```python
import torch

def _softmax(x, axis=-1):
    return torch.softmax(x, dim=axis)

def _gelu(x):
    return torch.nn.functional.gelu(x, approximate='tanh')

def _silu(x):
    return torch.nn.functional.silu(x)

def _sigmoid(x):
    return torch.sigmoid(x)

def _layernorm(x, axis=-1, eps=1e-05):
    m = x.mean(dim=axis, keepdim=True)
    v = x.var(dim=axis, keepdim=True, unbiased=False)
    return (x - m) / torch.sqrt(v + eps)

def _scatter_add(tgt, idx, src, axis=0):
    return tgt.clone().index_add_(axis, idx, src)

def _segment_sum(src, seg, n):
    out = torch.zeros((n,) + src.shape[1:], dtype=src.dtype)
    return out.index_add_(0, seg, src)

def _pad(x, pw):
    flat = tuple((v for pair in reversed(pw) for v in pair))
    return torch.nn.functional.pad(x, flat)
kernel_name = 'recurrent_scan__ssm_conv_h3__mixer_gmlp__prompt'

def setup_inputs():

    _w = lambda a: (lambda t: t.to(torch.bfloat16) if t.is_floating_point() else t)(torch.as_tensor(a))

    def _A_setup():
        B, S, D = (1, 2048, 640)
        ks = 4
        return {'q': _w((lambda *s: torch.randn(*s, dtype=torch.bfloat16) * 0.05)(B, S, D)), 'k': _w((lambda *s: torch.randn(*s, dtype=torch.bfloat16) * 0.2)(B, S, D)), 'v': _w((lambda *s: torch.randn(*s, dtype=torch.bfloat16) * 0.2)(B, S, D)), 'shift_k': _w((lambda *s: torch.randn(*s, dtype=torch.bfloat16) * 0.2)(D, ks) * 0.5), 'ssm_k': _w((lambda *s: torch.randn(*s, dtype=torch.bfloat16) * 0.2)(D, S) * (1.0 / S ** 0.5)), 'ssm_D': _w((lambda *s: torch.randn(*s, dtype=torch.bfloat16) * 0.2)(D))}
    _A_in = _A_setup()

    def _B_setup():
        B, S, D, F = (1, 2048, 640, 1536)
        Fh = F // 2
        return {'x': _w((lambda *s: torch.randn(*s, dtype=torch.bfloat16) * 0.05)(B, S, D)), 'w_in': _w((lambda *s: torch.randn(*s, dtype=torch.bfloat16) * 0.8)(D, 2 * Fh)), 'w_sp': _w(0.01 * (lambda *s: torch.randn(*s, dtype=torch.bfloat16) * 0.8)(S, S)), 'b_sp': _w(1.0 + 0.01 * (lambda *s: torch.randn(*s, dtype=torch.bfloat16) * 0.1)(S)), 'w_out': _w((lambda *s: torch.randn(*s, dtype=torch.bfloat16) * 0.1)(Fh, D))}
    _B_in = _B_setup()
    return {'A_q': _A_in['q'], 'A_k': _A_in['k'], 'A_v': _A_in['v'], 'A_shift_k': _A_in['shift_k'], 'A_ssm_k': _A_in['ssm_k'], 'A_ssm_D': _A_in['ssm_D'], 'B_x': _B_in['x'], 'B_w_in': _B_in['w_in'], 'B_w_sp': _B_in['w_sp'], 'B_b_sp': _B_in['b_sp'], 'B_w_out': _B_in['w_out']}

def reference(A_q, A_k, A_v, A_shift_k, A_ssm_k, A_ssm_D, B_x, B_w_in, B_w_sp, B_b_sp, B_w_out):

    def _A_ref(q, k, v, shift_k, ssm_k, ssm_D):
        B, S, D = q.shape
        ks = shift_k.shape[-1]
        k_pad = _pad(k, ((0, 0), (ks - 1, 0), (0, 0)))
        k_shift = torch.zeros_like(k)
        for t in range(ks):
            k_shift = k_shift + k_pad[:, t:t + S, :] * shift_k[:, ks - 1 - t][None, None, :]
        kv = k_shift * v
        L = 2 * S
        kv_pad = _pad(kv, ((0, 0), (0, S), (0, 0)))
        kf_pad = _pad(ssm_k, ((0, 0), (0, S)))
        kv_f = (lambda x, axes=(-2, -1): torch.fft.rfft2(x.float(), dim=axes))(kv_pad[:, None, :, :], axes=(1, 2))
        kf_f = (lambda x, axes=(-2, -1): torch.fft.rfft2(x.float(), dim=axes))(kf_pad[:, None, :], axes=(1, 2))
        kf_f = (lambda x, *d: x.permute(*d))(kf_f, 1, 2, 0)
        y_f = kv_f * kf_f[None, :, :, :]
        y = (lambda x, s, axes=(-2, -1): torch.fft.irfft2(x, s=s, dim=axes).to(torch.bfloat16))(y_f, (1, L), axes=(1, 2))[:, 0, :S, :]
        y = y + kv * ssm_D[None, None, :]
        return q * y

    def _B_ref(x, w_in, w_sp, b_sp, w_out):
        B, S, D = x.shape
        F = w_in.shape[-1]
        Fh = F // 2
        z = _gelu(torch.einsum('bsd,df->bsf', x, w_in))
        z1 = z[..., :Fh].clone()
        z2 = _layernorm(z[..., Fh:], axis=-1)
        g = torch.einsum('ts,bsf->btf', w_sp, z2) + b_sp[None, :, None]
        return torch.einsum('bsf,fd->bsd', z1 * g, w_out)
    x = A_q
    xn = _layernorm(x, axis=-1)
    s = _A_ref(xn, A_k, A_v, A_shift_k, A_ssm_k, A_ssm_D)
    g = _silu(_B_ref(xn, B_w_in, B_w_sp, B_b_sp, B_w_out))
    return x + s * g
```

```cpp
#define PROF_RT
#define HQ 0
#include <hip/hip_runtime.h>
#include <hip/hip_bf16.h>
#include <stdint.h>
#include <type_traits>

typedef unsigned short u16;
typedef __attribute__((ext_vector_type(8))) short bf16x8;
typedef __attribute__((ext_vector_type(4))) short s16x4;
typedef __attribute__((ext_vector_type(4))) float f32x4;
typedef __attribute__((ext_vector_type(16))) float f32x16;
typedef __attribute__((ext_vector_type(4))) unsigned int u32x4;
typedef __attribute__((ext_vector_type(3))) unsigned int u32x3;
typedef __attribute__((ext_vector_type(2))) unsigned int u32x2;

#define LDS_AS __attribute__((address_space(3)))
#define IC(n) std::integral_constant<int, n>{}
#define SB() __builtin_amdgcn_sched_barrier(0)

#define STAMP(k, i) do {} while (0)
#define STAMPW(k, i, w) do {} while (0)
#define TS_DECL do {} while (0)
#define TS(i) do {} while (0)
#define TS_OUT(k, base, n, w) do {} while (0)

__device__ unsigned long long g_rt[3][256][16][4];
#define RT(k, i) do { if ((threadIdx.x & 63) == 0) { g_rt[k][blockIdx.x][threadIdx.x >> 6][i] = __builtin_amdgcn_s_memrealtime(); g_rt[k][blockIdx.x][threadIdx.x >> 6][2 + i] = __builtin_amdgcn_s_memtime(); } } while (0)

#define AB_DECL do {} while (0)
#define AB(i) do {} while (0)
#define ABS(i) do {} while (0)
#define AB_OUT(n) do {} while (0)

#define AK_DECL do {} while (0)
#define AK(i) do {} while (0)
#define AK_OUT(n) do {} while (0)
#define MAPM 0
#define XE 0
constexpr int S_ = 2048, D_ = 640, FH_ = 768, F2_ = 1536;

__device__ __forceinline__ float bf2f(u16 v) { return __uint_as_float(((unsigned)v) << 16); }
typedef __attribute__((ext_vector_type(2))) __bf16 bf16x2n;
typedef __attribute__((ext_vector_type(2))) float f32x2;
__device__ __forceinline__ unsigned pack2(float lo, float hi) {
  f32x2 v = {lo, hi};
  bf16x2n r = __builtin_convertvector(v, bf16x2n);
  return __builtin_bit_cast(unsigned, r);
}
__device__ __forceinline__ float lo16(unsigned w) { return __uint_as_float(w << 16); }
__device__ __forceinline__ float hi16(unsigned w) { return __uint_as_float(w & 0xFFFF0000u); }
__device__ __forceinline__ u16 f2bf(float f) { return (u16)(pack2(f, f) & 0xFFFFu); }
__device__ __forceinline__ float rbf(float f) { return lo16(pack2(f, f)); }

template <int CTRL>
__device__ __forceinline__ float dppf(float v) {
  return __builtin_bit_cast(float, __builtin_amdgcn_update_dpp(0, __builtin_bit_cast(int, v), CTRL, 0xF, 0xF, true));
}
__device__ __forceinline__ float sum16(float v) {
  v += dppf<0xB1>(v); v += dppf<0x4E>(v); v += dppf<0x141>(v); v += dppf<0x140>(v);
  return v;
}
__device__ __forceinline__ float gelu_tanh(float x) {
  const float kBeta = 0.7978845608028654f;
  const float kKappa = 0.044715f;
  float inner = kBeta * (x + kKappa * x * x * x);
  return x * __builtin_amdgcn_rcpf(1.0f + __expf(-2.0f * inner));
}

template <int NSUB>
__device__ __forceinline__ unsigned boff(unsigned row, unsigned ch) {
  return (unsigned)(NSUB * 512) * (row >> 3) + 512u * (ch >> 2) + 64u * (row & 7) + 16u * ((ch & 3) ^ ((row >> 2) & 3));
}
template <int OFF>
__device__ __forceinline__ s16x4 tr_asm(unsigned a) {
  s16x4 r;
  asm volatile("ds_read_b64_tr_b16 %0, %1 offset:%2" : "=&v"(r) : "v"(a), "i"(OFF) : "memory");
  return r;
}
struct TrBase { unsigned b[2][2]; };
template <int NSUB>
__device__ __forceinline__ TrBase tr_base(unsigned lane, unsigned ldsbase) {
  const unsigned g = lane >> 4, q = (lane & 15) >> 2, p = lane & 3;
  TrBase t;
#pragma unroll
  for (int c1 = 0; c1 < 2; ++c1)
#pragma unroll
    for (int h = 0; h < 2; ++h)
      t.b[c1][h] = ldsbase + boff<NSUB>(8 * g + 4 * h + q, 2 * c1 + (p >> 1)) + 8 * (p & 1);
  return t;
}
template <int NSUB, int KS, int C>
__device__ __forceinline__ void tr_frag_issue(const TrBase& t, unsigned stage_off, s16x4& lo, s16x4& hi) {
  constexpr int OFF = NSUB * 512 * 4 * KS + 512 * (C >> 1);
  lo = tr_asm<OFF>(t.b[C & 1][0] + stage_off);
  hi = tr_asm<OFF>(t.b[C & 1][1] + stage_off);
}
__device__ __forceinline__ bf16x8 join8(s16x4 lo, s16x4 hi) {
  bf16x8 r;
  r[0] = lo[0]; r[1] = lo[1]; r[2] = lo[2]; r[3] = lo[3];
  r[4] = hi[0]; r[5] = hi[1]; r[6] = hi[2]; r[7] = hi[3];
  return r;
}
#define WAIT_LGKM0() asm volatile("s_waitcnt lgkmcnt(0)" ::: "memory")
__device__ __forceinline__ unsigned lds_addr(const void* p) {
  return (unsigned)(uintptr_t)(LDS_AS const char*)p;
}

#define RAW_BARRIER() do { asm volatile("s_waitcnt lgkmcnt(0)" ::: "memory"); __builtin_amdgcn_s_barrier(); } while (0)
#define WAIT_VM(n) asm volatile("s_waitcnt vmcnt(" #n ")" ::: "memory")

__device__ __forceinline__ void flag_wait(unsigned faddr, unsigned target) {
  while (true) {
    unsigned v;
    asm volatile("ds_read_b32 %0, %1\n\ts_waitcnt lgkmcnt(0)" : "=&v"(v) : "v"(faddr) : "memory");
    v = __builtin_amdgcn_readfirstlane(v);
    if (v >= target) break;
    __builtin_amdgcn_s_sleep(1);
  }
}
__device__ __forceinline__ void flag_wait_asm(unsigned faddr, unsigned target) {
  unsigned v, sv;
  asm volatile(".Lfw_%=:\n\tds_read_b32 %0, %2\n\ts_waitcnt lgkmcnt(0)\n\tv_readfirstlane_b32 %1, %0\n\ts_nop 0\n\t"
               "s_cmp_ge_u32 %1, %3\n\ts_cbranch_scc1 .Lfd_%=\n\ts_sleep 1\n\ts_branch .Lfw_%=\n\t.Lfd_%=:"
               : "=&v"(v), "=&s"(sv) : "v"(faddr), "s"(target) : "memory", "scc");
}
__device__ __forceinline__ void flag_signal(unsigned faddr, int lane) {
  if (lane == 0) {
    unsigned one = 1u;
    asm volatile("ds_add_u32 %0, %1" :: "v"(faddr), "v"(one) : "memory");
  }
}
__device__ __forceinline__ void glds16(const void* g, char* lds) {
  __builtin_amdgcn_global_load_lds((const unsigned*)g, (unsigned*)lds, 16, 0, 0);
}
__device__ __forceinline__ void glds_a(const u16* gbase, int ld, char* stageA, int ins, int lane) {
  const int row = ins * 8 + (lane >> 3);
  const int ch = (lane & 7) ^ (row & 7);
  glds16(gbase + (size_t)row * ld + ch * 8, stageA + ins * 1024);
}
__device__ __forceinline__ bf16x8 read_a(const char* stageA, int row, int kchunk) {
  return *(const bf16x8*)(stageA + row * 128 + ((kchunk ^ (row & 7)) << 4));
}
template <int NSUB, int BNV>
__device__ __forceinline__ void glds_b(const u16* gbase, int ld, char* stageB, int ins, int lane) {
  const int o = ins * 1024 + lane * 16;
  const int rg = o / (NSUB * 512), rem = o % (NSUB * 512);
  const int sub = rem >> 9, rr = (rem >> 6) & 7, slot = (rem >> 4) & 3;
  const int row = rg * 8 + rr;
  int ch = sub * 4 + (slot ^ ((row >> 2) & 3));
  if (BNV < NSUB * 32) ch = min(ch, BNV / 8 - 1);
  glds16(gbase + (size_t)row * ld + ch * 8, stageB + ins * 1024);
}

constexpr int L1_A_BYTES = 64 * 128;
constexpr int L1_B_BYTES = 64 * 192 * 2;
constexpr int L1_STAGE = L1_A_BYTES + L1_B_BYTES;
constexpr int L1_LDS = 5 * L1_STAGE;
constexpr unsigned HOLE = 0xFFFFFFFFu;
constexpr int Z1X = S_ * FH_ * 2;
constexpr int KVX = D_ * S_ * 2;
#define S0 7

__device__ __forceinline__ f32x2 unpk(unsigned w) { f32x2 r = {lo16(w), hi16(w)}; return r; }
__device__ __forceinline__ unsigned pk2(f32x2 v) { return pack2(v[0], v[1]); }
__device__ __forceinline__ f32x2 gelu2(f32x2 x) {
  const f32x2 c1 = {-2.3022082f, -2.3022082f}, c2 = {-0.10294324f, -0.10294324f};
  const f32x2 a = x * __builtin_elementwise_fma(x * x, c2, c1);
  f32x2 e = {__builtin_amdgcn_exp2f(a[0]), __builtin_amdgcn_exp2f(a[1])};
  e = e + (f32x2){1.0f, 1.0f};
  const f32x2 r = {__builtin_amdgcn_rcpf(e[0]), __builtin_amdgcn_rcpf(e[1])};
  return x * r;
}
__device__ __forceinline__ float allred_8(float v) {
  v += __uint_as_float(__builtin_amdgcn_mov_dpp(__float_as_uint(v), 0xB1, 0xF, 0xF, true));
  v += __uint_as_float(__builtin_amdgcn_mov_dpp(__float_as_uint(v), 0x4E, 0xF, 0xF, true));
  v += __uint_as_float(__builtin_amdgcn_mov_dpp(__float_as_uint(v), 0x141, 0xF, 0xF, true));
  return v;
}
__device__ __forceinline__ float allred_g(float v) {
  const auto a = __builtin_amdgcn_permlane16_swap(__float_as_uint(v), __float_as_uint(v), false, false);
  v = __uint_as_float(a[0]) + __uint_as_float(a[1]);
  const auto c = __builtin_amdgcn_permlane32_swap(__float_as_uint(v), __float_as_uint(v), false, false);
  return __uint_as_float(c[0]) + __uint_as_float(c[1]);
}

#define WT1 0
#define WTS "sc1"
#define ST16(p, v, bit) do { if (WT1 & (bit)) asm volatile("global_store_dwordx4 %0, %1, off " WTS "\n\ts_nop 1" :: "v"(p), "v"(v) : "memory"); else *(u32x4*)(p) = (v); } while (0)
__global__ __launch_bounds__(768) void k_l1(
    const u16* __restrict__ x, const u16* __restrict__ kin, const u16* __restrict__ vin,
    const u16* __restrict__ shift_k, const u16* __restrict__ w_in,
    u16* __restrict__ z1, u16* __restrict__ z2, float2* __restrict__ st2, float2* __restrict__ xst,
    u16* __restrict__ kvT, const u16* __restrict__ ssm_k, const u16* __restrict__ w_out, const u16* __restrict__ w_sp,
    u16* __restrict__ woF) {
  extern __shared__ __attribute__((aligned(16))) char smem[];
  const int tid = threadIdx.x, lane = tid & 63, wave = __builtin_amdgcn_readfirstlane(tid >> 6);
  const int b = blockIdx.x;
  const int xcd = b & 7, idx = b >> 3;
  const int rb = 8 * (xcd >> 1) + (idx >> 2), cb = 4 * (xcd & 1) + (idx & 3);
  const int row0 = rb * 64;
  const int n0 = cb * 192;
  const int g4 = lane >> 4, l15 = lane & 15;
  STAMP(0, 0);
  RT(0, 0);
  AK_DECL;
  AK(0);

  char* sE = smem + L1_STAGE;
  float2* sRow = (float2*)sE;
  float* sCs = (float*)(sE + 1024);
  float* sStat = (float*)(sE + 2048);
  char* sHand = sE + 8192;
  char* sTr = sE + 40960 + wave * 2048;
  const bool is_z2 = cb >= 4;
  auto epi = [&](const f32x4 (&t)[2][2], int wm, int cg, int swp, int rsw) {
    f32x4 csv[2];
#pragma unroll
    for (int jj = 0; jj < 2; ++jj) csv[jj] = *(const f32x4*)(sCs + cg * 32 + (jj ^ swp) * 16 + 4 * g4);
    f32x2 gv[2][2][2];
    unsigned pk[2][2][2];
#pragma unroll
    for (int i = 0; i < 2; ++i) {
      const float2 rs = sRow[wm * 32 + (i ^ rsw) * 16 + l15];
      const f32x2 nm = {-rs.x, -rs.x}, rd = {rs.y, rs.y};
#pragma unroll
      for (int jj = 0; jj < 2; ++jj)
#pragma unroll
        for (int h = 0; h < 2; ++h) {
          const f32x2 av = {t[i][jj][2 * h], t[i][jj][2 * h + 1]};
          const f32x2 cv = {csv[jj][2 * h], csv[jj][2 * h + 1]};
          gv[i][jj][h] = gelu2(__builtin_elementwise_fma(nm, cv, av) * rd);
          pk[i][jj][h] = pk2(gv[i][jj][h]);
        }
    }
    if (!is_z2) {
      const int lp = l15 + 16 * (2 * ((g4 & 1) ^ swp) + (g4 >> 1));
      char* zdst = (char*)z1 + (size_t)((rb * 4 + wm * 2) * 24 + cb * 6 + cg) * 1024 + lp * 16;
      u32x4 o[2];
#pragma unroll
      for (int i = 0; i < 2; ++i) {
        const auto sa = __builtin_amdgcn_permlane16_swap(pk[i][0][0], pk[i][1][0], false, false);
        const auto sb = __builtin_amdgcn_permlane16_swap(pk[i][0][1], pk[i][1][1], false, false);
        o[i][0] = sa[0]; o[i][1] = sb[0]; o[i][2] = sa[1]; o[i][3] = sb[1];
      }
#pragma unroll
      for (int i = 0; i < 2; ++i) ST16(zdst + (i ^ rsw) * 24 * 1024, ((u32x4){o[i][0], o[i][1], o[i][2], HOLE}), 1);
      *(u32x2*)((char*)z1 + Z1X + ((rb * 2 + wm) * 24 + cb * 6 + cg) * 512 + lp * 8) = (u32x2){rsw ? o[1][3] : o[0][3], rsw ? o[0][3] : o[1][3]};
    } else {
#pragma unroll
      for (int jj = 0; jj < 2; ++jj) {
        char* wb = sTr + l15 * 64 + 8 * ((4 * (jj ^ swp) + g4) ^ (l15 >> 1));
#pragma unroll
        for (int i = 0; i < 2; ++i) *(u32x2*)(wb + (i ^ rsw) * 1024) = (u32x2){pk[i][jj][0], pk[i][jj][1]};
      }
      const unsigned q = l15 >> 2, p = lane & 3;
      const unsigned ra = lds_addr(sTr) + (8 * g4 + q) * 64 + 8 * (p ^ (4 * (g4 & 1) + (q >> 1)));
      s16x4 tr[2][2];
      tr[0][0] = tr_asm<0>(ra); tr[0][1] = tr_asm<256>(ra ^ 16u);
      tr[1][0] = tr_asm<0>(ra ^ 32u); tr[1][1] = tr_asm<256>(ra ^ 48u);
      float sv[2][2], pv[2];
#pragma unroll
      for (int i = 0; i < 2; ++i) {
        const unsigned p0 = __float_as_uint(gv[i][0][0][0]);
        const auto pa = __builtin_amdgcn_permlane16_swap(p0, p0, false, false);
        const auto pb = __builtin_amdgcn_permlane32_swap(pa[0], pa[0], false, false);
        pv[i] = __uint_as_float(pb[0]);
        const f32x2 pp = {pv[i], pv[i]};
        const f32x2 d0 = gv[i][0][0] - pp, d1 = gv[i][0][1] - pp, d2 = gv[i][1][0] - pp, d3 = gv[i][1][1] - pp;
        const f32x2 s1 = (d0 + d1) + (d2 + d3);
        const f32x2 s2 = __builtin_elementwise_fma(d0, d0, d1 * d1) + __builtin_elementwise_fma(d2, d2, d3 * d3);
        sv[i][0] = s1[0] + s1[1]; sv[i][1] = s2[0] + s2[1];
      }
      const auto u0 = __builtin_amdgcn_permlane16_swap(__float_as_uint(sv[0][0]), __float_as_uint(sv[0][1]), false, false);
      const auto u1 = __builtin_amdgcn_permlane16_swap(__float_as_uint(sv[1][0]), __float_as_uint(sv[1][1]), false, false);
      const float w0 = __uint_as_float(u0[0]) + __uint_as_float(u0[1]), w1 = __uint_as_float(u1[0]) + __uint_as_float(u1[1]);
      const auto u2 = __builtin_amdgcn_permlane32_swap(__float_as_uint(w0), __float_as_uint(w1), false, false);
      float* sd = sStat + ((wm * 32 + ((g4 >> 1) ^ rsw) * 16 + l15) * 6 + cg) * 4 + (g4 & 1);
      sd[0] = __uint_as_float(u2[0]) + __uint_as_float(u2[1]);
      sd[2] = (g4 >> 1) ? pv[1] : pv[0];
      asm volatile("s_waitcnt lgkmcnt(0)" : "+v"(tr[0][0]), "+v"(tr[0][1]), "+v"(tr[1][0]), "+v"(tr[1][1]) :: "memory");
      char* zdst = (char*)z2 + (size_t)((rb * 2 + wm) * 48 + (cb - 4) * 12 + cg * 2) * 1024 + lane * 16;
      const bool tag = ((rb * 2 + wm) & 15) >= S0;
      u32x4 o[2];
#pragma unroll
      for (int jj = 0; jj < 2; ++jj) {
        o[jj] = __builtin_bit_cast(u32x4, join8(tr[jj][0], tr[jj][1]));
        ST16(zdst + jj * 1024, ((u32x4){o[jj][0], o[jj][1], o[jj][2], tag ? HOLE : o[jj][3]}), 2);
      }
      *(u32x2*)((char*)z2 + Z1X + ((rb * 2 + wm) * 24 + (cb - 4) * 6 + cg) * 512 + lane * 8) = (u32x2){o[0][3], o[1][3]};
    }
  };

  if (__builtin_expect(wave >= 4, 1)) {
    const int lw = wave - 4;
    const unsigned voA = (unsigned)((lw * 8 + (lane >> 3)) * (D_ * 2) + (((lane & 7) ^ (lane >> 3)) << 4));
    const unsigned voB = (unsigned)((lw * 8 + ((lane >> 2) & 7)) * (F2_ * 2) + (lane >> 5) * 64 + (((lane & 3) ^ ((lw * 2 + ((lane >> 4) & 1)) & 3)) << 4));
    const char* gA = (const char*)x + (size_t)row0 * (D_ * 2);
    const char* gB = (const char*)w_in + n0 * 2;
    const unsigned lbase = __builtin_amdgcn_readfirstlane(lds_addr(smem));
    auto issue = [&](int kt) {
      const unsigned st = lbase + (kt % 5) * L1_STAGE;
      asm volatile("s_mov_b32 m0, %2\n\ts_nop 0\n\tglobal_load_lds_dwordx4 %0, %1" :: "v"(voA), "s"(gA + kt * 128), "s"(st + lw * 1024) : "memory");
#pragma unroll
      for (int i = 0; i < 3; ++i)
        asm volatile("s_mov_b32 m0, %2\n\ts_nop 0\n\tglobal_load_lds_dwordx4 %0, %1"
                     :: "v"(voB), "s"(gB + (size_t)kt * (64 * F2_ * 2) + i * 128), "s"(st + L1_A_BYTES + (lw * 3 + i) * 1024) : "memory");
    };
#define LPRIO 2
    __builtin_amdgcn_s_setprio(LPRIO);
    issue(0);
    AK(1);
    __builtin_amdgcn_s_barrier();
    issue(1); issue(2);
    AK(2);
    const int ks0 = row0 + lw * 8, kd = cb * 80 + min(lane, 39) * 2;
    const int wfr = ((xcd >> 1) * 32 + idx) * 8 + lw, wfi = min(wfr, 959);
    u32x4 sw;
    const char* kvp[3];
#pragma unroll
    for (int i = 0; i < 3; ++i) {
      const int id = min(i * 64 + lane, 189), r = id / 10, c = id % 10;
      kvp[i] = (r < 11 ? (const char*)kin + (size_t)max(ks0 - 3 + r, 0) * (D_ * 2) : (const char*)vin + (size_t)(ks0 + r - 11) * (D_ * 2)) + cb * 160 + c * 16;
    }
    auto kvload = [&]() {
#pragma unroll
      for (int i = 0; i < 3; ++i)
        asm volatile("s_mov_b32 m0, %1\n\ts_nop 0\n\tglobal_load_lds_dwordx4 %0, off" :: "v"(kvp[i]), "s"(lbase + lw * 3072 + i * 1024) : "memory");
      asm volatile("s_mov_b32 m0, %2\n\ts_nop 0\n\tglobal_load_lds_dwordx4 %0, %1"
                   :: "v"((unsigned)((lane >> 1) * (D_ * 2) + (lane & 1) * 16)), "s"(w_out + (size_t)(wfi / 40) * 32 * D_ + (wfi % 40) * 16), "s"(lbase + 24576 + lw * 1024) : "memory");
      asm volatile("global_load_dwordx4 %0, %1, %2" : "=&v"(sw) : "v"(kd * 8), "s"(shift_k) : "memory");
    };
    auto womath = [&]() {
      const unsigned ra = lbase + 24576 + lw * 1024 + (8 * g4 + (l15 >> 2)) * 32 + 8 * (lane & 3);
      s16x4 lo = tr_asm<0>(ra), hi = tr_asm<128>(ra);
      asm volatile("s_waitcnt lgkmcnt(0)" : "+v"(lo), "+v"(hi) :: "memory");
      ST16((char*)woF + (size_t)wfi * 1024 + lane * 16, __builtin_bit_cast(u32x4, join8(lo, hi)), 4);
    };
    auto kvmath = [&]() {
      unsigned kk[11], vv[8];
      const char* kl = smem + lw * 3072 + min(lane, 39) * 4;
#pragma unroll
      for (int i = 0; i < 11; ++i) kk[i] = *(const unsigned*)(kl + i * 160);
#pragma unroll
      for (int i = 0; i < 8; ++i) vv[i] = *(const unsigned*)(kl + (11 + i) * 160);
      const f32x2 w0 = {lo16(sw[0]), lo16(sw[2])}, w1 = {hi16(sw[0]), hi16(sw[2])};
      const f32x2 w2 = {lo16(sw[1]), lo16(sw[3])}, w3 = {hi16(sw[1]), hi16(sw[3])};
      f32x2 kf[11], rr[8];
#pragma unroll
      for (int i = 0; i < 11; ++i) kf[i] = unpk((i < 3 && ks0 == 0) ? 0u : kk[i]);
#pragma unroll
      for (int i = 0; i < 8; ++i) {
        f32x2 a0 = kf[i] * w3;
        a0 = __builtin_elementwise_fma(kf[i + 1], w2, a0);
        a0 = __builtin_elementwise_fma(kf[i + 2], w1, a0);
        a0 = __builtin_elementwise_fma(kf[i + 3], w0, a0);
        rr[i] = unpk(pk2(a0)) * unpk(vv[i]);
      }
      u32x4 oa, ob;
#pragma unroll
      for (int e = 0; e < 4; ++e) { oa[e] = pack2(rr[2 * e][0], rr[2 * e + 1][0]); ob[e] = pack2(rr[2 * e][1], rr[2 * e + 1][1]); }
      if (lane < 40) {
        ST16(kvT + (size_t)kd * S_ + ks0, ((u32x4){oa[0], oa[1], oa[2], HOLE}), 8);
        ST16(kvT + (size_t)(kd + 1) * S_ + ks0, ((u32x4){ob[0], ob[1], ob[2], HOLE}), 8);
        *(u32x2*)((char*)kvT + KVX + kd * 1024 + ks0) = (u32x2){oa[3], ob[3]};
      }
    };
#define PFK 8
#define PFF 4
#define KVM 1
#define SPF 0
#define SP0 1
#define SPN 3
    unsigned long long pfs = 0;
    const char* spb = (const char*)w_sp + (size_t)(row0 + (SPF == 1 ? cb * 8 + lw : (idx & 3) * 16 + lw * 2)) * 4096;
    auto spf = [&](int i) {
      const int r = i & (SPF == 1 ? 0 : 1), k = SPF == 1 ? i : i >> 1;
      asm volatile("s_load_dwordx2 %0, %1, %2" : "+s"(pfs) : "s"(spb), "i"(r * 4096 + (k & 3) * 1024 + (k >> 2) * 256 + 124) : "memory");
    };
    constexpr int NPF = PFF == 1 ? 2 : 1;
    unsigned pfv, pfw = 0;
    auto prefetch = [&]() {
      const int t_ = tid - 256;
      if (PFF == 1) {
        const char* pw = (const char*)w_sp + (size_t)(row0 + (idx & 3) * 16 + (t_ >> 5)) * 4096 + (t_ & 31) * 128;
        const int t2 = t_ % 96;
        const char* pa = (const char*)ssm_k + (t2 < 64 ? (size_t)b * 8192 + t2 * 128 : (size_t)(512 + (b & 127)) * 4096 + (t2 & 31) * 128);
        asm volatile("global_load_dword %0, %1, off" : "=&v"(pfw) : "v"(pa) : "memory");
        asm volatile("global_load_dword %0, %1, off" : "=&v"(pfv) : "v"(pw) : "memory");
      } else {
        constexpr int NW = 64 * PFF;
        const int t2 = min(max(t_ - NW, 0), 95);
        const char* pw = (const char*)w_sp + (size_t)(row0 + (idx & 3) * 16 + (t_ / (4 * PFF)) % 16) * 4096 + ((t_ / PFF) & 3) * 1024 + (t_ % PFF) * 128;
        const char* pa = (const char*)ssm_k + (t2 < 64 ? (size_t)b * 8192 + t2 * 128 : (size_t)(512 + (b & 127)) * 4096 + (t2 & 31) * 128);
        const char* pc_ = (!SPF && t_ < NW) ? pw : pa;
        asm volatile("global_load_dword %0, %1, off" : "=&v"(pfv) : "v"(pc_) : "memory");
      }
    };
#pragma unroll
    for (int kt = 0; kt < 10; ++kt) {
      const int nv = (kt + 1 < 10 ? 4 : 0) + (kt + 2 < 10 ? 4 : 0) + (kt >= 8 ? 5 : 0) + ((PFK < kt && kt <= PFK + 3) ? NPF : 0);
      asm volatile("s_waitcnt vmcnt(%0)" :: "n"(nv) : "memory");
      __builtin_amdgcn_s_barrier();
      if (kt == 0) AK(3); if (kt == 1) AK(4); if (kt == 3) AK(5); if (kt == 5) AK(6); if (kt == 7) AK(7); if (kt == 9) AK(8);
      if (kt + 3 < 10) issue(kt + 3);
      if (kt == 7) kvload();
      if (kt == PFK) prefetch();
      if (SPF && kt >= SP0) {
#pragma unroll
        for (int i = (kt - SP0) * SPN; i < (kt - SP0 + 1) * SPN && i < 16 * SPF; ++i) spf(i);
      }
    }
    __builtin_amdgcn_s_setprio(0);
    if (KVM == 0) {
      asm volatile("s_waitcnt vmcnt(%1)" : "+v"(sw) : "n"(PFK >= 7 ? NPF : 0) : "memory");
      kvmath();
      if (!is_z2 && wfr < 960) womath();
    }
    SB();
    asm volatile("s_waitcnt lgkmcnt(0)" : "+s"(pfs) :: "memory");
    RAW_BARRIER();
    AK(9);
    {
      const int w = lw >> 1, gs = lw & 1;
      f32x4 t4[2][2];
#pragma unroll
      for (int q = 0; q < 4; ++q) t4[q >> 1][q & 1] = *(const f32x4*)(sHand + ((w * 2 + gs) * 4 + q) * 1024 + lane * 16);
      epi(t4, w >> 1, (w & 1) * 3 + 1 + gs, 0, 0);
    }
    AK(10);
    if (is_z2) RAW_BARRIER();
    AK(11);
    if (KVM == 1) {
      SB();
      if (is_z2) asm volatile("s_waitcnt vmcnt(%0)" :: "n"(2 + (PFK >= 7 ? NPF : 0)) : "memory");
      asm volatile("s_waitcnt vmcnt(%1)" : "+v"(sw) : "n"(3 + (PFK >= 7 ? NPF : 0)) : "memory");
      kvmath();
      if (!is_z2 && wfr < 960) womath();
    }
    AK(12);
    asm volatile("s_waitcnt vmcnt(0)" : "+v"(pfw), "+v"(pfv) :: "memory");
    AK(13);
    AK_OUT(14);
    RT(0, 1);
    return;
  }

  unsigned pcv, pfx;
  {
    unsigned long long pc; unsigned clen;
    asm volatile("s_getpc_b64 %0\n.Lpc_here_%=:\n\ts_mov_b32 %1, k_end-.Lpc_here_%=" : "=s"(pc), "=s"(clen));
    const unsigned o = idx < 2 ? min((unsigned)((idx * 4 + wave) * 8192 + lane * 128), clen - 4u) & ~3u : 0u;
    asm volatile("global_load_dword %0, %1, %2" : "=&v"(pcv) : "v"(o), "s"(pc) : "memory");
  }
  __builtin_amdgcn_s_barrier();
  auto pfx_issue = [&]() {
    const int t2 = max(tid - 80, 0), r2 = min(t2 / 3, 39);
    const char* pa = (const char*)x + (size_t)(row0 + (idx & 3) * 16 + tid / 5) * (D_ * 2) + 640 + (tid % 5) * 128;
    const char* pb = (const char*)w_in + (size_t)(320 + (idx >> 2) * 40 + r2) * (F2_ * 2) + n0 * 2 + (t2 % 3) * 128;
    const char* pc_ = tid < 80 ? pa : pb;
    asm volatile("global_load_dword %0, %1, off" : "=&v"(pfx) : "v"(pc_) : "memory");
  };
#define PFXM 0
  if (PFXM == 0) pfx_issue();
  if (PFXM == 2) pfx = 0;
  const int wm = wave >> 1, wn = wave & 1;
  f32x4 acc[2][6], acs[3], accS = {0.f, 0.f, 0.f, 0.f}, accG = {0.f, 0.f, 0.f, 0.f};
#pragma unroll
  for (int j = 0; j < 6; ++j) {
    acc[0][j] = (f32x4){0.f, 0.f, 0.f, 0.f};
    acc[1][j] = (f32x4){0.f, 0.f, 0.f, 0.f};
  }
#pragma unroll
  for (int j = 0; j < 3; ++j) acs[j] = (f32x4){0.f, 0.f, 0.f, 0.f};
  unsigned tb[2][2], aA[2][2];
#pragma unroll
  for (int c1 = 0; c1 < 2; ++c1)
#pragma unroll
    for (int h = 0; h < 2; ++h)
      tb[c1][h] = lds_addr(smem) + L1_A_BYTES + wn * 3 * 512 + boff<6>(8 * g4 + 4 * h + (l15 >> 2), 2 * (c1 ^ wm) + ((lane & 3) >> 1)) + 8 * (lane & 1);
#pragma unroll
  for (int ks = 0; ks < 2; ++ks)
#pragma unroll
    for (int i = 0; i < 2; ++i) aA[ks][i] = lds_addr(smem) + (wm * 32 + (i ^ wn) * 16 + l15) * 128 + (((ks * 4 + g4) ^ (l15 & 7)) << 4);
  bf16x8 ones;
#pragma unroll
  for (int e = 0; e < 8; ++e) ones[e] = (short)0x3F80;
  int cnt = 10, slot = 0, sdl;
  SB();
  __builtin_amdgcn_s_barrier();
  STAMP(0, 1);
  if (PFXM == 1) pfx_issue();
  asm volatile(
      "s_nop 1\n\t"
      "ds_read_b128 v[96:99], %21\n\t"
      "ds_read_b64_tr_b16 v[104:105], %17 offset:0\n\t"
      "ds_read_b64_tr_b16 v[106:107], %18 offset:0\n\t"
      "ds_read_b128 v[100:103], %22\n\t"
      "ds_read_b64_tr_b16 v[108:109], %19 offset:0\n\t"
      "ds_read_b64_tr_b16 v[110:111], %20 offset:0\n\t"
      "ds_read_b64_tr_b16 v[112:113], %17 offset:512\n\t"
      "ds_read_b64_tr_b16 v[114:115], %18 offset:512\n\t"
      "ds_read_b64_tr_b16 v[116:117], %19 offset:512\n\t"
      "ds_read_b64_tr_b16 v[118:119], %20 offset:512\n\t"
      "ds_read_b64_tr_b16 v[120:121], %17 offset:1024\n\t"
      "ds_read_b64_tr_b16 v[122:123], %18 offset:1024\n\t"
      "ds_read_b64_tr_b16 v[124:125], %19 offset:1024\n\t"
      "ds_read_b64_tr_b16 v[126:127], %20 offset:1024\n\t"
      ".Ll1_%=:\n\t"
      "s_waitcnt lgkmcnt(11)\n\t"
      "v_mfma_f32_16x16x32_bf16 %0, v[104:107], v[96:99], %0\n\t"
      "ds_read_b128 v[128:131], %23\n\t"
      "s_waitcnt lgkmcnt(11)\n\t"
      "v_mfma_f32_16x16x32_bf16 %6, v[104:107], v[100:103], %6\n\t"
      "ds_read_b64_tr_b16 v[136:137], %17 offset:12288\n\t"
      "s_waitcnt lgkmcnt(10)\n\t"
      "v_mfma_f32_16x16x32_bf16 %1, v[108:111], v[96:99], %1\n\t"
      "ds_read_b64_tr_b16 v[138:139], %18 offset:12288\n\t"
      "v_mfma_f32_16x16x32_bf16 %7, v[108:111], v[100:103], %7\n\t"
      "ds_read_b128 v[132:135], %24\n\t"
      "s_waitcnt lgkmcnt(10)\n\t"
      "v_mfma_f32_16x16x32_bf16 %2, v[112:115], v[96:99], %2\n\t"
      "ds_read_b64_tr_b16 v[140:141], %19 offset:12288\n\t"
      "v_mfma_f32_16x16x32_bf16 %8, v[112:115], v[100:103], %8\n\t"
      "ds_read_b64_tr_b16 v[142:143], %20 offset:12288\n\t"
      "s_waitcnt lgkmcnt(10)\n\t"
      "v_mfma_f32_16x16x32_bf16 %3, v[116:119], v[96:99], %3\n\t"
      "ds_read_b64_tr_b16 v[144:145], %17 offset:12800\n\t"
      "v_mfma_f32_16x16x32_bf16 %9, v[116:119], v[100:103], %9\n\t"
      "ds_read_b64_tr_b16 v[146:147], %18 offset:12800\n\t"
      "s_waitcnt lgkmcnt(10)\n\t"
      "v_mfma_f32_16x16x32_bf16 %4, v[120:123], v[96:99], %4\n\t"
      "ds_read_b64_tr_b16 v[148:149], %19 offset:12800\n\t"
      "v_mfma_f32_16x16x32_bf16 %10, v[120:123], v[100:103], %10\n\t"
      "ds_read_b64_tr_b16 v[150:151], %20 offset:12800\n\t"
      "s_waitcnt lgkmcnt(10)\n\t"
      "v_mfma_f32_16x16x32_bf16 %5, v[124:127], v[96:99], %5\n\t"
      "ds_read_b64_tr_b16 v[152:153], %17 offset:13312\n\t"
      "v_mfma_f32_16x16x32_bf16 %11, v[124:127], v[100:103], %11\n\t"
      "ds_read_b64_tr_b16 v[154:155], %18 offset:13312\n\t"
      "v_mfma_f32_16x16x32_bf16 %15, %28, v[96:99], %15\n\t"
      "ds_read_b64_tr_b16 v[156:157], %19 offset:13312\n\t"
      "v_mfma_f32_16x16x32_bf16 %16, v[96:99], v[96:99], %16\n\t"
      "ds_read_b64_tr_b16 v[158:159], %20 offset:13312\n\t"
      "v_mfma_f32_16x16x32_bf16 %12, v[104:107], %28, %12\n\t"
      "v_mfma_f32_16x16x32_bf16 %13, v[112:115], %28, %13\n\t"
      "v_mfma_f32_16x16x32_bf16 %14, v[120:123], %28, %14\n\t"
      "s_add_u32 %26, %26, 1\n\t"
      "s_cmp_eq_u32 %26, 5\n\t"
      "s_cselect_b32 %27, 0xfffd8000, 0\n\t"
      "s_cselect_b32 %26, 0, %26\n\t"
      "s_add_u32 %27, %27, 0x8000\n\t"
      "s_sub_u32 %25, %25, 1\n\t"
      "s_cmp_eq_u32 %25, 0\n\t"
      "s_cbranch_scc1 .Ll1s_%=\n\t"
      "s_barrier\n\t"
      ".Ll1s_%=:\n\t"
      "s_waitcnt lgkmcnt(11)\n\t"
      "v_mfma_f32_16x16x32_bf16 %0, v[136:139], v[128:131], %0\n\t"
      "v_add_u32 %21, %27, %21\n\t"
      "ds_read_b128 v[96:99], %21\n\t"
      "s_waitcnt lgkmcnt(11)\n\t"
      "v_mfma_f32_16x16x32_bf16 %6, v[136:139], v[132:135], %6\n\t"
      "v_add_u32 %17, %27, %17\n\t"
      "ds_read_b64_tr_b16 v[104:105], %17 offset:0\n\t"
      "s_waitcnt lgkmcnt(10)\n\t"
      "v_mfma_f32_16x16x32_bf16 %1, v[140:143], v[128:131], %1\n\t"
      "v_add_u32 %18, %27, %18\n\t"
      "ds_read_b64_tr_b16 v[106:107], %18 offset:0\n\t"
      "v_mfma_f32_16x16x32_bf16 %7, v[140:143], v[132:135], %7\n\t"
      "v_add_u32 %22, %27, %22\n\t"
      "ds_read_b128 v[100:103], %22\n\t"
      "s_waitcnt lgkmcnt(10)\n\t"
      "v_mfma_f32_16x16x32_bf16 %2, v[144:147], v[128:131], %2\n\t"
      "v_add_u32 %19, %27, %19\n\t"
      "ds_read_b64_tr_b16 v[108:109], %19 offset:0\n\t"
      "v_mfma_f32_16x16x32_bf16 %8, v[144:147], v[132:135], %8\n\t"
      "v_add_u32 %20, %27, %20\n\t"
      "ds_read_b64_tr_b16 v[110:111], %20 offset:0\n\t"
      "s_waitcnt lgkmcnt(10)\n\t"
      "v_mfma_f32_16x16x32_bf16 %3, v[148:151], v[128:131], %3\n\t"
      "v_add_u32 %23, %27, %23\n\t"
      "ds_read_b64_tr_b16 v[112:113], %17 offset:512\n\t"
      "v_mfma_f32_16x16x32_bf16 %9, v[148:151], v[132:135], %9\n\t"
      "v_add_u32 %24, %27, %24\n\t"
      "ds_read_b64_tr_b16 v[114:115], %18 offset:512\n\t"
      "s_waitcnt lgkmcnt(10)\n\t"
      "v_mfma_f32_16x16x32_bf16 %4, v[152:155], v[128:131], %4\n\t"
      "ds_read_b64_tr_b16 v[116:117], %19 offset:512\n\t"
      "v_mfma_f32_16x16x32_bf16 %10, v[152:155], v[132:135], %10\n\t"
      "ds_read_b64_tr_b16 v[118:119], %20 offset:512\n\t"
      "s_waitcnt lgkmcnt(10)\n\t"
      "v_mfma_f32_16x16x32_bf16 %5, v[156:159], v[128:131], %5\n\t"
      "ds_read_b64_tr_b16 v[120:121], %17 offset:1024\n\t"
      "v_mfma_f32_16x16x32_bf16 %11, v[156:159], v[132:135], %11\n\t"
      "ds_read_b64_tr_b16 v[122:123], %18 offset:1024\n\t"
      "v_mfma_f32_16x16x32_bf16 %15, %28, v[128:131], %15\n\t"
      "ds_read_b64_tr_b16 v[124:125], %19 offset:1024\n\t"
      "v_mfma_f32_16x16x32_bf16 %16, v[128:131], v[128:131], %16\n\t"
      "ds_read_b64_tr_b16 v[126:127], %20 offset:1024\n\t"
      "v_mfma_f32_16x16x32_bf16 %12, v[136:139], %28, %12\n\t"
      "v_mfma_f32_16x16x32_bf16 %13, v[144:147], %28, %13\n\t"
      "v_mfma_f32_16x16x32_bf16 %14, v[152:155], %28, %14\n\t"
      "s_cmp_lg_u32 %25, 0\n\t"
      "s_cbranch_scc1 .Ll1_%=\n\t"
      "s_waitcnt lgkmcnt(0)\n\t"
      "s_nop 15\n\t"
      "s_nop 7\n\t"
      : "+v"(acc[0][0]), "+v"(acc[0][1]), "+v"(acc[0][2]), "+v"(acc[0][3]), "+v"(acc[0][4]), "+v"(acc[0][5]),
        "+v"(acc[1][0]), "+v"(acc[1][1]), "+v"(acc[1][2]), "+v"(acc[1][3]), "+v"(acc[1][4]), "+v"(acc[1][5]),
        "+v"(acs[0]), "+v"(acs[1]), "+v"(acs[2]), "+v"(accS), "+v"(accG), "+v"(tb[0][0]), "+v"(tb[0][1]), "+v"(tb[1][0]), "+v"(tb[1][1]),
        "+v"(aA[0][0]), "+v"(aA[0][1]), "+v"(aA[1][0]), "+v"(aA[1][1]), "+s"(cnt), "+s"(slot), "=&s"(sdl)
      : "v"(ones)
      : "memory", "scc", "v96", "v97", "v98", "v99", "v100", "v101", "v102", "v103", "v104", "v105", "v106", "v107", "v108", "v109", "v110", "v111", "v112", "v113", "v114", "v115", "v116", "v117", "v118", "v119", "v120", "v121", "v122", "v123", "v124", "v125", "v126", "v127", "v128", "v129", "v130", "v131", "v132", "v133", "v134", "v135", "v136", "v137", "v138", "v139", "v140", "v141", "v142", "v143", "v144", "v145", "v146", "v147", "v148", "v149", "v150", "v151", "v152", "v153", "v154", "v155", "v156", "v157", "v158", "v159");
  STAMP(0, 3);
  AK(8);
#define PFC 0
  unsigned pfc = 0;
  auto pfc_issue = [&]() {
    const char* pw = (const char*)w_sp + (size_t)(row0 + (idx & 3) * 16 + (tid >> 4)) * 4096 + ((tid >> 2) & 3) * 1024 + (4 + (tid & 3)) * 128;
    asm volatile("global_load_dword %0, %1, off" : "=&v"(pfc) : "v"(pw) : "memory");
  };
  if (PFC == 1) pfc_issue();

#pragma unroll
  for (int gs = 0; gs < 2; ++gs)
#pragma unroll
    for (int q = 0; q < 4; ++q)
      *(f32x4*)(sHand + ((wave * 2 + gs) * 4 + (q ^ wm ^ (2 * wn))) * 1024 + lane * 16) = acc[q >> 1][2 + 2 * gs + (q & 1)];
  if (l15 == 0) {
#pragma unroll
    for (int j = 0; j < 3; ++j) *(f32x4*)(sCs + wn * 96 + (2 * j + wm) * 16 + 4 * g4) = acs[j];
  }
  {
    const int r = wm * 32 + wn * 16 + l15;
    const bool dg = g4 == (l15 >> 2);
    const float mean = accS[0] * (1.0f / 640.0f);
    const float s2 = (l15 & 2) ? ((l15 & 1) ? accG[3] : accG[2]) : ((l15 & 1) ? accG[1] : accG[0]);
    float sq = s2 - accS[0] * mean;
    if (__builtin_expect(__builtin_amdgcn_ballot_w64(dg && !(sq >= 1e-3f * accS[0] * mean)) != 0, 0)) {
      const f32x2 mm = {mean, mean};
      f32x2 q2 = {0.f, 0.f};
      const u32x4* xp = (const u32x4*)(x + (size_t)(row0 + r) * D_ + g4 * 160);
#pragma unroll 1
      for (int c = 0; c < 20; ++c) {
        const u32x4 xv = xp[c];
#pragma unroll
        for (int e = 0; e < 4; ++e) { const f32x2 d = unpk(xv[e]) - mm; q2 = __builtin_elementwise_fma(d, d, q2); }
      }
      sq = allred_g(q2[0] + q2[1]);
    }
    const float mbf = rbf(mean);
    const float vbf = rbf(fmaxf(sq, 0.f) * (1.0f / 640.0f));
    const float den = rbf(__builtin_amdgcn_sqrtf(rbf(vbf + 1e-5f)));
    const float rden = __builtin_amdgcn_rcpf(den);
    if (dg) {
      sRow[r] = make_float2(mbf, rden);
      if (cb == 0) xst[row0 + r] = make_float2(mbf, rden);
    }
  }
  TS_DECL;
  TS(0);
  AK(7);
  RAW_BARRIER();
  AK(9);
  TS(1);
  if (PFC == 2) pfc_issue();
  {
    f32x4 t4[2][2];
#pragma unroll
    for (int q = 0; q < 4; ++q) t4[q >> 1][q & 1] = acc[q >> 1][q & 1];
    epi(t4, wm, wn * 3, wm, wn);
  }
  TS(2); AK(10);
  if (is_z2) RAW_BARRIER();
  TS(3); AK(11);
  TS(4);
  if (is_z2 && tid < 64) {
    float mc[6];
    float sum = 0.f, m2 = 0.f;
#pragma unroll
    for (int c = 0; c < 6; ++c) {
      const f32x4 pp = *(const f32x4*)(sStat + (tid * 6 + c) * 4);
      const float dl = pp[0] * (1.0f / 32.0f);
      mc[c] = pp[2] + dl;
      sum += mc[c];
      m2 += fmaxf(pp[1] - pp[0] * dl, 0.f);
    }
    const float mb = sum * (1.0f / 6.0f);
    float dq = 0.f;
#pragma unroll
    for (int c = 0; c < 6; ++c) { const float dd = mc[c] - mb; dq += dd * dd; }
    st2[(size_t)(cb - 4) * S_ + row0 + tid] = make_float2(mb, m2 + 32.0f * dq);
  }
  TS(5); AK(12);
  asm volatile("s_waitcnt vmcnt(0)" : "+v"(pcv), "+v"(pfx), "+v"(pfc) :: "memory");
  TS(6); AK(13);
  AK_OUT(14);
  TS_OUT(0, 8, 7, TSW);
  STAMP(0, 4);
  RT(0, 1);
}

constexpr int CV_C = 4608, CV_CS = 4160, CV_BUF = CV_C + 2 * CV_CS;
__device__ __forceinline__ void conv_core(unsigned buf, int lane, f32x4 (&ac)[8]) {
  const int l15 = lane & 15, g4 = lane >> 4;
  const int c = (l15 + 1) & 1;
  unsigned aa[4];
#pragma unroll
  for (int k = 0; k < 4; ++k) aa[k] = buf + CV_C + c * CV_CS + (2031 - l15 + 8 * g4 - c) * 2 - 512 - 1024 * k;
  unsigned bb = buf + 512 + 32 * l15 - 32 + 16 * g4;
  u32x2 A[2][8][2];
  u32x4 B[2][8];
  constexpr int TT[36] = {7, 6, 5, 4, 7, 6, 5, 4, 7, 6, 5, 3, 7, 6, 5, 4, 7, 6, 3, 2, 7, 6, 5, 4, 7, 3, 2, 1, 7, 6, 5, 4, 3, 2, 1, 0};
  constexpr int AA[36] = {0, 0, 0, 0, 1, 1, 1, 1, 2, 2, 2, 0, 3, 3, 3, 2, 4, 4, 1, 0, 5, 5, 4, 3, 6, 2, 1, 0, 7, 6, 5, 4, 3, 2, 1, 0};
  auto ld1 = [&](int p, int n) {
    if (n < 16) {
      const int a = n >> 1, o = ((a & 1) ? 0 : 128) + 2 * (n & 1);
      asm volatile("ds_read2_b32 %0, %1 offset0:%2 offset1:%3" : "=&v"(A[p][(n >> 1) & 7][n & 1]) : "v"(aa[(a >> 1) & 3]), "i"(o), "i"(o + 1) : "memory");
    } else
      asm volatile("ds_read_b128 %0, %1 offset:%2" : "=&v"(B[p][n & 7]) : "v"(bb), "i"(512 * (n & 7)) : "memory");
  };
  auto next = [&](int dl) {
#pragma unroll
    for (int k = 0; k < 4; ++k) aa[k] += dl;
    bb += dl;
    SB();
  };
  auto land = [&](int p) {
    asm volatile("s_waitcnt lgkmcnt(0)" : "+v"(A[p][0][0]), "+v"(A[p][0][1]), "+v"(A[p][1][0]), "+v"(A[p][1][1]), "+v"(A[p][2][0]), "+v"(A[p][2][1]),
                 "+v"(A[p][3][0]), "+v"(A[p][3][1]), "+v"(A[p][4][0]), "+v"(A[p][4][1]), "+v"(A[p][5][0]), "+v"(A[p][5][1]), "+v"(A[p][6][0]), "+v"(A[p][6][1]),
                 "+v"(A[p][7][0]), "+v"(A[p][7][1]), "+v"(B[p][0]), "+v"(B[p][1]), "+v"(B[p][2]), "+v"(B[p][3]), "+v"(B[p][4]), "+v"(B[p][5]),
                 "+v"(B[p][6]), "+v"(B[p][7]) :: "memory");
    SB();
  };
  auto mm = [&](int p) {
#pragma unroll
    for (int n = 0; n < 36; ++n) {
      const int t = TT[n], a = AA[n];
      const u32x4 av = {A[p][a][0][0], A[p][a][0][1], A[p][a][1][0], A[p][a][1][1]};
      asm volatile("v_mfma_f32_16x16x32_bf16 %0, %1, %2, %0" : "+v"(ac[t]) : "v"(av), "v"(B[p][t - a]));
      if (n < 24) ld1(p ^ 1, n);
      SB();
    }
    land(p ^ 1);
  };
#pragma unroll
  for (int t = 0; t < 8; ++t) ac[t] = (f32x4){0.f, 0.f, 0.f, 0.f};
#pragma unroll
  for (int n = 0; n < 24; ++n) ld1(0, n);
  land(0);
#pragma unroll 1
  for (int r = 0; r < (XE & 1 ? 0 : 8); r += 2) {
    next(-64);
    mm(0);
    next(r < 6 ? -64 : 0);
    mm(1);
  }
  asm volatile("s_nop 15" : "+v"(ac[0]), "+v"(ac[1]), "+v"(ac[2]), "+v"(ac[3]), "+v"(ac[4]), "+v"(ac[5]), "+v"(ac[6]), "+v"(ac[7]));
}

#define GTB 1
#define PMAP 1
#define GNL 0
#define PSL "1"
#define LP3 3
#define HPL 1
#define HSC ""
#define XSRC hf
#define CDR 0
#define SPR 1
#define DLY -1
#define PSLN 2
#define NUP 2
#define SGS 13
#define CVT 4u
#define BARP 0
#define XNS S0
#define CLP 1
#define NRP 0
#define CVW 0
#define ROT 0
#define XAM 7
#define XBM 15
#define YMP 3
#define YEX 1
#define ANT ""
#define BNT ""
#define XAS 128
#define TBL 1
#define PW0 4
#define WPF 0
#define ZSL 0
#define ZS2 0
#define PPF 4
#define WTA 16
#define NW1 10
#define NW0 0
constexpr int M_FLAG = 0;
constexpr int M_RT = 128;
constexpr int M_MT = M_RT + 8192;
constexpr int M_CV = M_MT + 4096;
constexpr int M_RING = M_CV + 3 * CV_BUF;
constexpr int P3_C = 128;
constexpr int P3_G = P3_C + 72 * 1024;
constexpr int P3_Y = M_RING + 4 * 24576;
constexpr int M_LDS = P3_Y + 8 * 1536;
static_assert(P3_G + 68 * 1024 <= P3_Y && M_LDS <= 163840 && M_RING <= P3_G, "LDS map");
#define GAS __attribute__((address_space(1)))
__device__ __forceinline__ void flag_publish(unsigned* f) { __hip_atomic_store((GAS unsigned*)f, 1u, __ATOMIC_RELAXED, __HIP_MEMORY_SCOPE_AGENT); }
__device__ __forceinline__ unsigned umx(unsigned a, unsigned b) { return a > b ? a : b; }
__device__ __forceinline__ unsigned max4(const u32x4& v, unsigned m) { return umx(umx(umx(m, v[0]), v[1]), umx(v[2], v[3])); }
__device__ __forceinline__ unsigned nohole(unsigned a) { return a < HOLE - 1 ? a : HOLE - 1; }

__device__ __forceinline__ void row_stat(const float (&mj)[4], const float (&qj)[4], float& mean, float& r) {
  mean = 0.25f * ((mj[0] + mj[1]) + (mj[2] + mj[3]));
  const float d0 = mj[0] - mean, d1 = mj[1] - mean, d2 = mj[2] - mean, d3 = mj[3] - mean;
  const float m2 = (qj[0] + qj[1]) + (qj[2] + qj[3]) + 192.0f * ((d0 * d0 + d1 * d1) + (d2 * d2 + d3 * d3));
  const float vbf = rbf(m2 * (1.0f / 768.0f));
  r = __builtin_amdgcn_rcpf(rbf(__builtin_amdgcn_sqrtf(rbf(vbf + 1e-5f))));
}
struct NormLd { u32x3 z[6]; u32x2 x[3]; f32x2 p[4]; };
__device__ __forceinline__ void norm_load(NormLd& n, const u16* z2f, const float2* st2, int ks, int cb, int lane) {
#pragma unroll
  for (int j = 0; j < 4; ++j) n.p[j] = *(const f32x2*)(st2 + j * S_ + ks * 32 + (lane >> 4) * 8 + (lane & 7));
#pragma unroll
  for (int j = 0; j < 6; ++j) n.z[j] = *(const u32x3*)((const char*)z2f + (ks * 48 + cb * 6 + j) * 1024 + lane * 16);
#pragma unroll
  for (int j = 0; j < 3; ++j) n.x[j] = *(const u32x2*)((const char*)z2f + Z1X + (ks * 24 + cb * 3 + j) * 512 + lane * 8);
  SB();
}
template <int E> __device__ __forceinline__ f32x2 bc2(float v) { return (f32x2){dppf<0x150 + 2 * E>(v), dppf<0x151 + 2 * E>(v)}; }
__device__ __forceinline__ void norm_store(const NormLd& n, u16* z2f, int ks, int cb, int lane) {
  const float mj[4] = {n.p[0][0], n.p[1][0], n.p[2][0], n.p[3][0]}, qj[4] = {n.p[0][1], n.p[1][1], n.p[2][1], n.p[3][1]};
  float mean, r;
  row_stat(mj, qj, mean, r);
  mean = rbf(mean);
  const f32x2 me[4] = {bc2<0>(mean), bc2<1>(mean), bc2<2>(mean), bc2<3>(mean)}, re[4] = {bc2<0>(r), bc2<1>(r), bc2<2>(r), bc2<3>(r)};
  const auto zrs = __builtin_amdgcn_make_buffer_rsrc(z2f, 0, S_ * FH_ * 2, 0x00020000);
#pragma unroll
  for (int j = 0; j < 6; ++j) {
    u32x4 o;
#pragma unroll
    for (int e = 0; e < 4; ++e) o[e] = pk2((unpk(e < 3 ? n.z[j][e < 3 ? e : 0] : n.x[j >> 1][j & 1]) - me[e]) * re[e]);
    o[3] = nohole(o[3]);
    __builtin_amdgcn_raw_buffer_store_b128(o, zrs, (ks * 48 + cb * 6 + j) * 1024 + lane * 16, 0, WTA);
  }
}
__global__ __launch_bounds__(64) void k_n(u16* z2f, const float2* __restrict__ st2) {
  const int ks = blockIdx.x >> 3;
  if ((ks & 15) < S0) return;
  NormLd n;
  norm_load(n, z2f, st2, ks, blockIdx.x & 7, threadIdx.x);
  norm_store(n, z2f, ks, blockIdx.x & 7, threadIdx.x);
}

__global__ __launch_bounds__(512) void k_m(
    const u16* __restrict__ w_sp, const u16* __restrict__ b_sp, u16* hf,
    u16* z2f, const float2* __restrict__ st2, u16* yT,
    const u16* __restrict__ ssm_k, const u16* __restrict__ ssm_D, const u16* __restrict__ woF,
    const u16* __restrict__ x, const float2* __restrict__ xst, u16* __restrict__ out) {
  extern __shared__ __attribute__((aligned(16))) char smem[];
  const int tid = threadIdx.x, lane = tid & 63, wave = __builtin_amdgcn_readfirstlane(tid >> 6);
  const int b = blockIdx.x;
  const int xcd = b & 7, idx = b >> 3;
  const int rb = 8 * (xcd >> 1) + (idx >> 2), cb = 4 * (xcd & 1) + (idx & 3);
  const int pw = (wave + ROT * cb) & 7;
  const int t0 = rb * 64;
  const int g4 = lane >> 4, l15 = lane & 15;
  STAMP(2, 0);
  RT(1, 0);
  AB_DECL;
  AB(0);
  const unsigned flg = lds_addr(smem) + M_FLAG;
  bf16x8 Wq[15];
  const char* wg = (const char*)woF + (size_t)(pw * 120 + cb * 5) * 1024;
  const int mt = wave & 3, kh = wave >> 2, d0 = cb * 80;
  const int row = t0 + mt * 16 + l15;
  const size_t xo = (size_t)row * D_ + d0;
  u32x2 xv[3], st;
  const auto hrs = __builtin_amdgcn_make_buffer_rsrc(hf, 0, S_ * FH_ * 2, 0x00020000);
  auto ldP1 = [&](int n) {
    if (n < 15)
      asm volatile("global_load_dwordx4 %0, %1, %2 offset:%3" : "=&v"(Wq[n < 15 ? n : 0]) : "v"((unsigned)(lane * 16)), "s"(wg + (n / 5) * 40960 + 2048), "i"((n % 5) * 1024 - 2048) : "memory");
    else if (n < 18) {
      const int q = n < 18 ? n - 15 : 0;
      asm volatile("global_load_dwordx2 %0, %1, %2 offset:%3" : "=&v"(xv[q]) : "v"((unsigned)(l15 * D_ * 2 + 8 * g4)),
                   "s"(x + (size_t)(t0 + mt * 16) * D_ + d0 + (q < 2 ? 32 * kh : 64)), "i"(q == 1 ? 32 : 0) : "memory");
    } else
      asm volatile("global_load_dwordx2 %0, %1, %2" : "=&v"(st) : "v"((unsigned)(l15 * 8)), "s"(xst + t0 + mt * 16) : "memory");
  };
  int tmo = 1 << 16;
  u32x4 yv[2];
  auto yoff = [&](int n) {
    const int c = n ? 64 + (lane & 31) : lane, r = c >> 1;
    return (unsigned)(((r < 32 ? 32 * kh + r : 32 + r) * S_ + 8 * (c & 1)) * 2);
  };
  const u16* const yb = yT + (size_t)d0 * S_ + t0 + 16 * mt;
  auto ldy0 = [&]() {
#pragma unroll
    for (int n = 0; n < 2; ++n) asm volatile("global_load_dwordx4 %0, %1, %2 " HSC : "=&v"(yv[n]) : "v"(yoff(n)), "s"(yb) : "memory");
  };
  auto signal1 = [&](unsigned a) {
    const unsigned one = 1u;
    asm volatile("s_mov_b64 exec, 1\n\tds_add_u32 %0, %1\n\ts_mov_b64 exec, -1" :: "v"(a), "v"(one) : "memory");
  };
  auto drain = [&](int nc) {
    SB();
    asm volatile("s_waitcnt vmcnt(%21)"  : "+v"(Wq[0]), "+v"(Wq[1]), "+v"(Wq[2]), "+v"(Wq[3]), "+v"(Wq[4]), "+v"(Wq[5]), "+v"(Wq[6]), "+v"(Wq[7]),
                 "+v"(Wq[8]), "+v"(Wq[9]), "+v"(Wq[10]), "+v"(Wq[11]), "+v"(Wq[12]), "+v"(Wq[13]), "+v"(Wq[14]),
                 "+v"(xv[0]), "+v"(xv[1]), "+v"(xv[2]), "+v"(st), "+v"(yv[0]), "+v"(yv[1]) : "i"(nc) : "memory");
  };
  auto drain1 = [&](int nc) {
    SB();
#pragma unroll
    for (int n = 0; n < NW1; ++n) asm volatile("s_waitcnt vmcnt(%1)" : "+v"(Wq[n]) : "i"(nc) : "memory");
  };
  const int cw = wave & 3;
  const unsigned pfo = (unsigned)((lane >> 4) * 4096 + ((lane >> 2) & 3) * 1024 + (4 + (lane & 3)) * 128);
  f32x4 sp[4][2];
  auto ldst = [&](int q) {
#pragma unroll
    for (int j = 0; j < 4; ++j)
      asm volatile("global_load_dwordx4 %0, %1, %2 offset:%3" : "=&v"(sp[j][q]) : "v"((unsigned)(lane * 16)), "s"(st2 + j * S_ + cw * 512), "i"(q * 1024) : "memory");
    SB();
  };
  auto table = [&](int q, int nv) {
    asm volatile("s_waitcnt vmcnt(%4)" : "+v"(sp[0][q]), "+v"(sp[1][q]), "+v"(sp[2][q]), "+v"(sp[3][q]) : "i"(nv) : "memory");
    float mv[2], rv[2];
#pragma unroll
    for (int h = 0; h < 2; ++h) {
      const float mj[4] = {sp[0][q][2 * h], sp[1][q][2 * h], sp[2][q][2 * h], sp[3][q][2 * h]};
      const float qj[4] = {sp[0][q][2 * h + 1], sp[1][q][2 * h + 1], sp[2][q][2 * h + 1], sp[3][q][2 * h + 1]};
      row_stat(mj, qj, mv[h], rv[h]);
    }
    *(f32x2*)(smem + M_RT + cw * 2048 + q * 512 + lane * 8) = (f32x2){rv[0], rv[1]};
    *(unsigned*)(smem + M_MT + cw * 1024 + q * 256 + lane * 4) = pack2(mv[0], mv[1]);
    if (!GTB || q) flag_signal(flg + 16 + cw * 4, lane);
    SB();
  };
  if (wave >= 4) {
    TS_DECL;
    auto pre = [&]() {
      ldst(0);
      if (TBL) ldst(1);
      __builtin_amdgcn_s_barrier();
      SB();
      if (!TBL) ldst(1);
    };
    TS(0);
    if (cw < (b < 128 ? 3 : 2)) {
      if (!GTB) pre();
      const int d = cw < 2 ? 2 * b + cw : 512 + b;
      char* cb_ = smem + M_CV + cw * CV_BUF;
      u32x4 hc[4], uc[4];
      unsigned hp[4], ux[4];
      float Dv;
      auto ldc = [&]() {
#pragma unroll
        for (int k = 0; k < 4; ++k) {
          const int a = k * 64 + lane;
          hc[k] = *(const u32x4*)(ssm_k + (size_t)d * S_ + a * 8);
          hp[k] = *(const unsigned*)(ssm_k + (size_t)d * S_ + max(a * 8 - 2, 0));
          uc[k] = *(const u32x4*)(yT + (size_t)d * S_ + a * 8);
          ux[k] = *(const unsigned*)((const char*)yT + KVX + (d >> 1) * 2048 + a * 8 + (d & 1) * 4);
        }
        Dv = bf2f(ssm_D[d]);
        SB();
      };
      ldst(1);
      if (GTB == 1) ldc();
      ABS(1);
      __builtin_amdgcn_s_barrier();
      SB();
      ABS(2);
      if (GTB == 2) ldc();
      ABS(3);
      table(1, 16);
      ABS(4);
      TS(1); AB(1);
      if (lane < 36) *(u32x4*)(cb_ + (lane < 32 ? lane * 16 : CV_C + ((lane >> 1) & 1) * CV_CS + 4096 + (lane & 1) * 16)) = (u32x4){0u, 0u, 0u, 0u};
#pragma unroll
      for (int k = 0; k < 4; ++k) {
        const int a = k * 64 + lane;
        const unsigned w[5] = {a ? hp[k] : 0u, hc[k][0], hc[k][1], hc[k][2], hc[k][3]};
        u32x4 sw, mx;
#pragma unroll
        for (int i = 0; i < 4; ++i) {
          sw[i] = (w[4 - i] >> 16) | (w[4 - i] << 16);
          mx[i] = (w[4 - i] & 0xFFFFu) | (w[3 - i] & 0xFFFF0000u);
        }
        char* cp = cb_ + CV_C + (2040 - 8 * a) * 2;
        *(u32x4*)(cp) = sw;
        *(u32x4*)(cp + CV_CS) = mx;
        *(u32x4*)(cb_ + 512 + a * 16) = (u32x4){uc[k][0], uc[k][1], uc[k][2], ux[k]};
      }
      TS(2); AB(2); ABS(5);
      f32x4 ac[8];
      conv_core(lds_addr(cb_), lane, ac);
      AB(1);
      const auto yrs = __builtin_amdgcn_make_buffer_rsrc(yT, 0, D_ * S_ * 2, 0x00020000);
      u32x2 kvp[8];
#pragma unroll
      for (int t = 0; t < 8; ++t) kvp[t] = *(const u32x2*)(cb_ + 512 + (256 * t + 16 * l15 + 4 * g4) * 2);
#pragma unroll
      for (int k = 0; k < 4; ++k) {
        u32x2 ov[2];
#pragma unroll
        for (int t = 0; t < 2; ++t)
#pragma unroll
          for (int h = 0; h < 2; ++h) {
            const unsigned cv = pack2(ac[2 * k + t][2 * h], ac[2 * k + t][2 * h + 1]);
            const unsigned kd = pack2(lo16(kvp[2 * k + t][h]) * Dv, hi16(kvp[2 * k + t][h]) * Dv);
            ov[t][h] = pack2(lo16(cv) + lo16(kd), hi16(cv) + hi16(kd));
          }
        const auto s0 = __builtin_amdgcn_permlane16_swap(ov[0][0], ov[1][0], false, false);
        const auto s1 = __builtin_amdgcn_permlane16_swap(ov[0][1], ov[1][1], false, false);
        const u32x4 o4 = {s0[0], s1[0], s0[1], nohole(s1[1])};
        __builtin_amdgcn_raw_buffer_store_b128(o4, yrs, (d * S_ + 256 * (2 * k + (g4 & 1)) + 16 * l15 + 8 * (g4 >> 1)) * 2, 0, WTA);
      }
    } else {
      const int ks = 2 * (rb & 24) + (cw == 3 ? 8 : 0) + (rb & 7);
      NormLd nl;
      ldst(1);
      if (GNL) norm_load(nl, z2f, st2, ks, cb, lane);
      SB();
      __builtin_amdgcn_s_barrier();
      SB();
      if (!GNL) norm_load(nl, z2f, st2, ks, cb, lane);
      unsigned pf = 0;
      auto pfw = [&]() {
#pragma unroll
        for (int n = 0; n < 4; ++n)
          asm volatile("global_load_dword %0, %1, %2" : "+v"(pf) : "v"(pfo), "s"(w_sp + (size_t)(t0 + (idx & 3) * 16 + n * 4) * S_) : "memory");
        SB();
      };
      if (PPF == 1) pfw();
      if (!GTB && TBL != 2) table(0, 17);
      if (PPF == 2) pfw();
      if (NRP == 0) table(1, 13);
      if (PPF == 3) pfw();
      AB(1);
      if (out && (ks & 15) >= S0) norm_store(nl, z2f, ks, cb, lane);
      SB();
      if (NRP == 1) table(1, 0);
      if (PPF == 4 && cw == 3) pfw();
      __builtin_amdgcn_s_waitcnt(0x0F70);
      asm volatile("" : "+v"(pf));
      AB(2);
    }
    SB();
    TS(3); TS_OUT(2, 8, 4, 4); AB(3);
    if (!out) { RT(1, 1); return; }
    WAIT_LGKM0();
    flag_signal(flg + 40, lane);
    flag_wait(flg + 44, CVT);
    AB(4);
#pragma unroll
    for (int n = 0; n < NW1; ++n) ldP1(n);
    drain1(0);
  } else {
  if (tid < 32) *(unsigned*)(smem + M_FLAG + tid * 4) = 0u;
  const u16* Ab = w_sp + (size_t)t0 * S_ + wave * 512;
  const char* Bs = (const char*)z2f + (size_t)(wave * 16 * 48 + cb * 6) * 1024 + 2048;
  const unsigned boff = (unsigned)(lane * 16);
  char* ring = smem + M_RING + wave * 24576;
  const unsigned ringl = __builtin_amdgcn_readfirstlane(lds_addr(ring));
  const unsigned aoff = (unsigned)((lane >> 3) * 4096 + (((lane & 7) ^ (lane >> 3)) << 4));
  bf16x8 Bf[3][6];
  TS_DECL;
  auto ldA1 = [&](auto dc, int i) {
    constexpr int d = decltype(dc)::value;
    const unsigned ao = (XE & 4) && d >= 2 ? aoff & 0x70u : aoff, rl = ringl;
    const char* ab = (const char*)Ab + ((wave + 1 + (i >> 1)) & 3) * 65536;
    asm volatile("s_mov_b32 m0, %2\n\ts_nop 0\n\tglobal_load_lds_dwordx4 %0, %1 " ANT
                 :: "v"(ao), "s"(ab + ((i & 1) * 32768 + (d & XAM) * XAS)), "s"(rl + (d % 3) * 8192 + i * 1024) : "memory");
  };
  auto ldA = [&](auto dc) {
#pragma unroll
    for (int i = 0; i < 8; ++i) ldA1(dc, i);
  };
  auto ldB1 = [&](auto sc, int j) {
    constexpr int s = decltype(sc)::value;
    bf16x8& B = Bf[s % 3][j];
    const unsigned bo = (XE & 2) && s >= 3 ? boff & 0x70u : boff;
    const char* bs = Bs + (size_t)(s & XBM) * 49152;
    asm volatile("global_load_dwordx4 %0, %1, %2 offset:%3 " BNT : "=&v"(B) : "v"(bo), "s"(bs), "i"(j * 1024 - 2048) : "memory");
  };
  auto ldB = [&](auto sc) {
#pragma unroll
    for (int j = 0; j < 6; ++j) ldB1(sc, j);
  };
  if (GTB) ldst(0);
  ldA(IC(0)); ldB(IC(0));
  if (BARP == 0) RAW_BARRIER();
  if (BARP == 3) table(0, 14);
  ldB(IC(1));
  if (BARP == 4) table(0, 20);
  ldA(IC(1));
  unsigned pfg = 0;
  if (PPF == 6)
    asm volatile("global_load_dword %0, %1, %2" : "=&v"(pfg) : "v"(pfo), "s"(w_sp + (size_t)(t0 + (idx & 3) * 16 + wave * 4) * S_) : "memory");
  if (BARP == 1) RAW_BARRIER();
  TS(6);
  if (GTB) { if (BARP < 3) table(0, 28 + (PPF == 6)); }
  else flag_wait_asm(flg + 16 + wave * 4, 1u);
  if (BARP >= 2) RAW_BARRIER();
  f32x4 acc[4][6], cacc[4];
  const unsigned rta = lds_addr(smem) + M_RT + (wave * 512 + g4 * 8) * 4;
  const unsigned mta = lds_addr(smem) + M_MT + (wave * 512 + g4 * 8) * 2;
  unsigned ara[2];
#pragma unroll
  for (int ks = 0; ks < 2; ++ks) ara[ks] = lds_addr(ring) + l15 * 128 + (((ks * 4 + g4) ^ (l15 & 7)) << 4);
  u32x4 af[4];
  f32x4 rT[2][2]; u32x4 mT[2];
  auto rdA = [&](auto sc, int i) {
    constexpr int s = decltype(sc)::value;
    u32x4& o = af[i];
    const unsigned ad = ara[s & 1];
    asm volatile("ds_read_b128 %0, %1 offset:%2" : "=&v"(o) : "v"(ad), "i"(((s >> 1) % 3) * 8192 + i * 2048) : "memory");
  };
  auto rdT = [&](auto sc) {
    constexpr int s = decltype(sc)::value;
    f32x4 &o0 = rT[s & 1][0], &o1 = rT[s & 1][1]; u32x4& om = mT[s & 1];
    const unsigned ra = rta, ma = mta;
    asm volatile("ds_read_b128 %0, %1 offset:%2" : "=&v"(o0) : "v"(ra), "i"(s * 128) : "memory");
    asm volatile("ds_read_b128 %0, %1 offset:%2" : "=&v"(o1) : "v"(ra), "i"(s * 128 + 16) : "memory");
    asm volatile("ds_read_b128 %0, %1 offset:%2" : "=&v"(om) : "v"(ma), "i"(s * 64) : "memory");
  };
  constexpr int NSC = 20, NPM = 3;
  float tl, th;
  auto scop = [&](int i, int p, int n) {
    const int e = n / 5, k = n % 5;
    if (k == 0) tl = lo16(af[i][e]);
    else if (k == 1) th = hi16(af[i][e]);
    else if (k == 2) tl = __builtin_fmaf(tl, rT[p][e >> 1][(e & 1) * 2], 0.0f);
    else if (k == 3) th *= rT[p][e >> 1][(e & 1) * 2 + 1];
    else af[i][e] = pack2(tl, th);
  };
  TS(7);
  asm volatile("s_waitcnt vmcnt(%0) lgkmcnt(0)" :: "n"(20 + (PPF == 6)) : "memory");
  rdT(IC(0));
#pragma unroll
  for (int i = 0; i < 4; ++i) rdA(IC(0), i);
  asm volatile("s_waitcnt lgkmcnt(3)" : "+v"(af[0]), "+v"(rT[0][0]), "+v"(rT[0][1]), "+v"(mT[0]) :: "memory");
#pragma unroll
  for (int n = 0; n < NSC; ++n) scop(0, 0, n);
  SB();
  const unsigned eo = (unsigned)((l15 + 16 * (2 * (g4 & 1) + (g4 >> 1))) * 16);
  const size_t eb = (size_t)((rb * 4 + wave) * 24 + cb * 3) * 1024;
  u32x3 zv[3];
  unsigned zx[3], bsv;
  auto ldZ = [&]() {
#pragma unroll
    for (int q = 0; q < 3; ++q)
      asm volatile("global_load_dwordx3 %0, %1, %2 offset:%3" : "=&v"(zv[q]) : "v"(eo), "s"((const char*)hf + eb), "i"(q * 1024) : "memory");
#pragma unroll
    for (int q = 0; q < 3; ++q)
      asm volatile("global_load_dword %0, %1, %2 offset:%3" : "=&v"(zx[q]) : "v"(eo >> 1),
                   "s"((const char*)hf + Z1X + ((rb * 2 + (wave >> 1)) * 24 + cb * 3) * 512 + (wave & 1) * 4), "i"(q * 512) : "memory");
    asm volatile("global_load_ushort %0, %1, %2" : "=&v"(bsv) : "v"((unsigned)(l15 * 2)), "s"(b_sp + t0 + wave * 16) : "memory");
  };
  const unsigned xa = ringl + lane * 16, xc = ringl + lane * 4;
  auto step = [&](auto sc) {
    constexpr int s = decltype(sc)::value, sl = s % 3, p = s & 1;
    constexpr int NV = s == 0 ? 14 + (PPF == 6) : (s == 2 ? 28 : (s < 12 ? 20 : (s == 12 ? 12 : (LP3 ? 19 - 6 * (s - 13) : 19))));
    constexpr int DA = s == 0 ? 2 : (((s & 1) && (s + 5) / 2 < 8) ? (s + 5) / 2 : -1);
    asm volatile("s_waitcnt vmcnt(%7) lgkmcnt(2)\n\ts_nop 1"
                 : "+v"(Bf[sl][0]), "+v"(Bf[sl][1]), "+v"(Bf[sl][2]), "+v"(Bf[sl][3]), "+v"(Bf[sl][4]), "+v"(Bf[sl][5]), "+v"(af[1]) : "n"(NV) : "memory");
    if constexpr (s == 1) asm volatile("" : "+v"(pfg));
    if constexpr (s == 3) flag_wait_asm(flg + 16 + wave * 4, GTB ? 1u : 2u);
    if constexpr (s >= S0) {
      auto d3 = [&](int j) { return __builtin_bit_cast(u32x4, Bf[sl][j])[3]; };
      auto bad = [&]() { return __builtin_amdgcn_ballot_w64(umx(umx(umx(d3(0), d3(1)), d3(2)), umx(umx(d3(3), d3(4)), d3(5))) == HOLE) != 0; };
      if (__builtin_expect(bad(), 0)) {
        do {
          --tmo;
          asm volatile("s_sleep 2\n\tglobal_load_dwordx4 %0, %6, %7 offset:-2048 sc1\n\tglobal_load_dwordx4 %1, %6, %7 offset:-1024 sc1\n\t"
                       "global_load_dwordx4 %2, %6, %7 sc1\n\tglobal_load_dwordx4 %3, %6, %7 offset:1024 sc1\n\t"
                       "global_load_dwordx4 %4, %6, %7 offset:2048 sc1\n\tglobal_load_dwordx4 %5, %6, %7 offset:3072 sc1\n\ts_waitcnt vmcnt(0)"
                       : "+v"(Bf[sl][0]), "+v"(Bf[sl][1]), "+v"(Bf[sl][2]), "+v"(Bf[sl][3]), "+v"(Bf[sl][4]), "+v"(Bf[sl][5])
                       : "v"(boff), "s"(Bs + (size_t)s * 49152) : "memory");
        } while (bad() && tmo > 0);
      }
    }
    if constexpr (s == SGS) signal1(flg + 44);
    if constexpr (s < 15) ABS(s + 1);
    SB();
#pragma unroll
    for (int i = 0; i < 4; ++i) {
      if (i == 1) asm volatile("s_waitcnt lgkmcnt(%2)" : "+v"(af[2]), "+v"(af[3]) : "n"(s < 15 ? (s + 1 < S0 ? 4 : 1) : 0) : "memory");
      if (i == 3 && s + 1 < S0) asm volatile("s_waitcnt lgkmcnt(2)" : "+v"(af[0]), "+v"(rT[p ^ 1][0]), "+v"(rT[p ^ 1][1]), "+v"(mT[p ^ 1]) :: "memory");
      if (i == 3 && s + 1 >= S0 && s < 15) asm volatile("s_waitcnt lgkmcnt(2)" : "+v"(af[0]) :: "memory");
      const bf16x8 av = __builtin_bit_cast(bf16x8, af[i]);
#pragma unroll
      for (int j = 0; j < 7; ++j) {
        f32x4& ac = j < 6 ? acc[i][j < 6 ? j : 0] : cacc[i];
        const bf16x8 bb = j < 6 ? Bf[sl][j < 6 ? j : 0] : __builtin_bit_cast(bf16x8, mT[p]);
        if constexpr (s == 0) asm volatile("v_mfma_f32_16x16x32_bf16 %0, %1, %2, 0" : "=&v"(ac) : "v"(bb), "v"(av));
        else if (j < 6 || s < XNS) asm volatile("v_mfma_f32_16x16x32_bf16 %0, %1, %2, %0" : "+v"(ac) : "v"(bb), "v"(av));
        SB();
#pragma unroll
        for (int n = NPM * j; n < NPM * j + NPM && n < NSC; ++n) {
          if (i < 3) { if (s < XNS) scop(i + 1, p, n); }
          else if (s < 15 && s + 1 < XNS) scop(0, p ^ 1, n);
          SB();
        }
        if constexpr (s == 0) { if (i == 0 && j < 6) ldB1(IC(2), j); }
        if constexpr (DA >= 0) { if ((i == 1 || i == 2) && j < 4) ldA1(IC(DA < 0 ? 0 : DA), (i - 1) * 4 + j); }
        if constexpr (s + 3 < 16) { if (i == 3 && j < 6) ldB1(IC(s + 3), j); }
        if constexpr (!LP3 && (s == 13 || s == 14)) { if (i == 3 && j < 6) ldP1((s - 13) * 6 + j); }
        if constexpr (s == 15) {
          if (i > 0 && j > 0) asm volatile("ds_write_b128 %0, %1 offset:%2" :: "v"(xa), "v"(acc[i > 0 ? i - 1 : 0][j > 0 ? j - 1 : 0]), "i"((i - 1) * 6144 + (j - 1) * 1024) : "memory");
          if (!LP3) {
            if (i == 2 && j > 0) ldP1(11 + j);
            if (i == 3 && j == 1) ldP1(18);
            if (i == 3 && j == 2) ldy0();
          }
          if (LP3 == 2) { if (i >= 2 && j < 6) ldP1((i - 2) * 6 + j); }
          if (LP3 == 3) { if (i >= 2 && j < 6 && (i - 2) * 6 + j < NW0) ldP1((i - 2) * 6 + j); }
        }
        SB();
      }
      if constexpr (s == 15) {
        if (i > 0) {
          asm volatile("ds_write_b32 %0, %1 offset:%2" :: "v"(xc), "v"(cacc[i > 0 ? i - 1 : 0][0]), "i"(18432 + (i - 1) * 256),
                       "v"(cacc[i > 0 ? i - 1 : 0]) : "memory");
          signal1(flg + ((wave + i) & 3) * 4);
        }
      }
      asm volatile("s_nop 1");
      if constexpr (s < 15) { if (i == 0 && s + 1 < S0) rdT(IC(s + 1)); rdA(IC(s + 1), i); }
      SB();
    }
    if constexpr (s == 12) ldZ();
    SB();
  };
  TS(0); AB(1);
  step(IC(0)); TS(1); step(IC(1)); step(IC(2)); step(IC(3)); TS(2); AB(3); step(IC(4)); step(IC(5)); step(IC(6)); step(IC(7)); TS(3);
  step(IC(8)); step(IC(9)); step(IC(10)); step(IC(11)); TS(4); step(IC(12)); step(IC(13)); step(IC(14)); step(IC(15)); TS(5); AB(2);
  STAMP(2, 1);
  if (LP3 && LP3 != 3) {
#pragma unroll
    for (int n = LP3 == 2 ? 12 : 0; n < 19; ++n) ldP1(n);
    ldy0();
    SB();
  }
  if (LP3 == 3) {
#pragma unroll
    for (int n = NW0; n < NW1; ++n) ldP1(n);
    SB();
  }
  flag_wait_asm(flg + wave * 4, 3u);
  AB(4);
  f32x4 own[6];
  {
    f32x4 pr[3][6];
    float pc[3];
#pragma unroll
    for (int q = 0; q < 3; ++q) {
      const char* src = smem + M_RING + ((wave + 3 - q) & 3) * 24576;
      pc[q] = *(const float*)(src + 18432 + q * 256 + lane * 4);
#pragma unroll
      for (int j = 0; j < 6; ++j) pr[q][j] = *(const f32x4*)(src + q * 6144 + j * 1024 + lane * 16);
    }
    asm volatile("s_nop 7" : "+v"(acc[3][0]), "+v"(acc[3][1]), "+v"(acc[3][2]), "+v"(acc[3][3]), "+v"(acc[3][4]), "+v"(acc[3][5]), "+v"(cacc[3]));
    const float c = (cacc[3][0] + pc[0]) + (pc[1] + pc[2]);
#pragma unroll
    for (int j = 0; j < 6; ++j) own[j] = ((acc[3][j] - c) + pr[0][j]) + (pr[1][j] + pr[2][j]);
  }
  asm volatile("s_waitcnt vmcnt(%7)" : "+v"(zv[0]), "+v"(zv[1]), "+v"(zv[2]), "+v"(zx[0]), "+v"(zx[1]), "+v"(zx[2]), "+v"(bsv) : "i"(LP3 == 3 ? NW1 : 21) : "memory");
  SB();
  {
    const float bs = lo16(bsv);
    unsigned pk[6][2];
#pragma unroll
    for (int j = 0; j < 6; ++j)
#pragma unroll
      for (int h = 0; h < 2; ++h) {
        const unsigned r0 = pack2(own[j][2 * h], own[j][2 * h + 1]);
        pk[j][h] = pack2(lo16(r0) + bs, hi16(r0) + bs);
      }
#pragma unroll
    for (int q = 0; q < 3; ++q) {
      const auto s0 = __builtin_amdgcn_permlane16_swap(pk[2 * q][0], pk[2 * q + 1][0], false, false);
      const auto s1 = __builtin_amdgcn_permlane16_swap(pk[2 * q][1], pk[2 * q + 1][1], false, false);
      u32x4 gv; gv[0] = s0[0]; gv[1] = s1[0]; gv[2] = s0[1]; gv[3] = s1[1];
      u32x4 o4;
#pragma unroll
      for (int e = 0; e < 4; ++e) {
        const unsigned z = e < 3 ? zv[q][e < 3 ? e : 0] : zx[q];
        o4[e] = pack2(lo16(z) * lo16(gv[e]), hi16(z) * hi16(gv[e]));
      }
      o4[3] = nohole(o4[3]);
      __builtin_amdgcn_raw_buffer_store_b128(o4, hrs, (int)eb + eo + q * 1024, 0, WTA);
    }
  }
  TS_OUT(1, 0, 8, 0); TS_OUT(1, 8, 8, 3); AB(5);
  drain1(3);
  if (!out) { RT(1, 1); return; }
  STAMP(2, 2);
  }
  TS_DECL;
  TS(0);
  unsigned hoff[4];
#pragma unroll
  for (int it = 0; it < 4; ++it) hoff[it] = (unsigned)(((rb * 4 + ((mt + 1 + it) & 3)) * 24 + 3 * pw) * 1024 + lane * 16);
  u32x4 Hq[4][3];
  auto ldy = [&]() {
#pragma unroll
    for (int n = 0; n < 2; ++n) asm volatile("global_load_dwordx4 %0, %1, %2 sc1" : "=&v"(yv[n]) : "v"(yoff(n)), "s"(yb) : "memory");
  };
  auto ldh = [&](int it) {
#pragma unroll
    for (int k = 0; k < 3; ++k)
      asm volatile("global_load_dwordx4 %0, %1, %2 offset:%3 sc1" : "=&v"(Hq[it][k]) : "v"(hoff[it]), "s"(XSRC), "i"(k * 1024) : "memory");
  };
  auto ldh1 = [&](int it, int k) {
    asm volatile("global_load_dwordx4 %0, %1, %2 offset:%3 " HSC : "=&v"(Hq[it][k]) : "v"(hoff[it]), "s"(XSRC), "i"(k * 1024) : "memory");
  };
  auto ldh0 = [&](int it) {
#pragma unroll
    for (int k = 0; k < 3; ++k) ldh1(it, k);
  };
  f32x2 xf[3][2], sv[3][2];
  auto ymath = [&]() {
    char* yi = smem + P3_Y + wave * 1536;
#pragma unroll
    for (int n = 0; n < 2; ++n) *(u32x4*)(yi + (n ? 64 + (lane & 31) : lane) * 16) = yv[n];
    const unsigned ya = lds_addr(yi) + (4 * g4 + (l15 >> 2)) * 32 + 8 * (lane & 3);
    s16x4 yr[3] = {tr_asm<0>(ya), tr_asm<512>(ya), tr_asm<1024>(ya)};
    asm volatile("s_waitcnt lgkmcnt(0)" : "+v"(yr[0]), "+v"(yr[1]), "+v"(yr[2]) :: "memory");
    const f32x2 mean = {__uint_as_float(st[0]), __uint_as_float(st[0])}, rd = {__uint_as_float(st[1]), __uint_as_float(st[1])};
#pragma unroll
    for (int q = 0; q < 3; ++q) {
      const u32x2 yw = __builtin_bit_cast(u32x2, yr[q]);
#pragma unroll
      for (int h = 0; h < 2; ++h) {
        xf[q][h] = unpk(xv[q][h]);
        sv[q][h] = ((xf[q][h] - mean) * rd) * unpk(yw[h]);
        asm volatile("" : "+v"(sv[q][h]), "+v"(xf[q][h]));
      }
    }
    SB();
  };
#pragma unroll
  for (int n = NW1; n < 19; ++n) ldP1(n);
  if (YMP == 5) ldy0();
  SB();
  if (wave >= 4) AB(5);
  asm volatile("s_waitcnt vmcnt(%0)" :: "i"(19 - NW1 + (YMP == 5 ? 2 : 0)) : "memory");
  bool ybad;
  AB(6);
  signal1(flg + 48 + 4 * kh);
  flag_wait_asm(flg + 48, 4u);
  TS(1); AB(7);
  if (SPR) __builtin_amdgcn_s_setprio(3);
  ldh0(0);
  if (SPR) __builtin_amdgcn_s_setprio(2);
  ldh0(1);
  if (SPR) __builtin_amdgcn_s_setprio(1);
  if (NUP > 2) ldh0(2);
  if (SPR) __builtin_amdgcn_s_setprio(0);
  if (NUP > 3) ldh0(3);
  if (YMP < 3) ldy0();
  SB();
  AB(8);
  TS(2); AB(9);
  flag_wait_asm(flg + 40, kh ? 4u : 0u);
  TS(3);
  char* const px = smem + (kh ? P3_C + mt * 18432 : P3_G + mt * 17408) + lane * 16;
  f32x4 ac3[5], o[3];
  auto comp = [&](auto itc) {
    constexpr int it = decltype(itc)::value;
    constexpr int NW = 3 * ((it + NUP < 4 ? it + NUP : 4) - it - 1) + (LP3 == 3 && YMP != 5 && (YMP < 3 ? it < 2 : it >= 2) ? 2 : 0);
    asm volatile("s_waitcnt vmcnt(%3)" : "+v"(Hq[it][0]), "+v"(Hq[it][1]), "+v"(Hq[it][2]) : "n"(NW) : "memory");
    if constexpr (LP3 == 3 && it == 0)
      asm volatile("" : "+v"(Wq[0]), "+v"(Wq[1]), "+v"(Wq[2]), "+v"(Wq[3]), "+v"(Wq[4]), "+v"(Wq[5]), "+v"(Wq[6]), "+v"(Wq[7]),
                   "+v"(Wq[8]), "+v"(Wq[9]), "+v"(Wq[10]), "+v"(Wq[11]), "+v"(Wq[12]), "+v"(Wq[13]), "+v"(Wq[14]),
                   "+v"(xv[0]), "+v"(xv[1]), "+v"(xv[2]), "+v"(st) :: "memory");
    auto bad = [&]() { return __builtin_amdgcn_ballot_w64(umx(umx(Hq[it][0][3], Hq[it][1][3]), Hq[it][2][3]) == HOLE) != 0; };
    if (__builtin_expect(bad(), 0)) {
      do {
        --tmo;
        asm volatile("s_sleep 2\n\tglobal_load_dwordx4 %0, %3, %4 sc1\n\tglobal_load_dwordx4 %1, %3, %4 offset:1024 sc1\n\t"
                     "global_load_dwordx4 %2, %3, %4 offset:2048 sc1\n\ts_waitcnt vmcnt(0)"
                     : "+v"(Hq[it][0]), "+v"(Hq[it][1]), "+v"(Hq[it][2]) : "v"(hoff[it]), "s"(hf) : "memory");
      } while (bad() && tmo > 0);
    }
    SB();
#pragma unroll
    for (int k = 0; k < 3; ++k) {
#pragma unroll
      for (int j = 0; j < 5; ++j) {
        ac3[j] = __builtin_amdgcn_mfma_f32_16x16x32_bf16(Wq[k * 5 + j], __builtin_bit_cast(bf16x8, Hq[it][k]), k ? ac3[j] : (f32x4){0.f, 0.f, 0.f, 0.f}, 0, 0, 0);
        if constexpr (it + NUP < 4) { if (k == 0 && j < 3) { SB(); ldh1(it + NUP < 4 ? it + NUP : 0, j); SB(); } }
        if constexpr (YMP >= 3 && YMP != 5 && it == 1) { if (k == 1 && j == 0) { SB(); ldy0(); SB(); } }
      }
    }
    SB();
    if constexpr (it < 3) {
#pragma unroll
      for (int j = 0; j < 5; ++j) *(f32x4*)(px + (it * 5 + j) * 1024) = ac3[j];
      signal1(flg + 64 + kh * 16 + ((mt + 1 + it) & 3) * 4);
    }
    SB();
  };
  auto gather = [&](auto cc) {
    constexpr int cv = decltype(cc)::value;
    flag_wait_asm(flg + 64 + cv * 16 + mt * 4, cv ? 5u : 3u);
#pragma unroll
    for (int it = 0; it < 3 + cv; ++it) {
      const int ms = (mt - 1 - it) & 3;
      const char* ps = smem + ((it < 3 ? cv : !kh) ? P3_C + ms * 18432 : P3_G + ms * 17408) + lane * 16;
      const char* p0 = ps + (it < 3 ? it * 5 + 2 * kh : 15) * 1024;
      const f32x4 a0 = *(const f32x4*)p0, a1 = *(const f32x4*)(p0 + 1024);
      if (!cv && it == 0) { o[0] = a0; o[1] = a1; } else { o[0] += a0; o[1] += a1; }
    }
    if (!kh) {
#pragma unroll
      for (int it = 0; it < 3 + cv; ++it) {
        const int ms = (mt - 1 - it) & 3;
        const char* ps = smem + ((it < 3 ? cv : 1) ? P3_C + ms * 18432 : P3_G + ms * 17408) + lane * 16;
        o[2] += *(const f32x4*)(ps + (it < 3 ? it * 5 + 4 : 17) * 1024);
      }
    }
    SB();
  };
  o[2] = (f32x4){0.f, 0.f, 0.f, 0.f};
  comp(IC(0)); AB(10); comp(IC(1));
  if (YMP == 5) {
    asm volatile("" : "+v"(yv[0]), "+v"(yv[1]) :: "memory");
    if (__builtin_amdgcn_ballot_w64(umx(yv[0][3], yv[1][3]) == HOLE) != 0) {
      asm volatile("global_load_dwordx4 %0, %2, %4 sc1\n\tglobal_load_dwordx4 %1, %3, %4 sc1" : "+v"(yv[0]), "+v"(yv[1]) : "v"(yoff(0)), "v"(yoff(1)), "s"(yb) : "memory");
    }
    SB();
  }
  if (YMP == 1) {
    asm volatile("s_waitcnt vmcnt(6)" : "+v"(yv[0]), "+v"(yv[1]) :: "memory");
    ybad = __builtin_amdgcn_ballot_w64(umx(yv[0][3], yv[1][3]) == HOLE) != 0;
    ymath();
  }
  comp(IC(2));
  if (YMP == 2) {
    asm volatile("" : "+v"(yv[0]), "+v"(yv[1]) :: "memory");
    ybad = __builtin_amdgcn_ballot_w64(umx(yv[0][3], yv[1][3]) == HOLE) != 0;
    ymath();
  }
  TS(4); AB(11);
  gather(IC(0));
  if (YMP == 4) {
    asm volatile("s_waitcnt vmcnt(0)" : "+v"(yv[0]), "+v"(yv[1]) :: "memory");
    ybad = __builtin_amdgcn_ballot_w64(umx(yv[0][3], yv[1][3]) == HOLE) != 0;
    ymath();
  }
  AB(12);
  comp(IC(3));
  TS(5); AB(13);
  {
    *(f32x4*)(px + 15 * 1024) = kh ? ac3[0] : ac3[2];
    *(f32x4*)(px + 16 * 1024) = kh ? ac3[1] : ac3[3];
    if (kh) *(f32x4*)(px + 17 * 1024) = ac3[4];
    signal1(flg + 80 + mt * 4);
    SB();
    o[0] += kh ? ac3[2] : ac3[0]; o[1] += kh ? ac3[3] : ac3[1]; o[2] += ac3[4];
    if (YMP == 3 || YMP == 5) {
      asm volatile("s_waitcnt vmcnt(0)" : "+v"(yv[0]), "+v"(yv[1]) :: "memory");
      ybad = __builtin_amdgcn_ballot_w64(umx(yv[0][3], yv[1][3]) == HOLE) != 0;
    }
    if (__builtin_expect(ybad, 0)) {
      do {
        --tmo;
        ldy();
        asm volatile("s_waitcnt vmcnt(0)" : "+v"(yv[0]), "+v"(yv[1]) :: "memory");
      } while (__builtin_amdgcn_ballot_w64(umx(yv[0][3], yv[1][3]) == HOLE) != 0 && tmo > 0);
      if (YMP != 3 && YMP != 5) ymath();
    }
    if (YMP == 3 || YMP == 5) ymath();
    TS(6); AB(14);
    gather(IC(1));
    auto fin = [&](const f32x4& o4, int q, unsigned (&pk)[2]) {
#pragma unroll
      for (int h = 0; h < 2; ++h) {
        const f32x2 o = {o4[2 * h], o4[2 * h + 1]};
        const f32x2 a = o * (f32x2){-1.4426950f, -1.4426950f};
        const f32x2 e = (f32x2){__builtin_amdgcn_exp2f(a[0]), __builtin_amdgcn_exp2f(a[1])} + (f32x2){1.0f, 1.0f};
        const f32x2 r = {__builtin_amdgcn_rcpf(e[0]), __builtin_amdgcn_rcpf(e[1])};
        pk[h] = pk2(__builtin_elementwise_fma(o * r, sv[q][h], xf[q][h]));
      }
    };
    unsigned pa[2], pb[2];
    fin(o[0], 0, pa);
    fin(o[1], 1, pb);
    const auto s0 = __builtin_amdgcn_permlane16_swap(pa[0], pb[0], false, false);
    const auto s1 = __builtin_amdgcn_permlane16_swap(pa[1], pb[1], false, false);
    *(u32x4*)(out + xo + 32 * kh + 16 * (g4 & 1) + 8 * (g4 >> 1)) = (u32x4){s0[0], s1[0], s0[1], s1[1]};
    if (!kh) {
      fin(o[2], 2, pa);
      *(u32x2*)(out + xo + 64 + 4 * g4) = (u32x2){pa[0], pa[1]};
    }
  }
  TS(7); AB(15);
  AB_OUT(16);
  RT(1, 1);
}

extern "C" __global__ void k_end() {}

#define X4 0
#define LA4 4
constexpr int G3_RED = 4 * 3072;
constexpr int G3_LDS = G3_RED + 4 * 15 * 1024;

__global__ __launch_bounds__(256) void k_l4(
    const u16* __restrict__ hf, const u16* __restrict__ woF, const u16* __restrict__ x,
    const float2* __restrict__ xst, const u16* __restrict__ yT, u16* __restrict__ out) {
  extern __shared__ __attribute__((aligned(16))) char smem[];
  const int tid = threadIdx.x, lane = tid & 63, wave = __builtin_amdgcn_readfirstlane(tid >> 6);
  const int b = blockIdx.x;
  const int xcd = b & 7, idx = b >> 3;
  const int rb = 8 * (xcd >> 1) + (idx >> 2), cb = 4 * (xcd & 1) + (idx & 3);
  const int t0 = rb * 64, d0 = cb * 80;
  const int g4 = lane >> 4, l15 = lane & 15;
  RT(2, 0);
  TS_DECL;
  TS(0);
  const unsigned boff16 = (unsigned)(lane * 16);
  const char* Hb = (const char*)hf + (size_t)(rb * 4 * 24 + wave * 6) * 1024;
  const char* Wb = (const char*)woF + (size_t)(wave * 6 * 40 + cb * 5) * 1024;
  u32x4 Hf[6][4], Wf[6][5];
  auto ldH = [&](int s, int k) { Hf[s][k] = *(const u32x4*)(Hb + (size_t)((wave + 1 + k) & 3) * 24576 + s * 1024 + boff16); };
  auto ldG = [&](int s) {
#pragma unroll
    for (int j = 0; j < 5; ++j) Wf[s][j] = *(const u32x4*)(Wb + (size_t)s * 40960 + j * 1024 + boff16);
    ldH(s, 0); ldH(s, 1);
    SB();
  };
  const unsigned ly = __builtin_amdgcn_readfirstlane(lds_addr(smem) + wave * 3072);
  {
    const char* yb = (const char*)(yT + (size_t)d0 * S_ + t0 + wave * 16);
#pragma unroll
    for (int n = 0; n < 3; ++n) {
      const int c = min(n * 64 + lane, 159);
      asm volatile("s_mov_b32 m0, %2\n\ts_nop 0\n\tglobal_load_lds_dwordx4 %0, %1"
                   :: "v"((unsigned)((c >> 1) * (S_ * 2) + (c & 1) * 16)), "s"(yb), "s"(ly + n * 1024) : "memory");
    }
  }
  SB();
  ldG(0); ldG(1);
  const int row = t0 + wave * 16 + l15;
  const size_t xo = (size_t)row * D_ + d0;
  u32x2 xv[5];
  const float2 st = xst[row];
#pragma unroll
  for (int j = 0; j < 5; ++j) xv[j] = *(const u32x2*)(x + xo + 16 * j + 4 * g4);
  SB();
  ldG(2); ldG(3);
  TS(1);
  f32x4 acc[4][5];
  auto mm = [&](int s, int k) {
#pragma unroll
    for (int j = 0; j < 5; ++j) {
      const f32x4 c = s ? acc[k][j] : (f32x4){0.f, 0.f, 0.f, 0.f};
      acc[k][j] = __builtin_amdgcn_mfma_f32_16x16x32_bf16(__builtin_bit_cast(bf16x8, Wf[s][j]), __builtin_bit_cast(bf16x8, Hf[s][k]), c, 0, 0, 0);
    }
    SB();
  };
  auto put = [&](int k, int j) {
    *(f32x4*)(smem + G3_RED + (((wave + 1 + k) & 3) * 3 + k) * 5120 + j * 1024 + lane * 16) = acc[k][j];
    SB();
  };
  f32x4 pr[3][5];
  auto get = [&](int q) {
#pragma unroll
    for (int j = 0; j < 5; ++j) pr[q][j] = *(const f32x4*)(smem + G3_RED + (wave * 3 + q) * 5120 + j * 1024 + lane * 16);
    SB();
  };
  auto ldN = [&](int n) {
    if (n < 6) ldG(n);
    else if (n < 8) {
#pragma unroll
      for (int s = 0; s < 6; ++s) ldH(s, n - 4);
      SB();
    }
  };
  mm(0, 0); mm(0, 1); ldN(LA4);
  s16x4 yr[5];
  {
    const unsigned ya = ly + (4 * g4 + (l15 >> 2)) * 32 + 8 * (lane & 3);
#pragma unroll
    for (int j = 0; j < 5; ++j)
      asm volatile("ds_read_b64_tr_b16 %0, %1 offset:%2" : "=&v"(yr[j]) : "v"(ya), "i"(j * 512) : "memory");
  }
  mm(1, 0); mm(1, 1); ldN(LA4 + 1);
  asm volatile("s_waitcnt lgkmcnt(0)" : "+v"(yr[0]), "+v"(yr[1]), "+v"(yr[2]), "+v"(yr[3]), "+v"(yr[4]) :: "memory");
  f32x2 xf[5][2], sv[5][2];
  {
    const f32x2 mean = {st.x, st.x}, rd = {st.y, st.y};
#pragma unroll
    for (int j = 0; j < 5; ++j) {
      const u32x2 yw = __builtin_bit_cast(u32x2, yr[j]);
#pragma unroll
      for (int h = 0; h < 2; ++h) {
        xf[j][h] = unpk(xv[j][h]);
        sv[j][h] = ((xf[j][h] - mean) * rd) * unpk(yw[h]);
      }
    }
    SB();
  }
  mm(2, 0); mm(2, 1); ldN(LA4 + 2);
  mm(3, 0); mm(3, 1); ldN(LA4 + 3);
  mm(4, 0); mm(4, 1);
  mm(5, 0); mm(5, 1);
  TS(2);
  mm(0, 2); put(0, 0); put(0, 1); put(0, 2);
  mm(1, 2); put(0, 3); put(0, 4); put(1, 0);
  mm(2, 2); put(1, 1); put(1, 2); put(1, 3);
  mm(3, 2); put(1, 4);
  mm(4, 2);
  mm(5, 2);
  TS(3);
  RAW_BARRIER();
  SB();
  get(0);
  mm(0, 3);
  put(2, 0); put(2, 1); put(2, 2); put(2, 3); put(2, 4);
  get(1);
  mm(1, 3);
  mm(2, 3);
  TS(4);
  RAW_BARRIER();
  SB();
  TS(5);
  get(2);
#pragma unroll
  for (int j = 0; j < 5; ++j) pr[0][j] += pr[1][j];
  SB();
  mm(3, 3);
  mm(4, 3);
#pragma unroll
  for (int j = 0; j < 5; ++j) pr[0][j] += pr[2][j];
  SB();
  mm(5, 3);
  TS(6);
  unsigned pk[5][2];
#pragma unroll
  for (int j = 0; j < 5; ++j) {
    const f32x4 o4 = acc[3][j] + pr[0][j];
#pragma unroll
    for (int h = 0; h < 2; ++h) {
      const f32x2 o = {o4[2 * h], o4[2 * h + 1]};
      const f32x2 a = o * (f32x2){-1.4426950f, -1.4426950f};
      const f32x2 e = (f32x2){__builtin_amdgcn_exp2f(a[0]), __builtin_amdgcn_exp2f(a[1])} + (f32x2){1.0f, 1.0f};
      const f32x2 r = {__builtin_amdgcn_rcpf(e[0]), __builtin_amdgcn_rcpf(e[1])};
      pk[j][h] = pk2(__builtin_elementwise_fma(o * r, sv[j][h], xf[j][h]));
    }
  }
  const int dq = 16 * (g4 & 1) + 8 * (g4 >> 1);
#pragma unroll
  for (int q = 0; q < 2; ++q) {
    const auto s0 = __builtin_amdgcn_permlane16_swap(pk[2 * q][0], pk[2 * q + 1][0], false, false);
    const auto s1 = __builtin_amdgcn_permlane16_swap(pk[2 * q][1], pk[2 * q + 1][1], false, false);
    u32x4 o4; o4[0] = s0[0]; o4[1] = s1[0]; o4[2] = s0[1]; o4[3] = s1[1];
    *(u32x4*)(out + xo + 32 * q + dq) = o4;
  }
  *(u32x2*)(out + xo + 64 + 4 * g4) = (u32x2){pk[4][0], pk[4][1]};
  TS(7);
  RT(2, 1);
}

__global__ __launch_bounds__(256) void k_dly() {
  const unsigned long long t0 = __builtin_amdgcn_s_memrealtime();
  __shared__ unsigned long long sm[6][256];
  const int b = threadIdx.x;
  unsigned long long s0[2], s1[2], e1[2];
  for (int k = 0; k < 2; ++k) {
    s0[k] = ~0ull; s1[k] = 0; e1[k] = 0;
    for (int w = 0; w < 16; ++w) {
      const unsigned long long a = g_rt[k][b][w][0], e = g_rt[k][b][w][1];
      if (a) { s0[k] = a < s0[k] ? a : s0[k]; s1[k] = a > s1[k] ? a : s1[k]; e1[k] = e > e1[k] ? e : e1[k]; }
    }
    sm[3 * k][b] = s0[k]; sm[3 * k + 1][b] = s1[k]; sm[3 * k + 2][b] = e1[k];
  }
  __syncthreads();
  if (b == 0) {
    unsigned long long S0_[2] = {~0ull, ~0ull}, S1_[2] = {0, 0}, E1_[2] = {0, 0}, dsum[2] = {0, 0};
    for (int k = 0; k < 2; ++k)
      for (int i = 0; i < 256; ++i) {
        S0_[k] = sm[3 * k][i] < S0_[k] ? sm[3 * k][i] : S0_[k];
        S1_[k] = sm[3 * k + 1][i] > S1_[k] ? sm[3 * k + 1][i] : S1_[k];
        E1_[k] = sm[3 * k + 2][i] > E1_[k] ? sm[3 * k + 2][i] : E1_[k];
        dsum[k] += sm[3 * k + 2][i] - sm[3 * k][i];
      }
    unsigned long long q = 0;
    if (HQ == 1) q = E1_[0] - S0_[0];
    if (HQ == 2) q = S0_[1] - E1_[0];
    if (HQ == 3) q = E1_[1] - S0_[1];
    if (HQ == 4) q = t0 - E1_[1];
    if (HQ == 5) q = S1_[0] - S0_[0];
    if (HQ == 6) q = dsum[0] / 256;
    if (HQ == 7) q = dsum[1] / 256;
    if (HQ == 8) q = (g_rt[0][5][0][3] - g_rt[0][5][0][2]) * 100 / (g_rt[0][5][0][1] - g_rt[0][5][0][0]);
    if (HQ == 9) q = (g_rt[1][5][0][3] - g_rt[1][5][0][2]) * 100 / (g_rt[1][5][0][1] - g_rt[1][5][0][0]);
    if (q > 5000) q = 5000;
    const unsigned long long te = t0 + 2000 + 10 * q;
    while (__builtin_amdgcn_s_memrealtime() < te) __builtin_amdgcn_s_sleep(1);
  }
}
extern "C" void kernel_launch(void* const* d_in, const int* in_sizes, int n_in,
                              void* d_out, int out_size, void* d_ws, size_t ws_size,
                              hipStream_t stream) {
  const u16* A_q = (const u16*)d_in[0];
  const u16* A_k = (const u16*)d_in[1];
  const u16* A_v = (const u16*)d_in[2];
  const u16* A_shift_k = (const u16*)d_in[3];
  const u16* A_ssm_k = (const u16*)d_in[4];
  const u16* A_ssm_D = (const u16*)d_in[5];
  const u16* B_w_in = (const u16*)d_in[7];
  const u16* B_w_sp = (const u16*)d_in[8];
  const u16* B_b_sp = (const u16*)d_in[9];
  const u16* B_w_out = (const u16*)d_in[10];
  u16* out = (u16*)d_out;

  char* ws = (char*)d_ws;
  size_t off = 0;
  auto carve = [&](size_t bytes) { char* p = ws + off; off += (bytes + 255) & ~(size_t)255; return p; };
  u16* z1 = (u16*)carve((size_t)S_ * FH_ * 2 * 5 / 4);
  u16* z2 = (u16*)carve((size_t)S_ * FH_ * 2 * 5 / 4);
  float2* st2 = (float2*)carve((size_t)4 * S_ * 8);
  float2* xst = (float2*)carve((size_t)S_ * 8);
  u16* kvT = (u16*)carve((size_t)D_ * S_ * 2 * 5 / 4);
  u16* woF = (u16*)carve((size_t)FH_ * D_ * 2);

  int dev = 0, cus = 0;
  (void)hipGetDevice(&dev);
  (void)hipDeviceGetAttribute(&cus, hipDeviceAttributeMultiprocessorCount, dev);
  const bool fused = cus >= 256;
  (void)hipFuncSetAttribute((const void*)k_l1, hipFuncAttributeMaxDynamicSharedMemorySize, L1_LDS);
  (void)hipFuncSetAttribute((const void*)k_m, hipFuncAttributeMaxDynamicSharedMemorySize, M_LDS);
  k_l1<<<256, 768, L1_LDS, stream>>>(A_q, A_k, A_v, A_shift_k, B_w_in, z1, z2, st2, xst, kvT, A_ssm_k, B_w_out, B_w_sp, woF);
  if (!fused) k_n<<<512, 64, 0, stream>>>(z2, st2);
  k_m<<<256, 512, M_LDS, stream>>>(B_w_sp, B_b_sp, z1, z2, st2, kvT, A_ssm_k, A_ssm_D, woF, A_q, xst, fused ? out : nullptr);
  if (!fused) {
    (void)hipFuncSetAttribute((const void*)k_l4, hipFuncAttributeMaxDynamicSharedMemorySize, G3_LDS);
    k_l4<<<256, 256, G3_LDS, stream>>>(z1, woF, A_q, xst, kvT, out);
  }
  k_dly<<<1, 256, 0, stream>>>();
}
```

```cpp
#include <hip/hip_runtime.h>
#include <hip/hip_bf16.h>
#include <stdint.h>
#include <type_traits>

typedef unsigned short u16;
typedef unsigned long long u64;
typedef __attribute__((ext_vector_type(8))) short bf16x8;
typedef __attribute__((ext_vector_type(4))) short s16x4;
typedef __attribute__((ext_vector_type(4))) float f32x4;
typedef __attribute__((ext_vector_type(2))) float f32x2;
typedef __attribute__((ext_vector_type(4))) unsigned int u32x4;
typedef __attribute__((ext_vector_type(2))) unsigned int u32x2;
typedef __attribute__((ext_vector_type(2))) __bf16 bf16x2n;

#define LDS_AS __attribute__((address_space(3)))
#define IC(n) std::integral_constant<int, (n)>{}
#define SB() __builtin_amdgcn_sched_barrier(0)

#define AB_DECL do {} while (0)
#define AB(i) do {} while (0)
#define AB_OUT() do {} while (0)
#define CB(i) do {} while (0)
constexpr int S_ = 2048, D_ = 640, FH_ = 768, F2_ = 1536;
constexpr unsigned HOLE = 0xFFFFFFFFu;

constexpr size_t WS_EP = 0;
constexpr size_t WS_EF = 4096;
constexpr size_t WS_LF = 6144;
constexpr size_t WS_YF = 8192;
constexpr size_t WS_XF = 16384;
constexpr size_t WS_ST = 32768;
constexpr size_t WS_Z2 = WS_ST + 32 * 12 * 1024;
constexpr size_t WS_H = WS_Z2 + 64 * 48 * 1024;
constexpr size_t WS_ZX = WS_H + 128 * 24 * 1024;
constexpr size_t WS_KV = WS_ZX + 32 * 4 * 4096;
constexpr size_t WS_Y = WS_KV + D_ * S_ * 2;
constexpr size_t WS_WO = WS_Y + D_ * S_ * 2;

__device__ __forceinline__ float bf2f(u16 v) { return __uint_as_float(((unsigned)v) << 16); }
__device__ __forceinline__ unsigned pack2(float lo, float hi) {
  f32x2 v = {lo, hi};
  bf16x2n r = __builtin_convertvector(v, bf16x2n);
  return __builtin_bit_cast(unsigned, r);
}
__device__ __forceinline__ float lo16(unsigned w) { return __uint_as_float(w << 16); }
__device__ __forceinline__ float hi16(unsigned w) { return __uint_as_float(w & 0xFFFF0000u); }
__device__ __forceinline__ float rbf(float f) { return lo16(pack2(f, f)); }
__device__ __forceinline__ f32x2 unpk(unsigned w) { f32x2 r = {lo16(w), hi16(w)}; return r; }
__device__ __forceinline__ unsigned pk2(f32x2 v) { return pack2(v[0], v[1]); }
__device__ __forceinline__ unsigned umx(unsigned a, unsigned b) { return a > b ? a : b; }
__device__ __forceinline__ unsigned nohole(unsigned a) { return a < HOLE - 1 ? a : HOLE - 1; }
__device__ __forceinline__ f32x2 gelu2(f32x2 x) {
  const f32x2 c1 = {-2.3022082f, -2.3022082f}, c2 = {-0.10294324f, -0.10294324f};
  const f32x2 a = x * __builtin_elementwise_fma(x * x, c2, c1);
  f32x2 e = {__builtin_amdgcn_exp2f(a[0]), __builtin_amdgcn_exp2f(a[1])};
  e = e + (f32x2){1.0f, 1.0f};
  const f32x2 r = {__builtin_amdgcn_rcpf(e[0]), __builtin_amdgcn_rcpf(e[1])};
  return x * r;
}
template <int OFF>
__device__ __forceinline__ s16x4 tr_asm(unsigned a) {
  s16x4 r;
  asm volatile("ds_read_b64_tr_b16 %0, %1 offset:%2" : "=&v"(r) : "v"(a), "i"(OFF) : "memory");
  return r;
}
__device__ __forceinline__ bf16x8 join8(s16x4 lo, s16x4 hi) {
  bf16x8 r;
  r[0] = lo[0]; r[1] = lo[1]; r[2] = lo[2]; r[3] = lo[3];
  r[4] = hi[0]; r[5] = hi[1]; r[6] = hi[2]; r[7] = hi[3];
  return r;
}
__device__ __forceinline__ unsigned lds_addr(const void* p) { return (unsigned)(uintptr_t)(LDS_AS const char*)p; }
#define WAIT_LGKM0() asm volatile("s_waitcnt lgkmcnt(0)" ::: "memory")
__device__ __forceinline__ void flag_wait_asm(unsigned faddr, unsigned target) {
  unsigned v, sv;
  asm volatile(".Lfw_%=:\n\tds_read_b32 %0, %2\n\ts_waitcnt lgkmcnt(0)\n\tv_readfirstlane_b32 %1, %0\n\ts_nop 0\n\t"
               "s_cmp_ge_u32 %1, %3\n\ts_cbranch_scc1 .Lfd_%=\n\ts_sleep 1\n\ts_branch .Lfw_%=\n\t.Lfd_%=:"
               : "=&v"(v), "=&s"(sv) : "v"(faddr), "s"(target) : "memory", "scc");
}
__device__ __forceinline__ void signal1(unsigned a) {
  const unsigned one = 1u;
  asm volatile("s_mov_b64 exec, 1\n\tds_add_u32 %0, %1\n\ts_mov_b64 exec, -1" :: "v"(a), "v"(one) : "memory");
}
__device__ __forceinline__ void dma16(unsigned vo, const void* sb, unsigned lds) {
  asm volatile("s_mov_b32 m0, %2\n\ts_nop 0\n\tglobal_load_lds_dwordx4 %0, %1" :: "v"(vo), "s"(sb), "s"(lds) : "memory");
}
template <int LO, int GO> __device__ __forceinline__ void dma32(unsigned vo0, unsigned vo1, const void* sb, unsigned lds) {
  asm volatile("s_add_u32 m0, %5, %6\n\ts_add_u32 vcc_lo, %2, %4\n\ts_addc_u32 vcc_hi, %3, 0\n\t"
               "global_load_lds_dwordx4 %0, vcc\n\tglobal_load_lds_dwordx4 %1, vcc offset:1024"
               :: "v"(vo0), "v"(vo1), "s"((unsigned)(uintptr_t)sb), "s"((unsigned)((uintptr_t)sb >> 32)), "i"(GO), "s"(lds), "i"(LO) : "memory", "scc", "vcc");
}
__device__ __forceinline__ void dma16o(unsigned vo, const void* sb, int go, unsigned lds, int lo) {
  asm volatile("s_add_u32 m0, %3, %4\n\ts_nop 0\n\tglobal_load_lds_dwordx4 %0, %1 offset:%2" :: "v"(vo), "s"(sb), "i"(go), "s"(lds), "i"(lo - go) : "memory", "scc");
}
__device__ __forceinline__ void st16(unsigned vo, const void* sb, u32x4 v) {
  asm volatile("global_store_dwordx4 %0, %1, %2 sc1\n\ts_nop 1" :: "v"(vo), "v"(v), "s"(sb) : "memory");
}
template <class A, class B> __device__ __forceinline__ void MFMA(f32x4& acc, const A& a, const B& b) {
  asm volatile("v_mfma_f32_16x16x32_bf16 %0, %1, %2, %0" : "+v"(acc) : "v"(a), "v"(b));
}
template <class A, class B> __device__ __forceinline__ void MFMA0(f32x4& acc, const A& a, const B& b) {
  asm volatile("v_mfma_f32_16x16x32_bf16 %0, %1, %2, 0" : "=&v"(acc) : "v"(a), "v"(b));
}
template <bool Z, class A, class B> __device__ __forceinline__ void MFMAW(int n, f32x4& acc, const A& a, const B& b) {
  if (n < 0) { if (Z) MFMA0(acc, a, b); else MFMA(acc, a, b); }
  else if (Z) asm volatile("s_waitcnt lgkmcnt(%3)\n\tv_mfma_f32_16x16x32_bf16 %0, %1, %2, 0" : "=&v"(acc) : "v"(a), "v"(b), "i"(n));
  else asm volatile("s_waitcnt lgkmcnt(%3)\n\tv_mfma_f32_16x16x32_bf16 %0, %1, %2, %0" : "+v"(acc) : "v"(a), "v"(b), "i"(n));
}
template <class T> __device__ __forceinline__ void pin1(T& r) { asm volatile("" : "+v"(r)); }
template <class T> __device__ __forceinline__ T opq(T r) { asm volatile("" : "+v"(r)); return r; }
template <class... T> __device__ __forceinline__ void pin(T&... r) { (pin1(r), ...); }
template <int N, class... T> __device__ __forceinline__ void wait_vm(T&... r) { asm volatile("s_waitcnt vmcnt(%0)" :: "n"(N) : "memory"); (pin1(r), ...); }
template <int N, class... T> __device__ __forceinline__ void wait_lg(T&... r) { asm volatile("s_waitcnt lgkmcnt(%0)" :: "n"(N) : "memory"); (pin1(r), ...); }
template <int OFF, class T> __device__ __forceinline__ void ds16(T& d, unsigned a) {
  asm volatile("ds_read_b128 %0, %1 offset:%2" : "=&v"(d) : "v"(a), "i"(OFF) : "memory");
}
template <int OFF> __device__ __forceinline__ void dsw16(unsigned a, const f32x4& v) {
  asm volatile("ds_write_b128 %0, %1 offset:%2" :: "v"(a), "v"(v), "i"(OFF) : "memory");
}
#define HSC ""
template <int OFF, class T> __device__ __forceinline__ void ld16h(T& d, unsigned vo, const void* sb) {
  asm volatile("global_load_dwordx4 %0, %1, %2 offset:%3 " HSC : "=&v"(d) : "v"(vo), "s"(sb), "i"(OFF) : "memory");
}
template <class T> __device__ __forceinline__ void reld6(T (&B)[6], unsigned vo, const void* sb) {
  asm volatile("global_load_dwordx4 %0, %6, %7 offset:-2048 sc1\n\tglobal_load_dwordx4 %1, %6, %7 offset:-1024 sc1\n\t"
               "global_load_dwordx4 %2, %6, %7 sc1\n\tglobal_load_dwordx4 %3, %6, %7 offset:1024 sc1\n\t"
               "global_load_dwordx4 %4, %6, %7 offset:2048 sc1\n\tglobal_load_dwordx4 %5, %6, %7 offset:3072 sc1"
               : "+v"(B[0]), "+v"(B[1]), "+v"(B[2]), "+v"(B[3]), "+v"(B[4]), "+v"(B[5]) : "v"(vo), "s"(sb) : "memory");
}

constexpr int L_FLAG = 0;
constexpr int L_XM = 128;
constexpr int L_XR = 384;
constexpr int L_CS = 640;
constexpr int L_ZS = 1664;
constexpr int L_CS2 = 2688;
constexpr int L_Z1 = 4096;
constexpr int L_RAW = L_Z1;
constexpr int L_SLOT = 16384;
constexpr int L_KV = 128, KV_P = 3088, L_WO = L_KV + 8 * KV_P + 16;
static_assert(L_WO % 16 == 0 && L_WO + 4 * 1024 <= L_SLOT + 2 * 8192, "staging");
constexpr int CV_C = 4608, CV_CS = 4160, CV_BUF = CV_C + 2 * CV_CS;
constexpr int M_CV = L_SLOT;
constexpr int M_RING = L_SLOT + 6 * 8192;
constexpr int P3_C = L_SLOT;
constexpr int P3_G = P3_C + 72 * 1024;
constexpr int P3_Y = L_Z1;
constexpr int M_LDS = 163840;
static_assert(3 * CV_BUF <= 5 * 8192 && P3_G + 68 * 1024 <= M_LDS && M_RING <= P3_G, "LDS map");

enum { UX, UA, UB, UV };
struct Unit { int kind, idx, need, free, iss, slot, grp; };
struct Plan { Unit u[40]; int first[14], nv[2][12]; bool ok; };
#define ISSUE {7, 3, 4, 4, 4, 4, 3, 2, 2, 2, 2, 2, 1}
#define NSTC {0, 0, 0, 0, 0, 0, 1, 1, 1, 0, 0, 0, 0}
#define GZ 6
#define FLAGB 10
constexpr int XNEED[10] = {1, 2, 2, 3, 3, 4, 4, 5, 5, 6};
constexpr int XSLOT[10] = {8, 11, 14, 6, 7, 17, 12, 8, 13, 11}, WSLOT[10] = {2, 4, 9, 0, 15, 2, 6, 4, 9, 12}, VSLOT[10] = {8, 11, 17, 14, 2, 3, 0, 4, 5, 1};
constexpr int NST[2][13] = {{0, 2}, NSTC};
constexpr Plan make_plan() {
  Plan p = {};
  int n = 0;
  for (int b = 1; b < 11; ++b) {
    for (int c = 0; c < 10; ++c) if (XNEED[c] == b) p.u[n++] = {UX, c, b, b + 1, 0, XSLOT[c], 1};
    p.u[n++] = {UA, b - 1, b, b + 2 < 11 ? b + 2 : 11, 0, WSLOT[b - 1], 1};
    p.u[n++] = {UB, b - 1, b, b + 2 < 11 ? b + 2 : 11, 0, WSLOT[b - 1] + 1, 1};
  }
  for (int c = 0; c < 10; ++c) p.u[n++] = {UV, c, 99, 99, 0, VSLOT[c], 1};
  const int is[13] = ISSUE;
  n = 0;
  for (int q = 0; q < 13; ++q) { p.first[q] = n; for (int i = 0; i < is[q]; ++i) { p.u[n].grp = q == 0 ? n & 1 : 1; p.u[n++].iss = q - 1; } }
  p.first[13] = n;
  p.ok = n == 40;
  int fr[18] = {1, 1, -1, -1, -1, -1, -1, -1, -1, -1, -1, -1, -1, -1, -1, -1, -1, -1};
  for (int i = 0; i < 40; ++i) {
    if (p.u[i].iss < fr[p.u[i].slot]) p.ok = false;
    fr[p.u[i].slot] = p.u[i].free;
  }
  int lasts = 3;
  for (int q = 0; q < 13; ++q) if (NST[1][q] && q - 1 > lasts) lasts = q - 1;
  for (int g = 0; g < 2; ++g)
    for (int b = 0; b < 12; ++b) {
      int cnt = 0, lastq = -9;
      for (int q = 0; q <= b; ++q) {
        for (int i = p.first[q]; i < p.first[q + 1]; ++i)
          if (p.u[i].grp == g) { if (p.u[i].need <= b) { cnt = 0; lastq = q - 1; } else cnt += 2; }
        cnt += NST[g][q];
        if (g == 1 && b == FLAGB && q - 1 == lasts) cnt = 0;
      }
      for (int i = p.first[b + 1]; i < 40; ++i) if (p.u[i].need <= b) p.ok = false;
      p.nv[g][b] = g == 0 && b >= GZ ? 0 : cnt;
    }
  if (FLAGB <= lasts || FLAGB < GZ) p.ok = false;
  return p;
}
constexpr Plan PL = make_plan();
static_assert(PL.ok, "phase A plan");

__device__ __forceinline__ void conv_core(unsigned buf, int lane, f32x4 (&ac)[8]) {
  const int l15 = lane & 15, g4 = lane >> 4;
  const int c = (l15 + 1) & 1;
  unsigned aa[4];
#pragma unroll
  for (int k = 0; k < 4; ++k) aa[k] = buf + CV_C + c * CV_CS + (2031 - l15 + 8 * g4 - c) * 2 - 512 - 1024 * k;
  unsigned bb = buf + 512 + 32 * l15 - 32 + 16 * g4;
  u32x2 A[2][8][2];
  u32x4 B[2][8];
  constexpr int TT[36] = {7, 6, 5, 4, 7, 6, 5, 4, 7, 6, 5, 3, 7, 6, 5, 4, 7, 6, 3, 2, 7, 6, 5, 4, 7, 3, 2, 1, 7, 6, 5, 4, 3, 2, 1, 0};
  constexpr int AA[36] = {0, 0, 0, 0, 1, 1, 1, 1, 2, 2, 2, 0, 3, 3, 3, 2, 4, 4, 1, 0, 5, 5, 4, 3, 6, 2, 1, 0, 7, 6, 5, 4, 3, 2, 1, 0};
  auto ld1 = [&](int p, int n) {
    if (n < 16) {
      const int a = n >> 1, o = ((a & 1) ? 0 : 128) + 2 * (n & 1);
      asm volatile("ds_read2_b32 %0, %1 offset0:%2 offset1:%3" : "=&v"(A[p][(n >> 1) & 7][n & 1]) : "v"(aa[(a >> 1) & 3]), "i"(o), "i"(o + 1) : "memory");
    } else
      asm volatile("ds_read_b128 %0, %1 offset:%2" : "=&v"(B[p][n & 7]) : "v"(bb), "i"(512 * (n & 7)) : "memory");
  };
  auto next = [&](int dl) {
#pragma unroll
    for (int k = 0; k < 4; ++k) aa[k] += dl;
    bb += dl;
    SB();
  };
  auto land = [&](int p) {
    asm volatile("s_waitcnt lgkmcnt(0)" : "+v"(A[p][0][0]), "+v"(A[p][0][1]), "+v"(A[p][1][0]), "+v"(A[p][1][1]), "+v"(A[p][2][0]), "+v"(A[p][2][1]),
                 "+v"(A[p][3][0]), "+v"(A[p][3][1]), "+v"(A[p][4][0]), "+v"(A[p][4][1]), "+v"(A[p][5][0]), "+v"(A[p][5][1]), "+v"(A[p][6][0]), "+v"(A[p][6][1]),
                 "+v"(A[p][7][0]), "+v"(A[p][7][1]), "+v"(B[p][0]), "+v"(B[p][1]), "+v"(B[p][2]), "+v"(B[p][3]), "+v"(B[p][4]), "+v"(B[p][5]),
                 "+v"(B[p][6]), "+v"(B[p][7]) :: "memory");
    SB();
  };
  auto mm = [&](int p) {
#pragma unroll
    for (int n = 0; n < 36; ++n) {
      const int t = TT[n], a = AA[n];
      const u32x4 av = {A[p][a][0][0], A[p][a][0][1], A[p][a][1][0], A[p][a][1][1]};
      MFMA(ac[t], av, B[p][t - a]);
      if (n < 24) ld1(p ^ 1, n);
      SB();
    }
    land(p ^ 1);
  };
#pragma unroll
  for (int t = 0; t < 8; ++t) ac[t] = (f32x4){0.f, 0.f, 0.f, 0.f};
#pragma unroll
  for (int n = 0; n < 24; ++n) ld1(0, n);
  land(0);
#pragma unroll 1
  for (int r = 0; r < 8; r += 2) {
    next(-64);
    mm(0);
    next(r < 6 ? -64 : 0);
    mm(1);
  }
  asm volatile("s_nop 15" : "+v"(ac[0]), "+v"(ac[1]), "+v"(ac[2]), "+v"(ac[3]), "+v"(ac[4]), "+v"(ac[5]), "+v"(ac[6]), "+v"(ac[7]));
}

#define HSL 4
#define NW1 10
#define SGS 13

__global__ __launch_bounds__(512) void k_f(
    const u16* __restrict__ x, const u16* __restrict__ kin, const u16* __restrict__ vin, const u16* __restrict__ shift_k,
    const u16* __restrict__ ssm_k, const u16* __restrict__ ssm_D, const u16* __restrict__ w_in, const u16* __restrict__ w_sp,
    const u16* __restrict__ b_sp, const u16* __restrict__ w_out, u16* __restrict__ out, char* ws) {
  extern __shared__ __attribute__((aligned(16))) char smem[];
  const int tid = threadIdx.x, lane = tid & 63, wave = __builtin_amdgcn_readfirstlane(tid >> 6);
  const int b = blockIdx.x;
  const int xcd = b & 7, idx = b >> 3;
  const int rb = 8 * (xcd >> 1) + (idx >> 2), cb = 4 * (xcd & 1) + (idx & 3);
  const int t0 = rb * 64, d0 = cb * 80;
  const int g4 = lane >> 4, l15 = lane & 15;
  const unsigned lin = (unsigned)(lane * 16);
  const unsigned lb = lds_addr(smem), flg = lb + L_FLAG;
  AB_DECL;
  AB(0);
  asm volatile("" :: "s"(x), "s"(kin), "s"(vin), "s"(shift_k), "s"(ssm_k), "s"(ssm_D), "s"(w_in), "s"(w_sp), "s"(b_sp), "s"(w_out), "s"(out), "s"(ws));
  u64 tag = *(const __attribute__((address_space(4))) u64*)(uintptr_t)ws;
  int tmo = 1 << 16;

  const int cw = wave & 3, wm = wave & 1, wl = (wave >> 1) & 1, ksb = wm ^ wl, odd = cb & 1, pr = cb >> 1;
  const int r8 = lane >> 3, c8 = lane & 7;
  unsigned voX[2], voW[2];
#pragma unroll
  for (int h = 0; h < 2; ++h) {
    voX[h] = (unsigned)((h * 8 + r8) * (D_ * 2) + ((c8 ^ (h * 4 + (r8 & 3))) << 4) - h * 1024);
    voW[h] = (unsigned)((h * 8 + r8) * (F2_ * 2) + ((c8 ^ (h * 4 + (r8 & 2))) << 4) - h * 1024);
  }
  const char* gX = (const char*)x + (size_t)(t0 + cw * 16) * (D_ * 2);
  const char* gW = (const char*)w_in + (size_t)(cw * 16) * (F2_ * 2) + pr * 384;
  const char *gV = gW + odd * 256, *gB = gW + (odd ? 128 : FH_ * 2 + 128);
  const unsigned ldw = lb + cw * 2048;
  auto issue = [&](auto nc) {
    constexpr Unit u = PL.u[decltype(nc)::value];
    if (u.kind == UX) dma32<L_SLOT + u.slot * 8192, u.idx * 128>(voX[0], voX[1], gX, ldw);
    else dma32<L_SLOT + u.slot * 8192, u.idx * (64 * F2_ * 2) + (u.kind == UA ? FH_ * 2 : 0)>(voW[0], voW[1], u.kind == UB ? gB : gV, ldw);
    SB();
  };
  auto issp = [&](auto gc, auto pc, auto kc) {
    constexpr int p = decltype(pc)::value, n = PL.first[p + 1] + decltype(kc)::value;
    if constexpr (n < PL.first[p + 2]) { if constexpr (PL.u[n].grp == decltype(gc)::value) issue(IC(n)); }
  };
  auto issall = [&](auto gc, auto pc) {
    issp(gc, pc, IC(0)); issp(gc, pc, IC(1)); issp(gc, pc, IC(2)); issp(gc, pc, IC(3)); issp(gc, pc, IC(4));
    issp(gc, pc, IC(5)); issp(gc, pc, IC(6)); issp(gc, pc, IC(7)); issp(gc, pc, IC(8)); issp(gc, pc, IC(9));
  };
  const int kd = d0 + min(wave, 4) * 16 + r8 * 2;
  u32x4 sw;
  {
    const int ks0 = t0 + wave * 8;
#pragma unroll
    for (int i = 0; i < 3; ++i) {
      const int id = min(i * 64 + lane, 189), r = id / 10, c = id % 10;
      const char* kp = (r < 11 ? (const char*)kin + (size_t)max(ks0 - 3 + r, 0) * (D_ * 2) : (const char*)vin + (size_t)(ks0 + r - 11) * (D_ * 2)) + cb * 160 + c * 16;
      asm volatile("s_mov_b32 m0, %1\n\ts_nop 0\n\tglobal_load_lds_dwordx4 %0, off" :: "v"(kp), "s"(lb + L_KV + wave * KV_P + i * 1024) : "memory");
    }
    asm volatile("global_load_dwordx4 %0, %1, %2" : "=&v"(sw) : "v"(kd * 8), "s"(shift_k) : "memory");
    SB();
  }
  unsigned kk[11], vv[8];
  u32x4 oa, ob;
  auto kvld = [&]() {
    const char* kl = smem + L_KV + c8 * KV_P + min(wave, 4) * 32 + r8 * 4;
#pragma unroll
    for (int i = 0; i < 11; ++i) kk[i] = *(const unsigned*)(kl + i * 160);
#pragma unroll
    for (int i = 0; i < 8; ++i) vv[i] = *(const unsigned*)(kl + (11 + i) * 160);
  };
  auto kvm = [&](int e0, int e1) {
    const f32x2 w0 = {lo16(sw[0]), lo16(sw[2])}, w1 = {hi16(sw[0]), hi16(sw[2])};
    const f32x2 w2 = {lo16(sw[1]), lo16(sw[3])}, w3 = {hi16(sw[1]), hi16(sw[3])};
    f32x2 kf[11], rr[8];
#pragma unroll
    for (int i = 2 * e0; i < 2 * e1 + 3; ++i) kf[i] = unpk((i < 3 && t0 + c8 == 0) ? 0u : kk[i]);
#pragma unroll
    for (int i = 2 * e0; i < 2 * e1; ++i) {
      f32x2 a0 = kf[i] * w3;
      a0 = __builtin_elementwise_fma(kf[i + 1], w2, a0);
      a0 = __builtin_elementwise_fma(kf[i + 2], w1, a0);
      a0 = __builtin_elementwise_fma(kf[i + 3], w0, a0);
      rr[i] = a0 * unpk(vv[i]);
    }
#pragma unroll
    for (int e = e0; e < e1; ++e) { oa[e] = pack2(rr[2 * e][0], rr[2 * e + 1][0]); ob[e] = pack2(rr[2 * e][1], rr[2 * e + 1][1]); }
  };
  auto kvst = [&]() {
    const unsigned ko = (unsigned)((kd * S_ + t0 + c8 * 8) * 2);
    st16(ko, ws + WS_KV, oa);
    st16(ko, ws + WS_KV + S_ * 2, ob);
    SB();
  };
  auto barrier0 = [&](int nv) {
    SB();
    asm volatile("s_waitcnt vmcnt(%0) lgkmcnt(0)\n\ts_barrier" :: "n"(nv) : "memory");
    SB();
    asm volatile("" : "+v"(sw), "+s"(tag));
    tag += 1;
  };
  unsigned pX[2], pW[2];
#pragma unroll
  for (int k = 0; k < 2; ++k) {
    const int q = l15 >> 2, p = lane & 3, sz = (q >> 1) | ((g4 & 1) << 1);
    pX[k] = lb + (wm * 32 + q * 8 + p) * 128 + ((((k ^ ksb) * 4 + g4) ^ (l15 & 7)) << 4);
    pW[k] = lb + ((k ^ ksb) * 32 + g4 * 8 + q) * 128 + (((sz << 1) | (p >> 1)) << 4) + (p & 1) * 8;
  }
  bf16x8 ones;
#pragma unroll
  for (int e = 0; e < 8; ++e) ones[e] = (short)0x3F80;
  f32x4 xm[2], xr[2];
  auto ldxs = [&]() {
#pragma unroll
    for (int i = 0; i < 2; ++i) {
      xm[i] = *(const f32x4*)(smem + L_XM + (wm * 32 + g4 * 8 + i * 4) * 4);
      xr[i] = *(const f32x4*)(smem + L_XR + (wm * 32 + g4 * 8 + i * 4) * 4);
    }
  };
  auto act = [&](const f32x4& a, float c, int i) {
    const f32x2 nc = {-c, -c};
    const f32x2 lo = gelu2(__builtin_elementwise_fma((f32x2){xm[i][0], xm[i][1]}, nc, (f32x2){a[0], a[1]}) * (f32x2){xr[i][0], xr[i][1]});
    const f32x2 hi = gelu2(__builtin_elementwise_fma((f32x2){xm[i][2], xm[i][3]}, nc, (f32x2){a[2], a[3]}) * (f32x2){xr[i][2], xr[i][3]});
    return (f32x4){lo[0], lo[1], hi[0], hi[1]};
  };
  auto pack8 = [&](const f32x4& a, const f32x4& c) { return (u32x4){pack2(a[0], a[1]), pack2(a[2], a[3]), pack2(c[0], c[1]), pack2(c[2], c[3])}; };
  auto z1put = [&](int f0, const u32x4& v) { *(u32x4*)(smem + L_Z1 + (f0 + l15) * 128 + (((wm * 4 + g4) ^ (l15 & 7)) << 4)) = v; };
  auto rdXF = [&](auto cc, bf16x8 (&X)[2][2]) {
    constexpr int so = L_SLOT + XSLOT[decltype(cc)::value] * 8192;
#pragma unroll
    for (int k = 0; k < 2; ++k) {
      const unsigned a = pX[k] + (so & ~0x7FFF);
      ds16<(so & 0x7FFF)>(X[k][0], a);
      ds16<(so & 0x7FFF) + 512>(X[k][1], a);
    }
    SB();
  };
  auto sttag = [&](const char* p) {
    asm volatile("s_mov_b64 exec, 1\n\tglobal_store_dwordx2 %0, %1, %2 sc1\n\ts_mov_b64 exec, -1" :: "v"(lin), "v"(tag), "s"(p) : "memory");
  };
  auto ldtag = [&](u32x2& f, unsigned vo, const char* p) { asm volatile("global_load_dwordx2 %0, %1, %2 sc1" : "=&v"(f) : "v"(vo), "s"(p) : "memory"); };
  auto notag = [&](const u32x2& f) { return __builtin_amdgcn_ballot_w64(f[0] != (unsigned)tag || f[1] != (unsigned)(tag >> 32)) != 0; };
  auto waittag = [&](u32x2& f, unsigned vo, const char* p) {
    while (__builtin_expect(notag(f) && tmo > 0, 0)) {
      --tmo;
      __builtin_amdgcn_s_sleep(4);
      ldtag(f, vo, p);
      wait_vm<0>(f);
    }
  };
  u32x2 ef;
  auto ldef = [&]() { asm volatile("global_load_dwordx2 %0, %1, %2 sc1" : "=&v"(ef) : "v"((unsigned)((lane & 31) * 8)), "s"(ws + WS_EF + cb * 256) : "memory"); };

  char* const hf = ws + WS_H;
  bf16x8 Wq[15];
  const char* wg = ws + WS_WO + (size_t)(wave * 120 + cb * 5) * 1024;
  const int mt = wave & 3, kh = wave >> 2;
  const size_t xo = (size_t)(t0 + mt * 16 + l15) * D_ + d0;
  u32x2 xv[3];
  auto ldP1 = [&](int n) {
    if (n < 15)
      asm volatile("global_load_dwordx4 %0, %1, %2 offset:%3 sc1" : "=&v"(Wq[n < 15 ? n : 0]) : "v"(lin), "s"(wg + (n / 5) * 40960 + 2048), "i"((n % 5) * 1024 - 2048) : "memory");
    else {
      const int q = n < 18 ? n - 15 : 0;
      asm volatile("global_load_dwordx2 %0, %1, %2 offset:%3" : "=&v"(xv[q]) : "v"((unsigned)(l15 * D_ * 2 + 8 * g4)),
                   "s"(x + (size_t)(t0 + mt * 16) * D_ + d0 + (q < 2 ? 32 * kh : 64)), "i"(q == 1 ? 32 : 0) : "memory");
    }
  };
  u32x4 yv[2];
  auto yoff = [&](int n) {
    const int c = n ? 64 + (lane & 31) : lane, r = c >> 1;
    return (unsigned)(((r < 32 ? 32 * kh + r : 32 + r) * S_ + 8 * (c & 1)) * 2);
  };
  const char* const yb = ws + WS_Y + ((size_t)d0 * S_ + t0 + 16 * mt) * 2;
  auto ldW1 = [&]() {
    ldtag(ef, 0u, ws + WS_LF + (rb * 8 + wave) * 8);
#pragma unroll
    for (int n = 0; n < NW1; ++n) ldP1(n);
    SB();
  };
  auto drain1 = [&](int nc) {
    SB();
    asm volatile("s_waitcnt vmcnt(%0)" :: "i"(nc) : "memory");
#pragma unroll
    for (int n = 0; n < NW1; ++n) pin(Wq[n]);
    pin(ef);
  };
  if (wave >= 4) {
    const int wid = rb * 4 + cw;
    __builtin_amdgcn_s_setprio(3);
    if (wid < 120) dma16((unsigned)((lane >> 1) * (D_ * 2) + (lane & 1) * 16), w_out + (size_t)(wid / 5) * 32 * D_ + d0 + (wid % 5) * 16, lb + L_WO + cw * 1024);
    SB();
    issall(IC(1), IC(-1));
    AB(1);
    barrier0(PL.nv[1][0]);
    AB(2);
    u32x4 hole = {HOLE, HOLE, HOLE, HOLE};
    pin(ones, hole);
    issp(IC(1), IC(0), IC(0));
    if (wave == 7) kvld();
    issp(IC(1), IC(0), IC(1));
    if (wid < 120) {
      const unsigned ra = lb + L_WO + cw * 1024 + (8 * g4 + (l15 >> 2)) * 32 + 8 * (lane & 3);
      s16x4 lo = tr_asm<0>(ra), hi = tr_asm<128>(ra);
      wait_lg<0>(lo, hi);
      st16(lin, ws + WS_WO + (size_t)((wid / 5) * 40 + cb * 5 + wid % 5) * 1024, __builtin_bit_cast(u32x4, join8(lo, hi)));
    }
    SB();
    issp(IC(1), IC(0), IC(2)); issp(IC(1), IC(0), IC(3)); issp(IC(1), IC(0), IC(4)); issp(IC(1), IC(0), IC(5));
    if (wave == 7) kvm(0, 2);
    AB(3);
    bf16x8 XF[10][2][2];
    auto ldx = [&](int c) {
      u32x4 xv;
      asm volatile("global_load_dwordx4 %0, %1, %2\n\ts_waitcnt vmcnt(0)" : "=&v"(xv) : "v"((unsigned)(lane * (D_ * 2) + c * 16)), "s"(x + (size_t)t0 * D_) : "memory");
      return xv;
    };
    float fm, fq;
    f32x2 fr[2];
    constexpr int FINP = 8;
    auto period = [&](auto pc) {
      constexpr int p = decltype(pc)::value;
      constexpr int c0 = p == 1 || p > 6 ? 0 : 2 * p - 3, nx = p == 1 || p == 6 ? 1 : (p < 6 ? 2 : 0);
      SB();
      asm volatile("s_waitcnt vmcnt(%0) lgkmcnt(0)\n\ts_barrier" :: "n"(PL.nv[1][p]) : "memory");
      CB(6 * (p - XBAR) + 1);
      SB();
      constexpr int pc0 = p == 2 || p > 7 ? 0 : 2 * p - 5, pnx = p < 2 || p > 7 ? 0 : (p == 2 || p == 7 ? 1 : 2);
#pragma unroll
      for (int c = pc0; c < pc0 + pnx; ++c) pin(XF[c][0][0], XF[c][0][1], XF[c][1][0], XF[c][1][1]);
      if constexpr (p == FINP) {
        if (wave == 6) {
          const char* rw = smem + L_RAW + ((lane >> 3) * 4 + (lane & 3)) * 16 + (lane & 4) * 2;
          fr[0] = *(const f32x2*)rw;
          fr[1] = *(const f32x2*)(rw + 512);
        }
        SB();
      }
      issp(IC(1), pc, IC(0));
      CB(6 * (p - XBAR) + 2);
      if constexpr (nx > 0) rdXF(IC(c0), XF[c0]);
      if constexpr (p == FLAGB) { if (wave == 5) sttag(ws + WS_EF + (cb * 32 + rb) * 8); }
      issp(IC(1), pc, IC(1));
      CB(6 * (p - XBAR) + 3);
      if constexpr (p <= 2) { if (wave == 7) kvm(p + 1, p + 2); SB(); }
      if constexpr (p == 3) { if (wave == 7) kvst(); SB(); }
      issp(IC(1), pc, IC(2));
      CB(6 * (p - XBAR) + 4);
      if constexpr (nx > 1) rdXF(IC(c0 + 1), XF[c0 + 1]);
      issp(IC(1), pc, IC(3)); issp(IC(1), pc, IC(4)); issp(IC(1), pc, IC(5));
      if constexpr (NST[1][p + 1] > 0) {
        int n0 = 0;
#pragma unroll
        for (int q = 0; q <= p; ++q) n0 += NST[1][q];
#pragma unroll
        for (int n = n0; n < n0 + NST[1][p + 1]; ++n) st16(lin, ws + WS_Z2 + (size_t)((rb * 2 + wl) * 48 + cb * 6 + wm * 3 + n) * 1024, hole);
        SB();
      }
      if constexpr (p == FINP) {
        if (wave == 6) {
          const f32x2 sa = fr[0] + fr[1];
          fm = sa[0] * (1.0f / 640.0f);
          fq = sa[1] - sa[0] * fm;
          if (__builtin_expect(__builtin_amdgcn_ballot_w64(!(fq >= 1e-3f * sa[0] * fm)) != 0, 0)) {
            const f32x2 mm = {fm, fm};
            f32x2 q2 = {0.f, 0.f};
#pragma unroll 1
            for (int c = 0; c < 80; ++c) {
              const u32x4 xv = ldx(c);
#pragma unroll
              for (int e = 0; e < 4; ++e) { const f32x2 d = unpk(xv[e]) - mm; q2 = __builtin_elementwise_fma(d, d, q2); }
            }
            fq = q2[0] + q2[1];
          }
          pin(fm, fq);
        }
      }
      if constexpr (p == FINP + 1) {
        if (wave == 6) {
          const float vbf = rbf(fmaxf(fq, 0.f) * (1.0f / 640.0f));
          fq = rbf(__builtin_amdgcn_sqrtf(rbf(vbf + 1e-5f)));
          fm = rbf(fm);
          pin(fm, fq);
        }
      }
      if constexpr (p == FINP + 2) {
        if (wave == 6) {
          *(float*)(smem + L_XM + lane * 4) = fm;
          *(float*)(smem + L_XR + lane * 4) = __builtin_amdgcn_rcpf(fq);
        }
      }
    };
    period(IC(1)); AB(4); period(IC(2)); period(IC(3)); period(IC(4)); period(IC(5)); AB(5);
    period(IC(6)); period(IC(7)); period(IC(8)); period(IC(9)); period(IC(10)); period(IC(11));
    AB(6);
    const bool docv = cw < ((rb & 1) ? 2 : 3);
    const int d = d0 + (cw < 2 ? 2 * rb + cw : 64 + (rb >> 1));
    u32x4 hc[4], uc[4];
    unsigned Dw;
#pragma unroll
    for (int k = 0; k < 4; ++k) asm volatile("global_load_dwordx4 %0, %1, %2 offset:%3" : "=&v"(hc[k]) : "v"(lin), "s"(ssm_k + (size_t)d * S_), "i"(k * 1024) : "memory");
    asm volatile("global_load_ushort %0, %1, %2" : "=&v"(Dw) : "v"(0u), "s"(ssm_D + d) : "memory");
    __builtin_amdgcn_s_setprio(0);
    wait_vm<5>();
    signal1(flg + 60);
    flag_wait_asm(flg + 60, 4u);
    AB(7);
    {
      f32x4 ac2[2][2], cs2[2];
      s16x4 VF[3][2][2];
      unsigned aV[2][2];
      auto rdV = [&](auto nc) {
        constexpr int n = decltype(nc)::value, so = VSLOT[n >> 1] * 8192, po = n < 2 ? -1 : (VSLOT[(n >> 1) - 1] * 8192) >> 16;
#pragma unroll
        for (int jj = 0; jj < 2; ++jj) {
          if ((so >> 16) != po) aV[n & 1][jj] = opq((pW[n & 1] ^ (unsigned)(wl * 64 + jj * 32)) + L_SLOT + (so & ~0xFFFF));
          VF[n % 3][jj][0] = tr_asm<(so & 0xFFFF)>(aV[n & 1][jj]);
          VF[n % 3][jj][1] = tr_asm<(so & 0xFFFF) + 512>(aV[n & 1][jj]);
        }
        SB();
      };
      auto mm2 = [&](auto nc) {
        constexpr int n = decltype(nc)::value, j = n >> 1, k = n & 1, nw = n < 19 ? 4 : 0;
        const bf16x8 w0 = join8(VF[n % 3][0][0], VF[n % 3][0][1]), w1 = join8(VF[n % 3][1][0], VF[n % 3][1][1]);
        if (k == 0) { MFMAW<n == 0>(nw, cs2[0], ones, w0); MFMAW<n == 0>(-1, cs2[1], ones, w1); }
        MFMAW<n == 0>(k ? nw : -1, ac2[0][0], XF[j][k][0], w0);
        SB();
        if constexpr (n < 18) rdV(IC(n + 2 < 20 ? n + 2 : 0));
        if constexpr (n == 12) ldef();
        if constexpr (n == 19) signal1(flg + 16);
        MFMAW<n == 0>(-1, ac2[1][0], XF[j][k][1], w0);
        MFMAW<n == 0>(-1, ac2[0][1], XF[j][k][0], w1);
        MFMAW<n == 0>(-1, ac2[1][1], XF[j][k][1], w1);
        SB();
        if constexpr (n == 19) {
          pin(cs2[0], cs2[1]);
          if (g4 < 2) *(float*)(smem + L_CS2 + (cw * 32 + lane) * 4) = g4 ? cs2[1][0] : cs2[0][0];
          signal1(flg + 20 + wl * 4);
        }
        SB();
      };
      rdV(IC(0)); rdV(IC(1));
      mm2(IC(0)); mm2(IC(1)); mm2(IC(2)); mm2(IC(3)); mm2(IC(4)); mm2(IC(5)); mm2(IC(6)); mm2(IC(7)); mm2(IC(8)); mm2(IC(9));
      mm2(IC(10)); mm2(IC(11)); mm2(IC(12)); mm2(IC(13)); mm2(IC(14)); mm2(IC(15)); mm2(IC(16)); mm2(IC(17)); mm2(IC(18)); mm2(IC(19));
      AB(8);
      wait_vm<0>(ef);
      waittag(ef, (unsigned)((lane & 31) * 8), ws + WS_EF + cb * 256);
#pragma unroll
      for (int k = 0; k < 4; ++k) asm volatile("global_load_dwordx4 %0, %1, %2 offset:%3 sc1" : "=&v"(uc[k]) : "v"(lin), "s"(ws + WS_KV + (size_t)d * (S_ * 2)), "i"(k * 1024) : "memory");
      ldxs();
      flag_wait_asm(flg + 20 + wl * 4, 2u);
      pin(ac2[0][0], ac2[0][1], ac2[1][0], ac2[1][1]);
#pragma unroll
      for (int jj = 0; jj < 2; ++jj) {
        const float ct = cs2[jj][0] + *(const float*)(smem + L_CS2 + ((cw ^ 1) * 32 + jj * 16 + l15) * 4);
        z1put(odd * 32 + wl * 32 + jj * 16, pack8(act(ac2[0][jj], ct, 0), act(ac2[1][jj], ct, 1)));
      }
      signal1(flg + 56);
      AB(9);
    }
    wait_vm<0>(hc[0], hc[1], hc[2], hc[3], uc[0], uc[1], uc[2], uc[3], Dw);
    if (docv) {
      char* cb_ = smem + M_CV + cw * CV_BUF;
      flag_wait_asm(flg + 16, 4u);
      if (lane < 36) *(u32x4*)(cb_ + (lane < 32 ? lane * 16 : CV_C + ((lane >> 1) & 1) * CV_CS + 4096 + (lane & 1) * 16)) = (u32x4){0u, 0u, 0u, 0u};
      const float Dv = lo16(Dw);
#pragma unroll
      for (int k = 0; k < 4; ++k) {
        const int a = k * 64 + lane;
        const int h0 = k ? __builtin_amdgcn_readlane((int)hc[k ? k - 1 : 0][3], 63) : 0;
        const unsigned hp = (unsigned)__builtin_amdgcn_update_dpp(h0, (int)hc[k][3], 0x138, 0xF, 0xF, false);
        const unsigned w[5] = {hp, hc[k][0], hc[k][1], hc[k][2], hc[k][3]};
        u32x4 sw, mx;
#pragma unroll
        for (int i = 0; i < 4; ++i) {
          sw[i] = (w[4 - i] >> 16) | (w[4 - i] << 16);
          mx[i] = (w[4 - i] & 0xFFFFu) | (w[3 - i] & 0xFFFF0000u);
        }
        char* cp = cb_ + CV_C + (2040 - 8 * a) * 2;
        *(u32x4*)(cp) = sw;
        *(u32x4*)(cp + CV_CS) = mx;
        *(u32x4*)(cb_ + 512 + a * 16) = uc[k];
      }
      AB(12);
      f32x4 ac[8];
      conv_core(lds_addr(cb_), lane, ac);
      AB(13);
      u32x2 kvp[8];
#pragma unroll
      for (int t = 0; t < 8; ++t) kvp[t] = *(const u32x2*)(cb_ + 512 + (256 * t + 16 * l15 + 4 * g4) * 2);
#pragma unroll
      for (int k = 0; k < 4; ++k) {
        u32x2 ov[2];
#pragma unroll
        for (int t = 0; t < 2; ++t)
#pragma unroll
          for (int h = 0; h < 2; ++h) {
            const unsigned cv = pack2(ac[2 * k + t][2 * h], ac[2 * k + t][2 * h + 1]);
            const unsigned kd = pack2(lo16(kvp[2 * k + t][h]) * Dv, hi16(kvp[2 * k + t][h]) * Dv);
            ov[t][h] = pack2(lo16(cv) + lo16(kd), hi16(cv) + hi16(kd));
          }
        const auto s0 = __builtin_amdgcn_permlane16_swap(ov[0][0], ov[1][0], false, false);
        const auto s1 = __builtin_amdgcn_permlane16_swap(ov[0][1], ov[1][1], false, false);
        st16((unsigned)((256 * (g4 & 1) + 16 * l15 + 8 * (g4 >> 1)) * 2), ws + WS_Y + (size_t)d * (S_ * 2) + k * 1024, (u32x4){s0[0], s1[0], s0[1], s1[1]});
      }
      wait_vm<0>();
      sttag(ws + WS_YF + d * 8);
    } else if (cw == 3) {
#pragma unroll
      for (int n = 0; n < 12; ++n) {
        st16(lin, ws + WS_H + (size_t)((rb * 4 + n / 3) * 24 + cb * 3 + n % 3) * 1024, hole);
        __builtin_amdgcn_s_sleep(HSL);
      }
      wait_vm<0>();
      sttag(ws + WS_LF + (rb * 8 + cb) * 8);
      if (!odd) {
        u32x4 zx[4];
        ldtag(ef, (unsigned)((lane & 1) * 8), ws + WS_XF + (rb * 4 + pr) * 16);
        wait_vm<0>(ef);
        waittag(ef, (unsigned)((lane & 1) * 8), ws + WS_XF + (rb * 4 + pr) * 16);
#pragma unroll
        for (int c = 0; c < 4; ++c)
          asm volatile("global_load_dwordx4 %0, %1, %2 offset:%3 sc1" : "=&v"(zx[c]) : "v"(lin), "s"(ws + WS_ZX + (size_t)(rb * 4 + pr) * 4096), "i"(c * 1024) : "memory");
        wait_vm<0>(zx[0], zx[1], zx[2], zx[3]);
#pragma unroll
        for (int c = 0; c < 4; ++c) *(u32x4*)(smem + L_Z1 + (64 + (c & 1) * 16 + l15) * 128 + ((((c >> 1) * 4 + g4) ^ (l15 & 7)) << 4)) = zx[c];
      }
      signal1(flg + 56);
    }
    SB();
    AB(14);
    WAIT_LGKM0();
    signal1(flg + 40);
    flag_wait_asm(flg + 44, 4u);
    AB(15);
    ldW1();
    drain1(0);
  } else {
    issall(IC(0), IC(-1));
    if (tid < 32) *(unsigned*)(smem + L_FLAG + tid * 4) = 0u;
    pin(ones);
    AB(1);
    barrier0(PL.nv[0][0]);
    AB(2);
    kvld();
    kvm(0, 4);
    kvst();
    AB(3);
    const bool isz2 = !(odd && wl);
    const char* Ab[4];
#pragma unroll
    for (int i = 0; i < 4; ++i) Ab[i] = (const char*)w_sp + (size_t)(t0 + ((wave + 1 + i) & 3) * 16) * (S_ * 2) + wave * 1024;
    const unsigned ringl = lb + M_RING + wave * 24576;
    const unsigned aoff[2] = {(unsigned)(r8 * 4096 + ((c8 ^ r8) << 4)), (unsigned)(r8 * 4096 + ((c8 ^ r8) << 4) + 32768)};
    auto ldA1 = [&](auto dc, int i) {
      constexpr int d = decltype(dc)::value;
      dma16o(aoff[i & 1], Ab[i >> 1], d * 128, ringl, (d % 3) * 8192 + i * 1024);
    };
    {
      f32x4 zv[2][4], cs[4];
      s16x4 WB[2][4][2];
      bf16x8 XG[10][2][2];
      unsigned aX[2][2], aW[2][2][4];
#pragma unroll
      for (int k = 0; k < 2; ++k)
#pragma unroll
        for (int w = 0; w < 2; ++w) {
          aX[w][k] = pX[k] + L_SLOT + 6 * 8192 + w * 65536;
          pin(aX[w][k]);
#pragma unroll
          for (int jj = 0; jj < 4; ++jj) { aW[w][k][jj] = (pW[k] ^ (unsigned)(jj * 32)) + L_SLOT + wl * 8192 + w * 65536; pin(aW[w][k][jj]); }
        }
      auto rd1 = [&](auto nc, int r) {
        constexpr int n = decltype(nc)::value, j = n >> 1, k = n & 1, c = n > 0 && n < 11 ? n - 1 : 0, sx = (XSLOT[c] - 6) * 8192, sa = WSLOT[j] * 8192;
        if (r < 0) { if (r & 1) ds16<(sx & 0xFFFF) + 512>(XG[c][(r + 4) >> 1][1], aX[sx >> 16][(r + 4) >> 1]); else ds16<(sx & 0xFFFF)>(XG[c][(r + 4) >> 1][0], aX[sx >> 16][(r + 4) >> 1]); }
        else if (r & 1) WB[k][r >> 1][1] = tr_asm<(sa & 0xFFFF) + 512>(aW[sa >> 16][k][r >> 1]);
        else WB[k][r >> 1][0] = tr_asm<(sa & 0xFFFF)>(aW[sa >> 16][k][r >> 1]);
        SB();
      };
      auto bar = [&](auto bc) {
        SB();
        constexpr int p = decltype(bc)::value;
        CB(6 * (p - XBAR));
        if constexpr (p == 1) asm volatile("s_waitcnt vmcnt(%0) lgkmcnt(0)\n\ts_barrier" :: "n"(PL.nv[0][p]) : "memory");
        else if constexpr (p <= GZ) asm volatile("s_waitcnt vmcnt(%0) lgkmcnt(8)\n\ts_barrier" :: "n"(PL.nv[0][p]) : "memory");
        else if constexpr (p == 8) asm volatile("s_waitcnt lgkmcnt(4)\n\ts_barrier" ::: "memory");
        else if constexpr (p == 11) asm volatile("s_waitcnt lgkmcnt(0)\n\ts_barrier" ::: "memory");
        else asm volatile("s_barrier" ::: "memory");
        CB(6 * (p - XBAR) + 1);
        SB();
      };
      float cp[4];
      f32x4 st[2][2], rv;
      const unsigned rwa = lb + L_RAW + (ksb * 2 + wm) * 256 + l15 * 16 + ((l15 >> 2) == g4 ? 0 : 1024);
      auto kstep = [&](auto nc) {
        constexpr int n = decltype(nc)::value, j = n >> 1, k = n & 1, nn = n < 19 ? n + 1 : 0, r0 = nn >= 2 && nn <= 10 ? -4 : 0;
        constexpr int sc = n == 1 ? 0 : n - 2, ns = !k || n > 11 ? 0 : (n == 1 || n == 11 ? 1 : 2);
        constexpr bool more = n < 19;
        if constexpr (more && k) bar(IC(nn / 2 + 1));
        int r = r0, c = 0;
        auto nxt = [&]() { if (more && r < 8) { rd1(IC(nn), r++); ++c; } };
        auto sel = [&](int q) {
          const f32x4& sq = st[q & 1][q >> 1];
          rv[q] = (l15 & 2) ? ((l15 & 1) ? sq[3] : sq[2]) : ((l15 & 1) ? sq[1] : sq[0]);
          SB();
        };
#pragma unroll
        for (int jj = 0; jj < 4; ++jj) {
          const bf16x8 wf = join8(WB[k][jj][0], WB[k][jj][1]);
          if (n == 19 && jj == 2) {
            asm volatile("s_nop 2");
            pin(cs[0], cs[1], cs[2], cs[3]);
            *(float*)(smem + L_CS + (wave * 64 + lane) * 4) = (g4 & 2) ? ((g4 & 1) ? cs[3][0] : cs[2][0]) : ((g4 & 1) ? cs[1][0] : cs[0][0]);
            bar(IC(11));
#pragma unroll
            for (int q = 0; q < 4; ++q) cp[q] = *(const float*)(smem + L_CS + ((wave ^ 1) * 64 + q * 16 + l15) * 4);
            ldxs();
            SB();
          }
          MFMAW<n == 0>(jj == 0 ? 4 : (jj == 2 && more ? c : -1), zv[0][jj], XG[j][k][0], wf);
          SB();
          if (n == 12 && jj < 2) sel(2 * jj); else nxt();
          MFMAW<n == 0>(-1, zv[1][jj], XG[j][k][1], wf);
          SB();
          nxt();
          if (k == 0) {
            MFMAW<n == 0>(-1, cs[jj], ones, wf);
            SB();
            if (n == 12 && jj < 2) sel(2 * jj + 1); else nxt();
            if (n == 12 && jj == 1) { nxt(); nxt(); dsw16<0>(rwa, rv); ++c; SB(); }
          }
#pragma unroll
          for (int q = 0; q < ns; ++q) {
            const bf16x8& xs = XG[sc + q][1][jj >> 1];
            MFMAW<n == 1>(-1, st[jj & 1][jj >> 1], xs, (jj & 1) ? xs : ones);
            SB();
            nxt();
          }
        }
      };
      bar(IC(1));
#pragma unroll
      for (int r = -4; r < 8; ++r) rd1(IC(0), r);
      kstep(IC(0)); AB(4); kstep(IC(1)); kstep(IC(2)); kstep(IC(3)); kstep(IC(4)); kstep(IC(5)); kstep(IC(6)); kstep(IC(7)); kstep(IC(8)); AB(5); kstep(IC(9));
      kstep(IC(10)); kstep(IC(11)); kstep(IC(12)); kstep(IC(13)); kstep(IC(14)); kstep(IC(15)); kstep(IC(16)); kstep(IC(17)); kstep(IC(18)); kstep(IC(19));
      __builtin_amdgcn_s_setprio(2);
      AB(6);
      float ct[4];
#pragma unroll
      for (int jj = 0; jj < 4; ++jj) ct[jj] = cs[jj][0] + cp[jj];
#pragma unroll
      for (int jj = 0; jj < 4; ++jj) {
        if (jj == 2) SB();
        if (!(jj & 1)) pin(zv[0][jj], zv[1][jj], zv[0][jj + 1], zv[1][jj + 1]);
#pragma unroll
        for (int i = 0; i < 2; ++i) zv[i][jj] = act(zv[i][jj], ct[jj], i);
      }
      if (isz2) {
        f32x4 pv[2], s1[2], s2[2];
#pragma unroll
        for (int i = 0; i < 2; ++i) {
#pragma unroll
          for (int e = 0; e < 4; ++e) {
            const float z0 = zv[i][0][e];
            pv[i][e] = __int_as_float(__builtin_amdgcn_update_dpp(0, __float_as_int(z0), 0x150, 0xF, 0xF, true));
          }
          const f32x4 a0 = zv[i][0] - pv[i], a1 = zv[i][1] - pv[i], a2 = zv[i][2] - pv[i], a3 = zv[i][3] - pv[i];
          s1[i] = (a0 + a1) + (a2 + a3);
          s2[i] = __builtin_elementwise_fma(a3, a3, __builtin_elementwise_fma(a2, a2, __builtin_elementwise_fma(a1, a1, a0 * a0)));
        }
        float r[8], q[4];
#define DA(d, s, ctl, bm) "v_add_f32_dpp " d ", " s ", " s " " ctl " row_mask:0xf bank_mask:" bm "\n\t"
        asm volatile("s_nop 1\n\t"
                     DA("%0", "%12", "row_ror:8", "0x3") DA("%1", "%13", "row_ror:8", "0x3") DA("%2", "%14", "row_ror:8", "0x3") DA("%3", "%15", "row_ror:8", "0x3")
                     DA("%4", "%16", "row_ror:8", "0x3") DA("%5", "%17", "row_ror:8", "0x3") DA("%6", "%18", "row_ror:8", "0x3") DA("%7", "%19", "row_ror:8", "0x3")
                     DA("%0", "%20", "row_ror:8", "0xc") DA("%1", "%21", "row_ror:8", "0xc") DA("%2", "%22", "row_ror:8", "0xc") DA("%3", "%23", "row_ror:8", "0xc")
                     DA("%4", "%24", "row_ror:8", "0xc") DA("%5", "%25", "row_ror:8", "0xc") DA("%6", "%26", "row_ror:8", "0xc") DA("%7", "%27", "row_ror:8", "0xc")
                     DA("%8", "%0", "row_half_mirror", "0x5") DA("%9", "%1", "row_half_mirror", "0x5") DA("%10", "%2", "row_half_mirror", "0x5") DA("%11", "%3", "row_half_mirror", "0x5")
                     DA("%8", "%4", "row_half_mirror", "0xa") DA("%9", "%5", "row_half_mirror", "0xa") DA("%10", "%6", "row_half_mirror", "0xa") DA("%11", "%7", "row_half_mirror", "0xa")
                     DA("%8", "%8", "quad_perm:[1,0,3,2]", "0xf") DA("%9", "%9", "quad_perm:[1,0,3,2]", "0xf") DA("%10", "%10", "quad_perm:[1,0,3,2]", "0xf") DA("%11", "%11", "quad_perm:[1,0,3,2]", "0xf")
                     DA("%8", "%8", "quad_perm:[2,3,0,1]", "0xf") DA("%9", "%9", "quad_perm:[2,3,0,1]", "0xf") DA("%10", "%10", "quad_perm:[2,3,0,1]", "0xf") DA("%11", "%11", "quad_perm:[2,3,0,1]", "0xf")
                     : "=&v"(r[0]), "=&v"(r[1]), "=&v"(r[2]), "=&v"(r[3]), "=&v"(r[4]), "=&v"(r[5]), "=&v"(r[6]), "=&v"(r[7]),
                       "=&v"(q[0]), "=&v"(q[1]), "=&v"(q[2]), "=&v"(q[3])
                     : "v"(s1[0][0]), "v"(s2[0][0]), "v"(s1[0][1]), "v"(s2[0][1]), "v"(s1[0][2]), "v"(s2[0][2]), "v"(s1[0][3]), "v"(s2[0][3]),
                       "v"(s1[1][0]), "v"(s2[1][0]), "v"(s1[1][1]), "v"(s2[1][1]), "v"(s1[1][2]), "v"(s2[1][2]), "v"(s1[1][3]), "v"(s2[1][3]));
#undef DA
        const f32x4 p4 = (lane & 8) ? pv[1] : pv[0];
        const float pa = (lane & 4) ? p4[2] : p4[0], pb = (lane & 4) ? p4[3] : p4[1];
        const float pp = (lane & 2) ? pb : pa, S1 = (lane & 2) ? q[2] : q[0], S2 = (lane & 2) ? q[3] : q[1];
        const float dl = S1 * (1.0f / 64.0f);
        st16((unsigned)((lane >> 1) * 16), ws + WS_ST + (size_t)((rb * 12 + pr * 3 + odd * 2 + wl) * 1024 + wm * 512),
             (u32x4){__float_as_uint(pp + dl), __float_as_uint(fmaxf(S2 - S1 * dl, 0.f)), (unsigned)tag, (unsigned)(tag >> 32)});
      } else {
#pragma unroll
        for (int jj = 0; jj < 4; ++jj) {
          u32x4 o = pack8(zv[0][jj], zv[1][jj]);
          if (jj >= 2) z1put(jj * 16 - 32, o);
          else st16(lin, ws + WS_ZX + (size_t)((rb * 4 + pr) * 4 + wm * 2 + jj) * 1024, o);
        }
      }
      SB();
      AB(7);
#pragma unroll
      for (int i = 0; i < 8; ++i) ldA1(IC(0), i);
#pragma unroll
      for (int i = 0; i < 8; ++i) ldA1(IC(1), i);
      AB(8);
      u32x4 sg[6];
      bool bad;
      do {
#pragma unroll
        for (int c = 0; c < 6; ++c)
          asm volatile("global_load_dwordx4 %0, %1, %2 offset:%3 sc1" : "=&v"(sg[c]) : "v"((unsigned)((lane >> 5) * 6144 + (lane & 31) * 16)),
                       "s"(ws + WS_ST + (size_t)rb * 12288 + wm * 512 + 2048), "i"(c * 1024 - 2048) : "memory");
        ldef();
        wait_vm<0>(sg[0], sg[1], sg[2], sg[3], sg[4], sg[5], ef);
        unsigned dl = ef[0] ^ (unsigned)tag, dh = ef[1] ^ (unsigned)(tag >> 32);
#pragma unroll
        for (int c = 0; c < 6; ++c) { dl |= sg[c][2] ^ (unsigned)tag; dh |= sg[c][3] ^ (unsigned)(tag >> 32); }
        bad = __builtin_amdgcn_ballot_w64((dl | dh) != 0) != 0;
        --tmo;
      } while (bad && tmo > 0);
      AB(9);
      if (isz2) {
        auto both = [&](float v) {
          const auto c = __builtin_amdgcn_permlane32_swap(__float_as_uint(v), __float_as_uint(v), false, false);
          return __uint_as_float(c[0]) + __uint_as_float(c[1]);
        };
        float sm = 0.f, m2 = 0.f, qq = 0.f;
#pragma unroll
        for (int c = 0; c < 6; ++c) { sm += __uint_as_float(sg[c][0]); m2 += __uint_as_float(sg[c][1]); }
        const float mean = both(sm) * (1.0f / 12.0f);
#pragma unroll
        for (int c = 0; c < 6; ++c) { const float a = __uint_as_float(sg[c][0]) - mean; qq = __builtin_fmaf(a, a, qq); }
        const float vbf = rbf(both(__builtin_fmaf(qq, 64.0f, m2)) * (1.0f / 768.0f));
        const float rz = __builtin_amdgcn_rcpf(rbf(__builtin_amdgcn_sqrtf(rbf(vbf + 1e-5f))));
        char* zs = smem + L_ZS + wave * 256;
        *(float*)(zs + lane * 4) = lane < 32 ? rbf(mean) : rz;
        f32x4 zm[2], zr[2];
#pragma unroll
        for (int i = 0; i < 2; ++i) {
          zm[i] = *(const f32x4*)(zs + (g4 * 8 + i * 4) * 4);
          zr[i] = *(const f32x4*)(zs + 128 + (g4 * 8 + i * 4) * 4);
        }
#pragma unroll
        for (int jj = 0; jj < 4; ++jj) {
          u32x4 o = pack8((zv[0][jj] - zm[0]) * zr[0], (zv[1][jj] - zm[1]) * zr[1]);
          o[3] = nohole(o[3]);
          st16(lin, ws + WS_Z2 + (size_t)((rb * 2 + wm) * 48 + pr * 12 + odd * 8 + wl * 4 + jj) * 1024, o);
        }
      } else sttag(ws + WS_XF + (rb * 4 + pr) * 16 + wm * 8);
      __builtin_amdgcn_s_setprio(0);
      SB();
      AB(10);
    }
    const char* Bs = ws + WS_Z2 + (size_t)(wave * 16 * 48 + cb * 6) * 1024 + 2048;
    bf16x8 Bf[3][6];
    auto ldB1 = [&](int s, int j) {
      asm volatile("global_load_dwordx4 %0, %1, %2 offset:%3 " HSC : "=&v"(Bf[s % 3][j]) : "v"(lin + s * 49152), "s"(Bs), "i"(j * 1024 - 2048) : "memory");
    };
    flag_wait_asm(flg + 16, 4u);
#pragma unroll
    for (int i = 0; i < 8; ++i) ldA1(IC(2), i);
    AB(11);
    f32x4 acc[4][6];
    unsigned ara[2];
#pragma unroll
    for (int ks = 0; ks < 2; ++ks) ara[ks] = ringl + l15 * 128 + (((ks * 4 + g4) ^ (l15 & 7)) << 4);
    u32x4 af[4];
    auto rdA = [&](int s, int i) {
      asm volatile("ds_read_b128 %0, %1 offset:%2" : "=&v"(af[i]) : "v"(ara[s & 1]), "i"(((s >> 1) % 3) * 8192 + i * 2048) : "memory");
    };
    asm volatile("s_waitcnt vmcnt(8)" ::: "memory");
    signal1(flg + 28);
    flag_wait_asm(flg + 28, 4u);
#pragma unroll
    for (int i = 0; i < 4; ++i) rdA(0, i);
    AB(12);
#pragma unroll
    for (int j = 0; j < 6; ++j) ldB1(0, j);
#pragma unroll
    for (int j = 0; j < 6; ++j) ldB1(1, j);
#pragma unroll
    for (int j = 0; j < 6; ++j) ldB1(2, j);
    asm volatile("s_waitcnt lgkmcnt(3)" : "+v"(af[0]) :: "memory");
    SB();
    unsigned bsv;
    const unsigned xa = ringl + lane * 16;
    auto putp = [&](int i, int j) { asm volatile("ds_write_b128 %0, %1 offset:%2" :: "v"(xa), "v"(acc[i][j]), "i"(i * 6144 + j * 1024) : "memory"); };
    auto ldbs = [&]() { asm volatile("global_load_ushort %0, %1, %2" : "=&v"(bsv) : "v"((unsigned)(l15 * 2)), "s"(b_sp + t0 + wave * 16) : "memory"); };
    auto step = [&](auto sc) {
      constexpr int s = decltype(sc)::value, sl = s % 3;
      constexpr int NV = s < 2 ? 12 : (s < 12 ? 20 : (s == 12 ? 12 : 13 - 6 * (s - 13)));
      constexpr int DA = ((s & 1) && (s + 5) / 2 < 8) ? (s + 5) / 2 : -1;
      asm volatile("s_waitcnt vmcnt(%0) lgkmcnt(2)" :: "n"(NV) : "memory");
      pin(Bf[sl][0], Bf[sl][1], Bf[sl][2], Bf[sl][3], Bf[sl][4], Bf[sl][5], af[1]);
      {
        auto d3 = [&](int j) { return __builtin_bit_cast(u32x4, Bf[sl][j])[3]; };
        auto bad = [&]() { return __builtin_amdgcn_ballot_w64(umx(umx(umx(d3(0), d3(1)), d3(2)), umx(umx(d3(3), d3(4)), d3(5))) == HOLE) != 0; };
        if (__builtin_expect(bad(), 0)) {
          do {
            --tmo;
            asm volatile("s_waitcnt vmcnt(0)\n\ts_sleep 2" ::: "memory");
#pragma unroll
            for (int q = 0; q < 3; ++q)
              if (s + q < 16) reld6(Bf[(s + q) % 3], opq(lin) + (s + q) * 49152, Bs);
            wait_vm<0>(Bf[sl][0], Bf[sl][1], Bf[sl][2], Bf[sl][3], Bf[sl][4], Bf[sl][5]);
          } while (bad() && tmo > 0);
        }
      }
      if constexpr (s == SGS) signal1(flg + 44);
      SB();
#pragma unroll
      for (int i = 0; i < 4; ++i) {
        if (i == 1) wait_lg<(s < 15 ? 1 : 0)>(af[2], af[3]);
        if (i == 3 && s < 15) wait_lg<2>(af[0]);
#pragma unroll
        for (int j = 0; j < 6; ++j) {
          if constexpr (s == 0) MFMA0(acc[i][j], Bf[sl][j], af[i]); else MFMA(acc[i][j], Bf[sl][j], af[i]);
          SB();
          if constexpr (DA >= 0) { if ((i == 1 || i == 2) && j < 4) ldA1(IC(DA < 0 ? 0 : DA), (i - 1) * 4 + j); }
          if constexpr (s + 3 < 16) { if (i == 3) ldB1(s + 3, j); }
          if constexpr (s == 15) {
            if (i > 0 && j > 1) putp(i - 1, j - 2);
          }
          SB();
        }
        if constexpr (s == 15) {
          if (i > 0) {
            putp(i - 1, 4); putp(i - 1, 5);
            signal1(flg + ((wave + i) & 3) * 4);
          }
        }
        if constexpr (s < 15) rdA(s + 1, i);
        SB();
      }
      if constexpr (s == 12) ldbs();
      SB();
    };
    step(IC(0)); AB(13); step(IC(1)); step(IC(2)); step(IC(3)); AB(14); step(IC(4)); step(IC(5)); step(IC(6)); step(IC(7));
    step(IC(8)); step(IC(9)); step(IC(10)); step(IC(11)); step(IC(12)); step(IC(13)); step(IC(14)); step(IC(15)); AB(15);
    ldW1();
    flag_wait_asm(flg + 56, 5u);
    s16x4 z1r[6];
    {
      const int fr = 4 * g4 + (l15 >> 2), p = lane & 3;
      const unsigned za = lb + L_Z1 + fr * 128 + (((wave * 2 + (p >> 1)) ^ (fr & 7)) << 4) + (p & 1) * 8;
      z1r[0] = tr_asm<0>(za); z1r[1] = tr_asm<2048>(za); z1r[2] = tr_asm<4096>(za);
      z1r[3] = tr_asm<6144>(za); z1r[4] = tr_asm<8192>(za); z1r[5] = tr_asm<10240>(za);
    }
    flag_wait_asm(flg + wave * 4, 3u);
    asm volatile("" : "+v"(z1r[0]), "+v"(z1r[1]), "+v"(z1r[2]), "+v"(z1r[3]), "+v"(z1r[4]), "+v"(z1r[5]));
    AB(16);
    f32x4 own[6];
    {
      f32x4 pr_[3][6];
#pragma unroll
      for (int q = 0; q < 3; ++q) {
        const char* src = smem + M_RING + ((wave + 3 - q) & 3) * 24576;
#pragma unroll
        for (int j = 0; j < 6; ++j) pr_[q][j] = *(const f32x4*)(src + q * 6144 + j * 1024 + lane * 16);
      }
      asm volatile("s_nop 7" : "+v"(acc[3][0]), "+v"(acc[3][1]), "+v"(acc[3][2]), "+v"(acc[3][3]), "+v"(acc[3][4]), "+v"(acc[3][5]));
#pragma unroll
      for (int j = 0; j < 6; ++j) own[j] = (acc[3][j] + pr_[0][j]) + (pr_[1][j] + pr_[2][j]);
    }
    asm volatile("s_waitcnt vmcnt(%1)" : "+v"(bsv) : "i"(NW1 + 1) : "memory");
    SB();
    {
      const float bs = lo16(bsv);
      unsigned pk[6][2];
#pragma unroll
      for (int j = 0; j < 6; ++j) {
        const u32x2 z = __builtin_bit_cast(u32x2, z1r[j]);
#pragma unroll
        for (int h = 0; h < 2; ++h) {
          const unsigned r0 = pack2(own[j][2 * h], own[j][2 * h + 1]);
          const unsigned g = pack2(lo16(r0) + bs, hi16(r0) + bs);
          pk[j][h] = pack2(lo16(z[h]) * lo16(g), hi16(z[h]) * hi16(g));
        }
      }
      const unsigned eo = (unsigned)((l15 + 16 * (2 * (g4 & 1) + (g4 >> 1))) * 16);
#pragma unroll
      for (int q = 0; q < 3; ++q) {
        const auto s0 = __builtin_amdgcn_permlane16_swap(pk[2 * q][0], pk[2 * q + 1][0], false, false);
        const auto s1 = __builtin_amdgcn_permlane16_swap(pk[2 * q][1], pk[2 * q + 1][1], false, false);
        st16(eo, hf + (size_t)((rb * 4 + wave) * 24 + cb * 3 + q) * 1024, (u32x4){s0[0], s1[0], s0[1], nohole(s1[1])});
      }
    }
    AB(17);
    drain1(3);
  }
  unsigned hoff[4];
#pragma unroll
  for (int it = 0; it < 4; ++it) hoff[it] = (unsigned)(((rb * 4 + ((mt + 1 + it) & 3)) * 24 + 3 * wave) * 1024 + lane * 16);
  u32x4 Hq[4][3];
  auto ldh1 = [&](int it, int k) {
    asm volatile("global_load_dwordx4 %0, %1, %2 offset:%3 " HSC : "=&v"(Hq[it][k]) : "v"(hoff[it]), "s"(hf), "i"(k * 1024) : "memory");
  };
  auto ldh0 = [&](int it) {
#pragma unroll
    for (int k = 0; k < 3; ++k) ldh1(it, k);
  };
  f32x2 xf[3][2], sv[3][2];
  auto ymath = [&]() {
    char* yi = smem + P3_Y + wave * 1536;
#pragma unroll
    for (int n = 0; n < 2; ++n) *(u32x4*)(yi + (n ? 64 + (lane & 31) : lane) * 16) = yv[n];
    const unsigned ya = lds_addr(yi) + (4 * g4 + (l15 >> 2)) * 32 + 8 * (lane & 3);
    const float sm = *(const float*)(smem + L_XM + (mt * 16 + l15) * 4), sr = *(const float*)(smem + L_XR + (mt * 16 + l15) * 4);
    s16x4 yr[3] = {tr_asm<0>(ya), tr_asm<512>(ya), tr_asm<1024>(ya)};
    asm volatile("s_waitcnt lgkmcnt(0)" : "+v"(yr[0]), "+v"(yr[1]), "+v"(yr[2]) :: "memory");
    const f32x2 mean = {sm, sm}, rd = {sr, sr};
#pragma unroll
    for (int q = 0; q < 3; ++q) {
      const u32x2 yw = __builtin_bit_cast(u32x2, yr[q]);
#pragma unroll
      for (int h = 0; h < 2; ++h) {
        xf[q][h] = unpk(xv[q][h]);
        sv[q][h] = ((xf[q][h] - mean) * rd) * unpk(yw[h]);
        asm volatile("" : "+v"(sv[q][h]), "+v"(xf[q][h]));
      }
    }
    SB();
  };
  u32x2 yf;
  const unsigned yfo = (unsigned)((lane < 32 ? 32 * kh + lane : 64 + (lane & 15)) * 8);
#pragma unroll
  for (int n = NW1; n < 18; ++n) ldP1(n);
  ldtag(yf, yfo, ws + WS_YF + d0 * 8);
  SB();
  asm volatile("s_waitcnt vmcnt(%0)" :: "i"(19 - NW1) : "memory");
  waittag(ef, 0u, ws + WS_LF + (rb * 8 + wave) * 8);
  AB(18);
  signal1(flg + 48 + 4 * kh);
  flag_wait_asm(flg + 48, 4u);
  AB(19);
  __builtin_amdgcn_s_setprio(3);
  ldh0(0);
  __builtin_amdgcn_s_setprio(2);
  ldh0(1);
  __builtin_amdgcn_s_setprio(0);
  SB();
  flag_wait_asm(flg + 40, kh ? 4u : 0u);
  char* const px = smem + (kh ? P3_C + mt * 18432 : P3_G + mt * 17408) + lane * 16;
  f32x4 ac3[5], o[3];
  auto ldy = [&]() {
#pragma unroll
    for (int n = 0; n < 2; ++n) asm volatile("global_load_dwordx4 %0, %1, %2 sc1" : "=&v"(yv[n]) : "v"(yoff(n)), "s"(yb) : "memory");
  };
  auto relh = [&](int it) {
    asm volatile("s_sleep 2\n\tglobal_load_dwordx4 %0, %3, %4 sc1\n\tglobal_load_dwordx4 %1, %3, %4 offset:1024 sc1\n\t"
                 "global_load_dwordx4 %2, %3, %4 offset:2048 sc1\n\ts_waitcnt vmcnt(0)"
                 : "+v"(Hq[it][0]), "+v"(Hq[it][1]), "+v"(Hq[it][2]) : "v"(hoff[it]), "s"(hf) : "memory");
  };
  auto comp = [&](auto itc) {
    constexpr int it = decltype(itc)::value;
    constexpr int NW = 3 * ((it + 2 < 4 ? it + 2 : 4) - it - 1) + (it >= 2 ? 2 : 0);
    wait_vm<NW>(Hq[it][0], Hq[it][1], Hq[it][2]);
    if constexpr (it == 0) pin(Wq[0], Wq[1], Wq[2], Wq[3], Wq[4], Wq[5], Wq[6], Wq[7], Wq[8], Wq[9], Wq[10], Wq[11], Wq[12], Wq[13], Wq[14], xv[0], xv[1], xv[2], yf);
    auto bad = [&]() { return __builtin_amdgcn_ballot_w64(umx(umx(Hq[it][0][3], Hq[it][1][3]), Hq[it][2][3]) == HOLE) != 0; };
    if (__builtin_expect(bad(), 0)) {
      do {
        --tmo;
        relh(it);
      } while (bad() && tmo > 0);
    }
    SB();
#pragma unroll
    for (int k = 0; k < 3; ++k) {
#pragma unroll
      for (int j = 0; j < 5; ++j) {
        ac3[j] = __builtin_amdgcn_mfma_f32_16x16x32_bf16(Wq[k * 5 + j], __builtin_bit_cast(bf16x8, Hq[it][k]), k ? ac3[j] : (f32x4){0.f, 0.f, 0.f, 0.f}, 0, 0, 0);
        if constexpr (it + 2 < 4) { if (k == 0 && j < 3) { SB(); ldh1(it + 2 < 4 ? it + 2 : 0, j); SB(); } }
        if constexpr (it == 1) {
          if (k == 1 && j == 0) {
            SB();
            ldy();
            SB();
          }
        }
      }
    }
    SB();
    if constexpr (it < 3) {
#pragma unroll
      for (int j = 0; j < 5; ++j) *(f32x4*)(px + (it * 5 + j) * 1024) = ac3[j];
      signal1(flg + 64 + kh * 16 + ((mt + 1 + it) & 3) * 4);
    }
    SB();
  };
  auto gather = [&](auto cc) {
    constexpr int cv = decltype(cc)::value;
    flag_wait_asm(flg + 64 + cv * 16 + mt * 4, cv ? 5u : 3u);
#pragma unroll
    for (int it = 0; it < 3 + cv; ++it) {
      const int ms = (mt - 1 - it) & 3;
      const char* ps = smem + ((it < 3 ? cv : !kh) ? P3_C + ms * 18432 : P3_G + ms * 17408) + lane * 16;
      const char* p0 = ps + (it < 3 ? it * 5 + 2 * kh : 15) * 1024;
      const f32x4 a0 = *(const f32x4*)p0, a1 = *(const f32x4*)(p0 + 1024);
      if (!cv && it == 0) { o[0] = a0; o[1] = a1; } else { o[0] += a0; o[1] += a1; }
    }
    if (!kh) {
#pragma unroll
      for (int it = 0; it < 3 + cv; ++it) {
        const int ms = (mt - 1 - it) & 3;
        const char* ps = smem + ((it < 3 ? cv : 1) ? P3_C + ms * 18432 : P3_G + ms * 17408) + lane * 16;
        o[2] += *(const f32x4*)(ps + (it < 3 ? it * 5 + 4 : 17) * 1024);
      }
    }
    SB();
  };
  o[2] = (f32x4){0.f, 0.f, 0.f, 0.f};
  comp(IC(0)); AB(20); comp(IC(1)); comp(IC(2));
  AB(21);
  gather(IC(0));
  comp(IC(3));
  AB(22);
  {
    *(f32x4*)(px + 15 * 1024) = kh ? ac3[0] : ac3[2];
    *(f32x4*)(px + 16 * 1024) = kh ? ac3[1] : ac3[3];
    if (kh) *(f32x4*)(px + 17 * 1024) = ac3[4];
    signal1(flg + 80 + mt * 4);
    SB();
    o[0] += kh ? ac3[2] : ac3[0]; o[1] += kh ? ac3[3] : ac3[1]; o[2] += ac3[4];
    asm volatile("s_waitcnt vmcnt(0)" : "+v"(yv[0]), "+v"(yv[1]) :: "memory");
    if (__builtin_expect(notag(yf), 0)) {
      waittag(yf, yfo, ws + WS_YF + d0 * 8);
      ldy();
      wait_vm<0>(yv[0], yv[1]);
    }
    ymath();
    AB(23);
    gather(IC(1));
    auto fin = [&](const f32x4& o4, int q, unsigned (&pk)[2]) {
#pragma unroll
      for (int h = 0; h < 2; ++h) {
        const f32x2 o = {o4[2 * h], o4[2 * h + 1]};
        const f32x2 a = o * (f32x2){-1.4426950f, -1.4426950f};
        const f32x2 e = (f32x2){__builtin_amdgcn_exp2f(a[0]), __builtin_amdgcn_exp2f(a[1])} + (f32x2){1.0f, 1.0f};
        const f32x2 r = {__builtin_amdgcn_rcpf(e[0]), __builtin_amdgcn_rcpf(e[1])};
        pk[h] = pk2(__builtin_elementwise_fma(o * r, sv[q][h], xf[q][h]));
      }
    };
    unsigned pa[2], pb[2];
    fin(o[0], 0, pa);
    fin(o[1], 1, pb);
    const auto s0 = __builtin_amdgcn_permlane16_swap(pa[0], pb[0], false, false);
    const auto s1 = __builtin_amdgcn_permlane16_swap(pa[1], pb[1], false, false);
    *(u32x4*)(out + xo + 32 * kh + 16 * (g4 & 1) + 8 * (g4 >> 1)) = (u32x4){s0[0], s1[0], s0[1], s1[1]};
    if (!kh) {
      fin(o[2], 2, pa);
      *(u32x2*)(out + xo + 64 + 4 * g4) = (u32x2){pa[0], pa[1]};
    }
  }
  if (b == 0 && tid == 0) *(u64*)(ws + WS_EP) = tag;
  AB(24);
  AB_OUT();
}

extern "C" void kernel_launch(void* const* d_in, const int* in_sizes, int n_in,
                              void* d_out, int out_size, void* d_ws, size_t ws_size,
                              hipStream_t stream) {
  (void)hipFuncSetAttribute((const void*)k_f, hipFuncAttributeMaxDynamicSharedMemorySize, M_LDS);
  k_f<<<256, 512, M_LDS, stream>>>((const u16*)d_in[0], (const u16*)d_in[1], (const u16*)d_in[2], (const u16*)d_in[3], (const u16*)d_in[4],
                                   (const u16*)d_in[5], (const u16*)d_in[7], (const u16*)d_in[8], (const u16*)d_in[9], (const u16*)d_in[10],
                                   (u16*)d_out, (char*)d_ws);
}
```

```cpp
#define PROF_AB
#define ABM (1u | (1u << 24))
#define HQ 24
#define HM 'z'
#include <hip/hip_runtime.h>
#include <hip/hip_bf16.h>
#include <stdint.h>
#include <type_traits>

typedef unsigned short u16;
typedef unsigned long long u64;
typedef __attribute__((ext_vector_type(8))) short bf16x8;
typedef __attribute__((ext_vector_type(4))) short s16x4;
typedef __attribute__((ext_vector_type(4))) float f32x4;
typedef __attribute__((ext_vector_type(2))) float f32x2;
typedef __attribute__((ext_vector_type(4))) unsigned int u32x4;
typedef __attribute__((ext_vector_type(2))) unsigned int u32x2;
typedef __attribute__((ext_vector_type(2))) __bf16 bf16x2n;

#define LDS_AS __attribute__((address_space(3)))
#define IC(n) std::integral_constant<int, (n)>{}
#define SB() __builtin_amdgcn_sched_barrier(0)

__device__ unsigned g_ab[256][8][32];
#define AB_DECL unsigned ab_[32] = {}
#define AB(i) do { SB(); if ((ABM >> (i)) & 1) ab_[i] = (unsigned)__builtin_amdgcn_s_memrealtime(); SB(); } while (0)
#define AB_OUT() do { if ((threadIdx.x & 63) == 0) for (int i_ = 0; i_ < 32; ++i_) g_ab[blockIdx.x][threadIdx.x >> 6][i_] = ab_[i_] ? ab_[i_] : ab_[0]; } while (0)
#define CB(i) do {} while (0)
constexpr int S_ = 2048, D_ = 640, FH_ = 768, F2_ = 1536;
constexpr unsigned HOLE = 0xFFFFFFFFu;

constexpr size_t WS_EP = 0;
constexpr size_t WS_EF = 4096;
constexpr size_t WS_LF = 6144;
constexpr size_t WS_YF = 8192;
constexpr size_t WS_XF = 16384;
constexpr size_t WS_ST = 32768;
constexpr size_t WS_Z2 = WS_ST + 32 * 12 * 1024;
constexpr size_t WS_H = WS_Z2 + 64 * 48 * 1024;
constexpr size_t WS_ZX = WS_H + 128 * 24 * 1024;
constexpr size_t WS_KV = WS_ZX + 32 * 4 * 4096;
constexpr size_t WS_Y = WS_KV + D_ * S_ * 2;
constexpr size_t WS_WO = WS_Y + D_ * S_ * 2;

__device__ __forceinline__ float bf2f(u16 v) { return __uint_as_float(((unsigned)v) << 16); }
__device__ __forceinline__ unsigned pack2(float lo, float hi) {
  f32x2 v = {lo, hi};
  bf16x2n r = __builtin_convertvector(v, bf16x2n);
  return __builtin_bit_cast(unsigned, r);
}
__device__ __forceinline__ float lo16(unsigned w) { return __uint_as_float(w << 16); }
__device__ __forceinline__ float hi16(unsigned w) { return __uint_as_float(w & 0xFFFF0000u); }
__device__ __forceinline__ float rbf(float f) { return lo16(pack2(f, f)); }
__device__ __forceinline__ f32x2 unpk(unsigned w) { f32x2 r = {lo16(w), hi16(w)}; return r; }
__device__ __forceinline__ unsigned pk2(f32x2 v) { return pack2(v[0], v[1]); }
__device__ __forceinline__ unsigned umx(unsigned a, unsigned b) { return a > b ? a : b; }
__device__ __forceinline__ unsigned nohole(unsigned a) { return a < HOLE - 1 ? a : HOLE - 1; }
__device__ __forceinline__ f32x2 gelu2(f32x2 x) {
  const f32x2 c1 = {-2.3022082f, -2.3022082f}, c2 = {-0.10294324f, -0.10294324f};
  const f32x2 a = x * __builtin_elementwise_fma(x * x, c2, c1);
  f32x2 e = {__builtin_amdgcn_exp2f(a[0]), __builtin_amdgcn_exp2f(a[1])};
  e = e + (f32x2){1.0f, 1.0f};
  const f32x2 r = {__builtin_amdgcn_rcpf(e[0]), __builtin_amdgcn_rcpf(e[1])};
  return x * r;
}
template <int OFF>
__device__ __forceinline__ s16x4 tr_asm(unsigned a) {
  s16x4 r;
  asm volatile("ds_read_b64_tr_b16 %0, %1 offset:%2" : "=&v"(r) : "v"(a), "i"(OFF) : "memory");
  return r;
}
__device__ __forceinline__ bf16x8 join8(s16x4 lo, s16x4 hi) {
  bf16x8 r;
  r[0] = lo[0]; r[1] = lo[1]; r[2] = lo[2]; r[3] = lo[3];
  r[4] = hi[0]; r[5] = hi[1]; r[6] = hi[2]; r[7] = hi[3];
  return r;
}
__device__ __forceinline__ unsigned lds_addr(const void* p) { return (unsigned)(uintptr_t)(LDS_AS const char*)p; }
#define WAIT_LGKM0() asm volatile("s_waitcnt lgkmcnt(0)" ::: "memory")
__device__ __forceinline__ void flag_wait_asm(unsigned faddr, unsigned target) {
  unsigned v, sv;
  asm volatile(".Lfw_%=:\n\tds_read_b32 %0, %2\n\ts_waitcnt lgkmcnt(0)\n\tv_readfirstlane_b32 %1, %0\n\ts_nop 0\n\t"
               "s_cmp_ge_u32 %1, %3\n\ts_cbranch_scc1 .Lfd_%=\n\ts_sleep 1\n\ts_branch .Lfw_%=\n\t.Lfd_%=:"
               : "=&v"(v), "=&s"(sv) : "v"(faddr), "s"(target) : "memory", "scc");
}
__device__ __forceinline__ void signal1(unsigned a) {
  const unsigned one = 1u;
  asm volatile("s_mov_b64 exec, 1\n\tds_add_u32 %0, %1\n\ts_mov_b64 exec, -1" :: "v"(a), "v"(one) : "memory");
}
__device__ __forceinline__ void dma16(unsigned vo, const void* sb, unsigned lds) {
  asm volatile("s_mov_b32 m0, %2\n\ts_nop 0\n\tglobal_load_lds_dwordx4 %0, %1" :: "v"(vo), "s"(sb), "s"(lds) : "memory");
}
template <int LO, int GO> __device__ __forceinline__ void dma32(unsigned vo0, unsigned vo1, const void* sb, unsigned lds) {
  asm volatile("s_add_u32 m0, %5, %6\n\ts_add_u32 vcc_lo, %2, %4\n\ts_addc_u32 vcc_hi, %3, 0\n\t"
               "global_load_lds_dwordx4 %0, vcc\n\tglobal_load_lds_dwordx4 %1, vcc offset:1024"
               :: "v"(vo0), "v"(vo1), "s"((unsigned)(uintptr_t)sb), "s"((unsigned)((uintptr_t)sb >> 32)), "i"(GO), "s"(lds), "i"(LO) : "memory", "scc", "vcc");
}
__device__ __forceinline__ void dma16o(unsigned vo, const void* sb, int go, unsigned lds, int lo) {
  asm volatile("s_add_u32 m0, %3, %4\n\ts_nop 0\n\tglobal_load_lds_dwordx4 %0, %1 offset:%2" :: "v"(vo), "s"(sb), "i"(go), "s"(lds), "i"(lo - go) : "memory", "scc");
}
__device__ __forceinline__ void st16(unsigned vo, const void* sb, u32x4 v) {
  asm volatile("global_store_dwordx4 %0, %1, %2 sc1\n\ts_nop 1" :: "v"(vo), "v"(v), "s"(sb) : "memory");
}
template <class A, class B> __device__ __forceinline__ void MFMA(f32x4& acc, const A& a, const B& b) {
  asm volatile("v_mfma_f32_16x16x32_bf16 %0, %1, %2, %0" : "+v"(acc) : "v"(a), "v"(b));
}
template <class A, class B> __device__ __forceinline__ void MFMA0(f32x4& acc, const A& a, const B& b) {
  asm volatile("v_mfma_f32_16x16x32_bf16 %0, %1, %2, 0" : "=&v"(acc) : "v"(a), "v"(b));
}
template <bool Z, class A, class B> __device__ __forceinline__ void MFMAW(int n, f32x4& acc, const A& a, const B& b) {
  if (n < 0) { if (Z) MFMA0(acc, a, b); else MFMA(acc, a, b); }
  else if (Z) asm volatile("s_waitcnt lgkmcnt(%3)\n\tv_mfma_f32_16x16x32_bf16 %0, %1, %2, 0" : "=&v"(acc) : "v"(a), "v"(b), "i"(n));
  else asm volatile("s_waitcnt lgkmcnt(%3)\n\tv_mfma_f32_16x16x32_bf16 %0, %1, %2, %0" : "+v"(acc) : "v"(a), "v"(b), "i"(n));
}
template <class T> __device__ __forceinline__ void pin1(T& r) { asm volatile("" : "+v"(r)); }
template <class T> __device__ __forceinline__ T opq(T r) { asm volatile("" : "+v"(r)); return r; }
template <class... T> __device__ __forceinline__ void pin(T&... r) { (pin1(r), ...); }
template <int N, class... T> __device__ __forceinline__ void wait_vm(T&... r) { asm volatile("s_waitcnt vmcnt(%0)" :: "n"(N) : "memory"); (pin1(r), ...); }
template <int N, class... T> __device__ __forceinline__ void wait_lg(T&... r) { asm volatile("s_waitcnt lgkmcnt(%0)" :: "n"(N) : "memory"); (pin1(r), ...); }
template <int OFF, class T> __device__ __forceinline__ void ds16(T& d, unsigned a) {
  asm volatile("ds_read_b128 %0, %1 offset:%2" : "=&v"(d) : "v"(a), "i"(OFF) : "memory");
}
template <int OFF> __device__ __forceinline__ void dsw16(unsigned a, const f32x4& v) {
  asm volatile("ds_write_b128 %0, %1 offset:%2" :: "v"(a), "v"(v), "i"(OFF) : "memory");
}
#define HSC ""
template <int OFF, class T> __device__ __forceinline__ void ld16h(T& d, unsigned vo, const void* sb) {
  asm volatile("global_load_dwordx4 %0, %1, %2 offset:%3 " HSC : "=&v"(d) : "v"(vo), "s"(sb), "i"(OFF) : "memory");
}
template <class T> __device__ __forceinline__ void reld6(T (&B)[6], unsigned vo, const void* sb) {
  asm volatile("global_load_dwordx4 %0, %6, %7 offset:-2048 sc1\n\tglobal_load_dwordx4 %1, %6, %7 offset:-1024 sc1\n\t"
               "global_load_dwordx4 %2, %6, %7 sc1\n\tglobal_load_dwordx4 %3, %6, %7 offset:1024 sc1\n\t"
               "global_load_dwordx4 %4, %6, %7 offset:2048 sc1\n\tglobal_load_dwordx4 %5, %6, %7 offset:3072 sc1"
               : "+v"(B[0]), "+v"(B[1]), "+v"(B[2]), "+v"(B[3]), "+v"(B[4]), "+v"(B[5]) : "v"(vo), "s"(sb) : "memory");
}

constexpr int L_FLAG = 0;
constexpr int L_XM = 128;
constexpr int L_XR = 384;
constexpr int L_CS = 640;
constexpr int L_ZS = 1664;
constexpr int L_CS2 = 2688;
constexpr int L_Z1 = 4096;
constexpr int L_RAW = L_Z1;
constexpr int L_SLOT = 16384;
constexpr int L_KV = 128, KV_P = 3088, L_WO = L_KV + 8 * KV_P + 16;
static_assert(L_WO % 16 == 0 && L_WO + 4 * 1024 <= L_SLOT + 2 * 8192, "staging");
constexpr int CV_C = 4608, CV_CS = 4160, CV_BUF = CV_C + 2 * CV_CS;
constexpr int M_CV = L_SLOT;
constexpr int M_RING = L_SLOT + 6 * 8192;
constexpr int P3_C = L_SLOT;
constexpr int P3_G = P3_C + 72 * 1024;
constexpr int P3_Y = L_Z1;
constexpr int M_LDS = 163840;
static_assert(3 * CV_BUF <= 5 * 8192 && P3_G + 68 * 1024 <= M_LDS && M_RING <= P3_G, "LDS map");

enum { UX, UA, UB, UV };
struct Unit { int kind, idx, need, free, iss, slot, grp; };
struct Plan { Unit u[40]; int first[14], nv[2][12]; bool ok; };
#define ISSUE {7, 3, 4, 4, 4, 4, 3, 2, 2, 2, 2, 2, 1}
#define NSTC {0, 0, 0, 0, 0, 0, 1, 1, 1, 0, 0, 0, 0}
#define GZ 6
#define FLAGB 10
constexpr int XNEED[10] = {1, 2, 2, 3, 3, 4, 4, 5, 5, 6};
constexpr int XSLOT[10] = {8, 11, 14, 6, 7, 17, 12, 8, 13, 11}, WSLOT[10] = {2, 4, 9, 0, 15, 2, 6, 4, 9, 12}, VSLOT[10] = {8, 11, 17, 14, 2, 3, 0, 4, 5, 1};
constexpr int NST[2][13] = {{0, 2}, NSTC};
constexpr Plan make_plan() {
  Plan p = {};
  int n = 0;
  for (int b = 1; b < 11; ++b) {
    for (int c = 0; c < 10; ++c) if (XNEED[c] == b) p.u[n++] = {UX, c, b, b + 1, 0, XSLOT[c], 1};
    p.u[n++] = {UA, b - 1, b, b + 2 < 11 ? b + 2 : 11, 0, WSLOT[b - 1], 1};
    p.u[n++] = {UB, b - 1, b, b + 2 < 11 ? b + 2 : 11, 0, WSLOT[b - 1] + 1, 1};
  }
  for (int c = 0; c < 10; ++c) p.u[n++] = {UV, c, 99, 99, 0, VSLOT[c], 1};
  const int is[13] = ISSUE;
  n = 0;
  for (int q = 0; q < 13; ++q) { p.first[q] = n; for (int i = 0; i < is[q]; ++i) { p.u[n].grp = q == 0 ? n & 1 : 1; p.u[n++].iss = q - 1; } }
  p.first[13] = n;
  p.ok = n == 40;
  int fr[18] = {1, 1, -1, -1, -1, -1, -1, -1, -1, -1, -1, -1, -1, -1, -1, -1, -1, -1};
  for (int i = 0; i < 40; ++i) {
    if (p.u[i].iss < fr[p.u[i].slot]) p.ok = false;
    fr[p.u[i].slot] = p.u[i].free;
  }
  int lasts = 3;
  for (int q = 0; q < 13; ++q) if (NST[1][q] && q - 1 > lasts) lasts = q - 1;
  for (int g = 0; g < 2; ++g)
    for (int b = 0; b < 12; ++b) {
      int cnt = 0, lastq = -9;
      for (int q = 0; q <= b; ++q) {
        for (int i = p.first[q]; i < p.first[q + 1]; ++i)
          if (p.u[i].grp == g) { if (p.u[i].need <= b) { cnt = 0; lastq = q - 1; } else cnt += 2; }
        cnt += NST[g][q];
        if (g == 1 && b == FLAGB && q - 1 == lasts) cnt = 0;
      }
      for (int i = p.first[b + 1]; i < 40; ++i) if (p.u[i].need <= b) p.ok = false;
      p.nv[g][b] = g == 0 && b >= GZ ? 0 : cnt;
    }
  if (FLAGB <= lasts || FLAGB < GZ) p.ok = false;
  return p;
}
constexpr Plan PL = make_plan();
static_assert(PL.ok, "phase A plan");

__device__ __forceinline__ void conv_core(unsigned buf, int lane, f32x4 (&ac)[8]) {
  const int l15 = lane & 15, g4 = lane >> 4;
  const int c = (l15 + 1) & 1;
  unsigned aa[4];
#pragma unroll
  for (int k = 0; k < 4; ++k) aa[k] = buf + CV_C + c * CV_CS + (2031 - l15 + 8 * g4 - c) * 2 - 512 - 1024 * k;
  unsigned bb = buf + 512 + 32 * l15 - 32 + 16 * g4;
  u32x2 A[2][8][2];
  u32x4 B[2][8];
  constexpr int TT[36] = {7, 6, 5, 4, 7, 6, 5, 4, 7, 6, 5, 3, 7, 6, 5, 4, 7, 6, 3, 2, 7, 6, 5, 4, 7, 3, 2, 1, 7, 6, 5, 4, 3, 2, 1, 0};
  constexpr int AA[36] = {0, 0, 0, 0, 1, 1, 1, 1, 2, 2, 2, 0, 3, 3, 3, 2, 4, 4, 1, 0, 5, 5, 4, 3, 6, 2, 1, 0, 7, 6, 5, 4, 3, 2, 1, 0};
  auto ld1 = [&](int p, int n) {
    if (n < 16) {
      const int a = n >> 1, o = ((a & 1) ? 0 : 128) + 2 * (n & 1);
      asm volatile("ds_read2_b32 %0, %1 offset0:%2 offset1:%3" : "=&v"(A[p][(n >> 1) & 7][n & 1]) : "v"(aa[(a >> 1) & 3]), "i"(o), "i"(o + 1) : "memory");
    } else
      asm volatile("ds_read_b128 %0, %1 offset:%2" : "=&v"(B[p][n & 7]) : "v"(bb), "i"(512 * (n & 7)) : "memory");
  };
  auto next = [&](int dl) {
#pragma unroll
    for (int k = 0; k < 4; ++k) aa[k] += dl;
    bb += dl;
    SB();
  };
  auto land = [&](int p) {
    asm volatile("s_waitcnt lgkmcnt(0)" : "+v"(A[p][0][0]), "+v"(A[p][0][1]), "+v"(A[p][1][0]), "+v"(A[p][1][1]), "+v"(A[p][2][0]), "+v"(A[p][2][1]),
                 "+v"(A[p][3][0]), "+v"(A[p][3][1]), "+v"(A[p][4][0]), "+v"(A[p][4][1]), "+v"(A[p][5][0]), "+v"(A[p][5][1]), "+v"(A[p][6][0]), "+v"(A[p][6][1]),
                 "+v"(A[p][7][0]), "+v"(A[p][7][1]), "+v"(B[p][0]), "+v"(B[p][1]), "+v"(B[p][2]), "+v"(B[p][3]), "+v"(B[p][4]), "+v"(B[p][5]),
                 "+v"(B[p][6]), "+v"(B[p][7]) :: "memory");
    SB();
  };
  auto mm = [&](int p) {
#pragma unroll
    for (int n = 0; n < 36; ++n) {
      const int t = TT[n], a = AA[n];
      const u32x4 av = {A[p][a][0][0], A[p][a][0][1], A[p][a][1][0], A[p][a][1][1]};
      MFMA(ac[t], av, B[p][t - a]);
      if (n < 24) ld1(p ^ 1, n);
      SB();
    }
    land(p ^ 1);
  };
#pragma unroll
  for (int t = 0; t < 8; ++t) ac[t] = (f32x4){0.f, 0.f, 0.f, 0.f};
#pragma unroll
  for (int n = 0; n < 24; ++n) ld1(0, n);
  land(0);
#pragma unroll 1
  for (int r = 0; r < 8; r += 2) {
    next(-64);
    mm(0);
    next(r < 6 ? -64 : 0);
    mm(1);
  }
  asm volatile("s_nop 15" : "+v"(ac[0]), "+v"(ac[1]), "+v"(ac[2]), "+v"(ac[3]), "+v"(ac[4]), "+v"(ac[5]), "+v"(ac[6]), "+v"(ac[7]));
}

#define HSL 4
#define NW1 10
#define SGS 13

__global__ __launch_bounds__(512) void k_f(
    const u16* __restrict__ x, const u16* __restrict__ kin, const u16* __restrict__ vin, const u16* __restrict__ shift_k,
    const u16* __restrict__ ssm_k, const u16* __restrict__ ssm_D, const u16* __restrict__ w_in, const u16* __restrict__ w_sp,
    const u16* __restrict__ b_sp, const u16* __restrict__ w_out, u16* __restrict__ out, char* ws) {
  extern __shared__ __attribute__((aligned(16))) char smem[];
  const int tid = threadIdx.x, lane = tid & 63, wave = __builtin_amdgcn_readfirstlane(tid >> 6);
  const int b = blockIdx.x;
  const int xcd = b & 7, idx = b >> 3;
  const int rb = 8 * (xcd >> 1) + (idx >> 2), cb = 4 * (xcd & 1) + (idx & 3);
  const int t0 = rb * 64, d0 = cb * 80;
  const int g4 = lane >> 4, l15 = lane & 15;
  const unsigned lin = (unsigned)(lane * 16);
  const unsigned lb = lds_addr(smem), flg = lb + L_FLAG;
  AB_DECL;
  AB(0);
  asm volatile("" :: "s"(x), "s"(kin), "s"(vin), "s"(shift_k), "s"(ssm_k), "s"(ssm_D), "s"(w_in), "s"(w_sp), "s"(b_sp), "s"(w_out), "s"(out), "s"(ws));
  u64 tag = *(const __attribute__((address_space(4))) u64*)(uintptr_t)ws;
  int tmo = 1 << 16;

  const int cw = wave & 3, wm = wave & 1, wl = (wave >> 1) & 1, ksb = wm ^ wl, odd = cb & 1, pr = cb >> 1;
  const int r8 = lane >> 3, c8 = lane & 7;
  unsigned voX[2], voW[2];
#pragma unroll
  for (int h = 0; h < 2; ++h) {
    voX[h] = (unsigned)((h * 8 + r8) * (D_ * 2) + ((c8 ^ (h * 4 + (r8 & 3))) << 4) - h * 1024);
    voW[h] = (unsigned)((h * 8 + r8) * (F2_ * 2) + ((c8 ^ (h * 4 + (r8 & 2))) << 4) - h * 1024);
  }
  const char* gX = (const char*)x + (size_t)(t0 + cw * 16) * (D_ * 2);
  const char* gW = (const char*)w_in + (size_t)(cw * 16) * (F2_ * 2) + pr * 384;
  const char *gV = gW + odd * 256, *gB = gW + (odd ? 128 : FH_ * 2 + 128);
  const unsigned ldw = lb + cw * 2048;
  auto issue = [&](auto nc) {
    constexpr Unit u = PL.u[decltype(nc)::value];
    if (u.kind == UX) dma32<L_SLOT + u.slot * 8192, u.idx * 128>(voX[0], voX[1], gX, ldw);
    else dma32<L_SLOT + u.slot * 8192, u.idx * (64 * F2_ * 2) + (u.kind == UA ? FH_ * 2 : 0)>(voW[0], voW[1], u.kind == UB ? gB : gV, ldw);
    SB();
  };
  auto issp = [&](auto gc, auto pc, auto kc) {
    constexpr int p = decltype(pc)::value, n = PL.first[p + 1] + decltype(kc)::value;
    if constexpr (n < PL.first[p + 2]) { if constexpr (PL.u[n].grp == decltype(gc)::value) issue(IC(n)); }
  };
  auto issall = [&](auto gc, auto pc) {
    issp(gc, pc, IC(0)); issp(gc, pc, IC(1)); issp(gc, pc, IC(2)); issp(gc, pc, IC(3)); issp(gc, pc, IC(4));
    issp(gc, pc, IC(5)); issp(gc, pc, IC(6)); issp(gc, pc, IC(7)); issp(gc, pc, IC(8)); issp(gc, pc, IC(9));
  };
  const int kd = d0 + min(wave, 4) * 16 + r8 * 2;
  u32x4 sw;
  {
    const int ks0 = t0 + wave * 8;
#pragma unroll
    for (int i = 0; i < 3; ++i) {
      const int id = min(i * 64 + lane, 189), r = id / 10, c = id % 10;
      const char* kp = (r < 11 ? (const char*)kin + (size_t)max(ks0 - 3 + r, 0) * (D_ * 2) : (const char*)vin + (size_t)(ks0 + r - 11) * (D_ * 2)) + cb * 160 + c * 16;
      asm volatile("s_mov_b32 m0, %1\n\ts_nop 0\n\tglobal_load_lds_dwordx4 %0, off" :: "v"(kp), "s"(lb + L_KV + wave * KV_P + i * 1024) : "memory");
    }
    asm volatile("global_load_dwordx4 %0, %1, %2" : "=&v"(sw) : "v"(kd * 8), "s"(shift_k) : "memory");
    SB();
  }
  unsigned kk[11], vv[8];
  u32x4 oa, ob;
  auto kvld = [&]() {
    const char* kl = smem + L_KV + c8 * KV_P + min(wave, 4) * 32 + r8 * 4;
#pragma unroll
    for (int i = 0; i < 11; ++i) kk[i] = *(const unsigned*)(kl + i * 160);
#pragma unroll
    for (int i = 0; i < 8; ++i) vv[i] = *(const unsigned*)(kl + (11 + i) * 160);
  };
  auto kvm = [&](int e0, int e1) {
    const f32x2 w0 = {lo16(sw[0]), lo16(sw[2])}, w1 = {hi16(sw[0]), hi16(sw[2])};
    const f32x2 w2 = {lo16(sw[1]), lo16(sw[3])}, w3 = {hi16(sw[1]), hi16(sw[3])};
    f32x2 kf[11], rr[8];
#pragma unroll
    for (int i = 2 * e0; i < 2 * e1 + 3; ++i) kf[i] = unpk((i < 3 && t0 + c8 == 0) ? 0u : kk[i]);
#pragma unroll
    for (int i = 2 * e0; i < 2 * e1; ++i) {
      f32x2 a0 = kf[i] * w3;
      a0 = __builtin_elementwise_fma(kf[i + 1], w2, a0);
      a0 = __builtin_elementwise_fma(kf[i + 2], w1, a0);
      a0 = __builtin_elementwise_fma(kf[i + 3], w0, a0);
      rr[i] = a0 * unpk(vv[i]);
    }
#pragma unroll
    for (int e = e0; e < e1; ++e) { oa[e] = pack2(rr[2 * e][0], rr[2 * e + 1][0]); ob[e] = pack2(rr[2 * e][1], rr[2 * e + 1][1]); }
  };
  auto kvst = [&]() {
    const unsigned ko = (unsigned)((kd * S_ + t0 + c8 * 8) * 2);
    st16(ko, ws + WS_KV, oa);
    st16(ko, ws + WS_KV + S_ * 2, ob);
    SB();
  };
  auto barrier0 = [&](int nv) {
    SB();
    asm volatile("s_waitcnt vmcnt(%0) lgkmcnt(0)\n\ts_barrier" :: "n"(nv) : "memory");
    SB();
    asm volatile("" : "+v"(sw), "+s"(tag));
    tag += 1;
  };
  unsigned pX[2], pW[2];
#pragma unroll
  for (int k = 0; k < 2; ++k) {
    const int q = l15 >> 2, p = lane & 3, sz = (q >> 1) | ((g4 & 1) << 1);
    pX[k] = lb + (wm * 32 + q * 8 + p) * 128 + ((((k ^ ksb) * 4 + g4) ^ (l15 & 7)) << 4);
    pW[k] = lb + ((k ^ ksb) * 32 + g4 * 8 + q) * 128 + (((sz << 1) | (p >> 1)) << 4) + (p & 1) * 8;
  }
  bf16x8 ones;
#pragma unroll
  for (int e = 0; e < 8; ++e) ones[e] = (short)0x3F80;
  f32x4 xm[2], xr[2];
  auto ldxs = [&]() {
#pragma unroll
    for (int i = 0; i < 2; ++i) {
      xm[i] = *(const f32x4*)(smem + L_XM + (wm * 32 + g4 * 8 + i * 4) * 4);
      xr[i] = *(const f32x4*)(smem + L_XR + (wm * 32 + g4 * 8 + i * 4) * 4);
    }
  };
  auto act = [&](const f32x4& a, float c, int i) {
    const f32x2 nc = {-c, -c};
    const f32x2 lo = gelu2(__builtin_elementwise_fma((f32x2){xm[i][0], xm[i][1]}, nc, (f32x2){a[0], a[1]}) * (f32x2){xr[i][0], xr[i][1]});
    const f32x2 hi = gelu2(__builtin_elementwise_fma((f32x2){xm[i][2], xm[i][3]}, nc, (f32x2){a[2], a[3]}) * (f32x2){xr[i][2], xr[i][3]});
    return (f32x4){lo[0], lo[1], hi[0], hi[1]};
  };
  auto pack8 = [&](const f32x4& a, const f32x4& c) { return (u32x4){pack2(a[0], a[1]), pack2(a[2], a[3]), pack2(c[0], c[1]), pack2(c[2], c[3])}; };
  auto z1put = [&](int f0, const u32x4& v) { *(u32x4*)(smem + L_Z1 + (f0 + l15) * 128 + (((wm * 4 + g4) ^ (l15 & 7)) << 4)) = v; };
  auto rdXF = [&](auto cc, bf16x8 (&X)[2][2]) {
    constexpr int so = L_SLOT + XSLOT[decltype(cc)::value] * 8192;
#pragma unroll
    for (int k = 0; k < 2; ++k) {
      const unsigned a = pX[k] + (so & ~0x7FFF);
      ds16<(so & 0x7FFF)>(X[k][0], a);
      ds16<(so & 0x7FFF) + 512>(X[k][1], a);
    }
    SB();
  };
  auto sttag = [&](const char* p) {
    asm volatile("s_mov_b64 exec, 1\n\tglobal_store_dwordx2 %0, %1, %2 sc1\n\ts_mov_b64 exec, -1" :: "v"(lin), "v"(tag), "s"(p) : "memory");
  };
  auto ldtag = [&](u32x2& f, unsigned vo, const char* p) { asm volatile("global_load_dwordx2 %0, %1, %2 sc1" : "=&v"(f) : "v"(vo), "s"(p) : "memory"); };
  auto notag = [&](const u32x2& f) { return __builtin_amdgcn_ballot_w64(f[0] != (unsigned)tag || f[1] != (unsigned)(tag >> 32)) != 0; };
  auto waittag = [&](u32x2& f, unsigned vo, const char* p) {
    while (__builtin_expect(notag(f) && tmo > 0, 0)) {
      --tmo;
      __builtin_amdgcn_s_sleep(4);
      ldtag(f, vo, p);
      wait_vm<0>(f);
    }
  };
  u32x2 ef;
  auto ldef = [&]() { asm volatile("global_load_dwordx2 %0, %1, %2 sc1" : "=&v"(ef) : "v"((unsigned)((lane & 31) * 8)), "s"(ws + WS_EF + cb * 256) : "memory"); };

  char* const hf = ws + WS_H;
  bf16x8 Wq[15];
  const char* wg = ws + WS_WO + (size_t)(wave * 120 + cb * 5) * 1024;
  const int mt = wave & 3, kh = wave >> 2;
  const size_t xo = (size_t)(t0 + mt * 16 + l15) * D_ + d0;
  u32x2 xv[3];
  auto ldP1 = [&](int n) {
    if (n < 15)
      asm volatile("global_load_dwordx4 %0, %1, %2 offset:%3 sc1" : "=&v"(Wq[n < 15 ? n : 0]) : "v"(lin), "s"(wg + (n / 5) * 40960 + 2048), "i"((n % 5) * 1024 - 2048) : "memory");
    else {
      const int q = n < 18 ? n - 15 : 0;
      asm volatile("global_load_dwordx2 %0, %1, %2 offset:%3" : "=&v"(xv[q]) : "v"((unsigned)(l15 * D_ * 2 + 8 * g4)),
                   "s"(x + (size_t)(t0 + mt * 16) * D_ + d0 + (q < 2 ? 32 * kh : 64)), "i"(q == 1 ? 32 : 0) : "memory");
    }
  };
  u32x4 yv[2];
  auto yoff = [&](int n) {
    const int c = n ? 64 + (lane & 31) : lane, r = c >> 1;
    return (unsigned)(((r < 32 ? 32 * kh + r : 32 + r) * S_ + 8 * (c & 1)) * 2);
  };
  const char* const yb = ws + WS_Y + ((size_t)d0 * S_ + t0 + 16 * mt) * 2;
  auto ldW1 = [&]() {
    ldtag(ef, 0u, ws + WS_LF + (rb * 8 + wave) * 8);
#pragma unroll
    for (int n = 0; n < NW1; ++n) ldP1(n);
    SB();
  };
  auto drain1 = [&](int nc) {
    SB();
    asm volatile("s_waitcnt vmcnt(%0)" :: "i"(nc) : "memory");
#pragma unroll
    for (int n = 0; n < NW1; ++n) pin(Wq[n]);
    pin(ef);
  };
  if (wave >= 4) {
    const int wid = rb * 4 + cw;
    __builtin_amdgcn_s_setprio(3);
    if (wid < 120) dma16((unsigned)((lane >> 1) * (D_ * 2) + (lane & 1) * 16), w_out + (size_t)(wid / 5) * 32 * D_ + d0 + (wid % 5) * 16, lb + L_WO + cw * 1024);
    SB();
    issall(IC(1), IC(-1));
    AB(1);
    barrier0(PL.nv[1][0]);
    AB(2);
    u32x4 hole = {HOLE, HOLE, HOLE, HOLE};
    pin(ones, hole);
    issp(IC(1), IC(0), IC(0));
    if (wave == 7) kvld();
    issp(IC(1), IC(0), IC(1));
    if (wid < 120) {
      const unsigned ra = lb + L_WO + cw * 1024 + (8 * g4 + (l15 >> 2)) * 32 + 8 * (lane & 3);
      s16x4 lo = tr_asm<0>(ra), hi = tr_asm<128>(ra);
      wait_lg<0>(lo, hi);
      st16(lin, ws + WS_WO + (size_t)((wid / 5) * 40 + cb * 5 + wid % 5) * 1024, __builtin_bit_cast(u32x4, join8(lo, hi)));
    }
    SB();
    issp(IC(1), IC(0), IC(2)); issp(IC(1), IC(0), IC(3)); issp(IC(1), IC(0), IC(4)); issp(IC(1), IC(0), IC(5));
    if (wave == 7) kvm(0, 2);
    AB(3);
    bf16x8 XF[10][2][2];
    auto ldx = [&](int c) {
      u32x4 xv;
      asm volatile("global_load_dwordx4 %0, %1, %2\n\ts_waitcnt vmcnt(0)" : "=&v"(xv) : "v"((unsigned)(lane * (D_ * 2) + c * 16)), "s"(x + (size_t)t0 * D_) : "memory");
      return xv;
    };
    float fm, fq;
    f32x2 fr[2];
    constexpr int FINP = 8;
    auto period = [&](auto pc) {
      constexpr int p = decltype(pc)::value;
      constexpr int c0 = p == 1 || p > 6 ? 0 : 2 * p - 3, nx = p == 1 || p == 6 ? 1 : (p < 6 ? 2 : 0);
      SB();
      asm volatile("s_waitcnt vmcnt(%0) lgkmcnt(0)\n\ts_barrier" :: "n"(PL.nv[1][p]) : "memory");
      CB(6 * (p - XBAR) + 1);
      SB();
      constexpr int pc0 = p == 2 || p > 7 ? 0 : 2 * p - 5, pnx = p < 2 || p > 7 ? 0 : (p == 2 || p == 7 ? 1 : 2);
#pragma unroll
      for (int c = pc0; c < pc0 + pnx; ++c) pin(XF[c][0][0], XF[c][0][1], XF[c][1][0], XF[c][1][1]);
      if constexpr (p == FINP) {
        if (wave == 6) {
          const char* rw = smem + L_RAW + ((lane >> 3) * 4 + (lane & 3)) * 16 + (lane & 4) * 2;
          fr[0] = *(const f32x2*)rw;
          fr[1] = *(const f32x2*)(rw + 512);
        }
        SB();
      }
      issp(IC(1), pc, IC(0));
      CB(6 * (p - XBAR) + 2);
      if constexpr (nx > 0) rdXF(IC(c0), XF[c0]);
      if constexpr (p == FLAGB) { if (wave == 5) sttag(ws + WS_EF + (cb * 32 + rb) * 8); }
      issp(IC(1), pc, IC(1));
      CB(6 * (p - XBAR) + 3);
      if constexpr (p <= 2) { if (wave == 7) kvm(p + 1, p + 2); SB(); }
      if constexpr (p == 3) { if (wave == 7) kvst(); SB(); }
      issp(IC(1), pc, IC(2));
      CB(6 * (p - XBAR) + 4);
      if constexpr (nx > 1) rdXF(IC(c0 + 1), XF[c0 + 1]);
      issp(IC(1), pc, IC(3)); issp(IC(1), pc, IC(4)); issp(IC(1), pc, IC(5));
      if constexpr (NST[1][p + 1] > 0) {
        int n0 = 0;
#pragma unroll
        for (int q = 0; q <= p; ++q) n0 += NST[1][q];
#pragma unroll
        for (int n = n0; n < n0 + NST[1][p + 1]; ++n) st16(lin, ws + WS_Z2 + (size_t)((rb * 2 + wl) * 48 + cb * 6 + wm * 3 + n) * 1024, hole);
        SB();
      }
      if constexpr (p == FINP) {
        if (wave == 6) {
          const f32x2 sa = fr[0] + fr[1];
          fm = sa[0] * (1.0f / 640.0f);
          fq = sa[1] - sa[0] * fm;
          if (__builtin_expect(__builtin_amdgcn_ballot_w64(!(fq >= 1e-3f * sa[0] * fm)) != 0, 0)) {
            const f32x2 mm = {fm, fm};
            f32x2 q2 = {0.f, 0.f};
#pragma unroll 1
            for (int c = 0; c < 80; ++c) {
              const u32x4 xv = ldx(c);
#pragma unroll
              for (int e = 0; e < 4; ++e) { const f32x2 d = unpk(xv[e]) - mm; q2 = __builtin_elementwise_fma(d, d, q2); }
            }
            fq = q2[0] + q2[1];
          }
          pin(fm, fq);
        }
      }
      if constexpr (p == FINP + 1) {
        if (wave == 6) {
          const float vbf = rbf(fmaxf(fq, 0.f) * (1.0f / 640.0f));
          fq = rbf(__builtin_amdgcn_sqrtf(rbf(vbf + 1e-5f)));
          fm = rbf(fm);
          pin(fm, fq);
        }
      }
      if constexpr (p == FINP + 2) {
        if (wave == 6) {
          *(float*)(smem + L_XM + lane * 4) = fm;
          *(float*)(smem + L_XR + lane * 4) = __builtin_amdgcn_rcpf(fq);
        }
      }
    };
    period(IC(1)); AB(4); period(IC(2)); period(IC(3)); period(IC(4)); period(IC(5)); AB(5);
    period(IC(6)); period(IC(7)); period(IC(8)); period(IC(9)); period(IC(10)); period(IC(11));
    AB(6);
    const bool docv = cw < ((rb & 1) ? 2 : 3);
    const int d = d0 + (cw < 2 ? 2 * rb + cw : 64 + (rb >> 1));
    u32x4 hc[4], uc[4];
    unsigned Dw;
#pragma unroll
    for (int k = 0; k < 4; ++k) asm volatile("global_load_dwordx4 %0, %1, %2 offset:%3" : "=&v"(hc[k]) : "v"(lin), "s"(ssm_k + (size_t)d * S_), "i"(k * 1024) : "memory");
    asm volatile("global_load_ushort %0, %1, %2" : "=&v"(Dw) : "v"(0u), "s"(ssm_D + d) : "memory");
    __builtin_amdgcn_s_setprio(0);
    wait_vm<5>();
    signal1(flg + 60);
    flag_wait_asm(flg + 60, 4u);
    AB(7);
    {
      f32x4 ac2[2][2], cs2[2];
      s16x4 VF[3][2][2];
      unsigned aV[2][2];
      auto rdV = [&](auto nc) {
        constexpr int n = decltype(nc)::value, so = VSLOT[n >> 1] * 8192, po = n < 2 ? -1 : (VSLOT[(n >> 1) - 1] * 8192) >> 16;
#pragma unroll
        for (int jj = 0; jj < 2; ++jj) {
          if ((so >> 16) != po) aV[n & 1][jj] = opq((pW[n & 1] ^ (unsigned)(wl * 64 + jj * 32)) + L_SLOT + (so & ~0xFFFF));
          VF[n % 3][jj][0] = tr_asm<(so & 0xFFFF)>(aV[n & 1][jj]);
          VF[n % 3][jj][1] = tr_asm<(so & 0xFFFF) + 512>(aV[n & 1][jj]);
        }
        SB();
      };
      auto mm2 = [&](auto nc) {
        constexpr int n = decltype(nc)::value, j = n >> 1, k = n & 1, nw = n < 19 ? 4 : 0;
        const bf16x8 w0 = join8(VF[n % 3][0][0], VF[n % 3][0][1]), w1 = join8(VF[n % 3][1][0], VF[n % 3][1][1]);
        if (k == 0) { MFMAW<n == 0>(nw, cs2[0], ones, w0); MFMAW<n == 0>(-1, cs2[1], ones, w1); }
        MFMAW<n == 0>(k ? nw : -1, ac2[0][0], XF[j][k][0], w0);
        SB();
        if constexpr (n < 18) rdV(IC(n + 2 < 20 ? n + 2 : 0));
        if constexpr (n == 12) ldef();
        if constexpr (n == 19) signal1(flg + 16);
        MFMAW<n == 0>(-1, ac2[1][0], XF[j][k][1], w0);
        MFMAW<n == 0>(-1, ac2[0][1], XF[j][k][0], w1);
        MFMAW<n == 0>(-1, ac2[1][1], XF[j][k][1], w1);
        SB();
        if constexpr (n == 19) {
          pin(cs2[0], cs2[1]);
          if (g4 < 2) *(float*)(smem + L_CS2 + (cw * 32 + lane) * 4) = g4 ? cs2[1][0] : cs2[0][0];
          signal1(flg + 20 + wl * 4);
        }
        SB();
      };
      rdV(IC(0)); rdV(IC(1));
      mm2(IC(0)); mm2(IC(1)); mm2(IC(2)); mm2(IC(3)); mm2(IC(4)); mm2(IC(5)); mm2(IC(6)); mm2(IC(7)); mm2(IC(8)); mm2(IC(9));
      mm2(IC(10)); mm2(IC(11)); mm2(IC(12)); mm2(IC(13)); mm2(IC(14)); mm2(IC(15)); mm2(IC(16)); mm2(IC(17)); mm2(IC(18)); mm2(IC(19));
      AB(8);
      wait_vm<0>(ef);
      waittag(ef, (unsigned)((lane & 31) * 8), ws + WS_EF + cb * 256);
#pragma unroll
      for (int k = 0; k < 4; ++k) asm volatile("global_load_dwordx4 %0, %1, %2 offset:%3 sc1" : "=&v"(uc[k]) : "v"(lin), "s"(ws + WS_KV + (size_t)d * (S_ * 2)), "i"(k * 1024) : "memory");
      ldxs();
      flag_wait_asm(flg + 20 + wl * 4, 2u);
      pin(ac2[0][0], ac2[0][1], ac2[1][0], ac2[1][1]);
#pragma unroll
      for (int jj = 0; jj < 2; ++jj) {
        const float ct = cs2[jj][0] + *(const float*)(smem + L_CS2 + ((cw ^ 1) * 32 + jj * 16 + l15) * 4);
        z1put(odd * 32 + wl * 32 + jj * 16, pack8(act(ac2[0][jj], ct, 0), act(ac2[1][jj], ct, 1)));
      }
      signal1(flg + 56);
      AB(9);
    }
    wait_vm<0>(hc[0], hc[1], hc[2], hc[3], uc[0], uc[1], uc[2], uc[3], Dw);
    if (docv) {
      char* cb_ = smem + M_CV + cw * CV_BUF;
      flag_wait_asm(flg + 16, 4u);
      if (lane < 36) *(u32x4*)(cb_ + (lane < 32 ? lane * 16 : CV_C + ((lane >> 1) & 1) * CV_CS + 4096 + (lane & 1) * 16)) = (u32x4){0u, 0u, 0u, 0u};
      const float Dv = lo16(Dw);
#pragma unroll
      for (int k = 0; k < 4; ++k) {
        const int a = k * 64 + lane;
        const int h0 = k ? __builtin_amdgcn_readlane((int)hc[k ? k - 1 : 0][3], 63) : 0;
        const unsigned hp = (unsigned)__builtin_amdgcn_update_dpp(h0, (int)hc[k][3], 0x138, 0xF, 0xF, false);
        const unsigned w[5] = {hp, hc[k][0], hc[k][1], hc[k][2], hc[k][3]};
        u32x4 sw, mx;
#pragma unroll
        for (int i = 0; i < 4; ++i) {
          sw[i] = (w[4 - i] >> 16) | (w[4 - i] << 16);
          mx[i] = (w[4 - i] & 0xFFFFu) | (w[3 - i] & 0xFFFF0000u);
        }
        char* cp = cb_ + CV_C + (2040 - 8 * a) * 2;
        *(u32x4*)(cp) = sw;
        *(u32x4*)(cp + CV_CS) = mx;
        *(u32x4*)(cb_ + 512 + a * 16) = uc[k];
      }
      AB(12);
      f32x4 ac[8];
      conv_core(lds_addr(cb_), lane, ac);
      AB(13);
      u32x2 kvp[8];
#pragma unroll
      for (int t = 0; t < 8; ++t) kvp[t] = *(const u32x2*)(cb_ + 512 + (256 * t + 16 * l15 + 4 * g4) * 2);
#pragma unroll
      for (int k = 0; k < 4; ++k) {
        u32x2 ov[2];
#pragma unroll
        for (int t = 0; t < 2; ++t)
#pragma unroll
          for (int h = 0; h < 2; ++h) {
            const unsigned cv = pack2(ac[2 * k + t][2 * h], ac[2 * k + t][2 * h + 1]);
            const unsigned kd = pack2(lo16(kvp[2 * k + t][h]) * Dv, hi16(kvp[2 * k + t][h]) * Dv);
            ov[t][h] = pack2(lo16(cv) + lo16(kd), hi16(cv) + hi16(kd));
          }
        const auto s0 = __builtin_amdgcn_permlane16_swap(ov[0][0], ov[1][0], false, false);
        const auto s1 = __builtin_amdgcn_permlane16_swap(ov[0][1], ov[1][1], false, false);
        st16((unsigned)((256 * (g4 & 1) + 16 * l15 + 8 * (g4 >> 1)) * 2), ws + WS_Y + (size_t)d * (S_ * 2) + k * 1024, (u32x4){s0[0], s1[0], s0[1], s1[1]});
      }
      wait_vm<0>();
      sttag(ws + WS_YF + d * 8);
    } else if (cw == 3) {
#pragma unroll
      for (int n = 0; n < 12; ++n) {
        st16(lin, ws + WS_H + (size_t)((rb * 4 + n / 3) * 24 + cb * 3 + n % 3) * 1024, hole);
        __builtin_amdgcn_s_sleep(HSL);
      }
      wait_vm<0>();
      sttag(ws + WS_LF + (rb * 8 + cb) * 8);
      if (!odd) {
        u32x4 zx[4];
        ldtag(ef, (unsigned)((lane & 1) * 8), ws + WS_XF + (rb * 4 + pr) * 16);
        wait_vm<0>(ef);
        waittag(ef, (unsigned)((lane & 1) * 8), ws + WS_XF + (rb * 4 + pr) * 16);
#pragma unroll
        for (int c = 0; c < 4; ++c)
          asm volatile("global_load_dwordx4 %0, %1, %2 offset:%3 sc1" : "=&v"(zx[c]) : "v"(lin), "s"(ws + WS_ZX + (size_t)(rb * 4 + pr) * 4096), "i"(c * 1024) : "memory");
        wait_vm<0>(zx[0], zx[1], zx[2], zx[3]);
#pragma unroll
        for (int c = 0; c < 4; ++c) *(u32x4*)(smem + L_Z1 + (64 + (c & 1) * 16 + l15) * 128 + ((((c >> 1) * 4 + g4) ^ (l15 & 7)) << 4)) = zx[c];
      }
      signal1(flg + 56);
    }
    SB();
    AB(14);
    WAIT_LGKM0();
    signal1(flg + 40);
    flag_wait_asm(flg + 44, 4u);
    AB(15);
    ldW1();
    drain1(0);
  } else {
    issall(IC(0), IC(-1));
    if (tid < 32) *(unsigned*)(smem + L_FLAG + tid * 4) = 0u;
    pin(ones);
    AB(1);
    barrier0(PL.nv[0][0]);
    AB(2);
    kvld();
    kvm(0, 4);
    kvst();
    AB(3);
    const bool isz2 = !(odd && wl);
    const char* Ab[4];
#pragma unroll
    for (int i = 0; i < 4; ++i) Ab[i] = (const char*)w_sp + (size_t)(t0 + ((wave + 1 + i) & 3) * 16) * (S_ * 2) + wave * 1024;
    const unsigned ringl = lb + M_RING + wave * 24576;
    const unsigned aoff[2] = {(unsigned)(r8 * 4096 + ((c8 ^ r8) << 4)), (unsigned)(r8 * 4096 + ((c8 ^ r8) << 4) + 32768)};
    auto ldA1 = [&](auto dc, int i) {
      constexpr int d = decltype(dc)::value;
      dma16o(aoff[i & 1], Ab[i >> 1], d * 128, ringl, (d % 3) * 8192 + i * 1024);
    };
    {
      f32x4 zv[2][4], cs[4];
      s16x4 WB[2][4][2];
      bf16x8 XG[10][2][2];
      unsigned aX[2][2], aW[2][2][4];
#pragma unroll
      for (int k = 0; k < 2; ++k)
#pragma unroll
        for (int w = 0; w < 2; ++w) {
          aX[w][k] = pX[k] + L_SLOT + 6 * 8192 + w * 65536;
          pin(aX[w][k]);
#pragma unroll
          for (int jj = 0; jj < 4; ++jj) { aW[w][k][jj] = (pW[k] ^ (unsigned)(jj * 32)) + L_SLOT + wl * 8192 + w * 65536; pin(aW[w][k][jj]); }
        }
      auto rd1 = [&](auto nc, int r) {
        constexpr int n = decltype(nc)::value, j = n >> 1, k = n & 1, c = n > 0 && n < 11 ? n - 1 : 0, sx = (XSLOT[c] - 6) * 8192, sa = WSLOT[j] * 8192;
        if (r < 0) { if (r & 1) ds16<(sx & 0xFFFF) + 512>(XG[c][(r + 4) >> 1][1], aX[sx >> 16][(r + 4) >> 1]); else ds16<(sx & 0xFFFF)>(XG[c][(r + 4) >> 1][0], aX[sx >> 16][(r + 4) >> 1]); }
        else if (r & 1) WB[k][r >> 1][1] = tr_asm<(sa & 0xFFFF) + 512>(aW[sa >> 16][k][r >> 1]);
        else WB[k][r >> 1][0] = tr_asm<(sa & 0xFFFF)>(aW[sa >> 16][k][r >> 1]);
        SB();
      };
      auto bar = [&](auto bc) {
        SB();
        constexpr int p = decltype(bc)::value;
        CB(6 * (p - XBAR));
        if constexpr (p == 1) asm volatile("s_waitcnt vmcnt(%0) lgkmcnt(0)\n\ts_barrier" :: "n"(PL.nv[0][p]) : "memory");
        else if constexpr (p <= GZ) asm volatile("s_waitcnt vmcnt(%0) lgkmcnt(8)\n\ts_barrier" :: "n"(PL.nv[0][p]) : "memory");
        else if constexpr (p == 8) asm volatile("s_waitcnt lgkmcnt(4)\n\ts_barrier" ::: "memory");
        else if constexpr (p == 11) asm volatile("s_waitcnt lgkmcnt(0)\n\ts_barrier" ::: "memory");
        else asm volatile("s_barrier" ::: "memory");
        CB(6 * (p - XBAR) + 1);
        SB();
      };
      float cp[4];
      f32x4 st[2][2], rv;
      const unsigned rwa = lb + L_RAW + (ksb * 2 + wm) * 256 + l15 * 16 + ((l15 >> 2) == g4 ? 0 : 1024);
      auto kstep = [&](auto nc) {
        constexpr int n = decltype(nc)::value, j = n >> 1, k = n & 1, nn = n < 19 ? n + 1 : 0, r0 = nn >= 2 && nn <= 10 ? -4 : 0;
        constexpr int sc = n == 1 ? 0 : n - 2, ns = !k || n > 11 ? 0 : (n == 1 || n == 11 ? 1 : 2);
        constexpr bool more = n < 19;
        if constexpr (more && k) bar(IC(nn / 2 + 1));
        int r = r0, c = 0;
        auto nxt = [&]() { if (more && r < 8) { rd1(IC(nn), r++); ++c; } };
        auto sel = [&](int q) {
          const f32x4& sq = st[q & 1][q >> 1];
          rv[q] = (l15 & 2) ? ((l15 & 1) ? sq[3] : sq[2]) : ((l15 & 1) ? sq[1] : sq[0]);
          SB();
        };
#pragma unroll
        for (int jj = 0; jj < 4; ++jj) {
          const bf16x8 wf = join8(WB[k][jj][0], WB[k][jj][1]);
          if (n == 19 && jj == 2) {
            asm volatile("s_nop 2");
            pin(cs[0], cs[1], cs[2], cs[3]);
            *(float*)(smem + L_CS + (wave * 64 + lane) * 4) = (g4 & 2) ? ((g4 & 1) ? cs[3][0] : cs[2][0]) : ((g4 & 1) ? cs[1][0] : cs[0][0]);
            bar(IC(11));
#pragma unroll
            for (int q = 0; q < 4; ++q) cp[q] = *(const float*)(smem + L_CS + ((wave ^ 1) * 64 + q * 16 + l15) * 4);
            ldxs();
            SB();
          }
          MFMAW<n == 0>(jj == 0 ? 4 : (jj == 2 && more ? c : -1), zv[0][jj], XG[j][k][0], wf);
          SB();
          if (n == 12 && jj < 2) sel(2 * jj); else nxt();
          MFMAW<n == 0>(-1, zv[1][jj], XG[j][k][1], wf);
          SB();
          nxt();
          if (k == 0) {
            MFMAW<n == 0>(-1, cs[jj], ones, wf);
            SB();
            if (n == 12 && jj < 2) sel(2 * jj + 1); else nxt();
            if (n == 12 && jj == 1) { nxt(); nxt(); dsw16<0>(rwa, rv); ++c; SB(); }
          }
#pragma unroll
          for (int q = 0; q < ns; ++q) {
            const bf16x8& xs = XG[sc + q][1][jj >> 1];
            MFMAW<n == 1>(-1, st[jj & 1][jj >> 1], xs, (jj & 1) ? xs : ones);
            SB();
            nxt();
          }
        }
      };
      bar(IC(1));
#pragma unroll
      for (int r = -4; r < 8; ++r) rd1(IC(0), r);
      kstep(IC(0)); AB(4); kstep(IC(1)); kstep(IC(2)); kstep(IC(3)); kstep(IC(4)); kstep(IC(5)); kstep(IC(6)); kstep(IC(7)); kstep(IC(8)); AB(5); kstep(IC(9));
      kstep(IC(10)); kstep(IC(11)); kstep(IC(12)); kstep(IC(13)); kstep(IC(14)); kstep(IC(15)); kstep(IC(16)); kstep(IC(17)); kstep(IC(18)); kstep(IC(19));
      __builtin_amdgcn_s_setprio(2);
      AB(6);
      float ct[4];
#pragma unroll
      for (int jj = 0; jj < 4; ++jj) ct[jj] = cs[jj][0] + cp[jj];
#pragma unroll
      for (int jj = 0; jj < 4; ++jj) {
        if (jj == 2) SB();
        if (!(jj & 1)) pin(zv[0][jj], zv[1][jj], zv[0][jj + 1], zv[1][jj + 1]);
#pragma unroll
        for (int i = 0; i < 2; ++i) zv[i][jj] = act(zv[i][jj], ct[jj], i);
      }
      if (isz2) {
        f32x4 pv[2], s1[2], s2[2];
#pragma unroll
        for (int i = 0; i < 2; ++i) {
#pragma unroll
          for (int e = 0; e < 4; ++e) {
            const float z0 = zv[i][0][e];
            pv[i][e] = __int_as_float(__builtin_amdgcn_update_dpp(0, __float_as_int(z0), 0x150, 0xF, 0xF, true));
          }
          const f32x4 a0 = zv[i][0] - pv[i], a1 = zv[i][1] - pv[i], a2 = zv[i][2] - pv[i], a3 = zv[i][3] - pv[i];
          s1[i] = (a0 + a1) + (a2 + a3);
          s2[i] = __builtin_elementwise_fma(a3, a3, __builtin_elementwise_fma(a2, a2, __builtin_elementwise_fma(a1, a1, a0 * a0)));
        }
        float r[8], q[4];
#define DA(d, s, ctl, bm) "v_add_f32_dpp " d ", " s ", " s " " ctl " row_mask:0xf bank_mask:" bm "\n\t"
        asm volatile("s_nop 1\n\t"
                     DA("%0", "%12", "row_ror:8", "0x3") DA("%1", "%13", "row_ror:8", "0x3") DA("%2", "%14", "row_ror:8", "0x3") DA("%3", "%15", "row_ror:8", "0x3")
                     DA("%4", "%16", "row_ror:8", "0x3") DA("%5", "%17", "row_ror:8", "0x3") DA("%6", "%18", "row_ror:8", "0x3") DA("%7", "%19", "row_ror:8", "0x3")
                     DA("%0", "%20", "row_ror:8", "0xc") DA("%1", "%21", "row_ror:8", "0xc") DA("%2", "%22", "row_ror:8", "0xc") DA("%3", "%23", "row_ror:8", "0xc")
                     DA("%4", "%24", "row_ror:8", "0xc") DA("%5", "%25", "row_ror:8", "0xc") DA("%6", "%26", "row_ror:8", "0xc") DA("%7", "%27", "row_ror:8", "0xc")
                     DA("%8", "%0", "row_half_mirror", "0x5") DA("%9", "%1", "row_half_mirror", "0x5") DA("%10", "%2", "row_half_mirror", "0x5") DA("%11", "%3", "row_half_mirror", "0x5")
                     DA("%8", "%4", "row_half_mirror", "0xa") DA("%9", "%5", "row_half_mirror", "0xa") DA("%10", "%6", "row_half_mirror", "0xa") DA("%11", "%7", "row_half_mirror", "0xa")
                     DA("%8", "%8", "quad_perm:[1,0,3,2]", "0xf") DA("%9", "%9", "quad_perm:[1,0,3,2]", "0xf") DA("%10", "%10", "quad_perm:[1,0,3,2]", "0xf") DA("%11", "%11", "quad_perm:[1,0,3,2]", "0xf")
                     DA("%8", "%8", "quad_perm:[2,3,0,1]", "0xf") DA("%9", "%9", "quad_perm:[2,3,0,1]", "0xf") DA("%10", "%10", "quad_perm:[2,3,0,1]", "0xf") DA("%11", "%11", "quad_perm:[2,3,0,1]", "0xf")
                     : "=&v"(r[0]), "=&v"(r[1]), "=&v"(r[2]), "=&v"(r[3]), "=&v"(r[4]), "=&v"(r[5]), "=&v"(r[6]), "=&v"(r[7]),
                       "=&v"(q[0]), "=&v"(q[1]), "=&v"(q[2]), "=&v"(q[3])
                     : "v"(s1[0][0]), "v"(s2[0][0]), "v"(s1[0][1]), "v"(s2[0][1]), "v"(s1[0][2]), "v"(s2[0][2]), "v"(s1[0][3]), "v"(s2[0][3]),
                       "v"(s1[1][0]), "v"(s2[1][0]), "v"(s1[1][1]), "v"(s2[1][1]), "v"(s1[1][2]), "v"(s2[1][2]), "v"(s1[1][3]), "v"(s2[1][3]));
#undef DA
        const f32x4 p4 = (lane & 8) ? pv[1] : pv[0];
        const float pa = (lane & 4) ? p4[2] : p4[0], pb = (lane & 4) ? p4[3] : p4[1];
        const float pp = (lane & 2) ? pb : pa, S1 = (lane & 2) ? q[2] : q[0], S2 = (lane & 2) ? q[3] : q[1];
        const float dl = S1 * (1.0f / 64.0f);
        st16((unsigned)((lane >> 1) * 16), ws + WS_ST + (size_t)((rb * 12 + pr * 3 + odd * 2 + wl) * 1024 + wm * 512),
             (u32x4){__float_as_uint(pp + dl), __float_as_uint(fmaxf(S2 - S1 * dl, 0.f)), (unsigned)tag, (unsigned)(tag >> 32)});
      } else {
#pragma unroll
        for (int jj = 0; jj < 4; ++jj) {
          u32x4 o = pack8(zv[0][jj], zv[1][jj]);
          if (jj >= 2) z1put(jj * 16 - 32, o);
          else st16(lin, ws + WS_ZX + (size_t)((rb * 4 + pr) * 4 + wm * 2 + jj) * 1024, o);
        }
      }
      SB();
      AB(7);
#pragma unroll
      for (int i = 0; i < 8; ++i) ldA1(IC(0), i);
#pragma unroll
      for (int i = 0; i < 8; ++i) ldA1(IC(1), i);
      AB(8);
      u32x4 sg[6];
      bool bad;
      do {
#pragma unroll
        for (int c = 0; c < 6; ++c)
          asm volatile("global_load_dwordx4 %0, %1, %2 offset:%3 sc1" : "=&v"(sg[c]) : "v"((unsigned)((lane >> 5) * 6144 + (lane & 31) * 16)),
                       "s"(ws + WS_ST + (size_t)rb * 12288 + wm * 512 + 2048), "i"(c * 1024 - 2048) : "memory");
        ldef();
        wait_vm<0>(sg[0], sg[1], sg[2], sg[3], sg[4], sg[5], ef);
        unsigned dl = ef[0] ^ (unsigned)tag, dh = ef[1] ^ (unsigned)(tag >> 32);
#pragma unroll
        for (int c = 0; c < 6; ++c) { dl |= sg[c][2] ^ (unsigned)tag; dh |= sg[c][3] ^ (unsigned)(tag >> 32); }
        bad = __builtin_amdgcn_ballot_w64((dl | dh) != 0) != 0;
        --tmo;
      } while (bad && tmo > 0);
      AB(9);
      if (isz2) {
        auto both = [&](float v) {
          const auto c = __builtin_amdgcn_permlane32_swap(__float_as_uint(v), __float_as_uint(v), false, false);
          return __uint_as_float(c[0]) + __uint_as_float(c[1]);
        };
        float sm = 0.f, m2 = 0.f, qq = 0.f;
#pragma unroll
        for (int c = 0; c < 6; ++c) { sm += __uint_as_float(sg[c][0]); m2 += __uint_as_float(sg[c][1]); }
        const float mean = both(sm) * (1.0f / 12.0f);
#pragma unroll
        for (int c = 0; c < 6; ++c) { const float a = __uint_as_float(sg[c][0]) - mean; qq = __builtin_fmaf(a, a, qq); }
        const float vbf = rbf(both(__builtin_fmaf(qq, 64.0f, m2)) * (1.0f / 768.0f));
        const float rz = __builtin_amdgcn_rcpf(rbf(__builtin_amdgcn_sqrtf(rbf(vbf + 1e-5f))));
        char* zs = smem + L_ZS + wave * 256;
        *(float*)(zs + lane * 4) = lane < 32 ? rbf(mean) : rz;
        f32x4 zm[2], zr[2];
#pragma unroll
        for (int i = 0; i < 2; ++i) {
          zm[i] = *(const f32x4*)(zs + (g4 * 8 + i * 4) * 4);
          zr[i] = *(const f32x4*)(zs + 128 + (g4 * 8 + i * 4) * 4);
        }
#pragma unroll
        for (int jj = 0; jj < 4; ++jj) {
          u32x4 o = pack8((zv[0][jj] - zm[0]) * zr[0], (zv[1][jj] - zm[1]) * zr[1]);
          o[3] = nohole(o[3]);
          st16(lin, ws + WS_Z2 + (size_t)((rb * 2 + wm) * 48 + pr * 12 + odd * 8 + wl * 4 + jj) * 1024, o);
        }
      } else sttag(ws + WS_XF + (rb * 4 + pr) * 16 + wm * 8);
      __builtin_amdgcn_s_setprio(0);
      SB();
      AB(10);
    }
    const char* Bs = ws + WS_Z2 + (size_t)(wave * 16 * 48 + cb * 6) * 1024 + 2048;
    bf16x8 Bf[3][6];
    auto ldB1 = [&](int s, int j) {
      asm volatile("global_load_dwordx4 %0, %1, %2 offset:%3 " HSC : "=&v"(Bf[s % 3][j]) : "v"(lin + s * 49152), "s"(Bs), "i"(j * 1024 - 2048) : "memory");
    };
    flag_wait_asm(flg + 16, 4u);
#pragma unroll
    for (int i = 0; i < 8; ++i) ldA1(IC(2), i);
    AB(11);
    f32x4 acc[4][6];
    unsigned ara[2];
#pragma unroll
    for (int ks = 0; ks < 2; ++ks) ara[ks] = ringl + l15 * 128 + (((ks * 4 + g4) ^ (l15 & 7)) << 4);
    u32x4 af[4];
    auto rdA = [&](int s, int i) {
      asm volatile("ds_read_b128 %0, %1 offset:%2" : "=&v"(af[i]) : "v"(ara[s & 1]), "i"(((s >> 1) % 3) * 8192 + i * 2048) : "memory");
    };
    asm volatile("s_waitcnt vmcnt(8)" ::: "memory");
    signal1(flg + 28);
    flag_wait_asm(flg + 28, 4u);
#pragma unroll
    for (int i = 0; i < 4; ++i) rdA(0, i);
    AB(12);
#pragma unroll
    for (int j = 0; j < 6; ++j) ldB1(0, j);
#pragma unroll
    for (int j = 0; j < 6; ++j) ldB1(1, j);
#pragma unroll
    for (int j = 0; j < 6; ++j) ldB1(2, j);
    asm volatile("s_waitcnt lgkmcnt(3)" : "+v"(af[0]) :: "memory");
    SB();
    unsigned bsv;
    const unsigned xa = ringl + lane * 16;
    auto putp = [&](int i, int j) { asm volatile("ds_write_b128 %0, %1 offset:%2" :: "v"(xa), "v"(acc[i][j]), "i"(i * 6144 + j * 1024) : "memory"); };
    auto ldbs = [&]() { asm volatile("global_load_ushort %0, %1, %2" : "=&v"(bsv) : "v"((unsigned)(l15 * 2)), "s"(b_sp + t0 + wave * 16) : "memory"); };
    auto step = [&](auto sc) {
      constexpr int s = decltype(sc)::value, sl = s % 3;
      constexpr int NV = s < 2 ? 12 : (s < 12 ? 20 : (s == 12 ? 12 : 13 - 6 * (s - 13)));
      constexpr int DA = ((s & 1) && (s + 5) / 2 < 8) ? (s + 5) / 2 : -1;
      asm volatile("s_waitcnt vmcnt(%0) lgkmcnt(2)" :: "n"(NV) : "memory");
      pin(Bf[sl][0], Bf[sl][1], Bf[sl][2], Bf[sl][3], Bf[sl][4], Bf[sl][5], af[1]);
      {
        auto d3 = [&](int j) { return __builtin_bit_cast(u32x4, Bf[sl][j])[3]; };
        auto bad = [&]() { return __builtin_amdgcn_ballot_w64(umx(umx(umx(d3(0), d3(1)), d3(2)), umx(umx(d3(3), d3(4)), d3(5))) == HOLE) != 0; };
        if (__builtin_expect(bad(), 0)) {
          do {
            --tmo;
            asm volatile("s_waitcnt vmcnt(0)\n\ts_sleep 2" ::: "memory");
#pragma unroll
            for (int q = 0; q < 3; ++q)
              if (s + q < 16) reld6(Bf[(s + q) % 3], opq(lin) + (s + q) * 49152, Bs);
            wait_vm<0>(Bf[sl][0], Bf[sl][1], Bf[sl][2], Bf[sl][3], Bf[sl][4], Bf[sl][5]);
          } while (bad() && tmo > 0);
        }
      }
      if constexpr (s == SGS) signal1(flg + 44);
      SB();
#pragma unroll
      for (int i = 0; i < 4; ++i) {
        if (i == 1) wait_lg<(s < 15 ? 1 : 0)>(af[2], af[3]);
        if (i == 3 && s < 15) wait_lg<2>(af[0]);
#pragma unroll
        for (int j = 0; j < 6; ++j) {
          if constexpr (s == 0) MFMA0(acc[i][j], Bf[sl][j], af[i]); else MFMA(acc[i][j], Bf[sl][j], af[i]);
          SB();
          if constexpr (DA >= 0) { if ((i == 1 || i == 2) && j < 4) ldA1(IC(DA < 0 ? 0 : DA), (i - 1) * 4 + j); }
          if constexpr (s + 3 < 16) { if (i == 3) ldB1(s + 3, j); }
          if constexpr (s == 15) {
            if (i > 0 && j > 1) putp(i - 1, j - 2);
          }
          SB();
        }
        if constexpr (s == 15) {
          if (i > 0) {
            putp(i - 1, 4); putp(i - 1, 5);
            signal1(flg + ((wave + i) & 3) * 4);
          }
        }
        if constexpr (s < 15) rdA(s + 1, i);
        SB();
      }
      if constexpr (s == 12) ldbs();
      SB();
    };
    step(IC(0)); AB(13); step(IC(1)); step(IC(2)); step(IC(3)); AB(14); step(IC(4)); step(IC(5)); step(IC(6)); step(IC(7));
    step(IC(8)); step(IC(9)); step(IC(10)); step(IC(11)); step(IC(12)); step(IC(13)); step(IC(14)); step(IC(15)); AB(15);
    ldW1();
    flag_wait_asm(flg + 56, 5u);
    s16x4 z1r[6];
    {
      const int fr = 4 * g4 + (l15 >> 2), p = lane & 3;
      const unsigned za = lb + L_Z1 + fr * 128 + (((wave * 2 + (p >> 1)) ^ (fr & 7)) << 4) + (p & 1) * 8;
      z1r[0] = tr_asm<0>(za); z1r[1] = tr_asm<2048>(za); z1r[2] = tr_asm<4096>(za);
      z1r[3] = tr_asm<6144>(za); z1r[4] = tr_asm<8192>(za); z1r[5] = tr_asm<10240>(za);
    }
    flag_wait_asm(flg + wave * 4, 3u);
    asm volatile("" : "+v"(z1r[0]), "+v"(z1r[1]), "+v"(z1r[2]), "+v"(z1r[3]), "+v"(z1r[4]), "+v"(z1r[5]));
    AB(16);
    f32x4 own[6];
    {
      f32x4 pr_[3][6];
#pragma unroll
      for (int q = 0; q < 3; ++q) {
        const char* src = smem + M_RING + ((wave + 3 - q) & 3) * 24576;
#pragma unroll
        for (int j = 0; j < 6; ++j) pr_[q][j] = *(const f32x4*)(src + q * 6144 + j * 1024 + lane * 16);
      }
      asm volatile("s_nop 7" : "+v"(acc[3][0]), "+v"(acc[3][1]), "+v"(acc[3][2]), "+v"(acc[3][3]), "+v"(acc[3][4]), "+v"(acc[3][5]));
#pragma unroll
      for (int j = 0; j < 6; ++j) own[j] = (acc[3][j] + pr_[0][j]) + (pr_[1][j] + pr_[2][j]);
    }
    asm volatile("s_waitcnt vmcnt(%1)" : "+v"(bsv) : "i"(NW1 + 1) : "memory");
    SB();
    {
      const float bs = lo16(bsv);
      unsigned pk[6][2];
#pragma unroll
      for (int j = 0; j < 6; ++j) {
        const u32x2 z = __builtin_bit_cast(u32x2, z1r[j]);
#pragma unroll
        for (int h = 0; h < 2; ++h) {
          const unsigned r0 = pack2(own[j][2 * h], own[j][2 * h + 1]);
          const unsigned g = pack2(lo16(r0) + bs, hi16(r0) + bs);
          pk[j][h] = pack2(lo16(z[h]) * lo16(g), hi16(z[h]) * hi16(g));
        }
      }
      const unsigned eo = (unsigned)((l15 + 16 * (2 * (g4 & 1) + (g4 >> 1))) * 16);
#pragma unroll
      for (int q = 0; q < 3; ++q) {
        const auto s0 = __builtin_amdgcn_permlane16_swap(pk[2 * q][0], pk[2 * q + 1][0], false, false);
        const auto s1 = __builtin_amdgcn_permlane16_swap(pk[2 * q][1], pk[2 * q + 1][1], false, false);
        st16(eo, hf + (size_t)((rb * 4 + wave) * 24 + cb * 3 + q) * 1024, (u32x4){s0[0], s1[0], s0[1], nohole(s1[1])});
      }
    }
    AB(17);
    drain1(3);
  }
  unsigned hoff[4];
#pragma unroll
  for (int it = 0; it < 4; ++it) hoff[it] = (unsigned)(((rb * 4 + ((mt + 1 + it) & 3)) * 24 + 3 * wave) * 1024 + lane * 16);
  u32x4 Hq[4][3];
  auto ldh1 = [&](int it, int k) {
    asm volatile("global_load_dwordx4 %0, %1, %2 offset:%3 " HSC : "=&v"(Hq[it][k]) : "v"(hoff[it]), "s"(hf), "i"(k * 1024) : "memory");
  };
  auto ldh0 = [&](int it) {
#pragma unroll
    for (int k = 0; k < 3; ++k) ldh1(it, k);
  };
  f32x2 xf[3][2], sv[3][2];
  auto ymath = [&]() {
    char* yi = smem + P3_Y + wave * 1536;
#pragma unroll
    for (int n = 0; n < 2; ++n) *(u32x4*)(yi + (n ? 64 + (lane & 31) : lane) * 16) = yv[n];
    const unsigned ya = lds_addr(yi) + (4 * g4 + (l15 >> 2)) * 32 + 8 * (lane & 3);
    const float sm = *(const float*)(smem + L_XM + (mt * 16 + l15) * 4), sr = *(const float*)(smem + L_XR + (mt * 16 + l15) * 4);
    s16x4 yr[3] = {tr_asm<0>(ya), tr_asm<512>(ya), tr_asm<1024>(ya)};
    asm volatile("s_waitcnt lgkmcnt(0)" : "+v"(yr[0]), "+v"(yr[1]), "+v"(yr[2]) :: "memory");
    const f32x2 mean = {sm, sm}, rd = {sr, sr};
#pragma unroll
    for (int q = 0; q < 3; ++q) {
      const u32x2 yw = __builtin_bit_cast(u32x2, yr[q]);
#pragma unroll
      for (int h = 0; h < 2; ++h) {
        xf[q][h] = unpk(xv[q][h]);
        sv[q][h] = ((xf[q][h] - mean) * rd) * unpk(yw[h]);
        asm volatile("" : "+v"(sv[q][h]), "+v"(xf[q][h]));
      }
    }
    SB();
  };
  u32x2 yf;
  const unsigned yfo = (unsigned)((lane < 32 ? 32 * kh + lane : 64 + (lane & 15)) * 8);
#pragma unroll
  for (int n = NW1; n < 18; ++n) ldP1(n);
  ldtag(yf, yfo, ws + WS_YF + d0 * 8);
  SB();
  asm volatile("s_waitcnt vmcnt(%0)" :: "i"(19 - NW1) : "memory");
  waittag(ef, 0u, ws + WS_LF + (rb * 8 + wave) * 8);
  AB(18);
  signal1(flg + 48 + 4 * kh);
  flag_wait_asm(flg + 48, 4u);
  AB(19);
  __builtin_amdgcn_s_setprio(3);
  ldh0(0);
  __builtin_amdgcn_s_setprio(2);
  ldh0(1);
  __builtin_amdgcn_s_setprio(0);
  SB();
  flag_wait_asm(flg + 40, kh ? 4u : 0u);
  char* const px = smem + (kh ? P3_C + mt * 18432 : P3_G + mt * 17408) + lane * 16;
  f32x4 ac3[5], o[3];
  auto ldy = [&]() {
#pragma unroll
    for (int n = 0; n < 2; ++n) asm volatile("global_load_dwordx4 %0, %1, %2 sc1" : "=&v"(yv[n]) : "v"(yoff(n)), "s"(yb) : "memory");
  };
  auto relh = [&](int it) {
    asm volatile("s_sleep 2\n\tglobal_load_dwordx4 %0, %3, %4 sc1\n\tglobal_load_dwordx4 %1, %3, %4 offset:1024 sc1\n\t"
                 "global_load_dwordx4 %2, %3, %4 offset:2048 sc1\n\ts_waitcnt vmcnt(0)"
                 : "+v"(Hq[it][0]), "+v"(Hq[it][1]), "+v"(Hq[it][2]) : "v"(hoff[it]), "s"(hf) : "memory");
  };
  auto comp = [&](auto itc) {
    constexpr int it = decltype(itc)::value;
    constexpr int NW = 3 * ((it + 2 < 4 ? it + 2 : 4) - it - 1) + (it >= 2 ? 2 : 0);
    wait_vm<NW>(Hq[it][0], Hq[it][1], Hq[it][2]);
    if constexpr (it == 0) pin(Wq[0], Wq[1], Wq[2], Wq[3], Wq[4], Wq[5], Wq[6], Wq[7], Wq[8], Wq[9], Wq[10], Wq[11], Wq[12], Wq[13], Wq[14], xv[0], xv[1], xv[2], yf);
    auto bad = [&]() { return __builtin_amdgcn_ballot_w64(umx(umx(Hq[it][0][3], Hq[it][1][3]), Hq[it][2][3]) == HOLE) != 0; };
    if (__builtin_expect(bad(), 0)) {
      do {
        --tmo;
        relh(it);
      } while (bad() && tmo > 0);
    }
    SB();
#pragma unroll
    for (int k = 0; k < 3; ++k) {
#pragma unroll
      for (int j = 0; j < 5; ++j) {
        ac3[j] = __builtin_amdgcn_mfma_f32_16x16x32_bf16(Wq[k * 5 + j], __builtin_bit_cast(bf16x8, Hq[it][k]), k ? ac3[j] : (f32x4){0.f, 0.f, 0.f, 0.f}, 0, 0, 0);
        if constexpr (it + 2 < 4) { if (k == 0 && j < 3) { SB(); ldh1(it + 2 < 4 ? it + 2 : 0, j); SB(); } }
        if constexpr (it == 1) {
          if (k == 1 && j == 0) {
            SB();
            ldy();
            SB();
          }
        }
      }
    }
    SB();
    if constexpr (it < 3) {
#pragma unroll
      for (int j = 0; j < 5; ++j) *(f32x4*)(px + (it * 5 + j) * 1024) = ac3[j];
      signal1(flg + 64 + kh * 16 + ((mt + 1 + it) & 3) * 4);
    }
    SB();
  };
  auto gather = [&](auto cc) {
    constexpr int cv = decltype(cc)::value;
    flag_wait_asm(flg + 64 + cv * 16 + mt * 4, cv ? 5u : 3u);
#pragma unroll
    for (int it = 0; it < 3 + cv; ++it) {
      const int ms = (mt - 1 - it) & 3;
      const char* ps = smem + ((it < 3 ? cv : !kh) ? P3_C + ms * 18432 : P3_G + ms * 17408) + lane * 16;
      const char* p0 = ps + (it < 3 ? it * 5 + 2 * kh : 15) * 1024;
      const f32x4 a0 = *(const f32x4*)p0, a1 = *(const f32x4*)(p0 + 1024);
      if (!cv && it == 0) { o[0] = a0; o[1] = a1; } else { o[0] += a0; o[1] += a1; }
    }
    if (!kh) {
#pragma unroll
      for (int it = 0; it < 3 + cv; ++it) {
        const int ms = (mt - 1 - it) & 3;
        const char* ps = smem + ((it < 3 ? cv : 1) ? P3_C + ms * 18432 : P3_G + ms * 17408) + lane * 16;
        o[2] += *(const f32x4*)(ps + (it < 3 ? it * 5 + 4 : 17) * 1024);
      }
    }
    SB();
  };
  o[2] = (f32x4){0.f, 0.f, 0.f, 0.f};
  comp(IC(0)); AB(20); comp(IC(1)); comp(IC(2));
  AB(21);
  gather(IC(0));
  comp(IC(3));
  AB(22);
  {
    *(f32x4*)(px + 15 * 1024) = kh ? ac3[0] : ac3[2];
    *(f32x4*)(px + 16 * 1024) = kh ? ac3[1] : ac3[3];
    if (kh) *(f32x4*)(px + 17 * 1024) = ac3[4];
    signal1(flg + 80 + mt * 4);
    SB();
    o[0] += kh ? ac3[2] : ac3[0]; o[1] += kh ? ac3[3] : ac3[1]; o[2] += ac3[4];
    asm volatile("s_waitcnt vmcnt(0)" : "+v"(yv[0]), "+v"(yv[1]) :: "memory");
    if (__builtin_expect(notag(yf), 0)) {
      waittag(yf, yfo, ws + WS_YF + d0 * 8);
      ldy();
      wait_vm<0>(yv[0], yv[1]);
    }
    ymath();
    AB(23);
    gather(IC(1));
    auto fin = [&](const f32x4& o4, int q, unsigned (&pk)[2]) {
#pragma unroll
      for (int h = 0; h < 2; ++h) {
        const f32x2 o = {o4[2 * h], o4[2 * h + 1]};
        const f32x2 a = o * (f32x2){-1.4426950f, -1.4426950f};
        const f32x2 e = (f32x2){__builtin_amdgcn_exp2f(a[0]), __builtin_amdgcn_exp2f(a[1])} + (f32x2){1.0f, 1.0f};
        const f32x2 r = {__builtin_amdgcn_rcpf(e[0]), __builtin_amdgcn_rcpf(e[1])};
        pk[h] = pk2(__builtin_elementwise_fma(o * r, sv[q][h], xf[q][h]));
      }
    };
    unsigned pa[2], pb[2];
    fin(o[0], 0, pa);
    fin(o[1], 1, pb);
    const auto s0 = __builtin_amdgcn_permlane16_swap(pa[0], pb[0], false, false);
    const auto s1 = __builtin_amdgcn_permlane16_swap(pa[1], pb[1], false, false);
    *(u32x4*)(out + xo + 32 * kh + 16 * (g4 & 1) + 8 * (g4 >> 1)) = (u32x4){s0[0], s1[0], s0[1], s1[1]};
    if (!kh) {
      fin(o[2], 2, pa);
      *(u32x2*)(out + xo + 64 + 4 * g4) = (u32x2){pa[0], pa[1]};
    }
  }
  if (b == 0 && tid == 0) *(u64*)(ws + WS_EP) = tag;
  AB(24);
  ab_[31] = ab_[0] + 100 * ((1 << 16) - tmo);
  AB_OUT();
}

__global__ __launch_bounds__(256) void k_dly() {
  const unsigned long long t0 = __builtin_amdgcn_s_memrealtime();
  __shared__ unsigned sm[3][256];
  const int b = threadIdx.x;
  const unsigned ref = g_ab[0][0][0];
  int mn = 1 << 30, mx = -(1 << 30), su = 0;
  for (int w = 0; w < 8; ++w) {
    const int a = (int)(g_ab[b][w][0] - ref), e = (int)(g_ab[b][w][HQ] - ref);
    mn = a < mn ? a : mn; mx = e > mx ? e : mx;
    if ((HM == 'g' && w < 4) || (HM == 'c' && w >= 4)) su += e;
  }
  sm[0][b] = mn; sm[1][b] = mx; sm[2][b] = su;
  __syncthreads();
  if (b == 0) {
    int MN = 1 << 30, MX = -(1 << 30), SU = 0;
    for (int i = 0; i < 256; ++i) { MN = (int)sm[0][i] < MN ? (int)sm[0][i] : MN; MX = (int)sm[1][i] > MX ? (int)sm[1][i] : MX; SU += (int)sm[2][i]; }
    int q = HM == 'z' ? 0 : (HM == 'x' ? MX - MN : SU / 1024 - MN);
    q = q < 0 ? 0 : (q > 5000 ? 5000 : q);
    const unsigned long long te = t0 + 2000 + 10 * q;
    while (__builtin_amdgcn_s_memrealtime() < te) __builtin_amdgcn_s_sleep(1);
  }
}
extern "C" void kernel_launch(void* const* d_in, const int* in_sizes, int n_in,
                              void* d_out, int out_size, void* d_ws, size_t ws_size,
                              hipStream_t stream) {
  (void)hipFuncSetAttribute((const void*)k_f, hipFuncAttributeMaxDynamicSharedMemorySize, M_LDS);
  k_f<<<256, 512, M_LDS, stream>>>((const u16*)d_in[0], (const u16*)d_in[1], (const u16*)d_in[2], (const u16*)d_in[3], (const u16*)d_in[4],
                                   (const u16*)d_in[5], (const u16*)d_in[7], (const u16*)d_in[8], (const u16*)d_in[9], (const u16*)d_in[10],
                                   (u16*)d_out, (char*)d_ws);
  k_dly<<<1, 256, 0, stream>>>();
}
```

```cpp
#define PROF_AB
#define ABM (1u | (1u << 6))
#define HQ 6
#define HM 'g'
#define CWARM 1
#include <hip/hip_runtime.h>
#include <hip/hip_bf16.h>
#include <stdint.h>
#include <type_traits>

typedef unsigned short u16;
typedef unsigned long long u64;
typedef __attribute__((ext_vector_type(8))) short bf16x8;
typedef __attribute__((ext_vector_type(4))) short s16x4;
typedef __attribute__((ext_vector_type(4))) float f32x4;
typedef __attribute__((ext_vector_type(2))) float f32x2;
typedef __attribute__((ext_vector_type(4))) unsigned int u32x4;
typedef __attribute__((ext_vector_type(2))) unsigned int u32x2;
typedef __attribute__((ext_vector_type(2))) __bf16 bf16x2n;

#define LDS_AS __attribute__((address_space(3)))
#define IC(n) std::integral_constant<int, (n)>{}
#define SB() __builtin_amdgcn_sched_barrier(0)

__device__ unsigned g_ab[256][8][32];
#define AB_DECL unsigned ab_[32] = {}
#define AB(i) do { SB(); if ((ABM >> (i)) & 1) ab_[i] = (unsigned)__builtin_amdgcn_s_memrealtime(); SB(); } while (0)
#define AB_OUT() do { if ((threadIdx.x & 63) == 0) for (int i_ = 0; i_ < 32; ++i_) g_ab[blockIdx.x][threadIdx.x >> 6][i_] = ab_[i_] ? ab_[i_] : ab_[0]; } while (0)
#define CB(i) do {} while (0)
constexpr int S_ = 2048, D_ = 640, FH_ = 768, F2_ = 1536;
constexpr unsigned HOLE = 0xFFFFFFFFu;

constexpr size_t WS_EP = 0;
constexpr size_t WS_EF = 4096;
constexpr size_t WS_LF = 6144;
constexpr size_t WS_YF = 8192;
constexpr size_t WS_XF = 16384;
constexpr size_t WS_ST = 32768;
constexpr size_t WS_Z2 = WS_ST + 32 * 12 * 1024;
constexpr size_t WS_H = WS_Z2 + 64 * 48 * 1024;
constexpr size_t WS_ZX = WS_H + 128 * 24 * 1024;
constexpr size_t WS_KV = WS_ZX + 32 * 4 * 4096;
constexpr size_t WS_Y = WS_KV + D_ * S_ * 2;
constexpr size_t WS_WO = WS_Y + D_ * S_ * 2;

__device__ __forceinline__ float bf2f(u16 v) { return __uint_as_float(((unsigned)v) << 16); }
__device__ __forceinline__ unsigned pack2(float lo, float hi) {
  f32x2 v = {lo, hi};
  bf16x2n r = __builtin_convertvector(v, bf16x2n);
  return __builtin_bit_cast(unsigned, r);
}
__device__ __forceinline__ float lo16(unsigned w) { return __uint_as_float(w << 16); }
__device__ __forceinline__ float hi16(unsigned w) { return __uint_as_float(w & 0xFFFF0000u); }
__device__ __forceinline__ float rbf(float f) { return lo16(pack2(f, f)); }
__device__ __forceinline__ f32x2 unpk(unsigned w) { f32x2 r = {lo16(w), hi16(w)}; return r; }
__device__ __forceinline__ unsigned pk2(f32x2 v) { return pack2(v[0], v[1]); }
__device__ __forceinline__ unsigned umx(unsigned a, unsigned b) { return a > b ? a : b; }
__device__ __forceinline__ unsigned nohole(unsigned a) { return a < HOLE - 1 ? a : HOLE - 1; }
__device__ __forceinline__ f32x2 gelu2(f32x2 x) {
  const f32x2 c1 = {-2.3022082f, -2.3022082f}, c2 = {-0.10294324f, -0.10294324f};
  const f32x2 a = x * __builtin_elementwise_fma(x * x, c2, c1);
  f32x2 e = {__builtin_amdgcn_exp2f(a[0]), __builtin_amdgcn_exp2f(a[1])};
  e = e + (f32x2){1.0f, 1.0f};
  const f32x2 r = {__builtin_amdgcn_rcpf(e[0]), __builtin_amdgcn_rcpf(e[1])};
  return x * r;
}
template <int OFF>
__device__ __forceinline__ s16x4 tr_asm(unsigned a) {
  s16x4 r;
  asm volatile("ds_read_b64_tr_b16 %0, %1 offset:%2" : "=&v"(r) : "v"(a), "i"(OFF) : "memory");
  return r;
}
__device__ __forceinline__ bf16x8 join8(s16x4 lo, s16x4 hi) {
  bf16x8 r;
  r[0] = lo[0]; r[1] = lo[1]; r[2] = lo[2]; r[3] = lo[3];
  r[4] = hi[0]; r[5] = hi[1]; r[6] = hi[2]; r[7] = hi[3];
  return r;
}
__device__ __forceinline__ unsigned lds_addr(const void* p) { return (unsigned)(uintptr_t)(LDS_AS const char*)p; }
#define WAIT_LGKM0() asm volatile("s_waitcnt lgkmcnt(0)" ::: "memory")
__device__ __forceinline__ void flag_wait_asm(unsigned faddr, unsigned target) {
  unsigned v, sv;
  asm volatile(".Lfw_%=:\n\tds_read_b32 %0, %2\n\ts_waitcnt lgkmcnt(0)\n\tv_readfirstlane_b32 %1, %0\n\ts_nop 0\n\t"
               "s_cmp_ge_u32 %1, %3\n\ts_cbranch_scc1 .Lfd_%=\n\ts_sleep 1\n\ts_branch .Lfw_%=\n\t.Lfd_%=:"
               : "=&v"(v), "=&s"(sv) : "v"(faddr), "s"(target) : "memory", "scc");
}
__device__ __forceinline__ void signal1(unsigned a) {
  const unsigned one = 1u;
  asm volatile("s_mov_b64 exec, 1\n\tds_add_u32 %0, %1\n\ts_mov_b64 exec, -1" :: "v"(a), "v"(one) : "memory");
}
__device__ __forceinline__ void dma16(unsigned vo, const void* sb, unsigned lds) {
  asm volatile("s_mov_b32 m0, %2\n\ts_nop 0\n\tglobal_load_lds_dwordx4 %0, %1" :: "v"(vo), "s"(sb), "s"(lds) : "memory");
}
template <int LO, int GO> __device__ __forceinline__ void dma32(unsigned vo0, unsigned vo1, const void* sb, unsigned lds) {
  asm volatile("s_add_u32 m0, %5, %6\n\ts_add_u32 vcc_lo, %2, %4\n\ts_addc_u32 vcc_hi, %3, 0\n\t"
               "global_load_lds_dwordx4 %0, vcc\n\tglobal_load_lds_dwordx4 %1, vcc offset:1024"
               :: "v"(vo0), "v"(vo1), "s"((unsigned)(uintptr_t)sb), "s"((unsigned)((uintptr_t)sb >> 32)), "i"(GO), "s"(lds), "i"(LO) : "memory", "scc", "vcc");
}
__device__ __forceinline__ void dma16o(unsigned vo, const void* sb, int go, unsigned lds, int lo) {
  asm volatile("s_add_u32 m0, %3, %4\n\ts_nop 0\n\tglobal_load_lds_dwordx4 %0, %1 offset:%2" :: "v"(vo), "s"(sb), "i"(go), "s"(lds), "i"(lo - go) : "memory", "scc");
}
__device__ __forceinline__ void st16(unsigned vo, const void* sb, u32x4 v) {
  asm volatile("global_store_dwordx4 %0, %1, %2 sc1\n\ts_nop 1" :: "v"(vo), "v"(v), "s"(sb) : "memory");
}
template <class A, class B> __device__ __forceinline__ void MFMA(f32x4& acc, const A& a, const B& b) {
  asm volatile("v_mfma_f32_16x16x32_bf16 %0, %1, %2, %0" : "+v"(acc) : "v"(a), "v"(b));
}
template <class A, class B> __device__ __forceinline__ void MFMA0(f32x4& acc, const A& a, const B& b) {
  asm volatile("v_mfma_f32_16x16x32_bf16 %0, %1, %2, 0" : "=&v"(acc) : "v"(a), "v"(b));
}
template <bool Z, class A, class B> __device__ __forceinline__ void MFMAW(int n, f32x4& acc, const A& a, const B& b) {
  if (n < 0) { if (Z) MFMA0(acc, a, b); else MFMA(acc, a, b); }
  else if (Z) asm volatile("s_waitcnt lgkmcnt(%3)\n\tv_mfma_f32_16x16x32_bf16 %0, %1, %2, 0" : "=&v"(acc) : "v"(a), "v"(b), "i"(n));
  else asm volatile("s_waitcnt lgkmcnt(%3)\n\tv_mfma_f32_16x16x32_bf16 %0, %1, %2, %0" : "+v"(acc) : "v"(a), "v"(b), "i"(n));
}
template <class T> __device__ __forceinline__ void pin1(T& r) { asm volatile("" : "+v"(r)); }
template <class T> __device__ __forceinline__ T opq(T r) { asm volatile("" : "+v"(r)); return r; }
template <class... T> __device__ __forceinline__ void pin(T&... r) { (pin1(r), ...); }
template <int N, class... T> __device__ __forceinline__ void wait_vm(T&... r) { asm volatile("s_waitcnt vmcnt(%0)" :: "n"(N) : "memory"); (pin1(r), ...); }
template <int N, class... T> __device__ __forceinline__ void wait_lg(T&... r) { asm volatile("s_waitcnt lgkmcnt(%0)" :: "n"(N) : "memory"); (pin1(r), ...); }
template <int OFF, class T> __device__ __forceinline__ void ds16(T& d, unsigned a) {
  asm volatile("ds_read_b128 %0, %1 offset:%2" : "=&v"(d) : "v"(a), "i"(OFF) : "memory");
}
template <int OFF> __device__ __forceinline__ void dsw16(unsigned a, const f32x4& v) {
  asm volatile("ds_write_b128 %0, %1 offset:%2" :: "v"(a), "v"(v), "i"(OFF) : "memory");
}
#define HSC ""
template <int OFF, class T> __device__ __forceinline__ void ld16h(T& d, unsigned vo, const void* sb) {
  asm volatile("global_load_dwordx4 %0, %1, %2 offset:%3 " HSC : "=&v"(d) : "v"(vo), "s"(sb), "i"(OFF) : "memory");
}
template <class T> __device__ __forceinline__ void reld6(T (&B)[6], unsigned vo, const void* sb) {
  asm volatile("global_load_dwordx4 %0, %6, %7 offset:-2048 sc1\n\tglobal_load_dwordx4 %1, %6, %7 offset:-1024 sc1\n\t"
               "global_load_dwordx4 %2, %6, %7 sc1\n\tglobal_load_dwordx4 %3, %6, %7 offset:1024 sc1\n\t"
               "global_load_dwordx4 %4, %6, %7 offset:2048 sc1\n\tglobal_load_dwordx4 %5, %6, %7 offset:3072 sc1"
               : "+v"(B[0]), "+v"(B[1]), "+v"(B[2]), "+v"(B[3]), "+v"(B[4]), "+v"(B[5]) : "v"(vo), "s"(sb) : "memory");
}

constexpr int L_FLAG = 0;
constexpr int L_XM = 128;
constexpr int L_XR = 384;
constexpr int L_CS = 640;
constexpr int L_ZS = 1664;
constexpr int L_CS2 = 2688;
constexpr int L_Z1 = 4096;
constexpr int L_RAW = L_Z1;
constexpr int L_SLOT = 16384;
constexpr int L_KV = 128, KV_P = 3088, L_WO = L_KV + 8 * KV_P + 16;
static_assert(L_WO % 16 == 0 && L_WO + 4 * 1024 <= L_SLOT + 2 * 8192, "staging");
constexpr int CV_C = 4608, CV_CS = 4160, CV_BUF = CV_C + 2 * CV_CS;
constexpr int M_CV = L_SLOT;
constexpr int M_RING = L_SLOT + 6 * 8192;
constexpr int P3_C = L_SLOT;
constexpr int P3_G = P3_C + 72 * 1024;
constexpr int P3_Y = L_Z1;
constexpr int M_LDS = 163840;
static_assert(3 * CV_BUF <= 5 * 8192 && P3_G + 68 * 1024 <= M_LDS && M_RING <= P3_G, "LDS map");

enum { UX, UA, UB, UV };
struct Unit { int kind, idx, need, free, iss, slot, grp; };
struct Plan { Unit u[40]; int first[14], nv[2][12]; bool ok; };
#define ISSUE {7, 3, 4, 4, 4, 4, 3, 2, 2, 2, 2, 2, 1}
#define NSTC {0, 0, 0, 0, 0, 0, 1, 1, 1, 0, 0, 0, 0}
#define GZ 6
#define FLAGB 10
constexpr int XNEED[10] = {1, 2, 2, 3, 3, 4, 4, 5, 5, 6};
constexpr int XSLOT[10] = {8, 11, 14, 6, 7, 17, 12, 8, 13, 11}, WSLOT[10] = {2, 4, 9, 0, 15, 2, 6, 4, 9, 12}, VSLOT[10] = {8, 11, 17, 14, 2, 3, 0, 4, 5, 1};
constexpr int NST[2][13] = {{0, 2}, NSTC};
constexpr Plan make_plan() {
  Plan p = {};
  int n = 0;
  for (int b = 1; b < 11; ++b) {
    for (int c = 0; c < 10; ++c) if (XNEED[c] == b) p.u[n++] = {UX, c, b, b + 1, 0, XSLOT[c], 1};
    p.u[n++] = {UA, b - 1, b, b + 2 < 11 ? b + 2 : 11, 0, WSLOT[b - 1], 1};
    p.u[n++] = {UB, b - 1, b, b + 2 < 11 ? b + 2 : 11, 0, WSLOT[b - 1] + 1, 1};
  }
  for (int c = 0; c < 10; ++c) p.u[n++] = {UV, c, 99, 99, 0, VSLOT[c], 1};
  const int is[13] = ISSUE;
  n = 0;
  for (int q = 0; q < 13; ++q) { p.first[q] = n; for (int i = 0; i < is[q]; ++i) { p.u[n].grp = q == 0 ? n & 1 : 1; p.u[n++].iss = q - 1; } }
  p.first[13] = n;
  p.ok = n == 40;
  int fr[18] = {1, 1, -1, -1, -1, -1, -1, -1, -1, -1, -1, -1, -1, -1, -1, -1, -1, -1};
  for (int i = 0; i < 40; ++i) {
    if (p.u[i].iss < fr[p.u[i].slot]) p.ok = false;
    fr[p.u[i].slot] = p.u[i].free;
  }
  int lasts = 3;
  for (int q = 0; q < 13; ++q) if (NST[1][q] && q - 1 > lasts) lasts = q - 1;
  for (int g = 0; g < 2; ++g)
    for (int b = 0; b < 12; ++b) {
      int cnt = 0, lastq = -9;
      for (int q = 0; q <= b; ++q) {
        for (int i = p.first[q]; i < p.first[q + 1]; ++i)
          if (p.u[i].grp == g) { if (p.u[i].need <= b) { cnt = 0; lastq = q - 1; } else cnt += 2; }
        cnt += NST[g][q];
        if (g == 1 && b == FLAGB && q - 1 == lasts) cnt = 0;
      }
      for (int i = p.first[b + 1]; i < 40; ++i) if (p.u[i].need <= b) p.ok = false;
      p.nv[g][b] = g == 0 && b >= GZ ? 0 : cnt;
    }
  if (FLAGB <= lasts || FLAGB < GZ) p.ok = false;
  return p;
}
constexpr Plan PL = make_plan();
static_assert(PL.ok, "phase A plan");

__device__ __forceinline__ void conv_core(unsigned buf, int lane, f32x4 (&ac)[8]) {
  const int l15 = lane & 15, g4 = lane >> 4;
  const int c = (l15 + 1) & 1;
  unsigned aa[4];
#pragma unroll
  for (int k = 0; k < 4; ++k) aa[k] = buf + CV_C + c * CV_CS + (2031 - l15 + 8 * g4 - c) * 2 - 512 - 1024 * k;
  unsigned bb = buf + 512 + 32 * l15 - 32 + 16 * g4;
  u32x2 A[2][8][2];
  u32x4 B[2][8];
  constexpr int TT[36] = {7, 6, 5, 4, 7, 6, 5, 4, 7, 6, 5, 3, 7, 6, 5, 4, 7, 6, 3, 2, 7, 6, 5, 4, 7, 3, 2, 1, 7, 6, 5, 4, 3, 2, 1, 0};
  constexpr int AA[36] = {0, 0, 0, 0, 1, 1, 1, 1, 2, 2, 2, 0, 3, 3, 3, 2, 4, 4, 1, 0, 5, 5, 4, 3, 6, 2, 1, 0, 7, 6, 5, 4, 3, 2, 1, 0};
  auto ld1 = [&](int p, int n) {
    if (n < 16) {
      const int a = n >> 1, o = ((a & 1) ? 0 : 128) + 2 * (n & 1);
      asm volatile("ds_read2_b32 %0, %1 offset0:%2 offset1:%3" : "=&v"(A[p][(n >> 1) & 7][n & 1]) : "v"(aa[(a >> 1) & 3]), "i"(o), "i"(o + 1) : "memory");
    } else
      asm volatile("ds_read_b128 %0, %1 offset:%2" : "=&v"(B[p][n & 7]) : "v"(bb), "i"(512 * (n & 7)) : "memory");
  };
  auto next = [&](int dl) {
#pragma unroll
    for (int k = 0; k < 4; ++k) aa[k] += dl;
    bb += dl;
    SB();
  };
  auto land = [&](int p) {
    asm volatile("s_waitcnt lgkmcnt(0)" : "+v"(A[p][0][0]), "+v"(A[p][0][1]), "+v"(A[p][1][0]), "+v"(A[p][1][1]), "+v"(A[p][2][0]), "+v"(A[p][2][1]),
                 "+v"(A[p][3][0]), "+v"(A[p][3][1]), "+v"(A[p][4][0]), "+v"(A[p][4][1]), "+v"(A[p][5][0]), "+v"(A[p][5][1]), "+v"(A[p][6][0]), "+v"(A[p][6][1]),
                 "+v"(A[p][7][0]), "+v"(A[p][7][1]), "+v"(B[p][0]), "+v"(B[p][1]), "+v"(B[p][2]), "+v"(B[p][3]), "+v"(B[p][4]), "+v"(B[p][5]),
                 "+v"(B[p][6]), "+v"(B[p][7]) :: "memory");
    SB();
  };
  auto mm = [&](int p) {
#pragma unroll
    for (int n = 0; n < 36; ++n) {
      const int t = TT[n], a = AA[n];
      const u32x4 av = {A[p][a][0][0], A[p][a][0][1], A[p][a][1][0], A[p][a][1][1]};
      MFMA(ac[t], av, B[p][t - a]);
      if (n < 24) ld1(p ^ 1, n);
      SB();
    }
    land(p ^ 1);
  };
#pragma unroll
  for (int t = 0; t < 8; ++t) ac[t] = (f32x4){0.f, 0.f, 0.f, 0.f};
#pragma unroll
  for (int n = 0; n < 24; ++n) ld1(0, n);
  land(0);
#pragma unroll 1
  for (int r = 0; r < 8; r += 2) {
    next(-64);
    mm(0);
    next(r < 6 ? -64 : 0);
    mm(1);
  }
  asm volatile("s_nop 15" : "+v"(ac[0]), "+v"(ac[1]), "+v"(ac[2]), "+v"(ac[3]), "+v"(ac[4]), "+v"(ac[5]), "+v"(ac[6]), "+v"(ac[7]));
}

#define HSL 4
#define NW1 10
#define SGS 13

__global__ __launch_bounds__(512) void k_f(
    const u16* __restrict__ x, const u16* __restrict__ kin, const u16* __restrict__ vin, const u16* __restrict__ shift_k,
    const u16* __restrict__ ssm_k, const u16* __restrict__ ssm_D, const u16* __restrict__ w_in, const u16* __restrict__ w_sp,
    const u16* __restrict__ b_sp, const u16* __restrict__ w_out, u16* __restrict__ out, char* ws) {
  extern __shared__ __attribute__((aligned(16))) char smem[];
  const int tid = threadIdx.x, lane = tid & 63, wave = __builtin_amdgcn_readfirstlane(tid >> 6);
  const int b = blockIdx.x;
  const int xcd = b & 7, idx = b >> 3;
  const int rb = 8 * (xcd >> 1) + (idx >> 2), cb = 4 * (xcd & 1) + (idx & 3);
  const int t0 = rb * 64, d0 = cb * 80;
  const int g4 = lane >> 4, l15 = lane & 15;
  const unsigned lin = (unsigned)(lane * 16);
  const unsigned lb = lds_addr(smem), flg = lb + L_FLAG;
  AB_DECL;
  AB(0);
  asm volatile("" :: "s"(x), "s"(kin), "s"(vin), "s"(shift_k), "s"(ssm_k), "s"(ssm_D), "s"(w_in), "s"(w_sp), "s"(b_sp), "s"(w_out), "s"(out), "s"(ws));
  u64 tag = *(const __attribute__((address_space(4))) u64*)(uintptr_t)ws;
  int tmo = 1 << 16;
#define TWARM 0
  unsigned pcv = 0, ptv = 0;
  if (CWARM && idx < CWARM) {
    u64 pc;
    unsigned clen;
    asm volatile("s_getpc_b64 %0\n.Lpc_here_%=:\n\ts_mov_b32 %1, k_end-.Lpc_here_%=" : "=s"(pc), "=s"(clen));
    asm volatile("global_load_dword %0, %1, %2" : "=&v"(pcv) : "v"(min((unsigned)(wave * 8192 + lane * 128), clen - 4u) & ~3u), "s"(pc) : "memory");
  }
  if (TWARM && idx == 1) {
    const char* tp = wave == 0 ? (const char*)w_in : (wave < 3 ? (const char*)w_sp + (wave - 1) * (4 << 20) : (wave < 7 ? ws + (wave - 3) * (4 << 20) : (const char*)ssm_k));
    const unsigned to = (unsigned)(lane * (wave == 0 ? 30720 : (wave == 7 ? 40960 : 65536)));
    asm volatile("global_load_dword %0, %1, %2" : "=&v"(ptv) : "v"(to), "s"(tp) : "memory");
  }

  const int cw = wave & 3, wm = wave & 1, wl = (wave >> 1) & 1, ksb = wm ^ wl, odd = cb & 1, pr = cb >> 1;
  const int r8 = lane >> 3, c8 = lane & 7;
  unsigned voX[2], voW[2];
#pragma unroll
  for (int h = 0; h < 2; ++h) {
    voX[h] = (unsigned)((h * 8 + r8) * (D_ * 2) + ((c8 ^ (h * 4 + (r8 & 3))) << 4) - h * 1024);
    voW[h] = (unsigned)((h * 8 + r8) * (F2_ * 2) + ((c8 ^ (h * 4 + (r8 & 2))) << 4) - h * 1024);
  }
  const char* gX = (const char*)x + (size_t)(t0 + cw * 16) * (D_ * 2);
  const char* gW = (const char*)w_in + (size_t)(cw * 16) * (F2_ * 2) + pr * 384;
  const char *gV = gW + odd * 256, *gB = gW + (odd ? 128 : FH_ * 2 + 128);
  const unsigned ldw = lb + cw * 2048;
  auto issue = [&](auto nc) {
    constexpr Unit u = PL.u[decltype(nc)::value];
    if (u.kind == UX) dma32<L_SLOT + u.slot * 8192, u.idx * 128>(voX[0], voX[1], gX, ldw);
    else dma32<L_SLOT + u.slot * 8192, u.idx * (64 * F2_ * 2) + (u.kind == UA ? FH_ * 2 : 0)>(voW[0], voW[1], u.kind == UB ? gB : gV, ldw);
    SB();
  };
  auto issp = [&](auto gc, auto pc, auto kc) {
    constexpr int p = decltype(pc)::value, n = PL.first[p + 1] + decltype(kc)::value;
    if constexpr (n < PL.first[p + 2]) { if constexpr (PL.u[n].grp == decltype(gc)::value) issue(IC(n)); }
  };
  auto issall = [&](auto gc, auto pc) {
    issp(gc, pc, IC(0)); issp(gc, pc, IC(1)); issp(gc, pc, IC(2)); issp(gc, pc, IC(3)); issp(gc, pc, IC(4));
    issp(gc, pc, IC(5)); issp(gc, pc, IC(6)); issp(gc, pc, IC(7)); issp(gc, pc, IC(8)); issp(gc, pc, IC(9));
  };
  const int kd = d0 + min(wave, 4) * 16 + r8 * 2;
  u32x4 sw;
  {
    const int ks0 = t0 + wave * 8;
#pragma unroll
    for (int i = 0; i < 3; ++i) {
      const int id = min(i * 64 + lane, 189), r = id / 10, c = id % 10;
      const char* kp = (r < 11 ? (const char*)kin + (size_t)max(ks0 - 3 + r, 0) * (D_ * 2) : (const char*)vin + (size_t)(ks0 + r - 11) * (D_ * 2)) + cb * 160 + c * 16;
      asm volatile("s_mov_b32 m0, %1\n\ts_nop 0\n\tglobal_load_lds_dwordx4 %0, off" :: "v"(kp), "s"(lb + L_KV + wave * KV_P + i * 1024) : "memory");
    }
    asm volatile("global_load_dwordx4 %0, %1, %2" : "=&v"(sw) : "v"(kd * 8), "s"(shift_k) : "memory");
    SB();
  }
  unsigned kk[11], vv[8];
  u32x4 oa, ob;
  auto kvld = [&]() {
    const char* kl = smem + L_KV + c8 * KV_P + min(wave, 4) * 32 + r8 * 4;
#pragma unroll
    for (int i = 0; i < 11; ++i) kk[i] = *(const unsigned*)(kl + i * 160);
#pragma unroll
    for (int i = 0; i < 8; ++i) vv[i] = *(const unsigned*)(kl + (11 + i) * 160);
  };
  auto kvm = [&](int e0, int e1) {
    const f32x2 w0 = {lo16(sw[0]), lo16(sw[2])}, w1 = {hi16(sw[0]), hi16(sw[2])};
    const f32x2 w2 = {lo16(sw[1]), lo16(sw[3])}, w3 = {hi16(sw[1]), hi16(sw[3])};
    f32x2 kf[11], rr[8];
#pragma unroll
    for (int i = 2 * e0; i < 2 * e1 + 3; ++i) kf[i] = unpk((i < 3 && t0 + c8 == 0) ? 0u : kk[i]);
#pragma unroll
    for (int i = 2 * e0; i < 2 * e1; ++i) {
      f32x2 a0 = kf[i] * w3;
      a0 = __builtin_elementwise_fma(kf[i + 1], w2, a0);
      a0 = __builtin_elementwise_fma(kf[i + 2], w1, a0);
      a0 = __builtin_elementwise_fma(kf[i + 3], w0, a0);
      rr[i] = a0 * unpk(vv[i]);
    }
#pragma unroll
    for (int e = e0; e < e1; ++e) { oa[e] = pack2(rr[2 * e][0], rr[2 * e + 1][0]); ob[e] = pack2(rr[2 * e][1], rr[2 * e + 1][1]); }
  };
  auto kvst = [&]() {
    const unsigned ko = (unsigned)((kd * S_ + t0 + c8 * 8) * 2);
    st16(ko, ws + WS_KV, oa);
    st16(ko, ws + WS_KV + S_ * 2, ob);
    SB();
  };
  auto barrier0 = [&](int nv) {
    SB();
    asm volatile("s_waitcnt vmcnt(%0) lgkmcnt(0)\n\ts_barrier" :: "n"(nv) : "memory");
    SB();
    asm volatile("" : "+v"(sw), "+s"(tag), "+v"(pcv), "+v"(ptv));
    tag += 1;
  };
  unsigned pX[2], pW[2];
#pragma unroll
  for (int k = 0; k < 2; ++k) {
    const int q = l15 >> 2, p = lane & 3, sz = (q >> 1) | ((g4 & 1) << 1);
    pX[k] = lb + (wm * 32 + q * 8 + p) * 128 + ((((k ^ ksb) * 4 + g4) ^ (l15 & 7)) << 4);
    pW[k] = lb + ((k ^ ksb) * 32 + g4 * 8 + q) * 128 + (((sz << 1) | (p >> 1)) << 4) + (p & 1) * 8;
  }
  bf16x8 ones;
#pragma unroll
  for (int e = 0; e < 8; ++e) ones[e] = (short)0x3F80;
  f32x4 xm[2], xr[2];
  auto ldxs = [&]() {
#pragma unroll
    for (int i = 0; i < 2; ++i) {
      xm[i] = *(const f32x4*)(smem + L_XM + (wm * 32 + g4 * 8 + i * 4) * 4);
      xr[i] = *(const f32x4*)(smem + L_XR + (wm * 32 + g4 * 8 + i * 4) * 4);
    }
  };
  auto act = [&](const f32x4& a, float c, int i) {
    const f32x2 nc = {-c, -c};
    const f32x2 lo = gelu2(__builtin_elementwise_fma((f32x2){xm[i][0], xm[i][1]}, nc, (f32x2){a[0], a[1]}) * (f32x2){xr[i][0], xr[i][1]});
    const f32x2 hi = gelu2(__builtin_elementwise_fma((f32x2){xm[i][2], xm[i][3]}, nc, (f32x2){a[2], a[3]}) * (f32x2){xr[i][2], xr[i][3]});
    return (f32x4){lo[0], lo[1], hi[0], hi[1]};
  };
  auto pack8 = [&](const f32x4& a, const f32x4& c) { return (u32x4){pack2(a[0], a[1]), pack2(a[2], a[3]), pack2(c[0], c[1]), pack2(c[2], c[3])}; };
  auto z1put = [&](int f0, const u32x4& v) { *(u32x4*)(smem + L_Z1 + (f0 + l15) * 128 + (((wm * 4 + g4) ^ (l15 & 7)) << 4)) = v; };
  auto rdXF = [&](auto cc, bf16x8 (&X)[2][2]) {
    constexpr int so = L_SLOT + XSLOT[decltype(cc)::value] * 8192;
#pragma unroll
    for (int k = 0; k < 2; ++k) {
      const unsigned a = pX[k] + (so & ~0x7FFF);
      ds16<(so & 0x7FFF)>(X[k][0], a);
      ds16<(so & 0x7FFF) + 512>(X[k][1], a);
    }
    SB();
  };
  auto sttag = [&](const char* p) {
    asm volatile("s_mov_b64 exec, 1\n\tglobal_store_dwordx2 %0, %1, %2 sc1\n\ts_mov_b64 exec, -1" :: "v"(lin), "v"(tag), "s"(p) : "memory");
  };
  auto ldtag = [&](u32x2& f, unsigned vo, const char* p) { asm volatile("global_load_dwordx2 %0, %1, %2 sc1" : "=&v"(f) : "v"(vo), "s"(p) : "memory"); };
  auto notag = [&](const u32x2& f) { return __builtin_amdgcn_ballot_w64(f[0] != (unsigned)tag || f[1] != (unsigned)(tag >> 32)) != 0; };
  auto waittag = [&](u32x2& f, unsigned vo, const char* p) {
    while (__builtin_expect(notag(f) && tmo > 0, 0)) {
      --tmo;
      __builtin_amdgcn_s_sleep(4);
      ldtag(f, vo, p);
      wait_vm<0>(f);
    }
  };
  u32x2 ef;
  auto ldef = [&]() { asm volatile("global_load_dwordx2 %0, %1, %2 sc1" : "=&v"(ef) : "v"((unsigned)((lane & 31) * 8)), "s"(ws + WS_EF + cb * 256) : "memory"); };

  char* const hf = ws + WS_H;
  bf16x8 Wq[15];
  const char* wg = ws + WS_WO + (size_t)(wave * 120 + cb * 5) * 1024;
  const int mt = wave & 3, kh = wave >> 2;
  const size_t xo = (size_t)(t0 + mt * 16 + l15) * D_ + d0;
  u32x2 xv[3];
  auto ldP1 = [&](int n) {
    if (n < 15)
      asm volatile("global_load_dwordx4 %0, %1, %2 offset:%3 sc1" : "=&v"(Wq[n < 15 ? n : 0]) : "v"(lin), "s"(wg + (n / 5) * 40960 + 2048), "i"((n % 5) * 1024 - 2048) : "memory");
    else {
      const int q = n < 18 ? n - 15 : 0;
      asm volatile("global_load_dwordx2 %0, %1, %2 offset:%3" : "=&v"(xv[q]) : "v"((unsigned)(l15 * D_ * 2 + 8 * g4)),
                   "s"(x + (size_t)(t0 + mt * 16) * D_ + d0 + (q < 2 ? 32 * kh : 64)), "i"(q == 1 ? 32 : 0) : "memory");
    }
  };
  u32x4 yv[2];
  auto yoff = [&](int n) {
    const int c = n ? 64 + (lane & 31) : lane, r = c >> 1;
    return (unsigned)(((r < 32 ? 32 * kh + r : 32 + r) * S_ + 8 * (c & 1)) * 2);
  };
  const char* const yb = ws + WS_Y + ((size_t)d0 * S_ + t0 + 16 * mt) * 2;
  auto ldW1 = [&]() {
    ldtag(ef, 0u, ws + WS_LF + (rb * 8 + wave) * 8);
#pragma unroll
    for (int n = 0; n < NW1; ++n) ldP1(n);
    SB();
  };
  auto drain1 = [&](int nc) {
    SB();
    asm volatile("s_waitcnt vmcnt(%0)" :: "i"(nc) : "memory");
#pragma unroll
    for (int n = 0; n < NW1; ++n) pin(Wq[n]);
    pin(ef);
  };
  if (wave >= 4) {
    const int wid = rb * 4 + cw;
    __builtin_amdgcn_s_setprio(3);
    if (wid < 120) dma16((unsigned)((lane >> 1) * (D_ * 2) + (lane & 1) * 16), w_out + (size_t)(wid / 5) * 32 * D_ + d0 + (wid % 5) * 16, lb + L_WO + cw * 1024);
    SB();
    issall(IC(1), IC(-1));
    AB(1);
    barrier0(PL.nv[1][0]);
    AB(2);
    u32x4 hole = {HOLE, HOLE, HOLE, HOLE};
    pin(ones, hole);
    issp(IC(1), IC(0), IC(0));
    if (wave == 7) kvld();
    issp(IC(1), IC(0), IC(1));
    if (wid < 120) {
      const unsigned ra = lb + L_WO + cw * 1024 + (8 * g4 + (l15 >> 2)) * 32 + 8 * (lane & 3);
      s16x4 lo = tr_asm<0>(ra), hi = tr_asm<128>(ra);
      wait_lg<0>(lo, hi);
      st16(lin, ws + WS_WO + (size_t)((wid / 5) * 40 + cb * 5 + wid % 5) * 1024, __builtin_bit_cast(u32x4, join8(lo, hi)));
    }
    SB();
    issp(IC(1), IC(0), IC(2)); issp(IC(1), IC(0), IC(3)); issp(IC(1), IC(0), IC(4)); issp(IC(1), IC(0), IC(5));
    if (wave == 7) kvm(0, 2);
    AB(3);
    bf16x8 XF[10][2][2];
    auto ldx = [&](int c) {
      u32x4 xv;
      asm volatile("global_load_dwordx4 %0, %1, %2\n\ts_waitcnt vmcnt(0)" : "=&v"(xv) : "v"((unsigned)(lane * (D_ * 2) + c * 16)), "s"(x + (size_t)t0 * D_) : "memory");
      return xv;
    };
    float fm, fq;
    f32x2 fr[2];
    constexpr int FINP = 8;
    auto period = [&](auto pc) {
      constexpr int p = decltype(pc)::value;
      constexpr int c0 = p == 1 || p > 6 ? 0 : 2 * p - 3, nx = p == 1 || p == 6 ? 1 : (p < 6 ? 2 : 0);
      SB();
      asm volatile("s_waitcnt vmcnt(%0) lgkmcnt(0)\n\ts_barrier" :: "n"(PL.nv[1][p]) : "memory");
      CB(6 * (p - XBAR) + 1);
      SB();
      constexpr int pc0 = p == 2 || p > 7 ? 0 : 2 * p - 5, pnx = p < 2 || p > 7 ? 0 : (p == 2 || p == 7 ? 1 : 2);
#pragma unroll
      for (int c = pc0; c < pc0 + pnx; ++c) pin(XF[c][0][0], XF[c][0][1], XF[c][1][0], XF[c][1][1]);
      if constexpr (p == FINP) {
        if (wave == 6) {
          const char* rw = smem + L_RAW + ((lane >> 3) * 4 + (lane & 3)) * 16 + (lane & 4) * 2;
          fr[0] = *(const f32x2*)rw;
          fr[1] = *(const f32x2*)(rw + 512);
        }
        SB();
      }
      issp(IC(1), pc, IC(0));
      CB(6 * (p - XBAR) + 2);
      if constexpr (nx > 0) rdXF(IC(c0), XF[c0]);
      if constexpr (p == FLAGB) { if (wave == 5) sttag(ws + WS_EF + (cb * 32 + rb) * 8); }
      issp(IC(1), pc, IC(1));
      CB(6 * (p - XBAR) + 3);
      if constexpr (p <= 2) { if (wave == 7) kvm(p + 1, p + 2); SB(); }
      if constexpr (p == 3) { if (wave == 7) kvst(); SB(); }
      issp(IC(1), pc, IC(2));
      CB(6 * (p - XBAR) + 4);
      if constexpr (nx > 1) rdXF(IC(c0 + 1), XF[c0 + 1]);
      issp(IC(1), pc, IC(3)); issp(IC(1), pc, IC(4)); issp(IC(1), pc, IC(5));
      if constexpr (NST[1][p + 1] > 0) {
        int n0 = 0;
#pragma unroll
        for (int q = 0; q <= p; ++q) n0 += NST[1][q];
#pragma unroll
        for (int n = n0; n < n0 + NST[1][p + 1]; ++n) st16(lin, ws + WS_Z2 + (size_t)((rb * 2 + wl) * 48 + cb * 6 + wm * 3 + n) * 1024, hole);
        SB();
      }
      if constexpr (p == FINP) {
        if (wave == 6) {
          const f32x2 sa = fr[0] + fr[1];
          fm = sa[0] * (1.0f / 640.0f);
          fq = sa[1] - sa[0] * fm;
          if (__builtin_expect(__builtin_amdgcn_ballot_w64(!(fq >= 1e-3f * sa[0] * fm)) != 0, 0)) {
            const f32x2 mm = {fm, fm};
            f32x2 q2 = {0.f, 0.f};
#pragma unroll 1
            for (int c = 0; c < 80; ++c) {
              const u32x4 xv = ldx(c);
#pragma unroll
              for (int e = 0; e < 4; ++e) { const f32x2 d = unpk(xv[e]) - mm; q2 = __builtin_elementwise_fma(d, d, q2); }
            }
            fq = q2[0] + q2[1];
          }
          pin(fm, fq);
        }
      }
      if constexpr (p == FINP + 1) {
        if (wave == 6) {
          const float vbf = rbf(fmaxf(fq, 0.f) * (1.0f / 640.0f));
          fq = rbf(__builtin_amdgcn_sqrtf(rbf(vbf + 1e-5f)));
          fm = rbf(fm);
          pin(fm, fq);
        }
      }
      if constexpr (p == FINP + 2) {
        if (wave == 6) {
          *(float*)(smem + L_XM + lane * 4) = fm;
          *(float*)(smem + L_XR + lane * 4) = __builtin_amdgcn_rcpf(fq);
        }
      }
    };
    period(IC(1)); AB(4); period(IC(2)); period(IC(3)); period(IC(4)); period(IC(5)); AB(5);
    period(IC(6)); period(IC(7)); period(IC(8)); period(IC(9)); period(IC(10)); period(IC(11));
    AB(6);
    const bool docv = cw < ((rb & 1) ? 2 : 3);
    const int d = d0 + (cw < 2 ? 2 * rb + cw : 64 + (rb >> 1));
    u32x4 hc[4], uc[4];
    unsigned Dw;
#pragma unroll
    for (int k = 0; k < 4; ++k) asm volatile("global_load_dwordx4 %0, %1, %2 offset:%3" : "=&v"(hc[k]) : "v"(lin), "s"(ssm_k + (size_t)d * S_), "i"(k * 1024) : "memory");
    asm volatile("global_load_ushort %0, %1, %2" : "=&v"(Dw) : "v"(0u), "s"(ssm_D + d) : "memory");
    __builtin_amdgcn_s_setprio(0);
    wait_vm<5>();
    signal1(flg + 60);
    flag_wait_asm(flg + 60, 4u);
    AB(7);
    {
      f32x4 ac2[2][2], cs2[2];
      s16x4 VF[3][2][2];
      unsigned aV[2][2];
      auto rdV = [&](auto nc) {
        constexpr int n = decltype(nc)::value, so = VSLOT[n >> 1] * 8192, po = n < 2 ? -1 : (VSLOT[(n >> 1) - 1] * 8192) >> 16;
#pragma unroll
        for (int jj = 0; jj < 2; ++jj) {
          if ((so >> 16) != po) aV[n & 1][jj] = opq((pW[n & 1] ^ (unsigned)(wl * 64 + jj * 32)) + L_SLOT + (so & ~0xFFFF));
          VF[n % 3][jj][0] = tr_asm<(so & 0xFFFF)>(aV[n & 1][jj]);
          VF[n % 3][jj][1] = tr_asm<(so & 0xFFFF) + 512>(aV[n & 1][jj]);
        }
        SB();
      };
      auto mm2 = [&](auto nc) {
        constexpr int n = decltype(nc)::value, j = n >> 1, k = n & 1, nw = n < 19 ? 4 : 0;
        const bf16x8 w0 = join8(VF[n % 3][0][0], VF[n % 3][0][1]), w1 = join8(VF[n % 3][1][0], VF[n % 3][1][1]);
        if (k == 0) { MFMAW<n == 0>(nw, cs2[0], ones, w0); MFMAW<n == 0>(-1, cs2[1], ones, w1); }
        MFMAW<n == 0>(k ? nw : -1, ac2[0][0], XF[j][k][0], w0);
        SB();
        if constexpr (n < 18) rdV(IC(n + 2 < 20 ? n + 2 : 0));
        if constexpr (n == 12) ldef();
        if constexpr (n == 19) signal1(flg + 16);
        MFMAW<n == 0>(-1, ac2[1][0], XF[j][k][1], w0);
        MFMAW<n == 0>(-1, ac2[0][1], XF[j][k][0], w1);
        MFMAW<n == 0>(-1, ac2[1][1], XF[j][k][1], w1);
        SB();
        if constexpr (n == 19) {
          pin(cs2[0], cs2[1]);
          if (g4 < 2) *(float*)(smem + L_CS2 + (cw * 32 + lane) * 4) = g4 ? cs2[1][0] : cs2[0][0];
          signal1(flg + 20 + wl * 4);
        }
        SB();
      };
      rdV(IC(0)); rdV(IC(1));
      mm2(IC(0)); mm2(IC(1)); mm2(IC(2)); mm2(IC(3)); mm2(IC(4)); mm2(IC(5)); mm2(IC(6)); mm2(IC(7)); mm2(IC(8)); mm2(IC(9));
      mm2(IC(10)); mm2(IC(11)); mm2(IC(12)); mm2(IC(13)); mm2(IC(14)); mm2(IC(15)); mm2(IC(16)); mm2(IC(17)); mm2(IC(18)); mm2(IC(19));
      AB(8);
      wait_vm<0>(ef);
      waittag(ef, (unsigned)((lane & 31) * 8), ws + WS_EF + cb * 256);
#pragma unroll
      for (int k = 0; k < 4; ++k) asm volatile("global_load_dwordx4 %0, %1, %2 offset:%3 sc1" : "=&v"(uc[k]) : "v"(lin), "s"(ws + WS_KV + (size_t)d * (S_ * 2)), "i"(k * 1024) : "memory");
      ldxs();
      flag_wait_asm(flg + 20 + wl * 4, 2u);
      pin(ac2[0][0], ac2[0][1], ac2[1][0], ac2[1][1]);
#pragma unroll
      for (int jj = 0; jj < 2; ++jj) {
        const float ct = cs2[jj][0] + *(const float*)(smem + L_CS2 + ((cw ^ 1) * 32 + jj * 16 + l15) * 4);
        z1put(odd * 32 + wl * 32 + jj * 16, pack8(act(ac2[0][jj], ct, 0), act(ac2[1][jj], ct, 1)));
      }
      signal1(flg + 56);
      AB(9);
    }
    wait_vm<0>(hc[0], hc[1], hc[2], hc[3], uc[0], uc[1], uc[2], uc[3], Dw);
    if (docv) {
      char* cb_ = smem + M_CV + cw * CV_BUF;
      flag_wait_asm(flg + 16, 4u);
      if (lane < 36) *(u32x4*)(cb_ + (lane < 32 ? lane * 16 : CV_C + ((lane >> 1) & 1) * CV_CS + 4096 + (lane & 1) * 16)) = (u32x4){0u, 0u, 0u, 0u};
      const float Dv = lo16(Dw);
#pragma unroll
      for (int k = 0; k < 4; ++k) {
        const int a = k * 64 + lane;
        const int h0 = k ? __builtin_amdgcn_readlane((int)hc[k ? k - 1 : 0][3], 63) : 0;
        const unsigned hp = (unsigned)__builtin_amdgcn_update_dpp(h0, (int)hc[k][3], 0x138, 0xF, 0xF, false);
        const unsigned w[5] = {hp, hc[k][0], hc[k][1], hc[k][2], hc[k][3]};
        u32x4 sw, mx;
#pragma unroll
        for (int i = 0; i < 4; ++i) {
          sw[i] = (w[4 - i] >> 16) | (w[4 - i] << 16);
          mx[i] = (w[4 - i] & 0xFFFFu) | (w[3 - i] & 0xFFFF0000u);
        }
        char* cp = cb_ + CV_C + (2040 - 8 * a) * 2;
        *(u32x4*)(cp) = sw;
        *(u32x4*)(cp + CV_CS) = mx;
        *(u32x4*)(cb_ + 512 + a * 16) = uc[k];
      }
      AB(12);
      f32x4 ac[8];
      conv_core(lds_addr(cb_), lane, ac);
      AB(13);
      u32x2 kvp[8];
#pragma unroll
      for (int t = 0; t < 8; ++t) kvp[t] = *(const u32x2*)(cb_ + 512 + (256 * t + 16 * l15 + 4 * g4) * 2);
#pragma unroll
      for (int k = 0; k < 4; ++k) {
        u32x2 ov[2];
#pragma unroll
        for (int t = 0; t < 2; ++t)
#pragma unroll
          for (int h = 0; h < 2; ++h) {
            const unsigned cv = pack2(ac[2 * k + t][2 * h], ac[2 * k + t][2 * h + 1]);
            const unsigned kd = pack2(lo16(kvp[2 * k + t][h]) * Dv, hi16(kvp[2 * k + t][h]) * Dv);
            ov[t][h] = pack2(lo16(cv) + lo16(kd), hi16(cv) + hi16(kd));
          }
        const auto s0 = __builtin_amdgcn_permlane16_swap(ov[0][0], ov[1][0], false, false);
        const auto s1 = __builtin_amdgcn_permlane16_swap(ov[0][1], ov[1][1], false, false);
        st16((unsigned)((256 * (g4 & 1) + 16 * l15 + 8 * (g4 >> 1)) * 2), ws + WS_Y + (size_t)d * (S_ * 2) + k * 1024, (u32x4){s0[0], s1[0], s0[1], s1[1]});
      }
      wait_vm<0>();
      sttag(ws + WS_YF + d * 8);
    } else if (cw == 3) {
#pragma unroll
      for (int n = 0; n < 12; ++n) {
        st16(lin, ws + WS_H + (size_t)((rb * 4 + n / 3) * 24 + cb * 3 + n % 3) * 1024, hole);
        __builtin_amdgcn_s_sleep(HSL);
      }
      wait_vm<0>();
      sttag(ws + WS_LF + (rb * 8 + cb) * 8);
      if (!odd) {
        u32x4 zx[4];
        ldtag(ef, (unsigned)((lane & 1) * 8), ws + WS_XF + (rb * 4 + pr) * 16);
        wait_vm<0>(ef);
        waittag(ef, (unsigned)((lane & 1) * 8), ws + WS_XF + (rb * 4 + pr) * 16);
#pragma unroll
        for (int c = 0; c < 4; ++c)
          asm volatile("global_load_dwordx4 %0, %1, %2 offset:%3 sc1" : "=&v"(zx[c]) : "v"(lin), "s"(ws + WS_ZX + (size_t)(rb * 4 + pr) * 4096), "i"(c * 1024) : "memory");
        wait_vm<0>(zx[0], zx[1], zx[2], zx[3]);
#pragma unroll
        for (int c = 0; c < 4; ++c) *(u32x4*)(smem + L_Z1 + (64 + (c & 1) * 16 + l15) * 128 + ((((c >> 1) * 4 + g4) ^ (l15 & 7)) << 4)) = zx[c];
      }
      signal1(flg + 56);
    }
    SB();
    AB(14);
    WAIT_LGKM0();
    signal1(flg + 40);
    flag_wait_asm(flg + 44, 4u);
    AB(15);
    ldW1();
    drain1(0);
  } else {
    issall(IC(0), IC(-1));
    if (tid < 32) *(unsigned*)(smem + L_FLAG + tid * 4) = 0u;
    pin(ones);
    AB(1);
    barrier0(PL.nv[0][0]);
    AB(2);
    kvld();
    kvm(0, 4);
    kvst();
    AB(3);
    const bool isz2 = !(odd && wl);
    const char* Ab[4];
#pragma unroll
    for (int i = 0; i < 4; ++i) Ab[i] = (const char*)w_sp + (size_t)(t0 + ((wave + 1 + i) & 3) * 16) * (S_ * 2) + wave * 1024;
    const unsigned ringl = lb + M_RING + wave * 24576;
    const unsigned aoff[2] = {(unsigned)(r8 * 4096 + ((c8 ^ r8) << 4)), (unsigned)(r8 * 4096 + ((c8 ^ r8) << 4) + 32768)};
    auto ldA1 = [&](auto dc, int i) {
      constexpr int d = decltype(dc)::value;
      dma16o(aoff[i & 1], Ab[i >> 1], d * 128, ringl, (d % 3) * 8192 + i * 1024);
    };
    {
      f32x4 zv[2][4], cs[4];
      s16x4 WB[2][4][2];
      bf16x8 XG[10][2][2];
      unsigned aX[2][2], aW[2][2][4];
#pragma unroll
      for (int k = 0; k < 2; ++k)
#pragma unroll
        for (int w = 0; w < 2; ++w) {
          aX[w][k] = pX[k] + L_SLOT + 6 * 8192 + w * 65536;
          pin(aX[w][k]);
#pragma unroll
          for (int jj = 0; jj < 4; ++jj) { aW[w][k][jj] = (pW[k] ^ (unsigned)(jj * 32)) + L_SLOT + wl * 8192 + w * 65536; pin(aW[w][k][jj]); }
        }
      auto rd1 = [&](auto nc, int r) {
        constexpr int n = decltype(nc)::value, j = n >> 1, k = n & 1, c = n > 0 && n < 11 ? n - 1 : 0, sx = (XSLOT[c] - 6) * 8192, sa = WSLOT[j] * 8192;
        if (r < 0) { if (r & 1) ds16<(sx & 0xFFFF) + 512>(XG[c][(r + 4) >> 1][1], aX[sx >> 16][(r + 4) >> 1]); else ds16<(sx & 0xFFFF)>(XG[c][(r + 4) >> 1][0], aX[sx >> 16][(r + 4) >> 1]); }
        else if (r & 1) WB[k][r >> 1][1] = tr_asm<(sa & 0xFFFF) + 512>(aW[sa >> 16][k][r >> 1]);
        else WB[k][r >> 1][0] = tr_asm<(sa & 0xFFFF)>(aW[sa >> 16][k][r >> 1]);
        SB();
      };
      auto bar = [&](auto bc) {
        SB();
        constexpr int p = decltype(bc)::value;
        CB(6 * (p - XBAR));
        if constexpr (p == 1) asm volatile("s_waitcnt vmcnt(%0) lgkmcnt(0)\n\ts_barrier" :: "n"(PL.nv[0][p]) : "memory");
        else if constexpr (p <= GZ) asm volatile("s_waitcnt vmcnt(%0) lgkmcnt(8)\n\ts_barrier" :: "n"(PL.nv[0][p]) : "memory");
        else if constexpr (p == 8) asm volatile("s_waitcnt lgkmcnt(4)\n\ts_barrier" ::: "memory");
        else if constexpr (p == 11) asm volatile("s_waitcnt lgkmcnt(0)\n\ts_barrier" ::: "memory");
        else asm volatile("s_barrier" ::: "memory");
        CB(6 * (p - XBAR) + 1);
        SB();
      };
      float cp[4];
      f32x4 st[2][2], rv;
      const unsigned rwa = lb + L_RAW + (ksb * 2 + wm) * 256 + l15 * 16 + ((l15 >> 2) == g4 ? 0 : 1024);
      auto kstep = [&](auto nc) {
        constexpr int n = decltype(nc)::value, j = n >> 1, k = n & 1, nn = n < 19 ? n + 1 : 0, r0 = nn >= 2 && nn <= 10 ? -4 : 0;
        constexpr int sc = n == 1 ? 0 : n - 2, ns = !k || n > 11 ? 0 : (n == 1 || n == 11 ? 1 : 2);
        constexpr bool more = n < 19;
        if constexpr (more && k) bar(IC(nn / 2 + 1));
        int r = r0, c = 0;
        auto nxt = [&]() { if (more && r < 8) { rd1(IC(nn), r++); ++c; } };
        auto sel = [&](int q) {
          const f32x4& sq = st[q & 1][q >> 1];
          rv[q] = (l15 & 2) ? ((l15 & 1) ? sq[3] : sq[2]) : ((l15 & 1) ? sq[1] : sq[0]);
          SB();
        };
#pragma unroll
        for (int jj = 0; jj < 4; ++jj) {
          const bf16x8 wf = join8(WB[k][jj][0], WB[k][jj][1]);
          if (n == 19 && jj == 2) {
            asm volatile("s_nop 2");
            pin(cs[0], cs[1], cs[2], cs[3]);
            *(float*)(smem + L_CS + (wave * 64 + lane) * 4) = (g4 & 2) ? ((g4 & 1) ? cs[3][0] : cs[2][0]) : ((g4 & 1) ? cs[1][0] : cs[0][0]);
            bar(IC(11));
#pragma unroll
            for (int q = 0; q < 4; ++q) cp[q] = *(const float*)(smem + L_CS + ((wave ^ 1) * 64 + q * 16 + l15) * 4);
            ldxs();
            SB();
          }
          MFMAW<n == 0>(jj == 0 ? 4 : (jj == 2 && more ? c : -1), zv[0][jj], XG[j][k][0], wf);
          SB();
          if (n == 12 && jj < 2) sel(2 * jj); else nxt();
          MFMAW<n == 0>(-1, zv[1][jj], XG[j][k][1], wf);
          SB();
          nxt();
          if (k == 0) {
            MFMAW<n == 0>(-1, cs[jj], ones, wf);
            SB();
            if (n == 12 && jj < 2) sel(2 * jj + 1); else nxt();
            if (n == 12 && jj == 1) { nxt(); nxt(); dsw16<0>(rwa, rv); ++c; SB(); }
          }
#pragma unroll
          for (int q = 0; q < ns; ++q) {
            const bf16x8& xs = XG[sc + q][1][jj >> 1];
            MFMAW<n == 1>(-1, st[jj & 1][jj >> 1], xs, (jj & 1) ? xs : ones);
            SB();
            nxt();
          }
        }
      };
      bar(IC(1));
#pragma unroll
      for (int r = -4; r < 8; ++r) rd1(IC(0), r);
      kstep(IC(0)); AB(4); kstep(IC(1)); kstep(IC(2)); kstep(IC(3)); kstep(IC(4)); kstep(IC(5)); kstep(IC(6)); kstep(IC(7)); kstep(IC(8)); AB(5); kstep(IC(9));
      kstep(IC(10)); kstep(IC(11)); kstep(IC(12)); kstep(IC(13)); kstep(IC(14)); kstep(IC(15)); kstep(IC(16)); kstep(IC(17)); kstep(IC(18)); kstep(IC(19));
      __builtin_amdgcn_s_setprio(2);
      AB(6);
      float ct[4];
#pragma unroll
      for (int jj = 0; jj < 4; ++jj) ct[jj] = cs[jj][0] + cp[jj];
#pragma unroll
      for (int jj = 0; jj < 4; ++jj) {
        if (jj == 2) SB();
        if (!(jj & 1)) pin(zv[0][jj], zv[1][jj], zv[0][jj + 1], zv[1][jj + 1]);
#pragma unroll
        for (int i = 0; i < 2; ++i) zv[i][jj] = act(zv[i][jj], ct[jj], i);
      }
      if (isz2) {
        f32x4 pv[2], s1[2], s2[2];
#pragma unroll
        for (int i = 0; i < 2; ++i) {
#pragma unroll
          for (int e = 0; e < 4; ++e) {
            const float z0 = zv[i][0][e];
            pv[i][e] = __int_as_float(__builtin_amdgcn_update_dpp(0, __float_as_int(z0), 0x150, 0xF, 0xF, true));
          }
          const f32x4 a0 = zv[i][0] - pv[i], a1 = zv[i][1] - pv[i], a2 = zv[i][2] - pv[i], a3 = zv[i][3] - pv[i];
          s1[i] = (a0 + a1) + (a2 + a3);
          s2[i] = __builtin_elementwise_fma(a3, a3, __builtin_elementwise_fma(a2, a2, __builtin_elementwise_fma(a1, a1, a0 * a0)));
        }
        float r[8], q[4];
#define DA(d, s, ctl, bm) "v_add_f32_dpp " d ", " s ", " s " " ctl " row_mask:0xf bank_mask:" bm "\n\t"
        asm volatile("s_nop 1\n\t"
                     DA("%0", "%12", "row_ror:8", "0x3") DA("%1", "%13", "row_ror:8", "0x3") DA("%2", "%14", "row_ror:8", "0x3") DA("%3", "%15", "row_ror:8", "0x3")
                     DA("%4", "%16", "row_ror:8", "0x3") DA("%5", "%17", "row_ror:8", "0x3") DA("%6", "%18", "row_ror:8", "0x3") DA("%7", "%19", "row_ror:8", "0x3")
                     DA("%0", "%20", "row_ror:8", "0xc") DA("%1", "%21", "row_ror:8", "0xc") DA("%2", "%22", "row_ror:8", "0xc") DA("%3", "%23", "row_ror:8", "0xc")
                     DA("%4", "%24", "row_ror:8", "0xc") DA("%5", "%25", "row_ror:8", "0xc") DA("%6", "%26", "row_ror:8", "0xc") DA("%7", "%27", "row_ror:8", "0xc")
                     DA("%8", "%0", "row_half_mirror", "0x5") DA("%9", "%1", "row_half_mirror", "0x5") DA("%10", "%2", "row_half_mirror", "0x5") DA("%11", "%3", "row_half_mirror", "0x5")
                     DA("%8", "%4", "row_half_mirror", "0xa") DA("%9", "%5", "row_half_mirror", "0xa") DA("%10", "%6", "row_half_mirror", "0xa") DA("%11", "%7", "row_half_mirror", "0xa")
                     DA("%8", "%8", "quad_perm:[1,0,3,2]", "0xf") DA("%9", "%9", "quad_perm:[1,0,3,2]", "0xf") DA("%10", "%10", "quad_perm:[1,0,3,2]", "0xf") DA("%11", "%11", "quad_perm:[1,0,3,2]", "0xf")
                     DA("%8", "%8", "quad_perm:[2,3,0,1]", "0xf") DA("%9", "%9", "quad_perm:[2,3,0,1]", "0xf") DA("%10", "%10", "quad_perm:[2,3,0,1]", "0xf") DA("%11", "%11", "quad_perm:[2,3,0,1]", "0xf")
                     : "=&v"(r[0]), "=&v"(r[1]), "=&v"(r[2]), "=&v"(r[3]), "=&v"(r[4]), "=&v"(r[5]), "=&v"(r[6]), "=&v"(r[7]),
                       "=&v"(q[0]), "=&v"(q[1]), "=&v"(q[2]), "=&v"(q[3])
                     : "v"(s1[0][0]), "v"(s2[0][0]), "v"(s1[0][1]), "v"(s2[0][1]), "v"(s1[0][2]), "v"(s2[0][2]), "v"(s1[0][3]), "v"(s2[0][3]),
                       "v"(s1[1][0]), "v"(s2[1][0]), "v"(s1[1][1]), "v"(s2[1][1]), "v"(s1[1][2]), "v"(s2[1][2]), "v"(s1[1][3]), "v"(s2[1][3]));
#undef DA
        const f32x4 p4 = (lane & 8) ? pv[1] : pv[0];
        const float pa = (lane & 4) ? p4[2] : p4[0], pb = (lane & 4) ? p4[3] : p4[1];
        const float pp = (lane & 2) ? pb : pa, S1 = (lane & 2) ? q[2] : q[0], S2 = (lane & 2) ? q[3] : q[1];
        const float dl = S1 * (1.0f / 64.0f);
        st16((unsigned)((lane >> 1) * 16), ws + WS_ST + (size_t)((rb * 12 + pr * 3 + odd * 2 + wl) * 1024 + wm * 512),
             (u32x4){__float_as_uint(pp + dl), __float_as_uint(fmaxf(S2 - S1 * dl, 0.f)), (unsigned)tag, (unsigned)(tag >> 32)});
      } else {
#pragma unroll
        for (int jj = 0; jj < 4; ++jj) {
          u32x4 o = pack8(zv[0][jj], zv[1][jj]);
          if (jj >= 2) z1put(jj * 16 - 32, o);
          else st16(lin, ws + WS_ZX + (size_t)((rb * 4 + pr) * 4 + wm * 2 + jj) * 1024, o);
        }
      }
      SB();
      AB(7);
#pragma unroll
      for (int i = 0; i < 8; ++i) ldA1(IC(0), i);
#pragma unroll
      for (int i = 0; i < 8; ++i) ldA1(IC(1), i);
      AB(8);
      u32x4 sg[6];
      bool bad;
      do {
#pragma unroll
        for (int c = 0; c < 6; ++c)
          asm volatile("global_load_dwordx4 %0, %1, %2 offset:%3 sc1" : "=&v"(sg[c]) : "v"((unsigned)((lane >> 5) * 6144 + (lane & 31) * 16)),
                       "s"(ws + WS_ST + (size_t)rb * 12288 + wm * 512 + 2048), "i"(c * 1024 - 2048) : "memory");
        ldef();
        wait_vm<0>(sg[0], sg[1], sg[2], sg[3], sg[4], sg[5], ef);
        unsigned dl = ef[0] ^ (unsigned)tag, dh = ef[1] ^ (unsigned)(tag >> 32);
#pragma unroll
        for (int c = 0; c < 6; ++c) { dl |= sg[c][2] ^ (unsigned)tag; dh |= sg[c][3] ^ (unsigned)(tag >> 32); }
        bad = __builtin_amdgcn_ballot_w64((dl | dh) != 0) != 0;
        --tmo;
      } while (bad && tmo > 0);
      AB(9);
      if (isz2) {
        auto both = [&](float v) {
          const auto c = __builtin_amdgcn_permlane32_swap(__float_as_uint(v), __float_as_uint(v), false, false);
          return __uint_as_float(c[0]) + __uint_as_float(c[1]);
        };
        float sm = 0.f, m2 = 0.f, qq = 0.f;
#pragma unroll
        for (int c = 0; c < 6; ++c) { sm += __uint_as_float(sg[c][0]); m2 += __uint_as_float(sg[c][1]); }
        const float mean = both(sm) * (1.0f / 12.0f);
#pragma unroll
        for (int c = 0; c < 6; ++c) { const float a = __uint_as_float(sg[c][0]) - mean; qq = __builtin_fmaf(a, a, qq); }
        const float vbf = rbf(both(__builtin_fmaf(qq, 64.0f, m2)) * (1.0f / 768.0f));
        const float rz = __builtin_amdgcn_rcpf(rbf(__builtin_amdgcn_sqrtf(rbf(vbf + 1e-5f))));
        char* zs = smem + L_ZS + wave * 256;
        *(float*)(zs + lane * 4) = lane < 32 ? rbf(mean) : rz;
        f32x4 zm[2], zr[2];
#pragma unroll
        for (int i = 0; i < 2; ++i) {
          zm[i] = *(const f32x4*)(zs + (g4 * 8 + i * 4) * 4);
          zr[i] = *(const f32x4*)(zs + 128 + (g4 * 8 + i * 4) * 4);
        }
#pragma unroll
        for (int jj = 0; jj < 4; ++jj) {
          u32x4 o = pack8((zv[0][jj] - zm[0]) * zr[0], (zv[1][jj] - zm[1]) * zr[1]);
          o[3] = nohole(o[3]);
          st16(lin, ws + WS_Z2 + (size_t)((rb * 2 + wm) * 48 + pr * 12 + odd * 8 + wl * 4 + jj) * 1024, o);
        }
      } else sttag(ws + WS_XF + (rb * 4 + pr) * 16 + wm * 8);
      __builtin_amdgcn_s_setprio(0);
      SB();
      AB(10);
    }
    const char* Bs = ws + WS_Z2 + (size_t)(wave * 16 * 48 + cb * 6) * 1024 + 2048;
    bf16x8 Bf[3][6];
    auto ldB1 = [&](int s, int j) {
      asm volatile("global_load_dwordx4 %0, %1, %2 offset:%3 " HSC : "=&v"(Bf[s % 3][j]) : "v"(lin + s * 49152), "s"(Bs), "i"(j * 1024 - 2048) : "memory");
    };
    flag_wait_asm(flg + 16, 4u);
#pragma unroll
    for (int i = 0; i < 8; ++i) ldA1(IC(2), i);
    AB(11);
    f32x4 acc[4][6];
    unsigned ara[2];
#pragma unroll
    for (int ks = 0; ks < 2; ++ks) ara[ks] = ringl + l15 * 128 + (((ks * 4 + g4) ^ (l15 & 7)) << 4);
    u32x4 af[4];
    auto rdA = [&](int s, int i) {
      asm volatile("ds_read_b128 %0, %1 offset:%2" : "=&v"(af[i]) : "v"(ara[s & 1]), "i"(((s >> 1) % 3) * 8192 + i * 2048) : "memory");
    };
    asm volatile("s_waitcnt vmcnt(8)" ::: "memory");
    signal1(flg + 28);
    flag_wait_asm(flg + 28, 4u);
#pragma unroll
    for (int i = 0; i < 4; ++i) rdA(0, i);
    AB(12);
#pragma unroll
    for (int j = 0; j < 6; ++j) ldB1(0, j);
#pragma unroll
    for (int j = 0; j < 6; ++j) ldB1(1, j);
#pragma unroll
    for (int j = 0; j < 6; ++j) ldB1(2, j);
    asm volatile("s_waitcnt lgkmcnt(3)" : "+v"(af[0]) :: "memory");
    SB();
    unsigned bsv;
    const unsigned xa = ringl + lane * 16;
    auto putp = [&](int i, int j) { asm volatile("ds_write_b128 %0, %1 offset:%2" :: "v"(xa), "v"(acc[i][j]), "i"(i * 6144 + j * 1024) : "memory"); };
    auto ldbs = [&]() { asm volatile("global_load_ushort %0, %1, %2" : "=&v"(bsv) : "v"((unsigned)(l15 * 2)), "s"(b_sp + t0 + wave * 16) : "memory"); };
    auto step = [&](auto sc) {
      constexpr int s = decltype(sc)::value, sl = s % 3;
      constexpr int NV = s < 2 ? 12 : (s < 12 ? 20 : (s == 12 ? 12 : 13 - 6 * (s - 13)));
      constexpr int DA = ((s & 1) && (s + 5) / 2 < 8) ? (s + 5) / 2 : -1;
      asm volatile("s_waitcnt vmcnt(%0) lgkmcnt(2)" :: "n"(NV) : "memory");
      pin(Bf[sl][0], Bf[sl][1], Bf[sl][2], Bf[sl][3], Bf[sl][4], Bf[sl][5], af[1]);
      {
        auto d3 = [&](int j) { return __builtin_bit_cast(u32x4, Bf[sl][j])[3]; };
        auto bad = [&]() { return __builtin_amdgcn_ballot_w64(umx(umx(umx(d3(0), d3(1)), d3(2)), umx(umx(d3(3), d3(4)), d3(5))) == HOLE) != 0; };
        if (__builtin_expect(bad(), 0)) {
          do {
            --tmo;
            asm volatile("s_waitcnt vmcnt(0)\n\ts_sleep 2" ::: "memory");
#pragma unroll
            for (int q = 0; q < 3; ++q)
              if (s + q < 16) reld6(Bf[(s + q) % 3], opq(lin) + (s + q) * 49152, Bs);
            wait_vm<0>(Bf[sl][0], Bf[sl][1], Bf[sl][2], Bf[sl][3], Bf[sl][4], Bf[sl][5]);
          } while (bad() && tmo > 0);
        }
      }
      if constexpr (s == SGS) signal1(flg + 44);
      SB();
#pragma unroll
      for (int i = 0; i < 4; ++i) {
        if (i == 1) wait_lg<(s < 15 ? 1 : 0)>(af[2], af[3]);
        if (i == 3 && s < 15) wait_lg<2>(af[0]);
#pragma unroll
        for (int j = 0; j < 6; ++j) {
          if constexpr (s == 0) MFMA0(acc[i][j], Bf[sl][j], af[i]); else MFMA(acc[i][j], Bf[sl][j], af[i]);
          SB();
          if constexpr (DA >= 0) { if ((i == 1 || i == 2) && j < 4) ldA1(IC(DA < 0 ? 0 : DA), (i - 1) * 4 + j); }
          if constexpr (s + 3 < 16) { if (i == 3) ldB1(s + 3, j); }
          if constexpr (s == 15) {
            if (i > 0 && j > 1) putp(i - 1, j - 2);
          }
          SB();
        }
        if constexpr (s == 15) {
          if (i > 0) {
            putp(i - 1, 4); putp(i - 1, 5);
            signal1(flg + ((wave + i) & 3) * 4);
          }
        }
        if constexpr (s < 15) rdA(s + 1, i);
        SB();
      }
      if constexpr (s == 12) ldbs();
      SB();
    };
    step(IC(0)); AB(13); step(IC(1)); step(IC(2)); step(IC(3)); AB(14); step(IC(4)); step(IC(5)); step(IC(6)); step(IC(7));
    step(IC(8)); step(IC(9)); step(IC(10)); step(IC(11)); step(IC(12)); step(IC(13)); step(IC(14)); step(IC(15)); AB(15);
    ldW1();
    flag_wait_asm(flg + 56, 5u);
    s16x4 z1r[6];
    {
      const int fr = 4 * g4 + (l15 >> 2), p = lane & 3;
      const unsigned za = lb + L_Z1 + fr * 128 + (((wave * 2 + (p >> 1)) ^ (fr & 7)) << 4) + (p & 1) * 8;
      z1r[0] = tr_asm<0>(za); z1r[1] = tr_asm<2048>(za); z1r[2] = tr_asm<4096>(za);
      z1r[3] = tr_asm<6144>(za); z1r[4] = tr_asm<8192>(za); z1r[5] = tr_asm<10240>(za);
    }
    flag_wait_asm(flg + wave * 4, 3u);
    asm volatile("" : "+v"(z1r[0]), "+v"(z1r[1]), "+v"(z1r[2]), "+v"(z1r[3]), "+v"(z1r[4]), "+v"(z1r[5]));
    AB(16);
    f32x4 own[6];
    {
      f32x4 pr_[3][6];
#pragma unroll
      for (int q = 0; q < 3; ++q) {
        const char* src = smem + M_RING + ((wave + 3 - q) & 3) * 24576;
#pragma unroll
        for (int j = 0; j < 6; ++j) pr_[q][j] = *(const f32x4*)(src + q * 6144 + j * 1024 + lane * 16);
      }
      asm volatile("s_nop 7" : "+v"(acc[3][0]), "+v"(acc[3][1]), "+v"(acc[3][2]), "+v"(acc[3][3]), "+v"(acc[3][4]), "+v"(acc[3][5]));
#pragma unroll
      for (int j = 0; j < 6; ++j) own[j] = (acc[3][j] + pr_[0][j]) + (pr_[1][j] + pr_[2][j]);
    }
    asm volatile("s_waitcnt vmcnt(%1)" : "+v"(bsv) : "i"(NW1 + 1) : "memory");
    SB();
    {
      const float bs = lo16(bsv);
      unsigned pk[6][2];
#pragma unroll
      for (int j = 0; j < 6; ++j) {
        const u32x2 z = __builtin_bit_cast(u32x2, z1r[j]);
#pragma unroll
        for (int h = 0; h < 2; ++h) {
          const unsigned r0 = pack2(own[j][2 * h], own[j][2 * h + 1]);
          const unsigned g = pack2(lo16(r0) + bs, hi16(r0) + bs);
          pk[j][h] = pack2(lo16(z[h]) * lo16(g), hi16(z[h]) * hi16(g));
        }
      }
      const unsigned eo = (unsigned)((l15 + 16 * (2 * (g4 & 1) + (g4 >> 1))) * 16);
#pragma unroll
      for (int q = 0; q < 3; ++q) {
        const auto s0 = __builtin_amdgcn_permlane16_swap(pk[2 * q][0], pk[2 * q + 1][0], false, false);
        const auto s1 = __builtin_amdgcn_permlane16_swap(pk[2 * q][1], pk[2 * q + 1][1], false, false);
        st16(eo, hf + (size_t)((rb * 4 + wave) * 24 + cb * 3 + q) * 1024, (u32x4){s0[0], s1[0], s0[1], nohole(s1[1])});
      }
    }
    AB(17);
    drain1(3);
  }
  unsigned hoff[4];
#pragma unroll
  for (int it = 0; it < 4; ++it) hoff[it] = (unsigned)(((rb * 4 + ((mt + 1 + it) & 3)) * 24 + 3 * wave) * 1024 + lane * 16);
  u32x4 Hq[4][3];
  auto ldh1 = [&](int it, int k) {
    asm volatile("global_load_dwordx4 %0, %1, %2 offset:%3 " HSC : "=&v"(Hq[it][k]) : "v"(hoff[it]), "s"(hf), "i"(k * 1024) : "memory");
  };
  auto ldh0 = [&](int it) {
#pragma unroll
    for (int k = 0; k < 3; ++k) ldh1(it, k);
  };
  f32x2 xf[3][2], sv[3][2];
  auto ymath = [&]() {
    char* yi = smem + P3_Y + wave * 1536;
#pragma unroll
    for (int n = 0; n < 2; ++n) *(u32x4*)(yi + (n ? 64 + (lane & 31) : lane) * 16) = yv[n];
    const unsigned ya = lds_addr(yi) + (4 * g4 + (l15 >> 2)) * 32 + 8 * (lane & 3);
    const float sm = *(const float*)(smem + L_XM + (mt * 16 + l15) * 4), sr = *(const float*)(smem + L_XR + (mt * 16 + l15) * 4);
    s16x4 yr[3] = {tr_asm<0>(ya), tr_asm<512>(ya), tr_asm<1024>(ya)};
    asm volatile("s_waitcnt lgkmcnt(0)" : "+v"(yr[0]), "+v"(yr[1]), "+v"(yr[2]) :: "memory");
    const f32x2 mean = {sm, sm}, rd = {sr, sr};
#pragma unroll
    for (int q = 0; q < 3; ++q) {
      const u32x2 yw = __builtin_bit_cast(u32x2, yr[q]);
#pragma unroll
      for (int h = 0; h < 2; ++h) {
        xf[q][h] = unpk(xv[q][h]);
        sv[q][h] = ((xf[q][h] - mean) * rd) * unpk(yw[h]);
        asm volatile("" : "+v"(sv[q][h]), "+v"(xf[q][h]));
      }
    }
    SB();
  };
  u32x2 yf;
  const unsigned yfo = (unsigned)((lane < 32 ? 32 * kh + lane : 64 + (lane & 15)) * 8);
#pragma unroll
  for (int n = NW1; n < 18; ++n) ldP1(n);
  ldtag(yf, yfo, ws + WS_YF + d0 * 8);
  SB();
  asm volatile("s_waitcnt vmcnt(%0)" :: "i"(19 - NW1) : "memory");
  waittag(ef, 0u, ws + WS_LF + (rb * 8 + wave) * 8);
  AB(18);
  signal1(flg + 48 + 4 * kh);
  flag_wait_asm(flg + 48, 4u);
  AB(19);
  __builtin_amdgcn_s_setprio(3);
  ldh0(0);
  __builtin_amdgcn_s_setprio(2);
  ldh0(1);
  __builtin_amdgcn_s_setprio(0);
  SB();
  flag_wait_asm(flg + 40, kh ? 4u : 0u);
  char* const px = smem + (kh ? P3_C + mt * 18432 : P3_G + mt * 17408) + lane * 16;
  f32x4 ac3[5], o[3];
  auto ldy = [&]() {
#pragma unroll
    for (int n = 0; n < 2; ++n) asm volatile("global_load_dwordx4 %0, %1, %2 sc1" : "=&v"(yv[n]) : "v"(yoff(n)), "s"(yb) : "memory");
  };
  auto relh = [&](int it) {
    asm volatile("s_sleep 2\n\tglobal_load_dwordx4 %0, %3, %4 sc1\n\tglobal_load_dwordx4 %1, %3, %4 offset:1024 sc1\n\t"
                 "global_load_dwordx4 %2, %3, %4 offset:2048 sc1\n\ts_waitcnt vmcnt(0)"
                 : "+v"(Hq[it][0]), "+v"(Hq[it][1]), "+v"(Hq[it][2]) : "v"(hoff[it]), "s"(hf) : "memory");
  };
  auto comp = [&](auto itc) {
    constexpr int it = decltype(itc)::value;
    constexpr int NW = 3 * ((it + 2 < 4 ? it + 2 : 4) - it - 1) + (it >= 2 ? 2 : 0);
    wait_vm<NW>(Hq[it][0], Hq[it][1], Hq[it][2]);
    if constexpr (it == 0) pin(Wq[0], Wq[1], Wq[2], Wq[3], Wq[4], Wq[5], Wq[6], Wq[7], Wq[8], Wq[9], Wq[10], Wq[11], Wq[12], Wq[13], Wq[14], xv[0], xv[1], xv[2], yf);
    auto bad = [&]() { return __builtin_amdgcn_ballot_w64(umx(umx(Hq[it][0][3], Hq[it][1][3]), Hq[it][2][3]) == HOLE) != 0; };
    if (__builtin_expect(bad(), 0)) {
      do {
        --tmo;
        relh(it);
      } while (bad() && tmo > 0);
    }
    SB();
#pragma unroll
    for (int k = 0; k < 3; ++k) {
#pragma unroll
      for (int j = 0; j < 5; ++j) {
        ac3[j] = __builtin_amdgcn_mfma_f32_16x16x32_bf16(Wq[k * 5 + j], __builtin_bit_cast(bf16x8, Hq[it][k]), k ? ac3[j] : (f32x4){0.f, 0.f, 0.f, 0.f}, 0, 0, 0);
        if constexpr (it + 2 < 4) { if (k == 0 && j < 3) { SB(); ldh1(it + 2 < 4 ? it + 2 : 0, j); SB(); } }
        if constexpr (it == 1) {
          if (k == 1 && j == 0) {
            SB();
            ldy();
            SB();
          }
        }
      }
    }
    SB();
    if constexpr (it < 3) {
#pragma unroll
      for (int j = 0; j < 5; ++j) *(f32x4*)(px + (it * 5 + j) * 1024) = ac3[j];
      signal1(flg + 64 + kh * 16 + ((mt + 1 + it) & 3) * 4);
    }
    SB();
  };
  auto gather = [&](auto cc) {
    constexpr int cv = decltype(cc)::value;
    flag_wait_asm(flg + 64 + cv * 16 + mt * 4, cv ? 5u : 3u);
#pragma unroll
    for (int it = 0; it < 3 + cv; ++it) {
      const int ms = (mt - 1 - it) & 3;
      const char* ps = smem + ((it < 3 ? cv : !kh) ? P3_C + ms * 18432 : P3_G + ms * 17408) + lane * 16;
      const char* p0 = ps + (it < 3 ? it * 5 + 2 * kh : 15) * 1024;
      const f32x4 a0 = *(const f32x4*)p0, a1 = *(const f32x4*)(p0 + 1024);
      if (!cv && it == 0) { o[0] = a0; o[1] = a1; } else { o[0] += a0; o[1] += a1; }
    }
    if (!kh) {
#pragma unroll
      for (int it = 0; it < 3 + cv; ++it) {
        const int ms = (mt - 1 - it) & 3;
        const char* ps = smem + ((it < 3 ? cv : 1) ? P3_C + ms * 18432 : P3_G + ms * 17408) + lane * 16;
        o[2] += *(const f32x4*)(ps + (it < 3 ? it * 5 + 4 : 17) * 1024);
      }
    }
    SB();
  };
  o[2] = (f32x4){0.f, 0.f, 0.f, 0.f};
  comp(IC(0)); AB(20); comp(IC(1)); comp(IC(2));
  AB(21);
  gather(IC(0));
  comp(IC(3));
  AB(22);
  {
    *(f32x4*)(px + 15 * 1024) = kh ? ac3[0] : ac3[2];
    *(f32x4*)(px + 16 * 1024) = kh ? ac3[1] : ac3[3];
    if (kh) *(f32x4*)(px + 17 * 1024) = ac3[4];
    signal1(flg + 80 + mt * 4);
    SB();
    o[0] += kh ? ac3[2] : ac3[0]; o[1] += kh ? ac3[3] : ac3[1]; o[2] += ac3[4];
    asm volatile("s_waitcnt vmcnt(0)" : "+v"(yv[0]), "+v"(yv[1]) :: "memory");
    if (__builtin_expect(notag(yf), 0)) {
      waittag(yf, yfo, ws + WS_YF + d0 * 8);
      ldy();
      wait_vm<0>(yv[0], yv[1]);
    }
    ymath();
    AB(23);
    gather(IC(1));
    auto fin = [&](const f32x4& o4, int q, unsigned (&pk)[2]) {
#pragma unroll
      for (int h = 0; h < 2; ++h) {
        const f32x2 o = {o4[2 * h], o4[2 * h + 1]};
        const f32x2 a = o * (f32x2){-1.4426950f, -1.4426950f};
        const f32x2 e = (f32x2){__builtin_amdgcn_exp2f(a[0]), __builtin_amdgcn_exp2f(a[1])} + (f32x2){1.0f, 1.0f};
        const f32x2 r = {__builtin_amdgcn_rcpf(e[0]), __builtin_amdgcn_rcpf(e[1])};
        pk[h] = pk2(__builtin_elementwise_fma(o * r, sv[q][h], xf[q][h]));
      }
    };
    unsigned pa[2], pb[2];
    fin(o[0], 0, pa);
    fin(o[1], 1, pb);
    const auto s0 = __builtin_amdgcn_permlane16_swap(pa[0], pb[0], false, false);
    const auto s1 = __builtin_amdgcn_permlane16_swap(pa[1], pb[1], false, false);
    *(u32x4*)(out + xo + 32 * kh + 16 * (g4 & 1) + 8 * (g4 >> 1)) = (u32x4){s0[0], s1[0], s0[1], s1[1]};
    if (!kh) {
      fin(o[2], 2, pa);
      *(u32x2*)(out + xo + 64 + 4 * g4) = (u32x2){pa[0], pa[1]};
    }
  }
  if (b == 0 && tid == 0) *(u64*)(ws + WS_EP) = tag;
  AB(24);
  ab_[31] = ab_[0] + 100 * ((1 << 16) - tmo);
  AB_OUT();
}

extern "C" __global__ void k_end() {}

__global__ __launch_bounds__(256) void k_dly() {
  const unsigned long long t0 = __builtin_amdgcn_s_memrealtime();
  __shared__ unsigned sm[3][256];
  const int b = threadIdx.x;
  const unsigned ref = g_ab[0][0][0];
  int mn = 1 << 30, mx = -(1 << 30), su = 0;
  for (int w = 0; w < 8; ++w) {
    const int a = (int)(g_ab[b][w][0] - ref), e = (int)(g_ab[b][w][HQ] - ref);
    mn = a < mn ? a : mn; mx = e > mx ? e : mx;
    if ((HM == 'g' && w < 4) || (HM == 'c' && w >= 4)) su += e;
  }
  sm[0][b] = mn; sm[1][b] = mx; sm[2][b] = su;
  __syncthreads();
  if (b == 0) {
    int MN = 1 << 30, MX = -(1 << 30), SU = 0;
    for (int i = 0; i < 256; ++i) { MN = (int)sm[0][i] < MN ? (int)sm[0][i] : MN; MX = (int)sm[1][i] > MX ? (int)sm[1][i] : MX; SU += (int)sm[2][i]; }
    int q = HM == 'z' ? 0 : (HM == 'x' ? MX - MN : SU / 1024 - MN);
    q = q < 0 ? 0 : (q > 5000 ? 5000 : q);
    const unsigned long long te = t0 + 2000 + 10 * q;
    while (__builtin_amdgcn_s_memrealtime() < te) __builtin_amdgcn_s_sleep(1);
  }
}
extern "C" void kernel_launch(void* const* d_in, const int* in_sizes, int n_in,
                              void* d_out, int out_size, void* d_ws, size_t ws_size,
                              hipStream_t stream) {
  (void)hipFuncSetAttribute((const void*)k_f, hipFuncAttributeMaxDynamicSharedMemorySize, M_LDS);
  k_f<<<256, 512, M_LDS, stream>>>((const u16*)d_in[0], (const u16*)d_in[1], (const u16*)d_in[2], (const u16*)d_in[3], (const u16*)d_in[4],
                                   (const u16*)d_in[5], (const u16*)d_in[7], (const u16*)d_in[8], (const u16*)d_in[9], (const u16*)d_in[10],
                                   (u16*)d_out, (char*)d_ws);
  k_dly<<<1, 256, 0, stream>>>();
}
```

```cpp
#include <hip/hip_runtime.h>
#include <hip/hip_bf16.h>
#include <stdint.h>
#include <type_traits>

typedef unsigned short u16;
typedef unsigned long long u64;
typedef __attribute__((ext_vector_type(8))) short bf16x8;
typedef __attribute__((ext_vector_type(4))) short s16x4;
typedef __attribute__((ext_vector_type(4))) float f32x4;
typedef __attribute__((ext_vector_type(2))) float f32x2;
typedef __attribute__((ext_vector_type(4))) unsigned int u32x4;
typedef __attribute__((ext_vector_type(2))) unsigned int u32x2;
typedef __attribute__((ext_vector_type(2))) __bf16 bf16x2n;

#define LDS_AS __attribute__((address_space(3)))
#define IC(n) std::integral_constant<int, (n)>{}
#define SB() __builtin_amdgcn_sched_barrier(0)

#define AB_DECL do {} while (0)
#define AB(i) do {} while (0)
#define AB_OUT() do {} while (0)
#define CB(i) do {} while (0)
#define SS(i) do {} while (0)
constexpr int S_ = 2048, D_ = 640, FH_ = 768, F2_ = 1536;
constexpr unsigned HOLE = 0xFFFFFFFFu;

constexpr size_t WS_EP = 0;
constexpr size_t WS_EF = 4096;
constexpr size_t WS_LF = 6144;
constexpr size_t WS_YF = 8192;
constexpr size_t WS_XF = 16384;
constexpr size_t WS_ST = 32768;
constexpr size_t WS_Z2 = WS_ST + 32 * 12 * 1024;
constexpr size_t WS_H = WS_Z2 + 64 * 48 * 1024;
constexpr size_t WS_ZX = WS_H + 128 * 24 * 1024;
constexpr size_t WS_KV = WS_ZX + 32 * 4 * 4096;
constexpr size_t WS_Y = WS_KV + D_ * S_ * 2;
constexpr size_t WS_ZH = WS_Y + D_ * S_ * 2 + FH_ * D_ * 2;
constexpr size_t WS_WO = WS_Y + D_ * S_ * 2;

__device__ __forceinline__ float bf2f(u16 v) { return __uint_as_float(((unsigned)v) << 16); }
__device__ __forceinline__ unsigned pack2(float lo, float hi) {
  f32x2 v = {lo, hi};
  bf16x2n r = __builtin_convertvector(v, bf16x2n);
  return __builtin_bit_cast(unsigned, r);
}
__device__ __forceinline__ float nv(float v) { asm("" : "+v"(v)); return v; }
__device__ __forceinline__ float fma1(float a, float b, float c) { float r; asm("v_fma_f32 %0, %1, %2, %3" : "=v"(r) : "v"(a), "v"(b), "v"(c)); return r; }
__device__ __forceinline__ float add1(float a, float b) { float r; asm("v_add_f32 %0, %1, %2" : "=v"(r) : "v"(a), "v"(b)); return r; }
__device__ __forceinline__ unsigned pack2s(float lo, float hi) { return pack2(nv(lo), nv(hi)); }
__device__ __forceinline__ float lo16(unsigned w) { return __uint_as_float(w << 16); }
__device__ __forceinline__ float hi16(unsigned w) { return __uint_as_float(w & 0xFFFF0000u); }
__device__ __forceinline__ float rbf(float f) { return lo16(pack2(f, f)); }
__device__ __forceinline__ f32x2 unpk(unsigned w) { f32x2 r = {lo16(w), hi16(w)}; return r; }
__device__ __forceinline__ unsigned pk2(f32x2 v) { return pack2(v[0], v[1]); }
__device__ __forceinline__ unsigned umx(unsigned a, unsigned b) { return a > b ? a : b; }
__device__ __forceinline__ unsigned nohole(unsigned a) { return a < HOLE - 1 ? a : HOLE - 1; }
__device__ __forceinline__ f32x2 gelu2(f32x2 x) {
  const f32x2 c1 = {-2.3022082f, -2.3022082f}, c2 = {-0.10294324f, -0.10294324f};
  const f32x2 a = x * __builtin_elementwise_fma(x * x, c2, c1);
  f32x2 e = {__builtin_amdgcn_exp2f(a[0]), __builtin_amdgcn_exp2f(a[1])};
  e = e + (f32x2){1.0f, 1.0f};
  const f32x2 r = {__builtin_amdgcn_rcpf(e[0]), __builtin_amdgcn_rcpf(e[1])};
  return x * r;
}
__device__ __forceinline__ float gelu1(float x) {
  const float a = x * __builtin_fmaf(x * x, -0.10294324f, -2.3022082f);
  return x * __builtin_amdgcn_rcpf(__builtin_amdgcn_exp2f(a) + 1.0f);
}
template <int OFF>
__device__ __forceinline__ s16x4 tr_asm(unsigned a) {
  s16x4 r;
  asm volatile("ds_read_b64_tr_b16 %0, %1 offset:%2" : "=&v"(r) : "v"(a), "i"(OFF) : "memory");
  return r;
}
__device__ __forceinline__ bf16x8 join8(s16x4 lo, s16x4 hi) {
  bf16x8 r;
  r[0] = lo[0]; r[1] = lo[1]; r[2] = lo[2]; r[3] = lo[3];
  r[4] = hi[0]; r[5] = hi[1]; r[6] = hi[2]; r[7] = hi[3];
  return r;
}
__device__ __forceinline__ unsigned lds_addr(const void* p) { return (unsigned)(uintptr_t)(LDS_AS const char*)p; }
#define WAIT_LGKM0() asm volatile("s_waitcnt lgkmcnt(0)" ::: "memory")
__device__ __forceinline__ void flag_wait_asm(unsigned faddr, unsigned target) {
  unsigned v, sv;
  asm volatile(".Lfw_%=:\n\tds_read_b32 %0, %2\n\ts_waitcnt lgkmcnt(0)\n\tv_readfirstlane_b32 %1, %0\n\ts_nop 0\n\t"
               "s_cmp_ge_u32 %1, %3\n\ts_cbranch_scc1 .Lfd_%=\n\ts_sleep 1\n\ts_branch .Lfw_%=\n\t.Lfd_%=:"
               : "=&v"(v), "=&s"(sv) : "v"(faddr), "s"(target) : "memory", "scc");
}
__device__ __forceinline__ void flag_wait4(unsigned faddr) {
  u32x4 v;
  asm volatile("ds_read_b128 %0, %1\n\ts_waitcnt lgkmcnt(0)" : "=&v"(v) : "v"(faddr) : "memory");
  while (__builtin_amdgcn_readfirstlane(v[0] & v[1] & v[2] & v[3]) == 0) asm volatile("s_sleep 1\n\tds_read_b128 %0, %1\n\ts_waitcnt lgkmcnt(0)" : "=&v"(v) : "v"(faddr) : "memory");
}
__device__ __forceinline__ void signal1(unsigned a) {
  const unsigned one = 1u;
  asm volatile("s_mov_b64 exec, 1\n\tds_add_u32 %0, %1\n\ts_mov_b64 exec, -1" :: "v"(a), "v"(one) : "memory");
}
__device__ __forceinline__ void dma16(unsigned vo, const void* sb, unsigned lds) {
  asm volatile("s_mov_b32 m0, %2\n\ts_nop 0\n\tglobal_load_lds_dwordx4 %0, %1" :: "v"(vo), "s"(sb), "s"(lds) : "memory");
}
template <int LO> __device__ __forceinline__ void dma32s(unsigned vo0, unsigned vo1, const void* sb, unsigned go, unsigned lds) {
  asm volatile("s_add_u32 m0, %5, %6\n\ts_add_u32 vcc_lo, %2, %4\n\ts_addc_u32 vcc_hi, %3, 0\n\t"
               "global_load_lds_dwordx4 %0, vcc\n\tglobal_load_lds_dwordx4 %1, vcc offset:1024"
               :: "v"(vo0), "v"(vo1), "s"((unsigned)(uintptr_t)sb), "s"((unsigned)((uintptr_t)sb >> 32)), "s"(go), "s"(lds), "i"(LO) : "memory", "scc", "vcc");
}
template <int LO, int GO, int WRAP, int LIM> __device__ __forceinline__ void dma32r(unsigned vo0, unsigned vo1, const void* sb, unsigned lds, int rot) {
  const char* s2 = (const char*)sb - WRAP;
  asm volatile("s_add_u32 m0, %5, %6\n\ts_cmp_ge_u32 %7, %8\n\ts_cselect_b64 vcc, %3, %2\n\ts_add_u32 vcc_lo, vcc_lo, %4\n\ts_addc_u32 vcc_hi, vcc_hi, 0\n\t"
               "global_load_lds_dwordx4 %0, vcc\n\tglobal_load_lds_dwordx4 %1, vcc offset:1024"
               :: "v"(vo0), "v"(vo1), "s"(sb), "s"(s2), "i"(GO), "s"(lds), "i"(LO), "s"(rot), "i"(LIM) : "memory", "scc", "vcc");
}
__device__ __forceinline__ void dma16o(unsigned vo, const void* sb, int go, unsigned lds, int lo) {
  asm volatile("s_add_u32 m0, %3, %4\n\ts_nop 0\n\tglobal_load_lds_dwordx4 %0, %1 offset:%2" :: "v"(vo), "s"(sb), "i"(go), "s"(lds), "i"(lo - go) : "memory", "scc");
}
__device__ __forceinline__ void st16(unsigned vo, const void* sb, u32x4 v) {
  asm volatile("global_store_dwordx4 %0, %1, %2 sc1\n\ts_nop 1" :: "v"(vo), "v"(v), "s"(sb) : "memory");
}
__device__ __forceinline__ void touch(unsigned vo, const void* sb, unsigned lds) {
  asm volatile("s_mov_b32 m0, %2\n\ts_nop 0\n\tglobal_load_lds_dword %0, %1" :: "v"(vo), "s"(sb), "s"(lds) : "memory");
}
__device__ __forceinline__ void st8(unsigned vo, const void* sb, u32x2 v) { asm volatile("global_store_dwordx2 %0, %1, %2 sc1" :: "v"(vo), "v"(v), "s"(sb) : "memory"); }
template <class A, class B> __device__ __forceinline__ void MFMA(f32x4& acc, const A& a, const B& b) {
  asm volatile("v_mfma_f32_16x16x32_bf16 %0, %1, %2, %0" : "+v"(acc) : "v"(a), "v"(b));
}
template <class A, class B> __device__ __forceinline__ void MFMA0(f32x4& acc, const A& a, const B& b) {
  asm volatile("v_mfma_f32_16x16x32_bf16 %0, %1, %2, 0" : "=&v"(acc) : "v"(a), "v"(b));
}
template <bool Z, class A, class B> __device__ __forceinline__ void MFMAW(int n, f32x4& acc, const A& a, const B& b) {
  if (n < 0) { if (Z) MFMA0(acc, a, b); else MFMA(acc, a, b); }
  else if (Z) asm volatile("s_waitcnt lgkmcnt(%3)\n\tv_mfma_f32_16x16x32_bf16 %0, %1, %2, 0" : "=&v"(acc) : "v"(a), "v"(b), "i"(n));
  else asm volatile("s_waitcnt lgkmcnt(%3)\n\tv_mfma_f32_16x16x32_bf16 %0, %1, %2, %0" : "+v"(acc) : "v"(a), "v"(b), "i"(n));
}
__device__ __forceinline__ float sel4(float a, float b, float c, float d, u64 m1, u64 m2) {
  float r, t;
  asm volatile("v_cndmask_b32_e64 %0, %2, %3, %6\n\tv_cndmask_b32_e64 %1, %4, %5, %6\n\tv_cndmask_b32_e64 %0, %0, %1, %7"
               : "=&v"(r), "=&v"(t) : "v"(a), "v"(b), "v"(c), "v"(d), "s"(m1), "s"(m2));
  return r;
}
template <class T> __device__ __forceinline__ void pin1(T& r) { asm volatile("" : "+v"(r)); }
template <class T> __device__ __forceinline__ T opq(T r) { asm volatile("" : "+v"(r)); return r; }
template <class... T> __device__ __forceinline__ void pin(T&... r) { (pin1(r), ...); }
template <int N, class... T> __device__ __forceinline__ void wait_vm(T&... r) { asm volatile("s_waitcnt vmcnt(%0)" :: "n"(N) : "memory"); (pin1(r), ...); }
template <int N, class... T> __device__ __forceinline__ void wait_lg(T&... r) { asm volatile("s_waitcnt lgkmcnt(%0)" :: "n"(N) : "memory"); (pin1(r), ...); }
template <int OFF, class T> __device__ __forceinline__ void ds16(T& d, unsigned a) {
  asm volatile("ds_read_b128 %0, %1 offset:%2" : "=&v"(d) : "v"(a), "i"(OFF) : "memory");
}
template <int OFF> __device__ __forceinline__ void dsw16(unsigned a, const f32x4& v) {
  asm volatile("ds_write_b128 %0, %1 offset:%2" :: "v"(a), "v"(v), "i"(OFF) : "memory");
}
__device__ __forceinline__ void dsw4(unsigned a, float v) { asm volatile("ds_write_b32 %0, %1" :: "v"(a), "v"(v) : "memory"); }
#define HSC ""
template <int OFF, class T> __device__ __forceinline__ void ld16h(T& d, unsigned vo, const void* sb) {
  asm volatile("global_load_dwordx4 %0, %1, %2 offset:%3 " HSC : "=&v"(d) : "v"(vo), "s"(sb), "i"(OFF) : "memory");
}
template <class T> __device__ __forceinline__ void reld6(T (&B)[6], unsigned vo, const void* sb) {
  asm volatile("global_load_dwordx4 %0, %6, %7 offset:-2048 sc1\n\tglobal_load_dwordx4 %1, %6, %7 offset:-1024 sc1\n\t"
               "global_load_dwordx4 %2, %6, %7 sc1\n\tglobal_load_dwordx4 %3, %6, %7 offset:1024 sc1\n\t"
               "global_load_dwordx4 %4, %6, %7 offset:2048 sc1\n\tglobal_load_dwordx4 %5, %6, %7 offset:3072 sc1"
               : "+v"(B[0]), "+v"(B[1]), "+v"(B[2]), "+v"(B[3]), "+v"(B[4]), "+v"(B[5]) : "v"(vo), "s"(sb) : "memory");
}

constexpr int L_FLAG = 0;
constexpr int L_DUM = 256;
constexpr int L_XM = 512;
constexpr int L_XR = 768;
constexpr int L_CS = 1024;
constexpr int L_ZS = 2048;
constexpr int L_CS2 = 3072;
constexpr int L_Z1 = 4096;
constexpr int L_RAW = L_Z1;
constexpr int L_SLOT = 16384;
constexpr int L_KV = 512, KV_P = 3088, L_WO = L_KV + 8 * KV_P + 16;
static_assert(L_WO % 16 == 0 && L_WO + 4 * 1024 <= L_SLOT + 2 * 8192, "staging");
constexpr int CV_C = 4608, CV_CS = 4160, CV_BUF = CV_C + 2 * CV_CS;
constexpr int M_CV = L_SLOT;
constexpr int M_RING = L_SLOT + 6 * 8192;
constexpr int P3_C = L_SLOT;
constexpr int P3_G = P3_C + 72 * 1024;
constexpr int P3_Y = L_Z1;
constexpr int M_LDS = 163840;
static_assert(3 * CV_BUF <= 5 * 8192 && P3_G + 68 * 1024 <= M_LDS && M_RING <= P3_G, "LDS map");

enum { UX, UA, UB, UV, US };
struct Unit { int kind, idx, need, free, iss, slot, grp; };
struct Plan { Unit u[48]; int first[14], nv[2][10]; bool ok; };
#define ISSUE {7, 3, 4, 4, 4, 4, 3, 3, 3, 3, 4, 6, 0}
#define NSTC {0, 0, 0, 0, 1, 1, 1, 0, 0, 0, 0, 0, 0}
#define GZ 6
#define PFM 0
#define PFA 0
#define PFB 0
#define PFC 0
#define HCP 9
#define FLAGB 8
constexpr int XNEED[10] = {1, 2, 2, 3, 3, 4, 4, 5, 5, 6};
constexpr int XSLOT[10] = {8, 11, 14, 6, 7, 17, 12, 8, 13, 11}, WSLOT[10] = {2, 4, 9, 0, 15, 2, 4, 6, 9, 12}, VSLOT[10] = {14, 17, 0, 1, 8, 11, 2, 3, 4, 5}, XW = 6;
constexpr int FORD[18] = {14, 17, 15, 16, 0, 1, 8, 11, 2, 3, 4, 5, 6, 9, 12, 7, 10, 13};
constexpr int NST[2][13] = {{0, 2}, NSTC};
constexpr Plan make_plan() {
  Plan p = {};
  int n = 0;
  for (int b = 1; b < 11; ++b) {
    for (int c = 0; c < 10; ++c) if (XNEED[c] == b) p.u[n++] = {UX, c, b, b + 1, 0, XSLOT[c], 1};
    p.u[n++] = {UA, b - 1, b < 8 ? b : 8, b < 7 ? b + 2 : 9, 0, WSLOT[b - 1], 1};
    p.u[n++] = {UB, b - 1, b < 8 ? b : 8, b < 7 ? b + 2 : 9, 0, WSLOT[b - 1] + 1, 1};
  }
  for (int c = 0, v = 0; c < 18; ++c) {
    if (FORD[c] == VSLOT[v < 10 ? v : 0]) p.u[n++] = {UV, v++, 99, 99, 0, FORD[c], 1};
    else p.u[n++] = {US, FORD[c] - 6, 99, 99, 0, FORD[c], 1};
  }
  const int is[13] = ISSUE;
  n = 0;
  for (int q = 0; q < 13; ++q) { p.first[q] = n; for (int i = 0; i < is[q]; ++i) { p.u[n].grp = q == 0 ? n & 1 : 1; p.u[n++].iss = q - 1; } }
  p.first[13] = n;
  p.ok = n == 48;
  int fr[18] = {1, 1, -1, -1, -1, -1, -1, -1, -1, -1, -1, -1, -1, -1, -1, -1, -1, -1};
  for (int i = 0; i < 48; ++i) {
    if (p.u[i].iss < fr[p.u[i].slot]) p.ok = false;
    fr[p.u[i].slot] = p.u[i].free;
  }
  int lasts = 0;
  for (int q = 0; q < 13; ++q) if (NST[1][q] && q - 1 > lasts) lasts = q - 1;
  for (int g = 0; g < 2; ++g)
    for (int b = 0; b < 10; ++b) {
      int cnt = 0, lastq = -9;
      for (int q = 0; q <= b; ++q) {
        for (int i = p.first[q]; i < p.first[q + 1]; ++i)
          if (p.u[i].grp == g) { if (p.u[i].need <= b) { cnt = 0; lastq = q - 1; } else cnt += 2; }
        cnt += NST[g][q] + (g == 1 && q > 0 ? PFM >> (q - 1) & 1 : 0) + (g == 1 && q - 1 == HCP ? 5 : 0);
        if (g == 1 && b == FLAGB && q - 1 == lasts) cnt = 0;
      }
      for (int i = p.first[b + 1]; i < 48; ++i) if (p.u[i].need <= b) p.ok = false;
      p.nv[g][b] = g == 0 && b >= GZ ? 0 : cnt;
    }
  if (FLAGB <= lasts || FLAGB < GZ) p.ok = false;
  return p;
}
constexpr Plan PL = make_plan();
constexpr int after_v() { int c = 0; for (int i = 0; i < PL.first[12]; ++i) c = PL.u[i].kind == UV ? 0 : c + 2; return c; }
static_assert(PL.ok, "phase A plan");

__device__ __forceinline__ void conv_core(unsigned buf, int lane, unsigned Dw, f32x4 (&ac)[8]) {
  const int l15 = lane & 15, g4 = lane >> 4;
  const int c = (l15 + 1) & 1;
  unsigned aa[4];
#pragma unroll
  for (int k = 0; k < 4; ++k) aa[k] = buf + CV_C + c * CV_CS + (2031 - l15 + 8 * g4 - c) * 2 - 512 - 1024 * k;
  unsigned bb = buf + 512 + 32 * l15 - 32 + 16 * g4;
  u32x2 A[2][8][2];
  u32x4 B[2][8];
  constexpr int TT[36] = {7, 6, 5, 4, 7, 6, 5, 4, 7, 6, 5, 3, 7, 6, 5, 4, 7, 6, 3, 2, 7, 6, 5, 4, 7, 3, 2, 1, 7, 6, 5, 4, 3, 2, 1, 0};
  constexpr int AA[36] = {0, 0, 0, 0, 1, 1, 1, 1, 2, 2, 2, 0, 3, 3, 3, 2, 4, 4, 1, 0, 5, 5, 4, 3, 6, 2, 1, 0, 7, 6, 5, 4, 3, 2, 1, 0};
  auto ld1 = [&](int p, int n) {
    if (n < 16) {
      const int a = n >> 1, o = ((a & 1) ? 0 : 128) + 2 * (n & 1);
      asm volatile("ds_read2_b32 %0, %1 offset0:%2 offset1:%3" : "=&v"(A[p][(n >> 1) & 7][n & 1]) : "v"(aa[(a >> 1) & 3]), "i"(o), "i"(o + 1) : "memory");
    } else
      asm volatile("ds_read_b128 %0, %1 offset:%2" : "=&v"(B[p][n & 7]) : "v"(bb), "i"(512 * (n & 7)) : "memory");
  };
  auto next = [&](int dl) {
#pragma unroll
    for (int k = 0; k < 4; ++k) aa[k] += dl;
    bb += dl;
    SB();
  };
  auto land = [&](int p) {
    asm volatile("s_waitcnt lgkmcnt(0)" : "+v"(A[p][0][0]), "+v"(A[p][0][1]), "+v"(A[p][1][0]), "+v"(A[p][1][1]), "+v"(A[p][2][0]), "+v"(A[p][2][1]),
                 "+v"(A[p][3][0]), "+v"(A[p][3][1]), "+v"(A[p][4][0]), "+v"(A[p][4][1]), "+v"(A[p][5][0]), "+v"(A[p][5][1]), "+v"(A[p][6][0]), "+v"(A[p][6][1]),
                 "+v"(A[p][7][0]), "+v"(A[p][7][1]), "+v"(B[p][0]), "+v"(B[p][1]), "+v"(B[p][2]), "+v"(B[p][3]), "+v"(B[p][4]), "+v"(B[p][5]),
                 "+v"(B[p][6]), "+v"(B[p][7]) :: "memory");
    SB();
  };
  auto mm = [&](int p) {
#pragma unroll
    for (int n = 0; n < 36; ++n) {
      const int t = TT[n], a = AA[n];
      const u32x4 av = {A[p][a][0][0], A[p][a][0][1], A[p][a][1][0], A[p][a][1][1]};
      MFMA(ac[t], av, B[p][t - a]);
      if (n < 24) ld1(p ^ 1, n);
      SB();
    }
    land(p ^ 1);
  };
#pragma unroll
  for (int n = 0; n < 24; ++n) ld1(0, n);
  u32x4 ad;
#pragma unroll
  for (int e = 0; e < 4; ++e) ad[e] = lane == 32 + 2 * e + (lane & 1) + (lane & 8) * 3 ? Dw << (16 * (lane & 1)) : 0u;
  pin(ad);
  land(0);
#pragma unroll
  for (int t = 0; t < 8; ++t) MFMA0(ac[t], ad, B[0][t]);
#pragma unroll 1
  for (int r = 0; r < 8; r += 2) {
    next(-64);
    mm(0);
    next(r < 6 ? -64 : 0);
    mm(1);
  }
  asm volatile("s_nop 15" : "+v"(ac[0]), "+v"(ac[1]), "+v"(ac[2]), "+v"(ac[3]), "+v"(ac[4]), "+v"(ac[5]), "+v"(ac[6]), "+v"(ac[7]));
}

#define ROTA 3
#define ROTB 2
#define THR 63
#define CPR 1
#define P2W 8u
#define PS0 2
#define PSD 6
#define EFS 0
#define HCL 0
#define PDL 0
#define STW 1
#define P2L 0
#define BDL 0
#define EFD 10
#define E2L 1
#define ZHP 0
#define ZHN 0
#define HST -1
#define HSL 4
#define NW1 10
#define SGS 13

__global__ __launch_bounds__(512) void k_f(
    const u16* __restrict__ x, const u16* __restrict__ kin, const u16* __restrict__ vin, const u16* __restrict__ shift_k,
    const u16* __restrict__ ssm_k, const u16* __restrict__ ssm_D, const u16* __restrict__ w_in, const u16* __restrict__ w_sp,
    const u16* __restrict__ b_sp, const u16* __restrict__ w_out, u16* __restrict__ out, char* ws) {
  extern __shared__ __attribute__((aligned(16))) char smem[];
  const int tid = threadIdx.x, lane = tid & 63, wave = __builtin_amdgcn_readfirstlane(tid >> 6);
  const int b = blockIdx.x;
  const int xcd = b & 7, idx = b >> 3;
  const int rb = 8 * (xcd >> 1) + (idx >> 2), cb = 4 * (xcd & 1) + (idx & 3);
  const int t0 = rb * 64, d0 = cb * 80;
  const int g4 = lane >> 4, l15 = lane & 15;
  const unsigned lin = (unsigned)(lane * 16);
  const unsigned lb = lds_addr(smem), flg = lb + L_FLAG;
  AB_DECL;
  AB(0);
  asm volatile("" :: "s"(x), "s"(kin), "s"(vin), "s"(shift_k), "s"(ssm_k), "s"(ssm_D), "s"(w_in), "s"(w_sp), "s"(b_sp), "s"(w_out), "s"(out), "s"(ws));
  u64 tag = *(const __attribute__((address_space(4))) u64*)(uintptr_t)ws;
  int tmo = 1 << 16;
#define CWARM 1
  auto cwarm = [&]() {
    u64 pc;
    unsigned clen;
    asm volatile("s_getpc_b64 %0\n.Lpc_here_%=:\n\ts_mov_b32 %1, k_end-.Lpc_here_%=" : "=s"(pc), "=s"(clen));
    touch(min((unsigned)(idx * 1280 + min(lane, 9) * 128), clen - 4u) & ~3u, (const void*)pc, lb + L_DUM);
  };

  const int cw = wave & 3, wm = wave & 1, wl = (wave >> 1) & 1, ksb = wm ^ wl, odd = cb & 1, pr = cb >> 1;
  const int r8 = lane >> 3, c8 = lane & 7;
  unsigned voX[2], voW[2];
#pragma unroll
  for (int h = 0; h < 2; ++h) {
    voX[h] = (unsigned)((h * 8 + r8) * (D_ * 2) + ((c8 ^ (h * 4 + (r8 & 3))) << 4) - h * 1024);
    voW[h] = (unsigned)((h * 8 + r8) * (F2_ * 2) + ((c8 ^ (h * 4 + (r8 & 2))) << 4) - h * 1024);
  }
  const int rot = ROTA ? (((rb & 7) * 5 >> 2) + ROTA * (cb & 3)) % 10 : 0, rot2 = ROTB * (cb & 3);
  const char* gX = (const char*)x + (size_t)(t0 + cw * 16) * (D_ * 2) + rot * 128;
  const char* gW = (const char*)w_in + (size_t)(cw * 16 + rot * 64) * (F2_ * 2) + pr * 384;
  const char *gV = gW + odd * 256, *gB = gW + (odd ? 128 : FH_ * 2 + 128);
  const char* gS = (const char*)w_sp + (size_t)t0 * (S_ * 2) + rot2 * 128;
  const unsigned aoff[2] = {(unsigned)(r8 * 4096 + ((c8 ^ r8) << 4)), (unsigned)(r8 * 4096 + ((c8 ^ r8) << 4) + 32768)};
  const unsigned ldw = lb + cw * 2048;
  auto issue = [&](auto nc) {
    constexpr Unit u = PL.u[decltype(nc)::value];
    constexpr int lo = L_SLOT + u.slot * 8192;
    if (u.kind == US) dma32s<lo>(aoff[0], aoff[1] - 1024, gS, (unsigned)(((u.idx / 3 + 1 + cw) & 3) * 65536 + (u.idx / 3) * 1024 + (u.idx % 3) * 128), ldw);
    else if (u.kind == UX) dma32r<lo, u.idx * 128, 1280, 10 - u.idx>(voX[0], voX[1], gX, ldw, rot);
    else dma32r<lo, u.idx * (64 * F2_ * 2) + (u.kind == UA ? FH_ * 2 : 0), 640 * F2_ * 2, 10 - u.idx>(voW[0], voW[1], u.kind == UB ? gB : gV, ldw, rot);
    SB();
  };
  auto issp = [&](auto gc, auto pc, auto kc) {
    constexpr int p = decltype(pc)::value, n = PL.first[p + 1] + decltype(kc)::value;
    if constexpr (n < PL.first[p + 2]) { if constexpr (PL.u[n].grp == decltype(gc)::value) issue(IC(n)); }
  };
  auto issall = [&](auto gc, auto pc) {
    issp(gc, pc, IC(0)); issp(gc, pc, IC(1)); issp(gc, pc, IC(2)); issp(gc, pc, IC(3)); issp(gc, pc, IC(4));
    issp(gc, pc, IC(5)); issp(gc, pc, IC(6)); issp(gc, pc, IC(7)); issp(gc, pc, IC(8)); issp(gc, pc, IC(9));
  };
  const int kd = d0 + min(wave, 4) * 16 + r8 * 2;
  u32x4 sw;
  {
    const int ks0 = t0 + wave * 8;
#pragma unroll
    for (int i = 0; i < 3; ++i) {
      const int id = min(i * 64 + lane, 189), r = id / 10, c = id % 10;
      const char* kp = (r < 11 ? (const char*)kin + (size_t)max(ks0 - 3 + r, 0) * (D_ * 2) : (const char*)vin + (size_t)(ks0 + r - 11) * (D_ * 2)) + cb * 160 + c * 16;
      asm volatile("s_mov_b32 m0, %1\n\ts_nop 0\n\tglobal_load_lds_dwordx4 %0, off" :: "v"(kp), "s"(lb + L_KV + wave * KV_P + i * 1024) : "memory");
    }
    if (CWARM && wave == 5) cwarm();
    asm volatile("global_load_dwordx4 %0, %1, %2" : "=&v"(sw) : "v"(kd * 8), "s"(shift_k) : "memory");
    SB();
  }
  unsigned kk[11], vv[8];
  u32x4 oa, ob;
  auto kvld = [&]() {
    const char* kl = smem + L_KV + c8 * KV_P + min(wave, 4) * 32 + r8 * 4;
#pragma unroll
    for (int i = 0; i < 11; ++i) kk[i] = *(const unsigned*)(kl + i * 160);
#pragma unroll
    for (int i = 0; i < 8; ++i) vv[i] = *(const unsigned*)(kl + (11 + i) * 160);
  };
  auto kvm = [&](int e0, int e1) {
    const f32x2 w0 = {lo16(sw[0]), lo16(sw[2])}, w1 = {hi16(sw[0]), hi16(sw[2])};
    const f32x2 w2 = {lo16(sw[1]), lo16(sw[3])}, w3 = {hi16(sw[1]), hi16(sw[3])};
    f32x2 kf[11], rr[8];
#pragma unroll
    for (int i = 2 * e0; i < 2 * e1 + 3; ++i) kf[i] = unpk((i < 3 && t0 + c8 == 0) ? 0u : kk[i]);
#pragma unroll
    for (int i = 2 * e0; i < 2 * e1; ++i) {
      f32x2 a0 = kf[i] * w3;
      a0 = __builtin_elementwise_fma(kf[i + 1], w2, a0);
      a0 = __builtin_elementwise_fma(kf[i + 2], w1, a0);
      a0 = __builtin_elementwise_fma(kf[i + 3], w0, a0);
      rr[i] = a0 * unpk(vv[i]);
    }
#pragma unroll
    for (int e = e0; e < e1; ++e) { oa[e] = pack2(rr[2 * e][0], rr[2 * e + 1][0]); ob[e] = pack2(rr[2 * e][1], rr[2 * e + 1][1]); }
  };
  auto kvst = [&]() {
    const unsigned ko = (unsigned)((kd * S_ + t0 + c8 * 8) * 2);
    st16(ko, ws + WS_KV, oa);
    st16(ko, ws + WS_KV + S_ * 2, ob);
    SB();
  };
  auto barrier0 = [&](int nv) {
    SB();
    asm volatile("s_waitcnt vmcnt(%0) lgkmcnt(0)\n\ts_barrier" :: "n"(nv) : "memory");
    SB();
    asm volatile("" : "+v"(sw), "+s"(tag));
    tag += 1;
    asm volatile("" : "+s"(tag));
  };
  unsigned pX[2], pW[2];
#pragma unroll
  for (int k = 0; k < 2; ++k) {
    const int q = l15 >> 2, p = lane & 3, sz = (q >> 1) | ((g4 & 1) << 1);
    pX[k] = lb + (wm * 32 + q * 8 + p) * 128 + ((((k ^ ksb) * 4 + g4) ^ (l15 & 7)) << 4);
    pW[k] = lb + ((k ^ ksb) * 32 + g4 * 8 + q) * 128 + (((sz << 1) | (p >> 1)) << 4) + (p & 1) * 8;
  }
  bf16x8 ones;
#pragma unroll
  for (int e = 0; e < 8; ++e) ones[e] = (short)0x3F80;
  f32x4 xm[2], xr[2];
  auto ldxs = [&]() {
#pragma unroll
    for (int i = 0; i < 2; ++i) {
      xm[i] = *(const f32x4*)(smem + L_XM + (wm * 32 + g4 * 8 + i * 4) * 4);
      xr[i] = *(const f32x4*)(smem + L_XR + (wm * 32 + g4 * 8 + i * 4) * 4);
    }
  };
  auto act = [&](const f32x4& a, float c, int i) {
    const f32x2 nc = {-c, -c};
    const f32x2 lo = gelu2(__builtin_elementwise_fma((f32x2){xm[i][0], xm[i][1]}, nc, (f32x2){a[0], a[1]}) * (f32x2){xr[i][0], xr[i][1]});
    const f32x2 hi = gelu2(__builtin_elementwise_fma((f32x2){xm[i][2], xm[i][3]}, nc, (f32x2){a[2], a[3]}) * (f32x2){xr[i][2], xr[i][3]});
    return (f32x4){lo[0], lo[1], hi[0], hi[1]};
  };
  auto pack8 = [&](const f32x4& a, const f32x4& c) { return (u32x4){pack2(a[0], a[1]), pack2(a[2], a[3]), pack2(c[0], c[1]), pack2(c[2], c[3])}; };
  auto z1put = [&](int f0, const u32x4& v) { *(u32x4*)(smem + L_Z1 + (f0 + l15) * 128 + (((wm * 4 + g4) ^ (l15 & 7)) << 4)) = v; };
  auto rdXF = [&](auto cc, bf16x8 (&X)[2][2]) {
    constexpr int so = L_SLOT + XSLOT[decltype(cc)::value] * 8192;
#pragma unroll
    for (int k = 0; k < 2; ++k) {
      const unsigned a = pX[k] + (so & ~0x7FFF);
      ds16<(so & 0x7FFF)>(X[k][0], a);
      ds16<(so & 0x7FFF) + 512>(X[k][1], a);
    }
    SB();
  };
  auto sttag = [&](const char* p) {
    asm volatile("s_mov_b64 exec, 1\n\tglobal_store_dwordx2 %0, %1, %2 sc1\n\ts_mov_b64 exec, -1" :: "v"(lin), "v"((u32x2){opq((unsigned)tag), opq((unsigned)(tag >> 32))}), "s"(p) : "memory");
  };
  auto ldtag = [&](u32x2& f, unsigned vo, const char* p) { asm volatile("global_load_dwordx2 %0, %1, %2 sc1" : "=&v"(f) : "v"(vo), "s"(p) : "memory"); };
  auto notag = [&](const u32x2& f) { return __builtin_amdgcn_ballot_w64(f[0] != (unsigned)tag || f[1] != (unsigned)(tag >> 32)) != 0; };
  auto waittag = [&](u32x2& f, unsigned vo, const char* p) {
    while (__builtin_expect(notag(f) && tmo > 0, 0)) {
      --tmo;
      __builtin_amdgcn_s_sleep(4);
      ldtag(f, vo, p);
      wait_vm<0>(f);
    }
  };
  auto ldpf = [&](int t, int n) {
    const int q = min(lane, 8 * n - 1);
    touch((unsigned)((q / n) * 4096 + (t + q % n) * 128), (const char*)w_sp + (size_t)(t0 + cb * 8) * 4096 + cw * 1024, lb + L_DUM);
  };
  u32x2 ef;
  auto ldef = [&]() { asm volatile("global_load_dwordx2 %0, %1, %2 sc1" : "=&v"(ef) : "v"((unsigned)((lane & 31) * 8)), "s"(ws + WS_EF + cb * 256) : "memory"); };

  char* const hf = ws + WS_H;
  bf16x8 Wq[15];
  const char* wg = ws + WS_WO + (size_t)(wave * 120 + cb * 5) * 1024;
  const int mt = wave & 3, kh = wave >> 2;
  const size_t xo = (size_t)(t0 + mt * 16 + l15) * D_ + d0;
  u32x2 xv[3];
  auto ldP1 = [&](int n) {
    if (n < 15)
      asm volatile("global_load_dwordx4 %0, %1, %2 offset:%3 sc1" : "=&v"(Wq[n < 15 ? n : 0]) : "v"(lin), "s"(wg + (n / 5) * 40960 + 2048), "i"((n % 5) * 1024 - 2048) : "memory");
    else {
      const int q = n < 18 ? n - 15 : 0;
      asm volatile("global_load_dwordx2 %0, %1, %2 offset:%3" : "=&v"(xv[q]) : "v"((unsigned)(l15 * D_ * 2 + 8 * g4)),
                   "s"(x + (size_t)(t0 + mt * 16) * D_ + d0 + (q < 2 ? 32 * kh : 64)), "i"(q == 1 ? 32 : 0) : "memory");
    }
  };
  u32x4 yv[2];
  auto yoff = [&](int n) {
    const int c = n ? 64 + (lane & 31) : lane, r = c >> 1;
    return (unsigned)(((r < 32 ? 32 * kh + r : 32 + r) * S_ + 8 * (c & 1)) * 2);
  };
  const char* const yb = ws + WS_Y + ((size_t)d0 * S_ + t0 + 16 * mt) * 2;
  auto ldW1 = [&]() {
    ldtag(ef, 0u, ws + WS_LF + (rb * 8 + wave) * 8);
#pragma unroll
    for (int n = 0; n < NW1; ++n) ldP1(n);
    SB();
  };
  auto drain1 = [&](int nc) {
    SB();
    asm volatile("s_waitcnt vmcnt(%0)" :: "i"(nc) : "memory");
#pragma unroll
    for (int n = 0; n < NW1; ++n) pin(Wq[n]);
    pin(ef);
  };
  if (wave >= 4) {
    const int wid = rb * 4 + cw;
    __builtin_amdgcn_s_setprio(3);
    if (wid < 120) dma16((unsigned)((lane >> 1) * (D_ * 2) + (lane & 1) * 16), w_out + (size_t)(wid / 5) * 32 * D_ + d0 + (wid % 5) * 16, lb + L_WO + cw * 1024);
    SB();
    issall(IC(1), IC(-1));
    AB(1);
    barrier0(PL.nv[1][0]);
    AB(2);
    u32x4 hole = {HOLE, HOLE, HOLE, HOLE};
    pin(ones, hole);
    issp(IC(1), IC(0), IC(0));
    if (wave == 7) kvld();
    issp(IC(1), IC(0), IC(1));
    if (wid < 120) {
      const unsigned ra = lb + L_WO + cw * 1024 + (8 * g4 + (l15 >> 2)) * 32 + 8 * (lane & 3);
      s16x4 lo = tr_asm<0>(ra), hi = tr_asm<128>(ra);
      wait_lg<0>(lo, hi);
      st16(lin, ws + WS_WO + (size_t)((wid / 5) * 40 + cb * 5 + wid % 5) * 1024, __builtin_bit_cast(u32x4, join8(lo, hi)));
    }
    SB();
    issp(IC(1), IC(0), IC(2)); issp(IC(1), IC(0), IC(3)); issp(IC(1), IC(0), IC(4)); issp(IC(1), IC(0), IC(5)); issp(IC(1), IC(0), IC(6)); issp(IC(1), IC(0), IC(7));
    if (wave == 7) { kvm(0, 4); kvst(); }
    AB(3);
    bf16x8 XF[10][2][2];
    auto ldx = [&](int c) {
      u32x4 xv;
      asm volatile("global_load_dwordx4 %0, %1, %2\n\ts_waitcnt vmcnt(0)" : "=&v"(xv) : "v"((unsigned)(lane * (D_ * 2) + c * 16)), "s"(x + (size_t)t0 * D_) : "memory");
      return xv;
    };
    const bool docv = cw < ((rb & 1) ? 2 : 3);
    const int d = d0 + (cw < 2 ? 2 * rb + cw : 64 + (rb >> 1));
    u32x4 hc[4], uc[4];
    unsigned Dw;
    auto ldhc = [&]() {
#pragma unroll
      for (int k = 0; k < 4; ++k) asm volatile("global_load_dwordx4 %0, %1, %2 offset:%3" : "=&v"(hc[k]) : "v"(lin), "s"(ssm_k + (size_t)d * S_), "i"(k * 1024) : "memory");
      asm volatile("global_load_ushort %0, %1, %2" : "=&v"(Dw) : "v"(0u), "s"(ssm_D + d) : "memory");
    };
    unsigned rwo;
    f32x2 ra, rc;
    auto rdraw = [&]() { asm volatile("ds_read_b64 %0, %2\n\tds_read_b64 %1, %2 offset:512" : "=&v"(ra), "=&v"(rc) : "v"(rwo) : "memory"); SB(); };
    auto period = [&](auto pc) {
      constexpr int p = decltype(pc)::value;
      constexpr int c0 = p == 1 || p > 6 ? 0 : 2 * p - 3, nx = p == 1 || p == 6 ? 1 : (p < 6 ? 2 : 0);
      SB();
      CB(6 * (p - XBAR));
      asm volatile("s_waitcnt vmcnt(%0) lgkmcnt(0)\n\ts_barrier" :: "n"(PL.nv[1][p]) : "memory");
      CB(6 * (p - XBAR) + 1);
      SB();
      constexpr int pc0 = p == 2 || p > 7 ? 0 : 2 * p - 5, pnx = p < 2 || p > 7 ? 0 : (p == 2 || p == 7 ? 1 : 2);
#pragma unroll
      for (int c = pc0; c < pc0 + pnx; ++c) pin(XF[c][0][0], XF[c][0][1], XF[c][1][0], XF[c][1][1]);
      if constexpr (p == 7) {
        rwo = lb + L_RAW + ((lane >> 3) * 4 + (lane & 3)) * 16 + (lane & 4) * 2;
        pin(rwo);
      }
      if constexpr (p == 8) rdraw();
      if constexpr (p == 9) __builtin_amdgcn_s_setprio(CPR);
      auto thr = [&]() { if constexpr (p >= 9 && THR < 63) wait_vm<THR>(); };
      issp(IC(1), pc, IC(0)); thr();
      CB(6 * (p - XBAR) + 2);
      if constexpr (nx > 0) rdXF(IC(c0), XF[c0]);
      issp(IC(1), pc, IC(1)); thr();
      CB(6 * (p - XBAR) + 3);
      issp(IC(1), pc, IC(2)); thr();
      CB(6 * (p - XBAR) + 4);
      if constexpr (nx > 1) rdXF(IC(c0 + 1), XF[c0 + 1]);
      issp(IC(1), pc, IC(3)); thr(); issp(IC(1), pc, IC(4)); thr(); issp(IC(1), pc, IC(5)); thr(); issp(IC(1), pc, IC(6)); thr(); issp(IC(1), pc, IC(7)); thr();
      issp(IC(1), pc, IC(8)); thr(); issp(IC(1), pc, IC(9)); thr(); issp(IC(1), pc, IC(10)); thr(); issp(IC(1), pc, IC(11));
      if constexpr (PFM >> p & 1) { ldpf(2 + __builtin_popcount(PFM & ((1 << p) - 1)), 1); SB(); }
      if constexpr (p == HCP) { ldhc(); SB(); }
      if constexpr (p == 8) {
        wait_lg<0>(ra, rc);
        if (wave == 5) sttag(ws + WS_EF + (cb * 32 + rb) * 8);
        if (wave == 6) {
          const float s1 = nv(ra[0]) + rc[0], fm = s1 * (1.0f / 640.0f), sm = s1 * fm;
          float fq = (ra[1] - sm) + rc[1];
          if (__builtin_expect(__builtin_amdgcn_ballot_w64(!(fq >= 1e-3f * sm)) != 0, 0)) {
            const f32x2 mm = {fm, fm};
            f32x2 q2 = {0.f, 0.f};
#pragma unroll 1
            for (int c = 0; c < 80; ++c) {
              const u32x4 xv = ldx(c);
#pragma unroll
              for (int e = 0; e < 4; ++e) { const f32x2 d = unpk(xv[e]) - mm; q2 = __builtin_elementwise_fma(d, d, q2); }
            }
            fq = q2[0] + q2[1];
          }
          const float vbf = rbf(fmaxf(fq, 0.f) * (1.0f / 640.0f));
          *(float*)(smem + L_XM + lane * 4) = rbf(fm);
          *(float*)(smem + L_XR + lane * 4) = __builtin_amdgcn_rcpf(rbf(__builtin_amdgcn_sqrtf(rbf(vbf + 1e-5f))));
        }
        SB();
      }
      if constexpr (NST[1][p + 1] > 0) {
        int n0 = 0;
#pragma unroll
        for (int q = 0; q <= p; ++q) n0 += NST[1][q];
#pragma unroll
        for (int n = n0; n < n0 + NST[1][p + 1]; ++n) st16(lin, ws + WS_Z2 + (size_t)((rb * 2 + wl) * 48 + cb * 6 + wm * 3 + n) * 1024, hole);
        SB();
      }
    };
    period(IC(1)); AB(4); period(IC(2)); period(IC(3)); period(IC(4)); period(IC(5)); AB(5);
    period(IC(6)); period(IC(7)); period(IC(8)); period(IC(9));
    AB(6);
    issall(IC(1), IC(10));
    if (PFA) ldpf(2, PFA);
    __builtin_amdgcn_s_setprio(0);
    wait_vm<after_v() + (PFA > 0)>();
    signal1(flg + 60);
    flag_wait_asm(flg + 60, 8u);
    AB(7);
    f32x4 ac2[2][2], cs2[2];
    auto lduc = [&]() {
#pragma unroll
      for (int k = 0; k < 4; ++k) asm volatile("global_load_dwordx4 %0, %1, %2 offset:%3 sc1" : "=&v"(uc[k]) : "v"(lin), "s"(ws + WS_KV + (size_t)d * (S_ * 2)), "i"(k * 1024) : "memory");
    };
    {
      s16x4 VF[3][2][2];
      unsigned aV[2][2];
      auto rdV = [&](auto nc, int r) {
        constexpr int n = decltype(nc)::value, so = VSLOT[n >> 1] * 8192, po = n < 2 ? -1 : (VSLOT[(n >> 1) - 1] * 8192) >> 16;
        const int jj = r >> 1;
        if (!(r & 1)) {
          if ((so >> 16) != po) aV[n & 1][jj] = opq((pW[n & 1] ^ (unsigned)(wl * 64 + jj * 32)) + L_SLOT + (so & ~0xFFFF));
          VF[n % 3][jj][0] = tr_asm<(so & 0xFFFF)>(aV[n & 1][jj]);
        } else VF[n % 3][jj][1] = tr_asm<(so & 0xFFFF) + 512>(aV[n & 1][jj]);
        SB();
      };
      auto mm2 = [&](auto nc) {
        constexpr int n = decltype(nc)::value, j = n >> 1, k = n & 1, nw = n < 19 ? 4 : 0;
        const bf16x8 w0 = join8(VF[n % 3][0][0], VF[n % 3][0][1]), w1 = join8(VF[n % 3][1][0], VF[n % 3][1][1]);
        auto nxt = [&](int r) { SB(); if constexpr (n < 18) rdV(IC(n + 2 < 20 ? n + 2 : 0), r); };
        if (k == 0) { MFMAW<n == 0>(nw, cs2[0], ones, w0); MFMAW<n == 0>(-1, cs2[1], ones, w1); }
        if constexpr (n == EFS) ldef();
        if constexpr (n == EFS + EFD) {
          wait_vm<0>(ef);
          waittag(ef, (unsigned)((lane & 31) * 8), ws + WS_EF + cb * 256);
          lduc();
          SB();
        }
        MFMAW<n == 0>(k ? nw : -1, ac2[0][0], XF[j][k][0], w0); nxt(0);
        MFMAW<n == 0>(-1, ac2[1][0], XF[j][k][1], w0); nxt(1);
        MFMAW<n == 0>(-1, ac2[0][1], XF[j][k][0], w1); nxt(2);
        MFMAW<n == 0>(-1, ac2[1][1], XF[j][k][1], w1); nxt(3);
        if constexpr (n == 19) {
          pin(cs2[0], cs2[1]);
          if (g4 < 2) *(float*)(smem + L_CS2 + (cw * 32 + lane) * 4) = g4 ? cs2[1][0] : cs2[0][0];
          signal1(flg + 20 + wl * 4);
        }
        SB();
      };
#pragma unroll
      for (int r = 0; r < 4; ++r) rdV(IC(0), r);
#pragma unroll
      for (int r = 0; r < 4; ++r) rdV(IC(1), r);
      mm2(IC(0)); mm2(IC(1)); mm2(IC(2)); mm2(IC(3)); mm2(IC(4)); mm2(IC(5)); mm2(IC(6)); mm2(IC(7)); mm2(IC(8)); mm2(IC(9));
      mm2(IC(10)); mm2(IC(11)); mm2(IC(12)); mm2(IC(13)); mm2(IC(14)); mm2(IC(15)); mm2(IC(16)); mm2(IC(17)); mm2(IC(18)); mm2(IC(19));
      AB(8);
      signal1(flg + 16);
    }
    auto epi2 = [&]() {
      ldxs();
      flag_wait_asm(flg + 20 + wl * 4, 2u);
      pin(ac2[0][0], ac2[0][1], ac2[1][0], ac2[1][1]);
#pragma unroll
      for (int jj = 0; jj < 2; ++jj) {
        const float ct = cs2[jj][0] + *(const float*)(smem + L_CS2 + ((cw ^ 1) * 32 + jj * 16 + l15) * 4);
        u32x4 o;
#pragma unroll
        for (int q = 0; q < 4; ++q) {
          const int i = q >> 1, e = (q & 1) * 2;
          o[q] = pack2s(gelu1(__builtin_fmaf(nv(xm[i][e]), -ct, ac2[i][jj][e]) * xr[i][e]), gelu1(__builtin_fmaf(xm[i][e + 1], -ct, ac2[i][jj][e + 1]) * nv(xr[i][e + 1])));
        }
        z1put(odd * 32 + wl * 32 + jj * 16, o);
      }
      signal1(flg + 56);
    };
    if (!E2L) epi2();
    AB(9);
    wait_vm<0>(hc[0], hc[1], hc[2], hc[3], uc[0], uc[1], uc[2], uc[3], Dw);
    if (docv) {
      char* cb_ = smem + M_CV + cw * CV_BUF;
      flag_wait_asm(flg + 16, 4u);
      if (lane < 36) *(u32x4*)(cb_ + (lane < 32 ? lane * 16 : CV_C + ((lane >> 1) & 1) * CV_CS + 4096 + (lane & 1) * 16)) = (u32x4){0u, 0u, 0u, 0u};
#pragma unroll
      for (int k = 0; k < 4; ++k) {
        const int a = k * 64 + lane;
        const int h0 = k ? __builtin_amdgcn_readlane((int)hc[k ? k - 1 : 0][3], 63) : 0;
        const unsigned hp = (unsigned)__builtin_amdgcn_update_dpp(h0, (int)hc[k][3], 0x138, 0xF, 0xF, false);
        const unsigned w[5] = {hp, hc[k][0], hc[k][1], hc[k][2], hc[k][3]};
        u32x4 sw, mx;
#pragma unroll
        for (int i = 0; i < 4; ++i) {
          sw[i] = (w[4 - i] >> 16) | (w[4 - i] << 16);
          mx[i] = (w[4 - i] & 0xFFFFu) | (w[3 - i] & 0xFFFF0000u);
        }
        char* cp = cb_ + CV_C + (2040 - 8 * a) * 2;
        *(u32x4*)(cp) = sw;
        *(u32x4*)(cp + CV_CS) = mx;
        *(u32x4*)(cb_ + 512 + a * 16) = uc[k];
      }
      AB(12);
      f32x4 ac[8];
      conv_core(lds_addr(cb_), lane, Dw, ac);
      AB(13);
#pragma unroll
      for (int k = 0; k < 4; ++k) {
        u32x2 ov[2];
#pragma unroll
        for (int t = 0; t < 2; ++t)
#pragma unroll
          for (int h = 0; h < 2; ++h) ov[t][h] = pack2(ac[2 * k + t][2 * h], ac[2 * k + t][2 * h + 1]);
        const auto s0 = __builtin_amdgcn_permlane16_swap(ov[0][0], ov[1][0], false, false);
        const auto s1 = __builtin_amdgcn_permlane16_swap(ov[0][1], ov[1][1], false, false);
        st16((unsigned)((256 * (g4 & 1) + 16 * l15 + 8 * (g4 >> 1)) * 2), ws + WS_Y + (size_t)d * (S_ * 2) + k * 1024, (u32x4){s0[0], s1[0], s0[1], s1[1]});
      }
      if (E2L) epi2();
      wait_vm<0>();
      sttag(ws + WS_YF + d * 8);
    } else if (E2L) epi2();
    if (cw == 3) {
      u32x4 hole2 = {HOLE, HOLE, HOLE, HOLE};
      pin(hole2);
      if (HST >= 0) flag_wait_asm(flg + 32, 1u);
#pragma unroll
      for (int n = 0; n < 12; ++n) {
        st16(lin, ws + WS_H + (size_t)((rb * 4 + n / 3) * 24 + cb * 3 + n % 3) * 1024, hole2);
        __builtin_amdgcn_s_sleep(HSL);
      }
      wait_vm<0>();
      sttag(ws + WS_LF + (rb * 8 + cb) * 8);
      if (!odd) {
        u32x4 zx[4];
        ldtag(ef, (unsigned)((lane & 1) * 8), ws + WS_XF + (rb * 4 + pr) * 16);
        wait_vm<0>(ef);
        waittag(ef, (unsigned)((lane & 1) * 8), ws + WS_XF + (rb * 4 + pr) * 16);
#pragma unroll
        for (int c = 0; c < 4; ++c)
          asm volatile("global_load_dwordx4 %0, %1, %2 offset:%3 sc1" : "=&v"(zx[c]) : "v"(lin), "s"(ws + WS_ZX + (size_t)(rb * 4 + pr) * 4096), "i"(c * 1024) : "memory");
        wait_vm<0>(zx[0], zx[1], zx[2], zx[3]);
#pragma unroll
        for (int c = 0; c < 4; ++c) *(u32x4*)(smem + L_Z1 + (64 + (c & 1) * 16 + l15) * 128 + ((((c >> 1) * 4 + g4) ^ (l15 & 7)) << 4)) = zx[c];
      }
      signal1(flg + 56);
    }
    SB();
    AB(14);
    WAIT_LGKM0();
    signal1(flg + 40);
    flag_wait_asm(flg + 44, 4u);
    AB(15);
    ldW1();
    drain1(0);
  } else {
    issall(IC(0), IC(-1));
    if (tid < 64) *(unsigned*)(smem + L_FLAG + tid * 4) = 0u;
    pin(ones);
    AB(1);
    barrier0(PL.nv[0][0]);
    AB(2);
    kvld();
    kvm(0, 4);
    kvst();
    const bool isz2 = !(odd && wl);
    const char* stp = ws + WS_ST + (size_t)((rb * 12 + pr * 3 + odd * 2 + wl) * 1024 + wm * 512);
    if (STW && isz2) st8(lin >> 1, stp, (u32x2){0u, ~(unsigned)tag});
    AB(3);
    const char* Ab[4];
#pragma unroll
    for (int i = 0; i < 4; ++i) Ab[i] = (const char*)w_sp + (size_t)(t0 + ((wave + 1 + i) & 3) * 16) * (S_ * 2) + wave * 1024;
    const unsigned ringl = lb + M_RING + wave * 24576;
    auto ldA1 = [&](auto dc, int i) {
      constexpr int d = decltype(dc)::value;
      dma16o(aoff[i & 1] + (unsigned)(((d + rot2) & 7) * 128), Ab[i >> 1], 0, ringl, (d % 3) * 8192 + i * 1024);
    };
    {
      f32x4 zv[2][4], cs[4];
      s16x4 WB[20][4][2];
      bf16x8 XG[10][2][2];
      unsigned aX[2][2], aW[2][2][4];
#pragma unroll
      for (int k = 0; k < 2; ++k)
#pragma unroll
        for (int w = 0; w < 2; ++w) {
          aX[w][k] = pX[k] + L_SLOT + XW * 8192 + w * 65536;
          pin(aX[w][k]);
#pragma unroll
          for (int jj = 0; jj < 4; ++jj) { aW[w][k][jj] = (pW[k] ^ (unsigned)(jj * 32)) + L_SLOT + wl * 8192 + w * 65536; pin(aW[w][k][jj]); }
        }
      auto rd1 = [&](auto nc, int r) {
        constexpr int n = decltype(nc)::value, j = n >> 1, k = n & 1, c = n > 0 && n < 11 ? n - 1 : 0, sx = (XSLOT[c] - XW) * 8192, sa = WSLOT[j] * 8192;
        if (r < 0) { if (r & 1) ds16<(sx & 0xFFFF) + 512>(XG[c][(r + 4) >> 1][1], aX[sx >> 16][(r + 4) >> 1]); else ds16<(sx & 0xFFFF)>(XG[c][(r + 4) >> 1][0], aX[sx >> 16][(r + 4) >> 1]); }
        else if (r & 1) WB[n][r >> 1][1] = tr_asm<(sa & 0xFFFF) + 512>(aW[sa >> 16][k][r >> 1]);
        else WB[n][r >> 1][0] = tr_asm<(sa & 0xFFFF)>(aW[sa >> 16][k][r >> 1]);
        SB();
      };
      auto bar = [&](auto bc) {
        SB();
        constexpr int p = decltype(bc)::value;
        CB(6 * (p - XBAR));
        if constexpr (p == 1) asm volatile("s_waitcnt vmcnt(%0) lgkmcnt(0)\n\ts_barrier" :: "n"(PL.nv[0][p]) : "memory");
        else if constexpr (p <= GZ) asm volatile("s_waitcnt vmcnt(%0) lgkmcnt(8)\n\ts_barrier" :: "n"(PL.nv[0][p]) : "memory");
        else if constexpr (p >= 8) asm volatile("s_waitcnt lgkmcnt(0)\n\ts_barrier" ::: "memory");
        else asm volatile("s_barrier" ::: "memory");
        CB(6 * (p - XBAR) + 1);
        SB();
      };
      float cp[4];
      f32x4 st[2][2], rv;
      const unsigned rwa = lb + L_RAW + (ksb * 2 + wm) * 256 + l15 * 16 + ((l15 >> 2) == g4 ? 0 : 1024);
      const unsigned csa = lb + L_CS + (wave * 64 + lane) * 4;
      auto kstep = [&](auto nc) {
        constexpr int n = decltype(nc)::value, j = n >> 1, k = n & 1, nn = n + 1, r0 = nn >= 2 && nn <= 10 ? -4 : 0;
        constexpr int sc = n == 1 ? 0 : n - 2, ns = !k ? 0 : (n == 1 || n == 11 ? 1 : 2);
        if constexpr (k) bar(IC(nn / 2 + 1));
        int r = r0, c = 0;
        auto nxt = [&]() { if (r < 8) { rd1(IC(nn), r++); ++c; } };
        auto sel = [&](int q) {
          const f32x4& sq = st[q & 1][q >> 1];
          rv[q] = sel4(sq[0], sq[1], sq[2], sq[3], 0xAAAAAAAAAAAAAAAAull, 0xCCCCCCCCCCCCCCCCull);
          SB();
        };
#pragma unroll
        for (int jj = 0; jj < 4; ++jj) {
          const bf16x8 wf = join8(WB[n][jj][0], WB[n][jj][1]);
          MFMAW<n == 0>(jj == 0 ? 4 : (jj == 2 ? c : -1), zv[0][jj], XG[j][k][0], wf);
          SB();
          nxt();
          if (n == 12) nxt();
          if (n == 12 && jj == 2) sel(2);
          MFMAW<n == 0>(-1, zv[1][jj], XG[j][k][1], wf);
          SB();
          nxt();
          if (n == 12) nxt();
          if (n == 12 && (jj == 1 || jj == 2)) sel(jj == 1 ? 0 : 3);
          if (n == 12 && jj == 2) { dsw16<0>(rwa, rv); SB(); }
          if (k == 0) {
            MFMAW<n == 0>(-1, cs[jj], ones, wf);
            SB();
            nxt();
            if (n == 12) nxt();
            if (n == 12 && jj == 1) sel(1);
          }
#pragma unroll
          for (int q = 0; q < ns; ++q) {
            const bf16x8& xs = XG[sc + q][1][jj >> 1];
            MFMAW<n == 1>(-1, st[jj & 1][jj >> 1], xs, (jj & 1) ? xs : ones);
            SB();
            nxt();
          }
        }
      };
      auto lstep = [&](auto nc, auto ac, auto bc, int w) {
        constexpr int n = decltype(nc)::value, j = n >> 1, k = n & 1, na = decltype(ac)::value, nb = decltype(bc)::value;
        int r = 0;
        auto nxt = [&]() { if (na > 0 && r < 16) { if (r < 8) rd1(ac, r); else rd1(bc, r - 8); ++r; } };
#pragma unroll
        for (int jj = 0; jj < 4; ++jj) {
          const bf16x8 wf = join8(WB[n][jj][0], WB[n][jj][1]);
          if (k == 0) { MFMAW<false>(jj == 0 ? w : -1, cs[jj], ones, wf); SB(); nxt(); nxt(); }
          MFMAW<false>(jj == 0 && k ? w : -1, zv[0][jj], XG[j][k][0], wf);
          SB();
          nxt();
          if (k) nxt();
          MFMAW<false>(-1, zv[1][jj], XG[j][k][1], wf);
          SB();
          nxt();
          if (k) nxt();
          if (n == 15 && jj == 2) {
            dsw4(csa, sel4(cs[0][0], cs[1][0], cs[2][0], cs[3][0], 0xFFFF0000FFFF0000ull, 0xFFFFFFFF00000000ull));
            SB();
          }
        }
      };
      bar(IC(1));
#pragma unroll
      for (int r = -4; r < 8; ++r) rd1(IC(0), r);
      kstep(IC(0)); AB(4); kstep(IC(1)); kstep(IC(2)); kstep(IC(3)); kstep(IC(4)); kstep(IC(5)); kstep(IC(6)); kstep(IC(7)); kstep(IC(8)); AB(5); kstep(IC(9));
      kstep(IC(10)); kstep(IC(11)); kstep(IC(12));
      bar(IC(8));
      lstep(IC(13), IC(14), IC(16), -1);
      lstep(IC(14), IC(18), IC(15), 8);
      lstep(IC(16), IC(17), IC(19), 15);
      lstep(IC(18), IC(0), IC(0), 15);
      lstep(IC(15), IC(0), IC(0), -1);
      lstep(IC(17), IC(0), IC(0), 9);
      bar(IC(9));
      __builtin_amdgcn_s_setprio(2);
#pragma unroll
      for (int q = 0; q < 4; ++q) cp[q] = *(const float*)(smem + L_CS + ((wave ^ 1) * 64 + q * 16 + l15) * 4);
      ldxs();
      SB();
      lstep(IC(19), IC(0), IC(0), -1);
      AB(6);
      float ct[4];
#pragma unroll
      for (int jj = 0; jj < 4; ++jj) ct[jj] = cs[jj][0] + cp[jj];
#pragma unroll
      for (int jj = 0; jj < 4; ++jj) {
        if (jj == 0) asm volatile("s_nop 3");
        if (jj == 2) SB();
        if (!(jj & 1)) pin(zv[0][jj], zv[1][jj], zv[0][jj + 1], zv[1][jj + 1]);
#pragma unroll
        for (int i = 0; i < 2; ++i) zv[i][jj] = act(zv[i][jj], ct[jj], i);
      }
      if (isz2) {
        f32x4 pv[2], s1[2], s2[2];
#pragma unroll
        for (int i = 0; i < 2; ++i) {
#pragma unroll
          for (int e = 0; e < 4; ++e) {
            const float z0 = zv[i][0][e];
            pv[i][e] = __int_as_float(__builtin_amdgcn_update_dpp(0, __float_as_int(z0), 0x150, 0xF, 0xF, true));
          }
          const f32x4 a0 = zv[i][0] - pv[i], a1 = zv[i][1] - pv[i], a2 = zv[i][2] - pv[i], a3 = zv[i][3] - pv[i];
          s1[i] = (a0 + a1) + (a2 + a3);
          s2[i] = __builtin_elementwise_fma(a3, a3, __builtin_elementwise_fma(a2, a2, __builtin_elementwise_fma(a1, a1, a0 * a0)));
        }
        float r[8], q[4];
#define DA(d, s, ctl, bm) "v_add_f32_dpp " d ", " s ", " s " " ctl " row_mask:0xf bank_mask:" bm "\n\t"
        asm volatile("s_nop 1\n\t"
                     DA("%0", "%12", "row_ror:8", "0x3") DA("%1", "%13", "row_ror:8", "0x3") DA("%2", "%14", "row_ror:8", "0x3") DA("%3", "%15", "row_ror:8", "0x3")
                     DA("%4", "%16", "row_ror:8", "0x3") DA("%5", "%17", "row_ror:8", "0x3") DA("%6", "%18", "row_ror:8", "0x3") DA("%7", "%19", "row_ror:8", "0x3")
                     DA("%0", "%20", "row_ror:8", "0xc") DA("%1", "%21", "row_ror:8", "0xc") DA("%2", "%22", "row_ror:8", "0xc") DA("%3", "%23", "row_ror:8", "0xc")
                     DA("%4", "%24", "row_ror:8", "0xc") DA("%5", "%25", "row_ror:8", "0xc") DA("%6", "%26", "row_ror:8", "0xc") DA("%7", "%27", "row_ror:8", "0xc")
                     DA("%8", "%0", "row_half_mirror", "0x5") DA("%9", "%1", "row_half_mirror", "0x5") DA("%10", "%2", "row_half_mirror", "0x5") DA("%11", "%3", "row_half_mirror", "0x5")
                     DA("%8", "%4", "row_half_mirror", "0xa") DA("%9", "%5", "row_half_mirror", "0xa") DA("%10", "%6", "row_half_mirror", "0xa") DA("%11", "%7", "row_half_mirror", "0xa")
                     DA("%8", "%8", "quad_perm:[1,0,3,2]", "0xf") DA("%9", "%9", "quad_perm:[1,0,3,2]", "0xf") DA("%10", "%10", "quad_perm:[1,0,3,2]", "0xf") DA("%11", "%11", "quad_perm:[1,0,3,2]", "0xf")
                     DA("%8", "%8", "quad_perm:[2,3,0,1]", "0xf") DA("%9", "%9", "quad_perm:[2,3,0,1]", "0xf") DA("%10", "%10", "quad_perm:[2,3,0,1]", "0xf") DA("%11", "%11", "quad_perm:[2,3,0,1]", "0xf")
                     : "=&v"(r[0]), "=&v"(r[1]), "=&v"(r[2]), "=&v"(r[3]), "=&v"(r[4]), "=&v"(r[5]), "=&v"(r[6]), "=&v"(r[7]),
                       "=&v"(q[0]), "=&v"(q[1]), "=&v"(q[2]), "=&v"(q[3])
                     : "v"(s1[0][0]), "v"(s2[0][0]), "v"(s1[0][1]), "v"(s2[0][1]), "v"(s1[0][2]), "v"(s2[0][2]), "v"(s1[0][3]), "v"(s2[0][3]),
                       "v"(s1[1][0]), "v"(s2[1][0]), "v"(s1[1][1]), "v"(s2[1][1]), "v"(s1[1][2]), "v"(s2[1][2]), "v"(s1[1][3]), "v"(s2[1][3]));
#undef DA
        const f32x4 p4 = (lane & 8) ? pv[1] : pv[0];
        const float pa = (lane & 4) ? p4[2] : p4[0], pb = (lane & 4) ? p4[3] : p4[1];
        const float pp = (lane & 2) ? pb : pa, S1 = (lane & 2) ? q[2] : q[0], S2 = (lane & 2) ? q[3] : q[1];
        const float dl = S1 * (1.0f / 64.0f);
        st8(lin >> 1, stp, (u32x2){__float_as_uint((lane & 1) ? fmaxf(S2 - S1 * dl, 0.f) : pp + dl), (unsigned)tag});
      } else {
#pragma unroll
        for (int jj = 0; jj < 4; ++jj) {
          u32x4 o = pack8(zv[0][jj], zv[1][jj]);
          if (jj >= 2) z1put(jj * 16 - 32, o);
          else st16(lin, ws + WS_ZX + (size_t)((rb * 4 + pr) * 4 + wm * 2 + jj) * 1024, o);
        }
      }
      signal1(flg + 60);
      ldef();
      if (PFB) ldpf(2 + PFA, PFB);
      SB();
      AB(7);
      if (isz2) {
        unsigned po = (unsigned)((lane >> 5) * 6144 + (lane & 31) * 16), zw = lb + L_ZS + wave * 256 + lane * 4, zr_ = lb + L_ZS + wave * 256 + g4 * 32;
        const char* pb = ws + WS_ST + (size_t)rb * 12288 + wm * 512 + 2048;
        const char* zb = ws + WS_Z2 + (size_t)((rb * 2 + wm) * 48 + pr * 12 + odd * 8 + wl * 4) * 1024;
        const char* zh = ws + WS_ZH + (size_t)((rb * 12 + pr * 3 + odd * 2 + wl) * 2 + wm) * 128;
        unsigned tg = (unsigned)tag;
        pin(po, zw, zr_, tg);
        asm volatile("" : "+s"(pb), "+s"(zb), "+s"(zh));
        auto zhint = [&]() { asm volatile("s_mov_b32 exec_hi, 0\n\tglobal_store_dword %0, %1, %2 sc1\n\ts_mov_b32 exec_hi, -1" :: "v"(lin >> 2), "v"(tg), "s"(zh) : "memory"); };
        wait_vm<1 + (PFB > 0)>();
        if (PDL) __builtin_amdgcn_s_sleep(PDL);
        AB(8);
        u32x4 sg[6];
        bool bad;
        do {
#pragma unroll
          for (int c = 0; c < 6; ++c) asm volatile("global_load_dwordx4 %0, %1, %2 offset:%3 sc1" : "=&v"(sg[c]) : "v"(po), "s"(pb), "i"(c * 1024 - 2048) : "memory");
          if (PFC) ldpf(2 + PFA + PFB, PFC);
          wait_vm<(PFC > 0)>(sg[0], sg[1], sg[2], sg[3], sg[4], sg[5]);
          unsigned an = sg[0][1] & sg[0][3], on = sg[0][1] | sg[0][3];
#pragma unroll
          for (int c = 1; c < 6; ++c) { an &= sg[c][1] & sg[c][3]; on |= sg[c][1] | sg[c][3]; }
          bad = __builtin_amdgcn_ballot_w64(((an ^ (unsigned)tag) | (on ^ (unsigned)tag)) != 0) != 0;
          --tmo;
        } while (__builtin_expect(bad && tmo > 0, 0));
        AB(9);
        if (ZHP == 0) zhint();
        auto f = [&](int c, int e) { return __uint_as_float(sg[c][e]); };
        auto sum6 = [&](int e) { return add1(add1(add1(f(0, e), f(1, e)), add1(f(2, e), f(3, e))), add1(f(4, e), f(5, e))); };
        const float nmh = sum6(0) * (-1.0f / 6.0f), mh = -nmh;
        float qa = sum6(2) * (1.0f / 64.0f), qb;
#pragma unroll
        for (int c = 0; c < 6; c += 2) { const float u = add1(f(c, 0), nmh), v = add1(f(c + 1, 0), nmh); qa = fma1(u, u, qa); qb = c ? fma1(v, v, qb) : v * v; }
        const float Mh = add1(qa, qb);
        const auto sm = __builtin_amdgcn_permlane32_swap(__float_as_uint(mh), __float_as_uint(mh), false, false);
        const auto sM = __builtin_amdgcn_permlane32_swap(__float_as_uint(Mh), __float_as_uint(Mh), false, false);
        const float dm = __uint_as_float(sm[0]) - __uint_as_float(sm[1]), mean = rbf(add1(__uint_as_float(sm[0]), __uint_as_float(sm[1])) * 0.5f);
        const float vbf = rbf(__builtin_fmaf(dm * dm, 3.0f, add1(__uint_as_float(sM[0]), __uint_as_float(sM[1]))) * (1.0f / 12.0f));
        const float rz = __builtin_amdgcn_rcpf(rbf(__builtin_amdgcn_sqrtf(rbf(vbf + 1e-5f))));
        dsw4(zw, lane < 32 ? -mean * rz : rz);
        f32x4 zm[2], zr[2];
        ds16<0>(zm[0], zr_); ds16<128>(zr[0], zr_); ds16<16>(zm[1], zr_); ds16<144>(zr[1], zr_);
        if (ZHP == 1) zhint();
        wait_lg<0>(zm[0], zm[1], zr[0], zr[1]);
#pragma unroll
        for (int jj = 0; jj < 4; ++jj) {
          u32x4 o;
#pragma unroll
          for (int q = 0; q < 4; ++q) {
            const int i = q >> 1, e = (q & 1) * 2;
            o[q] = pack2(fma1(zv[i][jj][e], zr[i][e], zm[i][e]), fma1(zv[i][jj][e + 1], zr[i][e + 1], zm[i][e + 1]));
          }
          o[3] = nohole(o[3]);
          asm volatile("global_store_dwordx4 %0, %1, %2 offset:%3 sc1\n\ts_nop 1" :: "v"(lin), "v"(o), "s"(zb), "i"(jj * 1024) : "memory");
          SB();
        }
        if (ZHP == 2) zhint();
      } else {
        wait_vm<0>();
        if (PFC) ldpf(2 + PFA + PFB, PFC);
        sttag(ws + WS_XF + (rb * 4 + pr) * 16 + wm * 8);
      }
      pin(ef);
      __builtin_amdgcn_s_setprio(0);
      SB();
      AB(10);
      waittag(ef, (unsigned)((lane & 31) * 8), ws + WS_EF + cb * 256);
    }
    const char* Bs = ws + WS_Z2 + (size_t)(wave * 16 * 48 + cb * 6) * 1024 + 2048;
    bf16x8 Bf[3][6];
    auto ldB1 = [&](int s, int j) {
      asm volatile("global_load_dwordx4 %0, %1, %2 offset:%3 " HSC : "=&v"(Bf[s % 3][j]) : "v"(lin + (unsigned)(((s + 2 * rot2) & 15) * 49152)), "s"(Bs), "i"(j * 1024 - 2048) : "memory");
    };
    f32x4 acc[4][6];
    unsigned ara[2];
#pragma unroll
    for (int ks = 0; ks < 2; ++ks) ara[ks] = ringl + l15 * 128 + (((ks * 4 + g4) ^ (l15 & 7)) << 4);
    u32x4 af[4];
    auto rdA = [&](int s, int i) {
      asm volatile("ds_read_b128 %0, %1 offset:%2" : "=&v"(af[i]) : "v"(ara[s & 1]), "i"(((s >> 1) % 3) * 8192 + i * 2048) : "memory");
    };
    signal1(flg + 28);
    flag_wait_asm(flg + 28, 4u);
    AB(11);
    {
      const unsigned ho = (unsigned)((min(lane >> 2, ZHN > 0 ? ZHN - 1 : 0) * 24 + (lane & 3)) * 128);
      const char* hb = ws + WS_ZH + (size_t)((wave * 8 * 12 + pr * 3 + odd) * 2) * 128;
      unsigned hv;
      do {
        asm volatile("global_load_dword %0, %1, %2 sc1\n\ts_waitcnt vmcnt(0)" : "=&v"(hv) : "v"(ho), "s"(hb) : "memory");
        --tmo;
      } while (ZHN > 0 && __builtin_amdgcn_ballot_w64(hv != (unsigned)tag) != 0 && tmo > 0);
    }
    if (BDL) __builtin_amdgcn_s_sleep(BDL);
    AB(12); SS(1 - 3 * XS);
#pragma unroll
    for (int j = 0; j < 6; ++j) ldB1(0, j);
#pragma unroll
    for (int j = 0; j < 6; ++j) ldB1(1, j);
#pragma unroll
    for (int j = 0; j < 6; ++j) ldB1(2, j);
    SS(2 - 3 * XS);
    flag_wait_asm(flg + 16, 4u);
#pragma unroll
    for (int i = 0; i < 4; ++i) rdA(0, i);
    asm volatile("s_waitcnt lgkmcnt(3)" : "+v"(af[0]) :: "memory");
    SB();
    unsigned bsv;
    const unsigned xa = ringl + lane * 16;
    auto putp = [&](int i, int j) { asm volatile("ds_write_b128 %0, %1 offset:%2" :: "v"(xa), "v"(acc[i][j]), "i"(i * 6144 + j * 1024) : "memory"); };
    auto ldbs = [&]() { asm volatile("global_load_ushort %0, %1, %2" : "=&v"(bsv) : "v"((unsigned)(l15 * 2)), "s"(b_sp + t0 + wave * 16) : "memory"); };
    auto step = [&](auto sc) {
      constexpr int s = decltype(sc)::value, sl = s % 3;
      constexpr int NV = s == 0 ? 12 : (s == 2 ? 28 : (s < 12 ? 20 : (s == 12 ? 12 : 13 - 6 * (s - 13))));
      constexpr int DA = s == 0 ? 2 : (((s & 1) && (s + 5) / 2 < 8) ? (s + 5) / 2 : -1);
      asm volatile("s_waitcnt vmcnt(%0) lgkmcnt(2)" :: "n"(NV) : "memory");
      pin(Bf[sl][0], Bf[sl][1], Bf[sl][2], Bf[sl][3], Bf[sl][4], Bf[sl][5], af[1]);
      SS(3 + 3 * (s - XS));
      {
        auto d3 = [&](int j) { return __builtin_bit_cast(u32x4, Bf[sl][j])[3]; };
        auto bad = [&]() { return __builtin_amdgcn_ballot_w64(umx(umx(umx(d3(0), d3(1)), d3(2)), umx(umx(d3(3), d3(4)), d3(5))) == HOLE) != 0; };
        if (__builtin_expect(bad(), 0)) {
          do {
            --tmo;
            asm volatile("s_waitcnt vmcnt(0)\n\ts_sleep 2" ::: "memory");
#pragma unroll
            for (int q = 0; q < 3; ++q)
              if (s + q < 16) reld6(Bf[(s + q) % 3], opq(lin) + (unsigned)(((s + q + 2 * rot2) & 15) * 49152), Bs);
            wait_vm<0>(Bf[sl][0], Bf[sl][1], Bf[sl][2], Bf[sl][3], Bf[sl][4], Bf[sl][5]);
          } while (bad() && tmo > 0);
        }
      }
      SS(4 + 3 * (s - XS));
      if constexpr (s == SGS) signal1(flg + 44);
      if constexpr (s == HST) signal1(flg + 32);
      SB();
#pragma unroll
      for (int i = 0; i < 4; ++i) {
        if (i == 1) wait_lg<(s < 15 ? 1 : 0)>(af[2], af[3]);
        if (i == 3 && s < 15) wait_lg<2>(af[0]);
#pragma unroll
        for (int j = 0; j < 6; ++j) {
          if constexpr (s == 0) MFMA0(acc[i][j], Bf[sl][j], af[i]); else MFMA(acc[i][j], Bf[sl][j], af[i]);
          SB();
          if constexpr (DA >= 0) { if ((i == 1 || i == 2) && j < 4) ldA1(IC(DA < 0 ? 0 : DA), (i - 1) * 4 + j); }
          if constexpr (s + 3 < 16) { if (i == 3) ldB1(s + 3, j); }
          if constexpr (s == 15) {
            if (i > 0 && j > 1) putp(i - 1, j - 2);
          }
          SB();
        }
        if constexpr (s == 15) {
          if (i > 0) {
            putp(i - 1, 4); putp(i - 1, 5);
            signal1(flg + ((wave + i) & 3) * 4);
          }
        }
        if constexpr (s < 15) rdA(s + 1, i);
        SB();
      }
      if constexpr (s == 12) ldbs();
      SS(5 + 3 * (s - XS));
      SB();
    };
    step(IC(0)); AB(13); step(IC(1)); step(IC(2)); step(IC(3)); AB(14); step(IC(4)); step(IC(5)); step(IC(6)); step(IC(7));
    step(IC(8)); step(IC(9)); step(IC(10)); step(IC(11)); step(IC(12)); step(IC(13)); step(IC(14)); step(IC(15)); AB(15);
    ldW1();
    flag_wait_asm(flg + 56, 5u);
    s16x4 z1r[6];
    {
      const int fr = 4 * g4 + (l15 >> 2), p = lane & 3;
      const unsigned za = lb + L_Z1 + fr * 128 + (((wave * 2 + (p >> 1)) ^ (fr & 7)) << 4) + (p & 1) * 8;
      z1r[0] = tr_asm<0>(za); z1r[1] = tr_asm<2048>(za); z1r[2] = tr_asm<4096>(za);
      z1r[3] = tr_asm<6144>(za); z1r[4] = tr_asm<8192>(za); z1r[5] = tr_asm<10240>(za);
    }
    flag_wait_asm(flg + wave * 4, 3u);
    asm volatile("" : "+v"(z1r[0]), "+v"(z1r[1]), "+v"(z1r[2]), "+v"(z1r[3]), "+v"(z1r[4]), "+v"(z1r[5]));
    AB(16);
    f32x4 own[6];
    {
      f32x4 pr_[3][6];
#pragma unroll
      for (int q = 0; q < 3; ++q) {
        const char* src = smem + M_RING + ((wave + 3 - q) & 3) * 24576;
#pragma unroll
        for (int j = 0; j < 6; ++j) pr_[q][j] = *(const f32x4*)(src + q * 6144 + j * 1024 + lane * 16);
      }
      asm volatile("s_nop 7" : "+v"(acc[3][0]), "+v"(acc[3][1]), "+v"(acc[3][2]), "+v"(acc[3][3]), "+v"(acc[3][4]), "+v"(acc[3][5]));
#pragma unroll
      for (int j = 0; j < 6; ++j) own[j] = (acc[3][j] + pr_[0][j]) + (pr_[1][j] + pr_[2][j]);
    }
    asm volatile("s_waitcnt vmcnt(%1)" : "+v"(bsv) : "i"(NW1 + 1) : "memory");
    SB();
    {
      const float bs = lo16(bsv);
      unsigned pk[6][2];
#pragma unroll
      for (int j = 0; j < 6; ++j) {
        const u32x2 z = __builtin_bit_cast(u32x2, z1r[j]);
#pragma unroll
        for (int h = 0; h < 2; ++h) {
          const unsigned r0 = pack2(own[j][2 * h], own[j][2 * h + 1]);
          const unsigned g = pack2(lo16(r0) + bs, hi16(r0) + bs);
          pk[j][h] = pack2(lo16(z[h]) * lo16(g), hi16(z[h]) * hi16(g));
        }
      }
      const unsigned eo = (unsigned)((l15 + 16 * (2 * (g4 & 1) + (g4 >> 1))) * 16);
#pragma unroll
      for (int q = 0; q < 3; ++q) {
        const auto s0 = __builtin_amdgcn_permlane16_swap(pk[2 * q][0], pk[2 * q + 1][0], false, false);
        const auto s1 = __builtin_amdgcn_permlane16_swap(pk[2 * q][1], pk[2 * q + 1][1], false, false);
        st16(eo, hf + (size_t)((rb * 4 + wave) * 24 + cb * 3 + q) * 1024, (u32x4){s0[0], s1[0], s0[1], nohole(s1[1])});
      }
    }
    AB(17);
    drain1(3);
  }
  unsigned hoff[4];
#pragma unroll
  for (int it = 0; it < 4; ++it) hoff[it] = (unsigned)(((rb * 4 + ((mt + 1 + it) & 3)) * 24 + 3 * wave) * 1024 + lane * 16);
  u32x4 Hq[4][3];
  auto ldh1 = [&](int it, int k) {
    asm volatile("global_load_dwordx4 %0, %1, %2 offset:%3 " HSC : "=&v"(Hq[it][k]) : "v"(hoff[it]), "s"(hf), "i"(k * 1024) : "memory");
  };
  auto ldh0 = [&](int it) {
#pragma unroll
    for (int k = 0; k < 3; ++k) ldh1(it, k);
  };
  f32x2 xf[3][2], sv[3][2];
  auto ymath = [&]() {
    char* yi = smem + P3_Y + wave * 1536;
#pragma unroll
    for (int n = 0; n < 2; ++n) *(u32x4*)(yi + (n ? 64 + (lane & 31) : lane) * 16) = yv[n];
    const unsigned ya = lds_addr(yi) + (4 * g4 + (l15 >> 2)) * 32 + 8 * (lane & 3);
    const float sm = *(const float*)(smem + L_XM + (mt * 16 + l15) * 4), sr = *(const float*)(smem + L_XR + (mt * 16 + l15) * 4);
    s16x4 yr[3] = {tr_asm<0>(ya), tr_asm<512>(ya), tr_asm<1024>(ya)};
    asm volatile("s_waitcnt lgkmcnt(0)" : "+v"(yr[0]), "+v"(yr[1]), "+v"(yr[2]) :: "memory");
    const f32x2 mean = {sm, sm}, rd = {sr, sr};
#pragma unroll
    for (int q = 0; q < 3; ++q) {
      const u32x2 yw = __builtin_bit_cast(u32x2, yr[q]);
#pragma unroll
      for (int h = 0; h < 2; ++h) {
        xf[q][h] = unpk(xv[q][h]);
        sv[q][h] = ((xf[q][h] - mean) * rd) * unpk(yw[h]);
        asm volatile("" : "+v"(sv[q][h]), "+v"(xf[q][h]));
      }
    }
    SB();
  };
  u32x2 yf;
  const unsigned yfo = (unsigned)((lane < 32 ? 32 * kh + lane : 64 + (lane & 15)) * 8);
#pragma unroll
  for (int n = NW1; n < 18; ++n) ldP1(n);
  ldtag(yf, yfo, ws + WS_YF + d0 * 8);
  SB();
  asm volatile("s_waitcnt vmcnt(%0)" :: "i"(19 - NW1) : "memory");
  waittag(ef, 0u, ws + WS_LF + (rb * 8 + wave) * 8);
  AB(18);
  signal1(flg + 48 + 4 * kh);
  flag_wait_asm(flg + 48, 4u);
  AB(19);
  __builtin_amdgcn_s_setprio(3);
  ldh0(0);
  __builtin_amdgcn_s_setprio(2);
  ldh0(1);
  __builtin_amdgcn_s_setprio(0);
  SB();
  flag_wait_asm(flg + 40, kh ? 4u : 0u);
  char* const px = smem + (kh ? P3_C + mt * 18432 : P3_G + mt * 17408) + lane * 16;
  f32x4 ac3[5], o[3];
  auto ldy = [&]() {
#pragma unroll
    for (int n = 0; n < 2; ++n) asm volatile("global_load_dwordx4 %0, %1, %2 sc1" : "=&v"(yv[n]) : "v"(yoff(n)), "s"(yb) : "memory");
  };
  auto relh = [&](int it) {
    asm volatile("s_sleep 2\n\tglobal_load_dwordx4 %0, %3, %4 sc1\n\tglobal_load_dwordx4 %1, %3, %4 offset:1024 sc1\n\t"
                 "global_load_dwordx4 %2, %3, %4 offset:2048 sc1\n\ts_waitcnt vmcnt(0)"
                 : "+v"(Hq[it][0]), "+v"(Hq[it][1]), "+v"(Hq[it][2]) : "v"(hoff[it]), "s"(hf) : "memory");
  };
  auto comp = [&](auto itc) {
    constexpr int it = decltype(itc)::value;
    constexpr int NW = 3 * ((it + 2 < 4 ? it + 2 : 4) - it - 1) + (it >= 2 ? 2 : 0);
    wait_vm<NW>(Hq[it][0], Hq[it][1], Hq[it][2]);
    if constexpr (it == 0) pin(Wq[0], Wq[1], Wq[2], Wq[3], Wq[4], Wq[5], Wq[6], Wq[7], Wq[8], Wq[9], Wq[10], Wq[11], Wq[12], Wq[13], Wq[14], xv[0], xv[1], xv[2], yf);
    auto bad = [&]() { return __builtin_amdgcn_ballot_w64(umx(umx(Hq[it][0][3], Hq[it][1][3]), Hq[it][2][3]) == HOLE) != 0; };
    if (__builtin_expect(bad(), 0)) {
      do {
        --tmo;
        relh(it);
      } while (bad() && tmo > 0);
    }
    SB();
#pragma unroll
    for (int k = 0; k < 3; ++k) {
#pragma unroll
      for (int j = 0; j < 5; ++j) {
        ac3[j] = __builtin_amdgcn_mfma_f32_16x16x32_bf16(Wq[k * 5 + j], __builtin_bit_cast(bf16x8, Hq[it][k]), k ? ac3[j] : (f32x4){0.f, 0.f, 0.f, 0.f}, 0, 0, 0);
        if constexpr (it + 2 < 4) { if (k == 0 && j < 3) { SB(); ldh1(it + 2 < 4 ? it + 2 : 0, j); SB(); } }
        if constexpr (it == 1) {
          if (k == 1 && j == 0) {
            SB();
            ldy();
            SB();
          }
        }
      }
    }
    SB();
    if constexpr (it < 3) {
#pragma unroll
      for (int j = 0; j < 5; ++j) *(f32x4*)(px + (it * 5 + j) * 1024) = ac3[j];
      signal1(flg + 64 + kh * 16 + ((mt + 1 + it) & 3) * 4);
    }
    SB();
  };
  auto gather = [&](auto cc) {
    constexpr int cv = decltype(cc)::value;
    flag_wait_asm(flg + 64 + cv * 16 + mt * 4, cv ? 5u : 3u);
#pragma unroll
    for (int it = 0; it < 3 + cv; ++it) {
      const int ms = (mt - 1 - it) & 3;
      const char* ps = smem + ((it < 3 ? cv : !kh) ? P3_C + ms * 18432 : P3_G + ms * 17408) + lane * 16;
      const char* p0 = ps + (it < 3 ? it * 5 + 2 * kh : 15) * 1024;
      const f32x4 a0 = *(const f32x4*)p0, a1 = *(const f32x4*)(p0 + 1024);
      if (!cv && it == 0) { o[0] = a0; o[1] = a1; } else { o[0] += a0; o[1] += a1; }
    }
    if (!kh) {
#pragma unroll
      for (int it = 0; it < 3 + cv; ++it) {
        const int ms = (mt - 1 - it) & 3;
        const char* ps = smem + ((it < 3 ? cv : 1) ? P3_C + ms * 18432 : P3_G + ms * 17408) + lane * 16;
        o[2] += *(const f32x4*)(ps + (it < 3 ? it * 5 + 4 : 17) * 1024);
      }
    }
    SB();
  };
  o[2] = (f32x4){0.f, 0.f, 0.f, 0.f};
  comp(IC(0)); AB(20); comp(IC(1)); comp(IC(2));
  AB(21);
  gather(IC(0));
  comp(IC(3));
  AB(22);
  {
    *(f32x4*)(px + 15 * 1024) = kh ? ac3[0] : ac3[2];
    *(f32x4*)(px + 16 * 1024) = kh ? ac3[1] : ac3[3];
    if (kh) *(f32x4*)(px + 17 * 1024) = ac3[4];
    signal1(flg + 80 + mt * 4);
    SB();
    o[0] += kh ? ac3[2] : ac3[0]; o[1] += kh ? ac3[3] : ac3[1]; o[2] += ac3[4];
    asm volatile("s_waitcnt vmcnt(0)" : "+v"(yv[0]), "+v"(yv[1]) :: "memory");
    if (__builtin_expect(notag(yf), 0)) {
      waittag(yf, yfo, ws + WS_YF + d0 * 8);
      ldy();
      wait_vm<0>(yv[0], yv[1]);
    }
    ymath();
    AB(23);
    gather(IC(1));
    auto fin = [&](const f32x4& o4, int q, unsigned (&pk)[2]) {
#pragma unroll
      for (int h = 0; h < 2; ++h) {
        const f32x2 o = {o4[2 * h], o4[2 * h + 1]};
        const f32x2 a = o * (f32x2){-1.4426950f, -1.4426950f};
        const f32x2 e = (f32x2){__builtin_amdgcn_exp2f(a[0]), __builtin_amdgcn_exp2f(a[1])} + (f32x2){1.0f, 1.0f};
        const f32x2 r = {__builtin_amdgcn_rcpf(e[0]), __builtin_amdgcn_rcpf(e[1])};
        pk[h] = pk2(__builtin_elementwise_fma(o * r, sv[q][h], xf[q][h]));
      }
    };
    unsigned pa[2], pb[2];
    fin(o[0], 0, pa);
    fin(o[1], 1, pb);
    const auto s0 = __builtin_amdgcn_permlane16_swap(pa[0], pb[0], false, false);
    const auto s1 = __builtin_amdgcn_permlane16_swap(pa[1], pb[1], false, false);
    *(u32x4*)(out + xo + 32 * kh + 16 * (g4 & 1) + 8 * (g4 >> 1)) = (u32x4){s0[0], s1[0], s0[1], s1[1]};
    if (!kh) {
      fin(o[2], 2, pa);
      *(u32x2*)(out + xo + 64 + 4 * g4) = (u32x2){pa[0], pa[1]};
    }
  }
  if (b == 0 && tid == 0) *(u64*)(ws + WS_EP) = tag;
  AB(24);
  AB_OUT();
}

extern "C" __global__ void k_end() {}

extern "C" void kernel_launch(void* const* d_in, const int* in_sizes, int n_in,
                              void* d_out, int out_size, void* d_ws, size_t ws_size,
                              hipStream_t stream) {
  (void)hipFuncSetAttribute((const void*)k_f, hipFuncAttributeMaxDynamicSharedMemorySize, M_LDS);
  k_f<<<256, 512, M_LDS, stream>>>((const u16*)d_in[0], (const u16*)d_in[1], (const u16*)d_in[2], (const u16*)d_in[3], (const u16*)d_in[4],
                                   (const u16*)d_in[5], (const u16*)d_in[7], (const u16*)d_in[8], (const u16*)d_in[9], (const u16*)d_in[10],
                                   (u16*)d_out, (char*)d_ws);
}
```

```cpp
#include <hip/hip_runtime.h>
#include <hip/hip_bf16.h>
#include <stdint.h>
#include <type_traits>

typedef unsigned short u16;
typedef unsigned long long u64;
typedef __attribute__((ext_vector_type(8))) short bf16x8;
typedef __attribute__((ext_vector_type(4))) short s16x4;
typedef __attribute__((ext_vector_type(4))) float f32x4;
typedef __attribute__((ext_vector_type(2))) float f32x2;
typedef __attribute__((ext_vector_type(4))) unsigned int u32x4;
typedef __attribute__((ext_vector_type(2))) unsigned int u32x2;
typedef __attribute__((ext_vector_type(2))) __bf16 bf16x2n;

#define LDS_AS __attribute__((address_space(3)))
#define IC(n) std::integral_constant<int, (n)>{}
#define SB() __builtin_amdgcn_sched_barrier(0)

#define AB_DECL do {} while (0)
#define AB(i) do {} while (0)
#define AB_OUT() do {} while (0)
#define CB(i) do {} while (0)
#define SS(i) do {} while (0)
constexpr int S_ = 2048, D_ = 640, FH_ = 768, F2_ = 1536;
constexpr unsigned HOLE = 0xFFFFFFFFu;

constexpr size_t WS_EP = 0;
constexpr size_t WS_EF = 4096;
constexpr size_t WS_LF = 22528;
constexpr size_t WS_YF = 14336;
constexpr size_t WS_XF = 20480;
constexpr size_t WS_ST = 32768;
constexpr size_t WS_Z2 = WS_ST + 32 * 12 * 1024;
constexpr size_t WS_H = WS_Z2 + 64 * 48 * 1024;
constexpr size_t WS_ZX = WS_H + 128 * 24 * 1024;
constexpr size_t WS_KV = WS_ZX + 32 * 4 * 4096;
constexpr size_t WS_Y = WS_KV + D_ * S_ * 2;
constexpr size_t WS_WO = WS_Y + D_ * S_ * 2;

__device__ __forceinline__ float bf2f(u16 v) { return __uint_as_float(((unsigned)v) << 16); }
__device__ __forceinline__ unsigned pack2(float lo, float hi) {
  f32x2 v = {lo, hi};
  bf16x2n r = __builtin_convertvector(v, bf16x2n);
  return __builtin_bit_cast(unsigned, r);
}
__device__ __forceinline__ float nv(float v) { asm("" : "+v"(v)); return v; }
__device__ __forceinline__ float fma1(float a, float b, float c) { float r; asm("v_fma_f32 %0, %1, %2, %3" : "=v"(r) : "v"(a), "v"(b), "v"(c)); return r; }
__device__ __forceinline__ float add1(float a, float b) { float r; asm("v_add_f32 %0, %1, %2" : "=v"(r) : "v"(a), "v"(b)); return r; }
__device__ __forceinline__ float mul1(float a, float b) { float r; asm("v_mul_f32 %0, %1, %2" : "=v"(r) : "v"(a), "v"(b)); return r; }
__device__ __forceinline__ unsigned pack2s(float lo, float hi) { return pack2(nv(lo), nv(hi)); }
__device__ __forceinline__ float lo16(unsigned w) { return __uint_as_float(w << 16); }
__device__ __forceinline__ float hi16(unsigned w) { return __uint_as_float(w & 0xFFFF0000u); }
__device__ __forceinline__ float rbf(float f) { return lo16(pack2(f, f)); }
__device__ __forceinline__ f32x2 unpk(unsigned w) { f32x2 r = {lo16(w), hi16(w)}; return r; }
__device__ __forceinline__ unsigned pk2(f32x2 v) { return pack2(v[0], v[1]); }
__device__ __forceinline__ unsigned umx(unsigned a, unsigned b) { return a > b ? a : b; }
__device__ __forceinline__ unsigned nohole(unsigned a) { return a < HOLE - 1 ? a : HOLE - 1; }
__device__ __forceinline__ f32x2 gelu2(f32x2 x) {
  const f32x2 c1 = {-2.3022082f, -2.3022082f}, c2 = {-0.10294324f, -0.10294324f};
  const f32x2 a = x * __builtin_elementwise_fma(x * x, c2, c1);
  f32x2 e = {__builtin_amdgcn_exp2f(a[0]), __builtin_amdgcn_exp2f(a[1])};
  e = e + (f32x2){1.0f, 1.0f};
  const f32x2 r = {__builtin_amdgcn_rcpf(e[0]), __builtin_amdgcn_rcpf(e[1])};
  return x * r;
}
__device__ __forceinline__ float gelu1(float x) {
  const float a = x * __builtin_fmaf(x * x, -0.10294324f, -2.3022082f);
  return x * __builtin_amdgcn_rcpf(__builtin_amdgcn_exp2f(a) + 1.0f);
}
template <int OFF>
__device__ __forceinline__ s16x4 tr_asm(unsigned a) {
  s16x4 r;
  asm volatile("ds_read_b64_tr_b16 %0, %1 offset:%2" : "=&v"(r) : "v"(a), "i"(OFF) : "memory");
  return r;
}
__device__ __forceinline__ bf16x8 join8(s16x4 lo, s16x4 hi) {
  bf16x8 r;
  r[0] = lo[0]; r[1] = lo[1]; r[2] = lo[2]; r[3] = lo[3];
  r[4] = hi[0]; r[5] = hi[1]; r[6] = hi[2]; r[7] = hi[3];
  return r;
}
__device__ __forceinline__ unsigned lds_addr(const void* p) { return (unsigned)(uintptr_t)(LDS_AS const char*)p; }
#define WAIT_LGKM0() asm volatile("s_waitcnt lgkmcnt(0)" ::: "memory")
__device__ __forceinline__ void flag_wait_asm(unsigned faddr, unsigned target) {
  unsigned v, sv;
  asm volatile(".Lfw_%=:\n\tds_read_b32 %0, %2\n\ts_waitcnt lgkmcnt(0)\n\tv_readfirstlane_b32 %1, %0\n\ts_nop 0\n\t"
               "s_cmp_ge_u32 %1, %3\n\ts_cbranch_scc1 .Lfd_%=\n\ts_sleep 1\n\ts_branch .Lfw_%=\n\t.Lfd_%=:"
               : "=&v"(v), "=&s"(sv) : "v"(faddr), "s"(target) : "memory", "scc");
}
__device__ __forceinline__ void flag_wait4(unsigned faddr) {
  u32x4 v;
  asm volatile("ds_read_b128 %0, %1\n\ts_waitcnt lgkmcnt(0)" : "=&v"(v) : "v"(faddr) : "memory");
  while (__builtin_amdgcn_readfirstlane(v[0] & v[1] & v[2] & v[3]) == 0) asm volatile("s_sleep 1\n\tds_read_b128 %0, %1\n\ts_waitcnt lgkmcnt(0)" : "=&v"(v) : "v"(faddr) : "memory");
}
__device__ __forceinline__ void signal1(unsigned a, unsigned one = 1u) {
  asm volatile("s_mov_b64 exec, 1\n\tds_add_u32 %0, %1\n\ts_mov_b64 exec, -1" :: "v"(a), "v"(one) : "memory");
}
__device__ __forceinline__ void dma16(unsigned vo, const void* sb, unsigned lds) {
  asm volatile("s_mov_b32 m0, %2\n\ts_nop 0\n\tglobal_load_lds_dwordx4 %0, %1" :: "v"(vo), "s"(sb), "s"(lds) : "memory");
}
template <int LO, int GO> __device__ __forceinline__ void dma32s(unsigned vo0, unsigned vo1, const void* sb, unsigned lds) {
  asm volatile("s_add_u32 m0, %5, %6\n\ts_add_u32 vcc_lo, %2, %4\n\ts_addc_u32 vcc_hi, %3, 0\n\t"
               "global_load_lds_dwordx4 %0, vcc\n\tglobal_load_lds_dwordx4 %1, vcc offset:1024"
               :: "v"(vo0), "v"(vo1), "s"((unsigned)(uintptr_t)sb), "s"((unsigned)((uintptr_t)sb >> 32)), "i"(GO), "s"(lds), "i"(LO) : "memory", "scc", "vcc");
}
template <int LO, int GO, int WRAP, int LIM> __device__ __forceinline__ void dma32r(unsigned vo0, unsigned vo1, const void* sb, unsigned lds, int rot) {
  const char* s2 = (const char*)sb - WRAP;
  asm volatile("s_add_u32 m0, %5, %6\n\ts_cmp_ge_u32 %7, %8\n\ts_cselect_b64 vcc, %3, %2\n\ts_add_u32 vcc_lo, vcc_lo, %4\n\ts_addc_u32 vcc_hi, vcc_hi, 0\n\t"
               "global_load_lds_dwordx4 %0, vcc\n\tglobal_load_lds_dwordx4 %1, vcc offset:1024"
               :: "v"(vo0), "v"(vo1), "s"(sb), "s"(s2), "i"(GO), "s"(lds), "i"(LO), "s"(rot), "i"(LIM) : "memory", "scc", "vcc");
}
__device__ __forceinline__ void dma16o(unsigned vo, const void* sb, int go, unsigned lds, int lo) {
  asm volatile("s_add_u32 m0, %3, %4\n\ts_nop 0\n\tglobal_load_lds_dwordx4 %0, %1 offset:%2" :: "v"(vo), "s"(sb), "i"(go), "s"(lds), "i"(lo - go) : "memory", "scc");
}
__device__ __forceinline__ void st16(unsigned vo, const void* sb, u32x4 v) {
  asm volatile("global_store_dwordx4 %0, %1, %2 sc1\n\ts_nop 1" :: "v"(vo), "v"(v), "s"(sb) : "memory");
}
__device__ __forceinline__ void touch(unsigned vo, const void* sb, unsigned lds) {
  asm volatile("s_mov_b32 m0, %2\n\ts_nop 0\n\tglobal_load_lds_dword %0, %1" :: "v"(vo), "s"(sb), "s"(lds) : "memory");
}
__device__ __forceinline__ void st8(unsigned vo, const void* sb, u32x2 v) { asm volatile("global_store_dwordx2 %0, %1, %2 sc1" :: "v"(vo), "v"(v), "s"(sb) : "memory"); }
template <class A, class B> __device__ __forceinline__ void MFMA(f32x4& acc, const A& a, const B& b) {
  asm volatile("v_mfma_f32_16x16x32_bf16 %0, %1, %2, %0" : "+v"(acc) : "v"(a), "v"(b));
}
template <class A, class B> __device__ __forceinline__ void MFMA0(f32x4& acc, const A& a, const B& b) {
  asm volatile("v_mfma_f32_16x16x32_bf16 %0, %1, %2, 0" : "=&v"(acc) : "v"(a), "v"(b));
}
template <bool Z, class A, class B> __device__ __forceinline__ void MFMAW(int n, f32x4& acc, const A& a, const B& b) {
  if (n < 0) { if (Z) MFMA0(acc, a, b); else MFMA(acc, a, b); }
  else if (Z) asm volatile("s_waitcnt lgkmcnt(%3)\n\tv_mfma_f32_16x16x32_bf16 %0, %1, %2, 0" : "=&v"(acc) : "v"(a), "v"(b), "i"(n));
  else asm volatile("s_waitcnt lgkmcnt(%3)\n\tv_mfma_f32_16x16x32_bf16 %0, %1, %2, %0" : "+v"(acc) : "v"(a), "v"(b), "i"(n));
}
__device__ __forceinline__ float sel4(float a, float b, float c, float d, u64 m1, u64 m2) {
  float r, t;
  asm volatile("v_cndmask_b32_e64 %0, %2, %3, %6\n\tv_cndmask_b32_e64 %1, %4, %5, %6\n\tv_cndmask_b32_e64 %0, %0, %1, %7"
               : "=&v"(r), "=&v"(t) : "v"(a), "v"(b), "v"(c), "v"(d), "s"(m1), "s"(m2));
  return r;
}
template <class T> __device__ __forceinline__ void pin1(T& r) { asm volatile("" : "+v"(r)); }
template <class T> __device__ __forceinline__ T opq(T r) { asm volatile("" : "+v"(r)); return r; }
template <class... T> __device__ __forceinline__ void pin(T&... r) { (pin1(r), ...); }
template <int N, class... T> __device__ __forceinline__ void wait_vm(T&... r) { asm volatile("s_waitcnt vmcnt(%0)" :: "n"(N) : "memory"); (pin1(r), ...); }
template <int N, class... T> __device__ __forceinline__ void wait_lg(T&... r) { asm volatile("s_waitcnt lgkmcnt(%0)" :: "n"(N) : "memory"); (pin1(r), ...); }
template <int OFF, class T> __device__ __forceinline__ void ds16(T& d, unsigned a) {
  asm volatile("ds_read_b128 %0, %1 offset:%2" : "=&v"(d) : "v"(a), "i"(OFF) : "memory");
}
template <int OFF> __device__ __forceinline__ void dsw16(unsigned a, const f32x4& v) {
  asm volatile("ds_write_b128 %0, %1 offset:%2" :: "v"(a), "v"(v), "i"(OFF) : "memory");
}
__device__ __forceinline__ void dsw4(unsigned a, float v) { asm volatile("ds_write_b32 %0, %1" :: "v"(a), "v"(v) : "memory"); }
#define HSC ""
template <int OFF, class T> __device__ __forceinline__ void ld16h(T& d, unsigned vo, const void* sb) {
  asm volatile("global_load_dwordx4 %0, %1, %2 offset:%3 " HSC : "=&v"(d) : "v"(vo), "s"(sb), "i"(OFF) : "memory");
}
template <class T> __device__ __forceinline__ void reld6(T (&B)[6], unsigned vo, const void* sb) {
  asm volatile("global_load_dwordx4 %0, %6, %7 offset:-2048 sc1\n\tglobal_load_dwordx4 %1, %6, %7 offset:-1024 sc1\n\t"
               "global_load_dwordx4 %2, %6, %7 sc1\n\tglobal_load_dwordx4 %3, %6, %7 offset:1024 sc1\n\t"
               "global_load_dwordx4 %4, %6, %7 offset:2048 sc1\n\tglobal_load_dwordx4 %5, %6, %7 offset:3072 sc1"
               : "+v"(B[0]), "+v"(B[1]), "+v"(B[2]), "+v"(B[3]), "+v"(B[4]), "+v"(B[5]) : "v"(vo), "s"(sb) : "memory");
}

constexpr int L_FLAG = 0;
constexpr int L_DUM = 256;
constexpr int L_XM = 512;
constexpr int L_XR = 768;
constexpr int L_CS = 1024;
constexpr int L_ZS = 2048;
constexpr int L_CS2 = 3072;
constexpr int L_Z1 = 4096;
constexpr int L_RAW = L_Z1;
constexpr int L_SLOT = 16384;
constexpr int L_KV = 512, KV_P = 3104, L_WO = L_KV + 8 * KV_P;
static_assert(L_WO % 16 == 0 && L_WO + 4 * 1024 <= L_SLOT + 2 * 8192, "staging");
constexpr int L_P2 = L_SLOT + 12 * 8192;
constexpr int CV_C = 4608, CV_CS = 4160, CV_BUF = CV_C + 2 * CV_CS;
constexpr int M_CV = L_SLOT;
constexpr int M_RING = L_SLOT + 6 * 8192;
constexpr int P3_C = L_SLOT;
constexpr int P3_G = P3_C + 72 * 1024;
constexpr int P3_Y = L_Z1;
constexpr int M_LDS = 163840;
static_assert(3 * CV_BUF + 8192 <= 6 * 8192 && P3_G + 68 * 1024 <= M_LDS && M_RING <= P3_G, "LDS map");

enum { UX, UA, UB, UV };
struct Unit { int kind, idx, need, free, slot; };
#define SCHED gugugugwBuluouk_uuxuu_uuxuxu_uuxuxuu_Euuuuh_vuumumuh_vuumumuhF_vuvumum_DRvuuUHmmmmSptt_VhhhttttttJItttttttt
#define STR_(x) #x
#define STR(x) STR_(x)
constexpr char SCH[] = STR(SCHED);
constexpr int NSCH = sizeof(SCH) - 1;
constexpr int XNEED[10] = {1, 2, 2, 3, 3, 4, 4, 5, 5, 6};
constexpr int VNEED[5] = {5, 6, 7, 7, 8};
constexpr int XSLOT[10] = {17, 16, 15, 13, 10, 14, 11, 17, 9, 16}, WSLOT[10] = {6, 2, 4, 0, 6, 12, 10, 4, 0, 8}, VSLOT[10] = {8, 15, 3, 2, 14, 17, 16, 15, 7, 6}, XW = 6;
constexpr int cnt(char c, int e = NSCH) { int n = 0; for (int i = 0; i < e; ++i) n += SCH[i] == c || (c == 'u' && SCH[i] == 'g'); return n; }
constexpr int pos(char c, int k = 0) { for (int i = 0; i < NSCH; ++i) if (SCH[i] == c && k-- == 0) return i; return NSCH; }
constexpr Unit unit(int n) {
  for (int b = 1; b < 11; ++b) {
    for (int c = 0; c < 10; ++c) if (XNEED[c] == b && n-- == 0) return {UX, c, b, b + 1, XSLOT[c]};
    for (int c = 0; c < 5; ++c) if (VNEED[c] == b && n-- == 0) return {UV, c, b, b + 1, VSLOT[c]};
    if (n < 2) return {n ? UB : UA, b - 1, b < 8 ? b : 8, b < 7 ? b + 2 : 9, WSLOT[b - 1] + n};
    n -= 2;
  }
  return {UV, n + 5, 10, 99, VSLOT[n + 5]};
}
constexpr int FLAGB = cnt('_', pos('D')), GZ = cnt('_', pos('E'));
constexpr int nvm(char c) { return c == 'u' ? 2 : (c == 'H' ? 5 : (c == 'U' ? 4 : (c == 'F' || c == 'h' || c == 't' || c == 'w'))); }
constexpr int behind(int g, int e, char w) {
  const int b = cnt('_', e) + 1;
  int n = 0;
  for (int i = 0; i < e; ++i) {
    const char c = SCH[i];
    if (g == 0) { if (c == 'g') n = w == '_' && unit(cnt('u', i)).need <= b ? 0 : n + 2; if (c == 'B') n += 2; continue; }
    n = (w == '_' ? (c == 'u' && unit(cnt('u', i)).need <= b) || (c == 'h' && b == FLAGB) || (c == 'k' && b == GZ) : c == w) ? 0 : n + nvm(c);
  }
  return g == 0 && b >= GZ ? 0 : n;
}
constexpr bool plan_ok() {
  if (cnt('u') != 40 || cnt('_') != 9 || cnt('x') != 5 || cnt('h') != 6 || cnt('t') != 16 || cnt('I') * cnt('J') * cnt('p') != 1 || cnt('v') != 5 || cnt('m') != 10) return false;
  if (cnt('B') * cnt('D') * cnt('E') * cnt('F') * cnt('l') * cnt('o') * cnt('k') * cnt('H') * cnt('U') * cnt('V') * cnt('R') * cnt('S') != 1) return false;
  if (pos('l') < pos('B') || pos('o') < pos('B') || pos('k') < pos('l') || pos('k') > pos('_') || pos('o') > pos('_') || cnt('g', pos('B')) != cnt('g')) return false;
  if (cnt('_', pos('h', 2)) >= FLAGB || GZ < 3 || GZ > 6 || FLAGB < GZ || FLAGB > 9 || pos('o') > pos('k') || pos('H') > pos('I') || pos('F') > pos('U')) return false;
  if (cnt('_', pos('R')) != 8 || cnt('_', pos('S')) != 8 || pos('S') < pos('R') || cnt('_', pos('I')) != 9 || pos('J') < pos('U') || cnt('_', pos('J')) != 9) return false;
  if (cnt('_', pos('V')) != 9 || cnt('u', pos('V')) < 40) return false;
  for (int c = 0; c < 5; ++c) if (cnt('_', pos('x', c)) != XNEED[c] || cnt('_', pos('v', c)) != VNEED[c] || pos('m', 2 * c) < pos('v', c)) return false;
  if (cnt('_', pos('p')) != 8 || pos('p') < pos('m', 9)) return false;
  int fr[18] = {1, 1};
  for (int i = 0; i < NSCH; ++i) if (SCH[i] == 'u' || SCH[i] == 'g') {
    const Unit u = unit(cnt('u', i));
    const int p = cnt('_', i);
    if (p < fr[u.slot] || p >= u.need) return false;
    fr[u.slot] = u.free;
  }
  return true;
}
static_assert(plan_ok(), "phase A plan");
template <int I, int E, class F> __device__ __forceinline__ void sfor(F&& f) { if constexpr (I < E) { f(IC(I)); sfor<I + 1, E>(f); } }

__device__ __forceinline__ void conv_core(unsigned buf, int lane, unsigned Dw, f32x4 (&ac)[8]) {
  const int l15 = lane & 15, g4 = lane >> 4;
  const int c = (l15 + 1) & 1;
  unsigned aa[4];
#pragma unroll
  for (int k = 0; k < 4; ++k) aa[k] = buf + CV_C + c * CV_CS + (2031 - l15 + 8 * g4 - c) * 2 - 512 - 1024 * k;
  unsigned bb = buf + 512 + 32 * l15 - 32 + 16 * g4;
  u32x2 A[2][8][2];
  u32x4 B[2][8];
  constexpr int TT[36] = {7, 6, 5, 4, 7, 6, 5, 4, 7, 6, 5, 3, 7, 6, 5, 4, 7, 6, 3, 2, 7, 6, 5, 4, 7, 3, 2, 1, 7, 6, 5, 4, 3, 2, 1, 0};
  constexpr int AA[36] = {0, 0, 0, 0, 1, 1, 1, 1, 2, 2, 2, 0, 3, 3, 3, 2, 4, 4, 1, 0, 5, 5, 4, 3, 6, 2, 1, 0, 7, 6, 5, 4, 3, 2, 1, 0};
  auto ld1 = [&](int p, int n) {
    if (n < 16) {
      const int a = n >> 1, o = ((a & 1) ? 0 : 128) + 2 * (n & 1);
      asm volatile("ds_read2_b32 %0, %1 offset0:%2 offset1:%3" : "=&v"(A[p][(n >> 1) & 7][n & 1]) : "v"(aa[(a >> 1) & 3]), "i"(o), "i"(o + 1) : "memory");
    } else
      asm volatile("ds_read_b128 %0, %1 offset:%2" : "=&v"(B[p][n & 7]) : "v"(bb), "i"(512 * (n & 7)) : "memory");
  };
  auto next = [&](int dl) {
#pragma unroll
    for (int k = 0; k < 4; ++k) aa[k] += dl;
    bb += dl;
    SB();
  };
  auto land = [&](int p) {
    asm volatile("s_waitcnt lgkmcnt(0)" : "+v"(A[p][0][0]), "+v"(A[p][0][1]), "+v"(A[p][1][0]), "+v"(A[p][1][1]), "+v"(A[p][2][0]), "+v"(A[p][2][1]),
                 "+v"(A[p][3][0]), "+v"(A[p][3][1]), "+v"(A[p][4][0]), "+v"(A[p][4][1]), "+v"(A[p][5][0]), "+v"(A[p][5][1]), "+v"(A[p][6][0]), "+v"(A[p][6][1]),
                 "+v"(A[p][7][0]), "+v"(A[p][7][1]), "+v"(B[p][0]), "+v"(B[p][1]), "+v"(B[p][2]), "+v"(B[p][3]), "+v"(B[p][4]), "+v"(B[p][5]),
                 "+v"(B[p][6]), "+v"(B[p][7]) :: "memory");
    SB();
  };
  auto mm = [&](int p) {
#pragma unroll
    for (int n = 0; n < 36; ++n) {
      const int t = TT[n], a = AA[n];
      const u32x4 av = {A[p][a][0][0], A[p][a][0][1], A[p][a][1][0], A[p][a][1][1]};
      MFMA(ac[t], av, B[p][t - a]);
      if (n < 24) ld1(p ^ 1, n);
      SB();
    }
    land(p ^ 1);
  };
#pragma unroll
  for (int n = 0; n < 24; ++n) ld1(0, n);
  u32x4 ad;
#pragma unroll
  for (int e = 0; e < 4; ++e) ad[e] = lane == 32 + 2 * e + (lane & 1) + (lane & 8) * 3 ? Dw << (16 * (lane & 1)) : 0u;
  pin(ad);
  land(0);
#pragma unroll
  for (int t = 0; t < 8; ++t) MFMA0(ac[t], ad, B[0][t]);
#pragma unroll 1
  for (int r = 0; r < 8; r += 2) {
    next(-64);
    mm(0);
    next(r < 6 ? -64 : 0);
    mm(1);
  }
  asm volatile("s_nop 15" : "+v"(ac[0]), "+v"(ac[1]), "+v"(ac[2]), "+v"(ac[3]), "+v"(ac[4]), "+v"(ac[5]), "+v"(ac[6]), "+v"(ac[7]));
}

#define ROTA 3
#define RA1 1
#define RA2 0
#define RA3 0
#define RB1 0
#define RB2 4
#define RB3 0
#define G2P 0
#define RB4 2
#define THR 24
#define RB5 2
#define CPR 1
#define STW 1
#define PN 10
#define P2P 2
#define NW1 10
#define SGS 12
#define ANUM {0, 0, 0, 0, 0, 3, 3, 2, 3, 3, 2, 0, 0, 0, 0, 0}
constexpr int cum16(const int (&a)[16], int n) { int c = 0; for (int i = 0; i < n; ++i) c += a[i]; return c; }

__global__ __launch_bounds__(512) void k_f(
    const u16* __restrict__ x, const u16* __restrict__ kin, const u16* __restrict__ vin, const u16* __restrict__ shift_k,
    const u16* __restrict__ ssm_k, const u16* __restrict__ ssm_D, const u16* __restrict__ w_in, const u16* __restrict__ w_sp,
    const u16* __restrict__ b_sp, const u16* __restrict__ w_out, u16* __restrict__ out, char* ws) {
  extern __shared__ __attribute__((aligned(16))) char smem[];
  const int tid = threadIdx.x, lane = tid & 63, wave = __builtin_amdgcn_readfirstlane(tid >> 6);
  const int b = blockIdx.x;
  const int xcd = b & 7, idx = b >> 3;
  const int rb = 8 * (xcd >> 1) + (idx >> 2), cb = 4 * (xcd & 1) + (idx & 3);
  const int t0 = rb * 64, d0 = cb * 80;
  const int g4 = lane >> 4, l15 = lane & 15;
  const unsigned lin = (unsigned)(lane * 16);
  const unsigned lb = lds_addr(smem), flg = lb + L_FLAG;
  AB_DECL;
  AB(0);
  asm volatile("" :: "s"(x), "s"(kin), "s"(vin), "s"(shift_k), "s"(ssm_k), "s"(ssm_D), "s"(w_in), "s"(w_sp), "s"(b_sp), "s"(w_out), "s"(out), "s"(ws));
  u64 tag = *(const __attribute__((address_space(4))) u64*)(uintptr_t)ws;
  int tmo = 1 << 16;
#define CWARM 1
  auto cwarm = [&]() {
    u64 pc;
    unsigned clen;
    asm volatile("s_getpc_b64 %0\n.Lpc_here_%=:\n\ts_mov_b32 %1, k_end-.Lpc_here_%=" : "=s"(pc), "=s"(clen));
    touch(min((unsigned)(idx * 1280 + min(lane, 9) * 128), clen - 4u) & ~3u, (const void*)pc, lb + L_DUM);
  };

  const int cw = wave & 3, wm = wave & 1, wl = (wave >> 1) & 1, ksb = wm ^ wl, odd = cb & 1, pr = cb >> 1;
  const int r8 = lane >> 3, c8 = lane & 7;
  unsigned voX[2], voW[2];
#pragma unroll
  for (int h = 0; h < 2; ++h) {
    voX[h] = (unsigned)((h * 8 + r8) * (D_ * 2) + ((c8 ^ (h * 4 + (r8 & 3))) << 4) - h * 1024);
    voW[h] = (unsigned)((h * 8 + r8) * (F2_ * 2) + ((c8 ^ (h * 4 + (r8 & 2))) << 4) - h * 1024);
  }
  const int rot = (RA1 * ((rb & 7) * 5 >> 2) + ROTA * (cb & 3) + RA2 * ((rb >> 3) * 5 >> 1) + RA3 * (cb >> 2)) % 10, rot2 = (RB1 * (cb & 3) + RB2 * (cb >> 2) + RB3 * (rb & 7) + RB4 * (rb >> 3) + RB5 * (cb & 1)) & 7;
  const char* gX = (const char*)x + (size_t)(t0 + cw * 16) * (D_ * 2) + rot * 128;
  const char* gW = (const char*)w_in + (size_t)(cw * 16 + rot * 64) * (F2_ * 2) + pr * 384;
  const char *gV = gW + odd * 256, *gB = gW + (odd ? 128 : FH_ * 2 + 128);
  const char* gS = (const char*)w_sp + (size_t)t0 * (S_ * 2);
  const unsigned aoff[2] = {(unsigned)(r8 * 4096 + ((c8 ^ r8) << 4)), (unsigned)(r8 * 4096 + ((c8 ^ r8) << 4) + 32768)};
  const unsigned ldw = lb + cw * 2048;
  auto issue = [&](auto nc) {
    constexpr Unit u = unit(decltype(nc)::value);
    constexpr int lo = L_SLOT + u.slot * 8192;
    if (u.kind == UX) dma32r<lo, u.idx * 128, 1280, 10 - u.idx>(voX[0], voX[1], gX, ldw, rot);
    else dma32r<lo, u.idx * (64 * F2_ * 2) + (u.kind == UA ? FH_ * 2 : 0), 640 * F2_ * 2, 10 - u.idx>(voW[0], voW[1], u.kind == UB ? gB : gV, ldw, rot);
    SB();
  };
  const int kd = d0 + min(wave, 4) * 16 + r8 * 2;
  u32x4 sw;
  {
    const int ks0 = t0 + wave * 8;
#pragma unroll
    for (int i = 0; i < 3; ++i) {
      const int id = min(i * 64 + lane, 189), r = id / 10, c = id % 10;
      const char* kp = (r < 11 ? (const char*)kin + (size_t)max(ks0 - 3 + r, 0) * (D_ * 2) : (const char*)vin + (size_t)(ks0 + r - 11) * (D_ * 2)) + cb * 160 + c * 16;
      asm volatile("s_mov_b32 m0, %1\n\ts_nop 0\n\tglobal_load_lds_dwordx4 %0, off" :: "v"(kp), "s"(lb + L_KV + wave * KV_P + i * 1024) : "memory");
    }
    if (CWARM && wave == 5) cwarm();
    SB();
  }
  auto ldsw = [&]() { asm volatile("global_load_dwordx4 %0, %1, %2" : "=&v"(sw) : "v"(kd * 8), "s"(shift_k) : "memory"); SB(); };
  unsigned kk[11], vv[8];
  u32x4 oa, ob;
  auto kvld = [&]() {
    const char* kl = smem + L_KV + c8 * KV_P + min(wave, 4) * 32 + r8 * 4;
#pragma unroll
    for (int i = 0; i < 11; ++i) kk[i] = *(const unsigned*)(kl + i * 160);
#pragma unroll
    for (int i = 0; i < 8; ++i) vv[i] = *(const unsigned*)(kl + (11 + i) * 160);
  };
  auto kvm = [&](int e0, int e1) {
    const f32x2 w0 = {lo16(sw[0]), lo16(sw[2])}, w1 = {hi16(sw[0]), hi16(sw[2])};
    const f32x2 w2 = {lo16(sw[1]), lo16(sw[3])}, w3 = {hi16(sw[1]), hi16(sw[3])};
    f32x2 kf[11], rr[8];
#pragma unroll
    for (int i = 2 * e0; i < 2 * e1 + 3; ++i) kf[i] = unpk((i < 3 && t0 + c8 == 0) ? 0u : kk[i]);
#pragma unroll
    for (int i = 2 * e0; i < 2 * e1; ++i) {
      f32x2 a0 = kf[i] * w3;
      a0 = __builtin_elementwise_fma(kf[i + 1], w2, a0);
      a0 = __builtin_elementwise_fma(kf[i + 2], w1, a0);
      a0 = __builtin_elementwise_fma(kf[i + 3], w0, a0);
      rr[i] = a0 * unpk(vv[i]);
    }
#pragma unroll
    for (int e = e0; e < e1; ++e) { oa[e] = pack2(rr[2 * e][0], rr[2 * e + 1][0]); ob[e] = pack2(rr[2 * e][1], rr[2 * e + 1][1]); }
  };
  auto kvst = [&]() {
    const unsigned ko = (unsigned)((kd * S_ + t0 + c8 * 8) * 2);
    st16(ko, ws + WS_KV, oa);
    st16(ko, ws + WS_KV + S_ * 2, ob);
    SB();
  };
  auto barrier0 = [&](int nv) {
    SB();
    asm volatile("s_waitcnt vmcnt(%0) lgkmcnt(0)\n\ts_barrier" :: "n"(nv) : "memory");
    SB();
    asm volatile("" : "+v"(sw), "+s"(tag));
    tag += 1;
    asm volatile("" : "+s"(tag));
  };
  unsigned pX[2], pW[2];
#pragma unroll
  for (int k = 0; k < 2; ++k) {
    const int q = l15 >> 2, p = lane & 3, sz = (q >> 1) | ((g4 & 1) << 1);
    pX[k] = lb + (wm * 32 + q * 8 + p) * 128 + ((((k ^ ksb) * 4 + g4) ^ (l15 & 7)) << 4);
    pW[k] = lb + ((k ^ ksb) * 32 + g4 * 8 + q) * 128 + (((sz << 1) | (p >> 1)) << 4) + (p & 1) * 8;
  }
  bf16x8 ones;
#pragma unroll
  for (int e = 0; e < 8; ++e) ones[e] = (short)0x3F80;
  f32x4 xm[2], xr[2];
  auto ldxs = [&]() {
#pragma unroll
    for (int i = 0; i < 2; ++i) {
      xm[i] = *(const f32x4*)(smem + L_XM + (wm * 32 + g4 * 8 + i * 4) * 4);
      xr[i] = *(const f32x4*)(smem + L_XR + (wm * 32 + g4 * 8 + i * 4) * 4);
    }
  };
  auto act = [&](const f32x4& a, float c, int i) {
    const f32x2 nc = {-c, -c};
    const f32x2 lo = gelu2(__builtin_elementwise_fma((f32x2){xm[i][0], xm[i][1]}, nc, (f32x2){a[0], a[1]}) * (f32x2){xr[i][0], xr[i][1]});
    const f32x2 hi = gelu2(__builtin_elementwise_fma((f32x2){xm[i][2], xm[i][3]}, nc, (f32x2){a[2], a[3]}) * (f32x2){xr[i][2], xr[i][3]});
    return (f32x4){lo[0], lo[1], hi[0], hi[1]};
  };
  auto pack8 = [&](const f32x4& a, const f32x4& c) { return (u32x4){pack2(a[0], a[1]), pack2(a[2], a[3]), pack2(c[0], c[1]), pack2(c[2], c[3])}; };
  auto z1at = [&](int f0) { return (u32x4*)(smem + L_Z1 + (f0 + l15) * 128 + (((wm * 4 + g4) ^ (l15 & 7)) << 4)); };
  auto z1put = [&](int f0, const u32x4& v) { *z1at(f0) = v; };
  f32x4 ac2[2][2], cs2[2];
  s16x4 VF[10][2][2];
  unsigned aV[2][2];
  auto rdV = [&](auto hc_, auto nc, int r) {
    constexpr int n = decltype(nc)::value, c = 5 * decltype(hc_)::value + (n >> 1), so = (VSLOT[c] - 2) * 8192, po = n < 2 ? -1 : ((VSLOT[n < 2 ? c : c - 1] - 2) * 8192) >> 16;
    const int jj = r >> 1;
    if (!(r & 1)) {
      if ((so >> 16) != po) aV[n & 1][jj] = opq((pW[n & 1] ^ (unsigned)(wl * 64 + jj * 32)) + L_SLOT + 2 * 8192 + (so & ~0xFFFF));
      VF[n][jj][0] = tr_asm<(so & 0xFFFF)>(aV[n & 1][jj]);
    } else VF[n][jj][1] = tr_asm<(so & 0xFFFF) + 512>(aV[n & 1][jj]);
    SB();
  };
  auto mm2 = [&](auto hc_, auto nc, bf16x8 (*X)[2][2], int nw, auto&& nxt) {
    constexpr int n = decltype(nc)::value, j = n >> 1, k = n & 1;
    constexpr bool z = n == 0 && !decltype(hc_)::value;
    const bf16x8 w0 = join8(VF[n][0][0], VF[n][0][1]), w1 = join8(VF[n][1][0], VF[n][1][1]);
    if (k == 0) { MFMAW<n == 0>(nw, cs2[0], ones, w0); MFMAW<n == 0>(-1, cs2[1], ones, w1); }
    MFMAW<z>(k ? nw : -1, ac2[0][0], X[j][k][0], w0); nxt(0);
    MFMAW<z>(-1, ac2[1][0], X[j][k][1], w0); nxt(1);
    MFMAW<z>(-1, ac2[0][1], X[j][k][0], w1); nxt(2);
    MFMAW<z>(-1, ac2[1][1], X[j][k][1], w1); nxt(3);
    if constexpr (n == 9) {
      pin(cs2[0], cs2[1]);
      if (g4 < 2) *(float*)(smem + L_CS2 + (wave * 32 + lane) * 4) = g4 ? cs2[1][0] : cs2[0][0];
    }
    SB();
  };
  const unsigned p2a = lb + L_P2 + cw * 4096 + lin;
  auto sttag = [&](const char* p) {
    asm volatile("s_mov_b64 exec, 1\n\tglobal_store_dwordx2 %0, %1, %2 sc1\n\ts_mov_b64 exec, -1" :: "v"(lin), "v"((u32x2){opq((unsigned)tag), opq((unsigned)(tag >> 32))}), "s"(p) : "memory");
  };
  auto ldtag = [&](u32x2& f, unsigned vo, const char* p) { asm volatile("global_load_dwordx2 %0, %1, %2 sc1" : "=&v"(f) : "v"(vo), "s"(p) : "memory"); };
  auto notag = [&](const u32x2& f) { return __builtin_amdgcn_ballot_w64(f[0] != (unsigned)tag || f[1] != (unsigned)(tag >> 32)) != 0; };
  auto waittag = [&](u32x2& f, unsigned vo, const char* p) {
    while (__builtin_expect(notag(f) && tmo > 0, 0)) {
      --tmo;
      __builtin_amdgcn_s_sleep(4);
      ldtag(f, vo, p);
      wait_vm<0>(f);
    }
  };
  u32x2 ef;

  char* const hf = ws + WS_H;
  bf16x8 Wq[15];
  const char* wg = ws + WS_WO + (size_t)(wave * 120 + cb * 5) * 1024;
  const int mt = wave & 3, kh = wave >> 2;
  const size_t xo = (size_t)(t0 + mt * 16 + l15) * D_ + d0;
  u32x2 xv[3];
  auto ldP1 = [&](int n) {
    if (n < 15)
      asm volatile("global_load_dwordx4 %0, %1, %2 offset:%3 sc1" : "=&v"(Wq[n < 15 ? n : 0]) : "v"(lin), "s"(wg + (n / 5) * 40960 + 2048), "i"((n % 5) * 1024 - 2048) : "memory");
    else {
      const int q = n < 18 ? n - 15 : 0;
      asm volatile("global_load_dwordx2 %0, %1, %2 offset:%3" : "=&v"(xv[q]) : "v"((unsigned)(l15 * D_ * 2 + 8 * g4)),
                   "s"(x + (size_t)(t0 + mt * 16) * D_ + d0 + (q < 2 ? 32 * kh : 64)), "i"(q == 1 ? 32 : 0) : "memory");
    }
  };
  u32x4 yv[2];
  auto yoff = [&](int n) {
    const int c = n ? 64 + (lane & 31) : lane, r = c >> 1;
    return (unsigned)(((r < 32 ? 32 * kh + r : 32 + r) * S_ + 8 * (c & 1)) * 2);
  };
  const char* const yb = ws + WS_Y + ((size_t)d0 * S_ + t0 + 16 * mt) * 2;
  auto ldW1 = [&]() {
    ldtag(ef, (unsigned)((lane & 3) * 8), ws + WS_LF + (rb * 8 + wave) * 32);
#pragma unroll
    for (int n = 0; n < NW1; ++n) ldP1(n);
    SB();
  };
  auto drain1 = [&](int nc) {
    SB();
    asm volatile("s_waitcnt vmcnt(%0)" :: "i"(nc) : "memory");
#pragma unroll
    for (int n = 0; n < NW1; ++n) pin(Wq[n]);
    pin(ef);
  };
  if (wave >= 4) {
    const int wid = rb * 4 + cw;
    __builtin_amdgcn_s_setprio(3);
    ldsw();
    if (wid < 120) dma16((unsigned)((lane >> 1) * (D_ * 2) + (lane & 1) * 16), w_out + (size_t)(wid / 5) * 32 * D_ + d0 + (wid % 5) * 16, lb + L_WO + cw * 1024);
    SB();
    u32x4 hole = {HOLE, HOLE, HOLE, HOLE};
    bf16x8 XF[5][2][2];
    auto rdXF = [&](auto cc) {
      constexpr int c = decltype(cc)::value, so = L_SLOT + XSLOT[c] * 8192;
#pragma unroll
      for (int k = 0; k < 2; ++k) {
        const unsigned a = pX[k] + (so & ~0x7FFF);
        ds16<(so & 0x7FFF)>(XF[c][k][0], a);
        ds16<(so & 0x7FFF) + 512>(XF[c][k][1], a);
      }
      SB();
    };
    u32x4 TA[16];
    auto ldT = [&](u32x4& r, int d, int w, int i) {
      asm volatile("global_load_dwordx4 %0, %1, %2" : "=&v"(r) : "v"(aoff[i & 1] + (unsigned)(((d + rot2) & 7) * 128)), "s"(gS + ((w + 1 + (i >> 1)) & 3) * 65536 + w * 1024) : "memory");
    };
    auto ldx = [&](int c) {
      u32x4 xv;
      asm volatile("global_load_dwordx4 %0, %1, %2\n\ts_waitcnt vmcnt(0)" : "=&v"(xv) : "v"((unsigned)(lane * (D_ * 2) + c * 16)), "s"(x + (size_t)t0 * D_) : "memory");
      return xv;
    };
    const bool docv = cw < ((rb & 1) ? 2 : 3);
    const int d = d0 + (cw < 2 ? 2 * rb + cw : 64 + (rb >> 1));
    u32x4 hc[4], uc[4];
    unsigned Dw;
    auto ldhc = [&]() {
#pragma unroll
      for (int k = 0; k < 4; ++k) asm volatile("global_load_dwordx4 %0, %1, %2 offset:%3" : "=&v"(hc[k]) : "v"(lin), "s"(ssm_k + (size_t)d * S_), "i"(k * 1024) : "memory");
      asm volatile("global_load_ushort %0, %1, %2" : "=&v"(Dw) : "v"(0u), "s"(ssm_D + d) : "memory");
    };
    unsigned rwo;
    f32x2 ra, rc;
    auto rdraw = [&]() { asm volatile("ds_read_b64 %0, %2\n\tds_read_b64 %1, %2 offset:512" : "=&v"(ra), "=&v"(rc) : "v"(rwo) : "memory"); SB(); };
    const char* efp = ws + WS_EF + cb * 256;
    char* cb_ = smem + M_CV + cw * CV_BUF;
    auto run = [&](auto ic) {
      constexpr int i = decltype(ic)::value, p = cnt('_', i) + 1;
      constexpr char c = SCH[i];
      if constexpr (c == '_') {
        CB(3 * (p - XBAR));
        asm volatile("s_waitcnt vmcnt(%0) lgkmcnt(0)" :: "n"(behind(1, i, '_')) : "memory");
        CB(3 * (p - XBAR) + 1);
        asm volatile("s_barrier" ::: "memory");
        CB(3 * (p - XBAR) + 2);
        SB();
#pragma unroll
        for (int q = 0; q < 5; ++q) if (XNEED[q] + 1 == p) pin(XF[q][0][0], XF[q][0][1], XF[q][1][0], XF[q][1][1]);
        if constexpr (p == 7) {
          rwo = lb + L_RAW + ((lane >> 3) * 4 + (lane & 3)) * 16 + (lane & 4) * 2;
          pin(rwo);
        }
        if constexpr (p == 9) __builtin_amdgcn_s_setprio(CPR);
        if constexpr (p == 2) AB(4);
        if constexpr (p == 6) AB(5);
        if constexpr (p == 9) AB(6);
      }
      if constexpr (c == 'u') issue(IC(cnt('u', i)));
      if constexpr (c == 'x') rdXF(IC(cnt('x', i)));
      if constexpr (c == 'B') {
        AB(1);
        barrier0(behind(1, i, 'B'));
        pin(ones, hole);
      }
      if constexpr (c == 'v') sfor<2 * cnt('v', i), 2 * cnt('v', i) + 2>([&](auto nc) { rdV(IC(0), nc, 0); rdV(IC(0), nc, 1); rdV(IC(0), nc, 2); rdV(IC(0), nc, 3); });
      if constexpr (c == 'm') {
        constexpr int n = cnt('m', i);
        mm2(IC(0), IC(n), XF, VNEED[n >> 1] < p - 1 || (n & 1) ? -1 : 0, [](int) {});
      }
      if constexpr (c == 'p') {
        asm volatile("s_nop 7" : "+v"(ac2[0][0]), "+v"(ac2[0][1]), "+v"(ac2[1][0]), "+v"(ac2[1][1]));
        dsw16<0>(p2a, ac2[0][0]); dsw16<1024>(p2a, ac2[1][0]); dsw16<2048>(p2a, ac2[0][1]); dsw16<3072>(p2a, ac2[1][1]);
      }
      if constexpr (c == 'w')
        touch((unsigned)(min(lane, 19) * (F2_ * 2)), (const char*)w_in + (size_t)((rb & 7) * 80 + cw * 20) * (F2_ * 2) + pr * 384 + odd * 256, lb + L_DUM);
      if constexpr (c == 'l') { if (wave == 7) kvld(); }
      if constexpr (c == 'k') { if (wave == 7) { kvm(0, 4); kvst(); } }
      if constexpr (c == 'E' || c == 'D') { if (wave == 5) sttag(ws + WS_EF + (c == 'D' ? 2048 : 0) + (cb * 32 + rb) * 8); }
      if constexpr (c == 'U') {
        wait_vm<behind(1, i, 'F')>(ef);
        waittag(ef, (unsigned)((lane & 31) * 8), efp);
#pragma unroll
        for (int k = 0; k < 4; ++k) asm volatile("global_load_dwordx4 %0, %1, %2 offset:%3 sc1" : "=&v"(uc[k]) : "v"(lin), "s"(ws + WS_KV + (size_t)d * (S_ * 2)), "i"(k * 1024) : "memory");
      }
      if constexpr (c == 'F') ldtag(ef, (unsigned)((lane & 31) * 8), efp);
      if constexpr (c == 'o') {
        if (wid < 120) {
          const unsigned ra = lb + L_WO + cw * 1024 + (8 * g4 + (l15 >> 2)) * 32 + 8 * (lane & 3);
          s16x4 lo = tr_asm<0>(ra), hi = tr_asm<128>(ra);
          wait_lg<0>(lo, hi);
          st16(lin, ws + WS_WO + (size_t)((wid / 5) * 40 + cb * 5 + wid % 5) * 1024, __builtin_bit_cast(u32x4, join8(lo, hi)));
        }
      }
      if constexpr (c == 'h') {
        constexpr int n = cnt('h', i);
        st16(lin, ws + (n < 3 ? WS_Z2 + (size_t)((rb * 2 + wl) * 48 + cb * 6 + wm * 3 + n) * 1024 : WS_H + (size_t)((rb * 4 + cw) * 24 + cb * 3 + n - 3) * 1024), hole);
      }
      if constexpr (c == 't') { constexpr int n = cnt('t', i); ldT(TA[n], 3 + (n >> 3), cw, n & 7); }
      if constexpr (c == 'H') ldhc();
      if constexpr (c == 'R') rdraw();
      if constexpr (c == 'V') {
        wait_vm<behind(1, i, '_')>();
        signal1(flg + 60);
        AB(2);
      }
      if constexpr (c == 'I') {
        wait_vm<behind(1, i, 'H')>(hc[0], hc[1], hc[2], hc[3], Dw);
        if (docv) {
          if (lane < 36) *(u32x4*)(cb_ + (lane < 32 ? lane * 16 : CV_C + ((lane >> 1) & 1) * CV_CS + 4096 + (lane & 1) * 16)) = (u32x4){0u, 0u, 0u, 0u};
#pragma unroll
          for (int k = 0; k < 4; ++k) {
            const int a = k * 64 + lane;
            const int h0 = k ? __builtin_amdgcn_readlane((int)hc[k ? k - 1 : 0][3], 63) : 0;
            const unsigned hp = (unsigned)__builtin_amdgcn_update_dpp(h0, (int)hc[k][3], 0x138, 0xF, 0xF, false);
            const unsigned w[5] = {hp, hc[k][0], hc[k][1], hc[k][2], hc[k][3]};
            u32x4 sw, mx;
#pragma unroll
            for (int q = 0; q < 4; ++q) {
              sw[q] = (w[4 - q] >> 16) | (w[4 - q] << 16);
              mx[q] = (w[4 - q] & 0xFFFFu) | (w[3 - q] & 0xFFFF0000u);
            }
            char* cp = cb_ + CV_C + (2040 - 8 * a) * 2;
            *(u32x4*)(cp) = sw;
            *(u32x4*)(cp + CV_CS) = mx;
          }
        }
      }
      if constexpr (c == 'J') {
        wait_vm<behind(1, i, 'U')>(uc[0], uc[1], uc[2], uc[3]);
        if (docv) {
#pragma unroll
          for (int k = 0; k < 4; ++k) *(u32x4*)(cb_ + 512 + (k * 64 + lane) * 16) = uc[k];
        }
      }
      if constexpr (c == 'S') {
        wait_lg<0>(ra, rc);
        if (wave == 6) {
          const float s1 = nv(ra[0]) + rc[0], fm = s1 * (1.0f / 640.0f), sm = s1 * fm;
          float fq = (ra[1] - sm) + rc[1];
          if (__builtin_expect(__builtin_amdgcn_ballot_w64(!(fq >= 1e-3f * sm)) != 0, 0)) {
            const f32x2 mm = {fm, fm};
            f32x2 q2 = {0.f, 0.f};
#pragma unroll 1
            for (int c = 0; c < 80; ++c) {
              const u32x4 xv = ldx(c);
#pragma unroll
              for (int e = 0; e < 4; ++e) { const f32x2 d = unpk(xv[e]) - mm; q2 = __builtin_elementwise_fma(d, d, q2); }
            }
            fq = q2[0] + q2[1];
          }
          const float vbf = rbf(fmaxf(fq, 0.f) * (1.0f / 640.0f));
          *(float*)(smem + L_XM + lane * 4) = rbf(fm);
          *(float*)(smem + L_XR + lane * 4) = __builtin_amdgcn_rcpf(rbf(__builtin_amdgcn_sqrtf(rbf(vbf + 1e-5f))));
        }
      }
      SB();
    };
    sfor<0, NSCH>(run);
    __builtin_amdgcn_s_setprio(0);
    AB(9);
    auto ldS = [&](auto dc, auto wc) {
      constexpr int dd = decltype(dc)::value, w = decltype(wc)::value;
      const unsigned to = (unsigned)(((dd + rot2) & 7) * 128);
      if (THR < 60) wait_vm<THR>();
      dma32s<M_RING + w * 24576 + (2 - dd) * 8192, ((w + 1) & 3) * 65536 + w * 1024>(aoff[0] + to, aoff[1] - 1024 + to, gS, lb);
      if (THR < 60) wait_vm<THR>();
      dma32s<M_RING + w * 24576 + (2 - dd) * 8192 + 2048, ((w + 2) & 3) * 65536 + w * 1024>(aoff[0] + to, aoff[1] - 1024 + to, gS, lb);
      if (THR < 60) wait_vm<THR>();
      dma32s<M_RING + w * 24576 + (2 - dd) * 8192 + 4096, ((w + 3) & 3) * 65536 + w * 1024>(aoff[0] + to, aoff[1] - 1024 + to, gS, lb);
      if (THR < 60) wait_vm<THR>();
      dma32s<M_RING + w * 24576 + (2 - dd) * 8192 + 6144, w * 65536 + w * 1024>(aoff[0] + to, aoff[1] - 1024 + to, gS, lb);
      SB();
    };
    auto epi2 = [&]() {
      flag_wait_asm(flg + 132, 4u);
#pragma unroll
      for (int jj = 0; jj < 2; ++jj) {
        u32x4* zp = z1at(odd * 32 + wl * 32 + jj * 16);
        u32x4 o = *zp;
#pragma unroll
        for (int q = 0; q < 4; ++q) o[q] = pack2s(gelu1(nv(lo16(o[q]))), gelu1(hi16(o[q])));
        *zp = o;
      }
      signal1(flg + 56);
      SB();
    };
    auto putT = [&](const u32x4* T, unsigned a, unsigned f) {
#pragma unroll
      for (int i = 0; i < 8; ++i) asm volatile("ds_write_b128 %0, %1 offset:%2" :: "v"(a), "v"(T[i]), "i"(i * 1024) : "memory");
      signal1(f);
    };
    const unsigned tx = lb + M_CV + cw * CV_BUF + lin;
    auto landT = [&]() {
      sttag(ws + WS_LF + (rb * 8 + cb) * 32 + cw * 8);
#pragma unroll
      for (int n = 0; n < 16; ++n) pin(TA[n]);
      putT(TA, tx, flg + 112 + cw * 4);
    };
    auto putT4 = [&]() {
      flag_wait_asm(flg + 96 + cw * 4, 1u);
      putT(TA + 8, lb + M_RING + cw * 24576 + 16384 + lin, flg + 112 + cw * 4);
    };
    if (docv) {
      flag_wait_asm(flg + 36, 4u);
      AB(12);
      f32x4 ac[8];
      conv_core(lds_addr(cb_), lane, Dw, ac);
      AB(13);
      wait_vm<0>();
      landT();
#pragma unroll
      for (int k = 0; k < 4; ++k) {
        u32x2 ov[2];
#pragma unroll
        for (int t = 0; t < 2; ++t)
#pragma unroll
          for (int h = 0; h < 2; ++h) ov[t][h] = pack2(ac[2 * k + t][2 * h], ac[2 * k + t][2 * h + 1]);
        const auto s0 = __builtin_amdgcn_permlane16_swap(ov[0][0], ov[1][0], false, false);
        const auto s1 = __builtin_amdgcn_permlane16_swap(ov[0][1], ov[1][1], false, false);
        st16((unsigned)((256 * (g4 & 1) + 16 * l15 + 8 * (g4 >> 1)) * 2), ws + WS_Y + (size_t)d * (S_ * 2) + k * 1024, (u32x4){s0[0], s1[0], s0[1], s1[1]});
      }
      putT4();
      wait_vm<0>();
      sttag(ws + WS_YF + d * 8);
      epi2();
    } else if (cw != 3) {
      wait_vm<0>();
      landT();
      putT4();
      epi2();
    } else {
      ldS(IC(0), IC(0)); ldS(IC(0), IC(1)); ldS(IC(0), IC(2)); ldS(IC(1), IC(1)); ldS(IC(2), IC(1));
      wait_vm<40>();
      landT();
      AB(12);
      flag_wait_asm(flg + 16, 4u);
      AB(8);
      wait_vm<24>();
      ldS(IC(0), IC(3)); ldS(IC(1), IC(0)); ldS(IC(1), IC(2)); ldS(IC(1), IC(3));
      wait_vm<24>();
      signal1(flg + 32);
      ldS(IC(2), IC(0)); ldS(IC(2), IC(2)); ldS(IC(2), IC(3));
      AB(10);
      u32x4 TB[32];
#pragma unroll
      for (int n = 0; n < 32; ++n) {
        if (n == 8) wait_vm<32>();
        if (THR < 60 && !(n & 1)) wait_vm<THR>();
        if (THR < 32 && n == THR) signal1(flg + 32);
        ldT(TB[n], 5, n >> 3, n & 7);
      }
      wait_vm<32>();
      if (THR >= 32) signal1(flg + 32);
      AB(11);
      wait_vm<0>();
#pragma unroll
      for (int n = 0; n < 32; ++n) pin(TB[n]);
      putT4();
#pragma unroll
      for (int w = 0; w < 4; ++w) {
        flag_wait_asm(flg + 96 + w * 4, 2u);
        putT(TB + 8 * w, lb + M_RING + w * 24576 + 8192 + lin, flg + 144 + w * 4);
      }
      epi2();
      if (!odd) {
        u32x4 zx[4];
        ldtag(ef, (unsigned)((lane & 1) * 8), ws + WS_XF + (rb * 4 + pr) * 16);
        wait_vm<0>(ef);
        waittag(ef, (unsigned)((lane & 1) * 8), ws + WS_XF + (rb * 4 + pr) * 16);
#pragma unroll
        for (int c = 0; c < 4; ++c)
          asm volatile("global_load_dwordx4 %0, %1, %2 offset:%3 sc1" : "=&v"(zx[c]) : "v"(lin), "s"(ws + WS_ZX + (size_t)(rb * 4 + pr) * 4096), "i"(c * 1024) : "memory");
        wait_vm<0>(zx[0], zx[1], zx[2], zx[3]);
#pragma unroll
        for (int c = 0; c < 4; ++c) *(u32x4*)(smem + L_Z1 + (64 + (c & 1) * 16 + l15) * 128 + ((((c >> 1) * 4 + g4) ^ (l15 & 7)) << 4)) = zx[c];
      }
      signal1(flg + 56);
      AB(13);
    }
    SB();
    AB(14);
    WAIT_LGKM0();
    signal1(flg + 40);
    flag_wait_asm(flg + 44, 4u);
    AB(15);
    ldW1();
    drain1(0);
  } else {
    ldsw();
    sfor<0, pos('B')>([&](auto ic) { if constexpr (SCH[ic] == 'g') issue(IC(cnt('u', ic))); });
    if (tid < 64) *(unsigned*)(smem + L_FLAG + tid * 4) = 0u;
    pin(ones);
    AB(1);
    barrier0(behind(0, pos('B'), 'B'));
    kvld();
    kvm(0, 4);
    kvst();
    const bool isz2 = !(odd && wl);
    const char* stp = ws + WS_ST + (size_t)((rb * 12 + pr * 3 + odd * 2 + wl) * 1024 + wm * 512);
    if (STW && isz2) st8(lin >> 1, stp, (u32x2){0u, ~(unsigned)tag});
    const char* Ab[4];
#pragma unroll
    for (int i = 0; i < 4; ++i) Ab[i] = (const char*)w_sp + (size_t)(t0 + ((wave + 1 + i) & 3) * 16) * (S_ * 2) + wave * 1024;
    const unsigned ringl = lb + M_RING + wave * 24576;
    auto ldA1 = [&](int d, int i) {
      dma16o(aoff[i & 1] + (unsigned)(((d + rot2) & 7) * 128), Ab[i >> 1], 0, d == 7 ? lb + M_CV + wave * CV_BUF : ringl, i * 1024);
    };
    unsigned bsv;
    {
      f32x4 zv[2][4], cs[4];
      s16x4 WB[20][4][2];
      bf16x8 XG[10][2][2];
      unsigned aX[2][2], aW[2][2][4];
#pragma unroll
      for (int k = 0; k < 2; ++k)
#pragma unroll
        for (int w = 0; w < 2; ++w) {
          aX[w][k] = pX[k] + L_SLOT + XW * 8192 + w * 65536;
          pin(aX[w][k]);
#pragma unroll
          for (int jj = 0; jj < 4; ++jj) { aW[w][k][jj] = (pW[k] ^ (unsigned)(jj * 32)) + L_SLOT + wl * 8192 + w * 65536; pin(aW[w][k][jj]); }
        }
      auto rd1 = [&](auto nc, int r) {
        constexpr int n = decltype(nc)::value, j = n >> 1, k = n & 1, c = n > 0 && n < 11 ? n - 1 : 0, sx = (XSLOT[c] - XW) * 8192, sa = WSLOT[j] * 8192;
        if (r < 0) { if (r & 1) ds16<(sx & 0xFFFF) + 512>(XG[c][(r + 4) >> 1][1], aX[sx >> 16][(r + 4) >> 1]); else ds16<(sx & 0xFFFF)>(XG[c][(r + 4) >> 1][0], aX[sx >> 16][(r + 4) >> 1]); }
        else if (r & 1) WB[n][r >> 1][1] = tr_asm<(sa & 0xFFFF) + 512>(aW[sa >> 16][k][r >> 1]);
        else WB[n][r >> 1][0] = tr_asm<(sa & 0xFFFF)>(aW[sa >> 16][k][r >> 1]);
        SB();
      };
      auto bar = [&](auto bc) {
        SB();
        constexpr int p = decltype(bc)::value;
        CB(3 * (p - XBAR));
        if constexpr (p == 1) asm volatile("s_waitcnt vmcnt(%0) lgkmcnt(0)\n\ts_barrier" :: "n"(behind(0, pos('_', p - 1), '_')) : "memory");
        else if constexpr (p <= GZ) asm volatile("s_waitcnt vmcnt(%0) lgkmcnt(8)\n\ts_barrier" :: "n"(behind(0, pos('_', p - 1), '_')) : "memory");
        else if constexpr (p >= 8) asm volatile("s_waitcnt lgkmcnt(0)\n\ts_barrier" ::: "memory");
        else if constexpr (p < 7) asm volatile("s_waitcnt lgkmcnt(8)\n\ts_barrier" ::: "memory");
        else asm volatile("s_barrier" ::: "memory");
        CB(3 * (p - XBAR) + 2);
        SB();
      };
      float cp[4];
      f32x4 st[2][2], rv;
      const unsigned rwa = lb + L_RAW + (ksb * 2 + wm) * 256 + l15 * 16 + ((l15 >> 2) == g4 ? 0 : 1024);
      const unsigned csa = lb + L_CS + (wave * 64 + lane) * 4;
      auto kstep = [&](auto nc) {
        constexpr int n = decltype(nc)::value, j = n >> 1, k = n & 1, nn = n + 1, r0 = nn >= 2 && nn <= 10 ? -4 : 0;
        constexpr int sc = n == 1 ? 0 : n - 2, ns = !k ? 0 : (n == 1 || n == 11 ? 1 : 2);
        if constexpr (k) bar(IC(nn / 2 + 1));
        int r = r0, c = 0;
        auto nxt = [&]() { if (r < 8) { rd1(IC(nn), r++); ++c; } };
        auto sel = [&](int q) {
          const f32x4& sq = st[q & 1][q >> 1];
          rv[q] = sel4(sq[0], sq[1], sq[2], sq[3], 0xAAAAAAAAAAAAAAAAull, 0xCCCCCCCCCCCCCCCCull);
          SB();
        };
#pragma unroll
        for (int jj = 0; jj < 4; ++jj) {
          const bf16x8 wf = join8(WB[n][jj][0], WB[n][jj][1]);
          MFMAW<n == 0>(jj == 0 ? 4 : (jj == 2 ? c : -1), zv[0][jj], XG[j][k][0], wf);
          SB();
          nxt();
          if (n == 12) nxt();
          if (n == 12 && jj == 2) sel(2);
          MFMAW<n == 0>(-1, zv[1][jj], XG[j][k][1], wf);
          SB();
          nxt();
          if (n == 12) nxt();
          if (n == 12 && (jj == 1 || jj == 2)) sel(jj == 1 ? 0 : 3);
          if (n == 12 && jj == 2) { dsw16<0>(rwa, rv); SB(); }
          if (k == 0) {
            MFMAW<n == 0>(-1, cs[jj], ones, wf);
            SB();
            nxt();
            if (n == 12) nxt();
            if (n == 12 && jj == 1) sel(1);
          }
#pragma unroll
          for (int q = 0; q < ns; ++q) {
            const bf16x8& xs = XG[sc + q][1][jj >> 1];
            MFMAW<n == 1>(-1, st[jj & 1][jj >> 1], xs, (jj & 1) ? xs : ones);
            SB();
            nxt();
          }
        }
      };
      auto lstep = [&](auto nc, auto ac, auto bc, int w) {
        constexpr int n = decltype(nc)::value, j = n >> 1, k = n & 1, na = decltype(ac)::value, nb = decltype(bc)::value;
        int r = 0;
        auto nxt = [&]() { if (na > 0 && r < 16) { if (r < 8) rd1(ac, r); else rd1(bc, r - 8); ++r; } };
#pragma unroll
        for (int jj = 0; jj < 4; ++jj) {
          const bf16x8 wf = join8(WB[n][jj][0], WB[n][jj][1]);
          if (k == 0) { MFMAW<false>(jj == 0 ? w : -1, cs[jj], ones, wf); SB(); nxt(); nxt(); }
          MFMAW<false>(jj == 0 && k ? w : -1, zv[0][jj], XG[j][k][0], wf);
          SB();
          nxt();
          if (k) nxt();
          MFMAW<false>(-1, zv[1][jj], XG[j][k][1], wf);
          SB();
          nxt();
          if (k) nxt();
          if (n == 15 && jj == 2) {
            dsw4(csa, sel4(cs[0][0], cs[1][0], cs[2][0], cs[3][0], 0xFFFF0000FFFF0000ull, 0xFFFFFFFF00000000ull));
            SB();
          }
        }
      };
      bar(IC(1));
      AB(2);
#pragma unroll
      for (int r = -4; r < 8; ++r) rd1(IC(0), r);
      kstep(IC(0)); AB(4); kstep(IC(1)); kstep(IC(2)); kstep(IC(3)); kstep(IC(4)); kstep(IC(5)); kstep(IC(6)); kstep(IC(7)); kstep(IC(8)); AB(5); kstep(IC(9));
      kstep(IC(10)); kstep(IC(11)); kstep(IC(12));
      bar(IC(8));
      lstep(IC(13), IC(14), IC(16), -1);
      lstep(IC(14), IC(18), IC(15), 8);
      lstep(IC(16), IC(17), IC(19), 15);
      lstep(IC(18), IC(0), IC(0), 15);
      lstep(IC(15), IC(0), IC(0), -1);
      lstep(IC(17), IC(0), IC(0), 9);
      bar(IC(9));
      __builtin_amdgcn_s_setprio(2);
#pragma unroll
      for (int q = 0; q < 4; ++q) cp[q] = *(const float*)(smem + L_CS + ((wave ^ 1) * 64 + q * 16 + l15) * 4);
      ldxs();
      SB();
      lstep(IC(19), IC(0), IC(0), -1);
      AB(6);
      float ct[4];
#pragma unroll
      for (int jj = 0; jj < 4; ++jj) ct[jj] = cs[jj][0] + cp[jj];
#pragma unroll
      for (int jj = 0; jj < 4; ++jj) {
        if (jj == 0) asm volatile("s_nop 3");
        if (jj == 2) SB();
        if (!(jj & 1)) pin(zv[0][jj], zv[1][jj], zv[0][jj + 1], zv[1][jj + 1]);
#pragma unroll
        for (int i = 0; i < 2; ++i) zv[i][jj] = act(zv[i][jj], ct[jj], i);
      }
      if (isz2) {
        f32x4 pv[2], s1[2], s2[2];
#pragma unroll
        for (int i = 0; i < 2; ++i) {
#pragma unroll
          for (int e = 0; e < 4; ++e) {
            const float z0 = zv[i][0][e];
            pv[i][e] = __int_as_float(__builtin_amdgcn_update_dpp(0, __float_as_int(z0), 0x150, 0xF, 0xF, true));
          }
          const f32x4 a0 = zv[i][0] - pv[i], a1 = zv[i][1] - pv[i], a2 = zv[i][2] - pv[i], a3 = zv[i][3] - pv[i];
          s1[i] = (a0 + a1) + (a2 + a3);
          s2[i] = __builtin_elementwise_fma(a3, a3, __builtin_elementwise_fma(a2, a2, __builtin_elementwise_fma(a1, a1, a0 * a0)));
        }
        float r[8], q[4];
#define DA(d, s, ctl, bm) "v_add_f32_dpp " d ", " s ", " s " " ctl " row_mask:0xf bank_mask:" bm "\n\t"
        asm volatile("s_nop 1\n\t"
                     DA("%0", "%12", "row_ror:8", "0x3") DA("%1", "%13", "row_ror:8", "0x3") DA("%2", "%14", "row_ror:8", "0x3") DA("%3", "%15", "row_ror:8", "0x3")
                     DA("%4", "%16", "row_ror:8", "0x3") DA("%5", "%17", "row_ror:8", "0x3") DA("%6", "%18", "row_ror:8", "0x3") DA("%7", "%19", "row_ror:8", "0x3")
                     DA("%0", "%20", "row_ror:8", "0xc") DA("%1", "%21", "row_ror:8", "0xc") DA("%2", "%22", "row_ror:8", "0xc") DA("%3", "%23", "row_ror:8", "0xc")
                     DA("%4", "%24", "row_ror:8", "0xc") DA("%5", "%25", "row_ror:8", "0xc") DA("%6", "%26", "row_ror:8", "0xc") DA("%7", "%27", "row_ror:8", "0xc")
                     DA("%8", "%0", "row_half_mirror", "0x5") DA("%9", "%1", "row_half_mirror", "0x5") DA("%10", "%2", "row_half_mirror", "0x5") DA("%11", "%3", "row_half_mirror", "0x5")
                     DA("%8", "%4", "row_half_mirror", "0xa") DA("%9", "%5", "row_half_mirror", "0xa") DA("%10", "%6", "row_half_mirror", "0xa") DA("%11", "%7", "row_half_mirror", "0xa")
                     DA("%8", "%8", "quad_perm:[1,0,3,2]", "0xf") DA("%9", "%9", "quad_perm:[1,0,3,2]", "0xf") DA("%10", "%10", "quad_perm:[1,0,3,2]", "0xf") DA("%11", "%11", "quad_perm:[1,0,3,2]", "0xf")
                     DA("%8", "%8", "quad_perm:[2,3,0,1]", "0xf") DA("%9", "%9", "quad_perm:[2,3,0,1]", "0xf") DA("%10", "%10", "quad_perm:[2,3,0,1]", "0xf") DA("%11", "%11", "quad_perm:[2,3,0,1]", "0xf")
                     : "=&v"(r[0]), "=&v"(r[1]), "=&v"(r[2]), "=&v"(r[3]), "=&v"(r[4]), "=&v"(r[5]), "=&v"(r[6]), "=&v"(r[7]),
                       "=&v"(q[0]), "=&v"(q[1]), "=&v"(q[2]), "=&v"(q[3])
                     : "v"(s1[0][0]), "v"(s2[0][0]), "v"(s1[0][1]), "v"(s2[0][1]), "v"(s1[0][2]), "v"(s2[0][2]), "v"(s1[0][3]), "v"(s2[0][3]),
                       "v"(s1[1][0]), "v"(s2[1][0]), "v"(s1[1][1]), "v"(s2[1][1]), "v"(s1[1][2]), "v"(s2[1][2]), "v"(s1[1][3]), "v"(s2[1][3]));
#undef DA
        const f32x4 p4 = (lane & 8) ? pv[1] : pv[0];
        const float pa = (lane & 4) ? p4[2] : p4[0], pb = (lane & 4) ? p4[3] : p4[1];
        const float pp = (lane & 2) ? pb : pa, S1 = (lane & 2) ? q[2] : q[0], S2 = (lane & 2) ? q[3] : q[1];
        const float dl = S1 * (1.0f / 64.0f);
        st8(lin >> 1, stp, (u32x2){__float_as_uint((lane & 1) ? fmaxf(S2 - S1 * dl, 0.f) : pp + dl), (unsigned)tag});
      } else {
#pragma unroll
        for (int jj = 0; jj < 4; ++jj) {
          u32x4 o = pack8(zv[0][jj], zv[1][jj]);
          if (jj >= 2) z1put(jj * 16 - 32, o);
          else st16(lin, ws + WS_ZX + (size_t)((rb * 4 + pr) * 4 + wm * 2 + jj) * 1024, o);
        }
      }
      signal1(flg + 36);
      SB();
      AB(7);
      __builtin_amdgcn_s_setprio(P2P);
      unsigned po = (unsigned)((lane >> 5) * 6144 + (lane & 31) * 16);
      const char* pb = ws + WS_ST + (size_t)rb * 12288 + wm * 512 + 2048;
      u32x4 sg[6];
      auto ldsg = [&]() {
#pragma unroll
        for (int c = 0; c < 6; ++c) asm volatile("global_load_dwordx4 %0, %1, %2 offset:%3 sc1" : "=&v"(sg[c]) : "v"(po), "s"(pb), "i"(c * 1024 - 2048) : "memory");
      };
      auto look = [&]() {
        wait_vm<0>();
        ldsg();
        asm volatile("global_load_ushort %0, %1, %2" : "=&v"(bsv) : "v"((unsigned)(l15 * 2)), "s"(b_sp + t0 + wave * 16) : "memory");
        if (!isz2) sttag(ws + WS_XF + (rb * 4 + pr) * 16 + wm * 8);
        SB();
      };
      flag_wait_asm(flg + 60, 4u);
      ds16<0>(ac2[0][0], p2a); ds16<1024>(ac2[1][0], p2a); ds16<2048>(ac2[0][1], p2a); ds16<3072>(ac2[1][1], p2a);
      sfor<0, 4>([&](auto nc) { rdV(IC(1), nc, 0); rdV(IC(1), nc, 1); rdV(IC(1), nc, 2); rdV(IC(1), nc, 3); });
      sfor<0, 10>([&](auto nc) {
        constexpr int n = decltype(nc)::value;
        mm2(IC(1), nc, XG + 5, n == 0 ? 12 : (n < 6 ? 15 : 4 * (9 - n)), [&](int r) {
          SB();
          if constexpr (n < 3) { if (r < 2) { rdV(IC(1), IC(4 + 2 * n), 2 * r); rdV(IC(1), IC(4 + 2 * n), 2 * r + 1); } else { rdV(IC(1), IC(5 + 2 * n), 2 * r - 4); rdV(IC(1), IC(5 + 2 * n), 2 * r - 3); } }
        });
        if constexpr (n == 2) signal1(flg + 16);
        if constexpr (n == PN) look();
      });
      signal1(flg + 20 + wl * 4);
      AB(3);
      if (PN > 9) look();
      AB(8);
      wait_vm<1>(sg[0], sg[1], sg[2], sg[3], sg[4], sg[5]);
      if (isz2) {
        const unsigned zw = lb + L_ZS + wave * 256 + lane * 4, zr_ = lb + L_ZS + wave * 256 + g4 * 32;
        const char* zb = ws + WS_Z2 + (size_t)((rb * 2 + wm) * 48 + pr * 12 + odd * 8 + wl * 4) * 1024;
        auto bad = [&]() {
          unsigned an = sg[0][1] & sg[0][3], on = sg[0][1] | sg[0][3];
#pragma unroll
          for (int c = 1; c < 6; ++c) { an &= sg[c][1] & sg[c][3]; on |= sg[c][1] | sg[c][3]; }
          return __builtin_amdgcn_ballot_w64(((an ^ (unsigned)tag) | (on ^ (unsigned)tag)) != 0) != 0;
        };
        if (__builtin_expect(bad(), 0)) {
          do {
            --tmo;
            reld6(sg, po, pb);
            wait_vm<0>(sg[0], sg[1], sg[2], sg[3], sg[4], sg[5]);
          } while (bad() && tmo > 0);
        }
        AB(9);
        auto f = [&](int c, int e) { return __uint_as_float(sg[c][e]); };
        auto sum6 = [&](int e) { return add1(add1(add1(f(0, e), f(1, e)), add1(f(2, e), f(3, e))), add1(f(4, e), f(5, e))); };
        const float nmh = sum6(0) * (-1.0f / 6.0f), mh = -nmh;
        float qa = sum6(2) * (1.0f / 64.0f), qb;
#pragma unroll
        for (int c = 0; c < 6; c += 2) { const float u = add1(f(c, 0), nmh), v = add1(f(c + 1, 0), nmh); qa = fma1(u, u, qa); qb = c ? fma1(v, v, qb) : v * v; }
        const float Mh = add1(qa, qb);
        const auto sm = __builtin_amdgcn_permlane32_swap(__float_as_uint(mh), __float_as_uint(mh), false, false);
        const auto sM = __builtin_amdgcn_permlane32_swap(__float_as_uint(Mh), __float_as_uint(Mh), false, false);
        const float dm = __uint_as_float(sm[0]) - __uint_as_float(sm[1]), mean = rbf(add1(__uint_as_float(sm[0]), __uint_as_float(sm[1])) * 0.5f);
        const float vbf = rbf(__builtin_fmaf(dm * dm, 3.0f, add1(__uint_as_float(sM[0]), __uint_as_float(sM[1]))) * (1.0f / 12.0f));
        const float rz = __builtin_amdgcn_rcpf(rbf(__builtin_amdgcn_sqrtf(rbf(vbf + 1e-5f))));
        dsw4(zw, lane < 32 ? -mean * rz : rz);
        f32x4 zm[2], zr[2];
        ds16<0>(zm[0], zr_); ds16<128>(zr[0], zr_); ds16<16>(zm[1], zr_); ds16<144>(zr[1], zr_);
        wait_lg<0>(zm[0], zm[1], zr[0], zr[1]);
#pragma unroll
        for (int jj = 0; jj < 4; ++jj) {
          u32x4 o;
#pragma unroll
          for (int q = 0; q < 4; ++q) {
            const int i = q >> 1, e = (q & 1) * 2;
            o[q] = pack2(fma1(zv[i][jj][e], zr[i][e], zm[i][e]), fma1(zv[i][jj][e + 1], zr[i][e + 1], zm[i][e + 1]));
          }
          o[3] = nohole(o[3]);
          asm volatile("global_store_dwordx4 %0, %1, %2 offset:%3 sc1\n\ts_nop 1" :: "v"(lin), "v"(o), "s"(zb), "i"(jj * 1024) : "memory");
          SB();
        }
      }
      SB();
      AB(10);
      ldtag(ef, (unsigned)((lane & 31) * 8), ws + WS_EF + 2048 + cb * 256);
      signal1(flg + 28);
    }
    const char* Bs = ws + WS_Z2 + (size_t)(wave * 16 * 48 + cb * 6) * 1024 + 2048;
    bf16x8 Bf[3][6];
    auto ldB1 = [&](int s, int j) {
      asm volatile("global_load_dwordx4 %0, %1, %2 offset:%3 " HSC : "=&v"(Bf[s % 3][j]) : "v"(lin + (unsigned)(((s + 2 * rot2) & 15) * 49152)), "s"(Bs), "i"(j * 1024 - 2048) : "memory");
    };
    f32x4 acc[4][6];
    unsigned ara[2][2];
#pragma unroll
    for (int ks = 0; ks < 2; ++ks) {
      ara[0][ks] = ringl + l15 * 128 + (((ks * 4 + g4) ^ (l15 & 7)) << 4);
      ara[1][ks] = ara[0][ks] + (M_CV - M_RING) + wave * (CV_BUF - 24576);
    }
    u32x4 af[2][4];
    auto rdA = [&](int s, int i) {
      const int q = (0x30123012 >> (4 * (s >> 1))) & 3;
      asm volatile("ds_read_b128 %0, %1 offset:%2" : "=&v"(af[s & 1][i]) : "v"(ara[q == 3][s & 1]), "i"((q % 3) * 8192 + i * 2048) : "memory");
    };
    flag_wait_asm(flg + 28, 4u);
    AB(11);
    wait_vm<0>(ef, bsv);
    waittag(ef, (unsigned)((lane & 31) * 8), ws + WS_EF + 2048 + cb * 256);
    AB(12); SS(1 - 3 * XS);
#pragma unroll
    for (int j = 0; j < 6; ++j) ldB1(0, j);
#pragma unroll
    for (int j = 0; j < 6; ++j) ldB1(1, j);
    SS(2 - 3 * XS);
    {
      flag_wait_asm(flg + 20 + wl * 4, 2u);
      const float* cp2 = (const float*)(smem + L_CS2 + (wl * 64 + l15) * 4);
      pin(ac2[0][0], ac2[0][1], ac2[1][0], ac2[1][1]);
#pragma unroll
      for (int jj = 0; jj < 2; ++jj) {
        const float nct = -add1(add1(cp2[jj * 16], cp2[32 + jj * 16]), add1(cp2[128 + jj * 16], cp2[160 + jj * 16]));
        u32x4 o;
#pragma unroll
        for (int q = 0; q < 4; ++q) {
          const int i = q >> 1, e = (q & 1) * 2;
          o[q] = pack2(mul1(fma1(xm[i][e], nct, ac2[i][jj][e]), xr[i][e]), mul1(fma1(xm[i][e + 1], nct, ac2[i][jj][e + 1]), xr[i][e + 1]));
        }
        z1put(odd * 32 + wl * 32 + jj * 16, o);
      }
      signal1(flg + 132);
      SB();
    }
    __builtin_amdgcn_s_setprio(G2P);
    f32x4 bs4 = {lo16(bsv), lo16(bsv), lo16(bsv), lo16(bsv)};
    pin(bs4);
    AB(25);
    flag_wait_asm(flg + 32, 1u);
    AB(26);
#pragma unroll
    for (int i = 0; i < 4; ++i) rdA(0, i);
    SB();
    const unsigned xa = ringl + lane * 16;
    auto putp = [&](int i, int j) { asm volatile("ds_write_b128 %0, %1 offset:%2" :: "v"(xa), "v"(acc[i][j]), "i"(i * 6144 + j * 1024) : "memory"); };
    unsigned mx[2], fv;
    u64 hm;
    auto chk = [&](auto sc, int nw, int k) {
      constexpr int s = decltype(sc)::value, sl = s % 3;
      auto d3 = [&](int j) { return __builtin_bit_cast(u32x4, Bf[sl][j])[3]; };
      if (k == 0) {
        asm volatile("s_waitcnt vmcnt(%0)" :: "i"(nw) : "memory");
        pin(Bf[sl][0], Bf[sl][1], Bf[sl][2], Bf[sl][3], Bf[sl][4], Bf[sl][5]);
        mx[0] = opq(umx(umx(d3(0), d3(1)), d3(2)));
      }
      if (k == 1) mx[1] = opq(umx(umx(d3(3), d3(4)), mx[0]));
      if (k == 2) { hm = __builtin_amdgcn_ballot_w64(umx(mx[1], d3(5)) == HOLE); asm volatile("" : "+s"(hm)); }
      if (k == 3 && __builtin_expect(hm != 0, 0)) {
        do {
          --tmo;
          asm volatile("s_waitcnt vmcnt(0)\n\ts_sleep 2" ::: "memory");
#pragma unroll
          for (int q = 0; q < (s ? 3 : 2); ++q)
            if (s + q < 16) reld6(Bf[(s + q) % 3], opq(lin) + (unsigned)(((s + q + 2 * rot2) & 15) * 49152), Bs);
          wait_vm<0>(Bf[sl][0], Bf[sl][1], Bf[sl][2], Bf[sl][3], Bf[sl][4], Bf[sl][5]);
        } while (__builtin_amdgcn_ballot_w64(umx(umx(umx(d3(0), d3(1)), d3(2)), umx(umx(d3(3), d3(4)), d3(5))) == HOLE) != 0 && tmo > 0);
      }
      SB();
    };
    chk(IC(0), 6, 0); chk(IC(0), 0, 1); chk(IC(0), 0, 2); chk(IC(0), 0, 3);
    auto mm = [&](int s, int i, int j) {
      if (s == 0 && i == 3) asm volatile("v_mfma_f32_16x16x32_bf16 %0, %1, %2, %3" : "=&v"(acc[i][j]) : "v"(Bf[0][j]), "v"(af[0][i]), "v"(bs4));
      else if (s == 0) MFMA0(acc[i][j], Bf[0][j], af[0][i]);
      else MFMA(acc[i][j], Bf[s % 3][j], af[s & 1][i]);
      SB();
    };
    auto step = [&](auto sc) {
      constexpr int s = decltype(sc)::value;
      constexpr int NA[16] = ANUM, a0 = s == 0 ? 0 : cum16(NA, s);
      static_assert(cum16(NA, 5) == 0 && cum16(NA, 7) <= 8 && cum16(NA, 9) >= 8 && cum16(NA, 11) == 16 && NA[s] <= 5, "");
      constexpr int NV = (s == 0 ? 5 : (s == 1 ? 1 : 6 + NA[s ? s - 1 : 0])) + (s < 13 ? 5 : 0) + NA[s];
      auto fad = [&](int q) { return flg + (q == 1 ? 32 : (q == 9 ? 144 : 112) + wave * 4); };
      wait_lg<0>(af[s & 1][0], af[s & 1][1], af[s & 1][2], af[s & 1][3]);
      if constexpr (s == 1 || s == 5 || s == 7 || s == 9) {
        pin(fv);
        constexpr unsigned tg = s == 1 || s == 7 ? 2u : 1u;
        if (__builtin_expect((unsigned)__builtin_amdgcn_readfirstlane((int)fv) < tg, 0)) flag_wait_asm(fad(s), tg);
      }
      SS(3 + 2 * (s - XS));
      SB();
#pragma unroll
      for (int j = 0; j < 6; ++j) {
        mm(s, 0, j);
        if (s == 0) ldB1(2, j);
        if (j == 5) chk(IC(s + 1), NV + (s == 0), 0);
        SB();
        mm(s, 1, j);
        if (j < NA[s]) ldA1(6 + ((a0 + j) >> 3), (a0 + j) & 7);
        if (j == 5) chk(IC(s + 1), 0, 1);
        SB();
        mm(s, 2, j);
        if (j < 4) rdA(s + 1, j);
        if (j == 4 && (s == 0 || s == 2)) signal1(flg + 96 + wave * 4);
        if (j == 4 && (s == 0 || s == 4 || s == 6 || s == 8)) asm volatile("ds_read_b32 %0, %1" : "=&v"(fv) : "v"(fad(s + 1)) : "memory");
        if (j == 4 && s == SGS) signal1(flg + 44);
        if (j == 5) chk(IC(s + 1), 0, 2);
        SB();
        mm(s, 3, j);
        if (s + 3 < 16) ldB1(s + 3, j);
        SB();
      }
      SS(4 + 2 * (s - XS));
      chk(IC(s + 1), 0, 3);
    };
    step(IC(0)); AB(13); step(IC(1)); step(IC(2)); step(IC(3)); AB(14); step(IC(4)); step(IC(5)); step(IC(6)); step(IC(7));
    step(IC(8)); step(IC(9)); step(IC(10)); step(IC(11)); step(IC(12)); step(IC(13));
    wait_lg<0>(af[0][0], af[0][1], af[0][2], af[0][3]);
    SS(3 + 2 * (14 - XS));
#pragma unroll
    for (int i = 0; i < 4; ++i) {
#pragma unroll
      for (int n = 0; n < 12; ++n) {
        if (i == 0 && n == 6) { chk(IC(15), 0, 3); wait_lg<0>(af[1][0], af[1][1], af[1][2], af[1][3]); }
        mm(14 + n / 6, i, n % 6);
        if (i == 0 && n < 2) { rdA(15, 2 * n); rdA(15, 2 * n + 1); }
        if (i == 0 && n >= 2 && n < 5) chk(IC(15), 0, n - 2);
        if (i > 0 && (n & 1)) putp(i - 1, n >> 1);
        SB();
      }
      if (i > 0) signal1(flg + ((wave + i) & 3) * 4);
      SB();
    }
    SS(4 + 2 * (14 - XS));
    AB(15);
    ldW1();
    flag_wait_asm(flg + 56, 5u);
    s16x4 z1r[6];
    {
      const int fr = 4 * g4 + (l15 >> 2), p = lane & 3;
      const unsigned za = lb + L_Z1 + fr * 128 + (((wave * 2 + (p >> 1)) ^ (fr & 7)) << 4) + (p & 1) * 8;
      z1r[0] = tr_asm<0>(za); z1r[1] = tr_asm<2048>(za); z1r[2] = tr_asm<4096>(za);
      z1r[3] = tr_asm<6144>(za); z1r[4] = tr_asm<8192>(za); z1r[5] = tr_asm<10240>(za);
    }
    flag_wait_asm(flg + wave * 4, 3u);
    asm volatile("" : "+v"(z1r[0]), "+v"(z1r[1]), "+v"(z1r[2]), "+v"(z1r[3]), "+v"(z1r[4]), "+v"(z1r[5]));
    AB(16);
    f32x4 own[6];
    {
      f32x4 pr_[3][6];
#pragma unroll
      for (int q = 0; q < 3; ++q) {
        const char* src = smem + M_RING + ((wave + 3 - q) & 3) * 24576;
#pragma unroll
        for (int j = 0; j < 6; ++j) pr_[q][j] = *(const f32x4*)(src + q * 6144 + j * 1024 + lane * 16);
      }
      asm volatile("s_nop 7" : "+v"(acc[3][0]), "+v"(acc[3][1]), "+v"(acc[3][2]), "+v"(acc[3][3]), "+v"(acc[3][4]), "+v"(acc[3][5]));
#pragma unroll
      for (int j = 0; j < 6; ++j) own[j] = (acc[3][j] + pr_[0][j]) + (pr_[1][j] + pr_[2][j]);
    }
    SB();
    {
      unsigned pk[6][2];
#pragma unroll
      for (int j = 0; j < 6; ++j) {
        const u32x2 z = __builtin_bit_cast(u32x2, z1r[j]);
#pragma unroll
        for (int h = 0; h < 2; ++h) pk[j][h] = pk2(unpk(z[h]) * (f32x2){own[j][2 * h], own[j][2 * h + 1]});
      }
      const unsigned eo = (unsigned)((l15 + 16 * (2 * (g4 & 1) + (g4 >> 1))) * 16);
#pragma unroll
      for (int q = 0; q < 3; ++q) {
        const auto s0 = __builtin_amdgcn_permlane16_swap(pk[2 * q][0], pk[2 * q + 1][0], false, false);
        const auto s1 = __builtin_amdgcn_permlane16_swap(pk[2 * q][1], pk[2 * q + 1][1], false, false);
        st16(eo, hf + (size_t)((rb * 4 + wave) * 24 + cb * 3 + q) * 1024, (u32x4){s0[0], s1[0], s0[1], nohole(s1[1])});
      }
    }
    AB(17);
    drain1(3);
  }
  unsigned hoff[4];
#pragma unroll
  for (int it = 0; it < 4; ++it) hoff[it] = (unsigned)(((rb * 4 + ((mt + 1 + it) & 3)) * 24 + 3 * wave) * 1024 + lane * 16);
  u32x4 Hq[4][3];
  auto ldh1 = [&](int it, int k) {
    asm volatile("global_load_dwordx4 %0, %1, %2 offset:%3 " HSC : "=&v"(Hq[it][k]) : "v"(hoff[it]), "s"(hf), "i"(k * 1024) : "memory");
  };
  auto ldh0 = [&](int it) {
#pragma unroll
    for (int k = 0; k < 3; ++k) ldh1(it, k);
  };
  f32x2 xf[3][2], sv[3][2];
  auto ymath = [&]() {
    char* yi = smem + P3_Y + wave * 1536;
#pragma unroll
    for (int n = 0; n < 2; ++n) *(u32x4*)(yi + (n ? 64 + (lane & 31) : lane) * 16) = yv[n];
    const unsigned ya = lds_addr(yi) + (4 * g4 + (l15 >> 2)) * 32 + 8 * (lane & 3);
    const float sm = *(const float*)(smem + L_XM + (mt * 16 + l15) * 4), sr = *(const float*)(smem + L_XR + (mt * 16 + l15) * 4);
    s16x4 yr[3] = {tr_asm<0>(ya), tr_asm<512>(ya), tr_asm<1024>(ya)};
    asm volatile("s_waitcnt lgkmcnt(0)" : "+v"(yr[0]), "+v"(yr[1]), "+v"(yr[2]) :: "memory");
    const f32x2 mean = {sm, sm}, rd = {sr, sr};
#pragma unroll
    for (int q = 0; q < 3; ++q) {
      const u32x2 yw = __builtin_bit_cast(u32x2, yr[q]);
#pragma unroll
      for (int h = 0; h < 2; ++h) {
        xf[q][h] = unpk(xv[q][h]);
        sv[q][h] = ((xf[q][h] - mean) * rd) * unpk(yw[h]);
        asm volatile("" : "+v"(sv[q][h]), "+v"(xf[q][h]));
      }
    }
    SB();
  };
  u32x2 yf;
  const unsigned yfo = (unsigned)((lane < 32 ? 32 * kh + lane : 64 + (lane & 15)) * 8);
#pragma unroll
  for (int n = NW1; n < 18; ++n) ldP1(n);
  ldtag(yf, yfo, ws + WS_YF + d0 * 8);
  SB();
  asm volatile("s_waitcnt vmcnt(%0)" :: "i"(19 - NW1) : "memory");
  waittag(ef, (unsigned)((lane & 3) * 8), ws + WS_LF + (rb * 8 + wave) * 32);
  AB(18);
  signal1(flg + 48 + 4 * kh);
  flag_wait_asm(flg + 48, 4u);
  AB(19);
  __builtin_amdgcn_s_setprio(3);
  ldh0(0);
  __builtin_amdgcn_s_setprio(2);
  ldh0(1);
  __builtin_amdgcn_s_setprio(0);
  SB();
  flag_wait_asm(flg + 40, kh ? 4u : 0u);
  char* const px = smem + (kh ? P3_C + mt * 18432 : P3_G + mt * 17408) + lane * 16;
  f32x4 ac3[5], o[3];
  auto ldy = [&]() {
#pragma unroll
    for (int n = 0; n < 2; ++n) asm volatile("global_load_dwordx4 %0, %1, %2 sc1" : "=&v"(yv[n]) : "v"(yoff(n)), "s"(yb) : "memory");
  };
  auto relh = [&](int it) {
    asm volatile("s_sleep 2\n\tglobal_load_dwordx4 %0, %3, %4 sc1\n\tglobal_load_dwordx4 %1, %3, %4 offset:1024 sc1\n\t"
                 "global_load_dwordx4 %2, %3, %4 offset:2048 sc1\n\ts_waitcnt vmcnt(0)"
                 : "+v"(Hq[it][0]), "+v"(Hq[it][1]), "+v"(Hq[it][2]) : "v"(hoff[it]), "s"(hf) : "memory");
  };
  auto comp = [&](auto itc) {
    constexpr int it = decltype(itc)::value;
    constexpr int NW = 3 * ((it + 2 < 4 ? it + 2 : 4) - it - 1) + (it >= 2 ? 2 : 0);
    wait_vm<NW>(Hq[it][0], Hq[it][1], Hq[it][2]);
    if constexpr (it == 0) pin(Wq[0], Wq[1], Wq[2], Wq[3], Wq[4], Wq[5], Wq[6], Wq[7], Wq[8], Wq[9], Wq[10], Wq[11], Wq[12], Wq[13], Wq[14], xv[0], xv[1], xv[2], yf);
    auto bad = [&]() { return __builtin_amdgcn_ballot_w64(umx(umx(Hq[it][0][3], Hq[it][1][3]), Hq[it][2][3]) == HOLE) != 0; };
    if (__builtin_expect(bad(), 0)) {
      do {
        --tmo;
        relh(it);
      } while (bad() && tmo > 0);
    }
    SB();
#pragma unroll
    for (int k = 0; k < 3; ++k) {
#pragma unroll
      for (int j = 0; j < 5; ++j) {
        ac3[j] = __builtin_amdgcn_mfma_f32_16x16x32_bf16(Wq[k * 5 + j], __builtin_bit_cast(bf16x8, Hq[it][k]), k ? ac3[j] : (f32x4){0.f, 0.f, 0.f, 0.f}, 0, 0, 0);
        if constexpr (it + 2 < 4) { if (k == 0 && j < 3) { SB(); ldh1(it + 2 < 4 ? it + 2 : 0, j); SB(); } }
        if constexpr (it == 1) {
          if (k == 1 && j == 0) {
            SB();
            ldy();
            SB();
          }
        }
      }
    }
    SB();
    if constexpr (it < 3) {
#pragma unroll
      for (int j = 0; j < 5; ++j) *(f32x4*)(px + (it * 5 + j) * 1024) = ac3[j];
      signal1(flg + 64 + kh * 16 + ((mt + 1 + it) & 3) * 4);
    }
    SB();
  };
  auto gather = [&](auto cc) {
    constexpr int cv = decltype(cc)::value;
    flag_wait_asm(flg + 64 + cv * 16 + mt * 4, cv ? 5u : 3u);
#pragma unroll
    for (int it = 0; it < 3 + cv; ++it) {
      const int ms = (mt - 1 - it) & 3;
      const char* ps = smem + ((it < 3 ? cv : !kh) ? P3_C + ms * 18432 : P3_G + ms * 17408) + lane * 16;
      const char* p0 = ps + (it < 3 ? it * 5 + 2 * kh : 15) * 1024;
      const f32x4 a0 = *(const f32x4*)p0, a1 = *(const f32x4*)(p0 + 1024);
      if (!cv && it == 0) { o[0] = a0; o[1] = a1; } else { o[0] += a0; o[1] += a1; }
    }
    if (!kh) {
#pragma unroll
      for (int it = 0; it < 3 + cv; ++it) {
        const int ms = (mt - 1 - it) & 3;
        const char* ps = smem + ((it < 3 ? cv : 1) ? P3_C + ms * 18432 : P3_G + ms * 17408) + lane * 16;
        o[2] += *(const f32x4*)(ps + (it < 3 ? it * 5 + 4 : 17) * 1024);
      }
    }
    SB();
  };
  o[2] = (f32x4){0.f, 0.f, 0.f, 0.f};
  comp(IC(0)); AB(20); comp(IC(1)); comp(IC(2));
  AB(21);
  gather(IC(0));
  comp(IC(3));
  AB(22);
  {
    *(f32x4*)(px + 15 * 1024) = kh ? ac3[0] : ac3[2];
    *(f32x4*)(px + 16 * 1024) = kh ? ac3[1] : ac3[3];
    if (kh) *(f32x4*)(px + 17 * 1024) = ac3[4];
    signal1(flg + 80 + mt * 4);
    SB();
    o[0] += kh ? ac3[2] : ac3[0]; o[1] += kh ? ac3[3] : ac3[1]; o[2] += ac3[4];
    asm volatile("s_waitcnt vmcnt(0)" : "+v"(yv[0]), "+v"(yv[1]) :: "memory");
    if (__builtin_expect(notag(yf), 0)) {
      waittag(yf, yfo, ws + WS_YF + d0 * 8);
      ldy();
      wait_vm<0>(yv[0], yv[1]);
    }
    ymath();
    AB(23);
    gather(IC(1));
    auto fin = [&](const f32x4& o4, int q, unsigned (&pk)[2]) {
#pragma unroll
      for (int h = 0; h < 2; ++h) {
        const f32x2 o = {o4[2 * h], o4[2 * h + 1]};
        const f32x2 a = o * (f32x2){-1.4426950f, -1.4426950f};
        const f32x2 e = (f32x2){__builtin_amdgcn_exp2f(a[0]), __builtin_amdgcn_exp2f(a[1])} + (f32x2){1.0f, 1.0f};
        const f32x2 r = {__builtin_amdgcn_rcpf(e[0]), __builtin_amdgcn_rcpf(e[1])};
        pk[h] = pk2(__builtin_elementwise_fma(o * r, sv[q][h], xf[q][h]));
      }
    };
    unsigned pa[2], pb[2];
    fin(o[0], 0, pa);
    fin(o[1], 1, pb);
    const auto s0 = __builtin_amdgcn_permlane16_swap(pa[0], pb[0], false, false);
    const auto s1 = __builtin_amdgcn_permlane16_swap(pa[1], pb[1], false, false);
    *(u32x4*)(out + xo + 32 * kh + 16 * (g4 & 1) + 8 * (g4 >> 1)) = (u32x4){s0[0], s1[0], s0[1], s1[1]};
    if (!kh) {
      fin(o[2], 2, pa);
      *(u32x2*)(out + xo + 64 + 4 * g4) = (u32x2){pa[0], pa[1]};
    }
  }
  if (b == 0 && tid == 0) *(u64*)(ws + WS_EP) = tag;
  AB(24);
  AB_OUT();
}

extern "C" __global__ void k_end() {}

extern "C" void kernel_launch(void* const* d_in, const int* in_sizes, int n_in,
                              void* d_out, int out_size, void* d_ws, size_t ws_size,
                              hipStream_t stream) {
  (void)hipFuncSetAttribute((const void*)k_f, hipFuncAttributeMaxDynamicSharedMemorySize, M_LDS);
  k_f<<<256, 512, M_LDS, stream>>>((const u16*)d_in[0], (const u16*)d_in[1], (const u16*)d_in[2], (const u16*)d_in[3], (const u16*)d_in[4],
                                   (const u16*)d_in[5], (const u16*)d_in[7], (const u16*)d_in[8], (const u16*)d_in[9], (const u16*)d_in[10],
                                   (u16*)d_out, (char*)d_ws);
}
```

```cpp
#include <hip/hip_runtime.h>
#include <hip/hip_bf16.h>
#include <stdint.h>
#include <type_traits>

typedef unsigned short u16;
typedef unsigned long long u64;
typedef __attribute__((ext_vector_type(8))) short bf16x8;
typedef __attribute__((ext_vector_type(4))) short s16x4;
typedef __attribute__((ext_vector_type(4))) float f32x4;
typedef __attribute__((ext_vector_type(2))) float f32x2;
typedef __attribute__((ext_vector_type(4))) unsigned int u32x4;
typedef __attribute__((ext_vector_type(2))) unsigned int u32x2;
typedef __attribute__((ext_vector_type(2))) __bf16 bf16x2n;

#define LDS_AS __attribute__((address_space(3)))
#define IC(n) std::integral_constant<int, (n)>{}
#define SB() __builtin_amdgcn_sched_barrier(0)

#define AB_DECL do {} while (0)
#define AB(i) do {} while (0)
#define AB_OUT() do {} while (0)
#define CB(i) do {} while (0)
#define SS(i) do {} while (0)
constexpr int S_ = 2048, D_ = 640, FH_ = 768, F2_ = 1536;
constexpr unsigned HOLE = 0xFFFFFFFFu;

constexpr size_t WS_EP = 0;
constexpr size_t WS_EF = 4096;
constexpr size_t WS_LF = 22528;
constexpr size_t WS_YF = 14336;
constexpr size_t WS_XF = 20480;
constexpr size_t WS_ST = 32768;
constexpr size_t WS_Z2 = WS_ST + 32 * 12 * 1024;
constexpr size_t WS_H = WS_Z2 + 64 * 48 * 1024;
constexpr size_t WS_ZX = WS_H + 128 * 24 * 1024;
constexpr size_t WS_KV = WS_ZX + 32 * 4 * 4096;
constexpr size_t WS_Y = WS_KV + D_ * S_ * 2;
constexpr size_t WS_WO = WS_Y + D_ * S_ * 2;

__device__ __forceinline__ float bf2f(u16 v) { return __uint_as_float(((unsigned)v) << 16); }
__device__ __forceinline__ unsigned pack2(float lo, float hi) {
  f32x2 v = {lo, hi};
  bf16x2n r = __builtin_convertvector(v, bf16x2n);
  return __builtin_bit_cast(unsigned, r);
}
__device__ __forceinline__ float nv(float v) { asm("" : "+v"(v)); return v; }
__device__ __forceinline__ float fma1(float a, float b, float c) { float r; asm("v_fma_f32 %0, %1, %2, %3" : "=v"(r) : "v"(a), "v"(b), "v"(c)); return r; }
__device__ __forceinline__ float add1(float a, float b) { float r; asm("v_add_f32 %0, %1, %2" : "=v"(r) : "v"(a), "v"(b)); return r; }
__device__ __forceinline__ float mul1(float a, float b) { float r; asm("v_mul_f32 %0, %1, %2" : "=v"(r) : "v"(a), "v"(b)); return r; }
__device__ __forceinline__ unsigned pack2s(float lo, float hi) { return pack2(nv(lo), nv(hi)); }
__device__ __forceinline__ float lo16(unsigned w) { return __uint_as_float(w << 16); }
__device__ __forceinline__ float hi16(unsigned w) { return __uint_as_float(w & 0xFFFF0000u); }
__device__ __forceinline__ float rbf(float f) { return lo16(pack2(f, f)); }
__device__ __forceinline__ f32x2 unpk(unsigned w) { f32x2 r = {lo16(w), hi16(w)}; return r; }
__device__ __forceinline__ unsigned pk2(f32x2 v) { return pack2(v[0], v[1]); }
__device__ __forceinline__ unsigned umx(unsigned a, unsigned b) { return a > b ? a : b; }
__device__ __forceinline__ unsigned nohole(unsigned a) { return a < HOLE - 1 ? a : HOLE - 1; }
__device__ __forceinline__ f32x2 gelu2(f32x2 x) {
  const f32x2 c1 = {-2.3022082f, -2.3022082f}, c2 = {-0.10294324f, -0.10294324f};
  const f32x2 a = x * __builtin_elementwise_fma(x * x, c2, c1);
  f32x2 e = {__builtin_amdgcn_exp2f(a[0]), __builtin_amdgcn_exp2f(a[1])};
  e = e + (f32x2){1.0f, 1.0f};
  const f32x2 r = {__builtin_amdgcn_rcpf(e[0]), __builtin_amdgcn_rcpf(e[1])};
  return x * r;
}
__device__ __forceinline__ float gelu1(float x) {
  const float a = x * __builtin_fmaf(x * x, -0.10294324f, -2.3022082f);
  return x * __builtin_amdgcn_rcpf(__builtin_amdgcn_exp2f(a) + 1.0f);
}
template <int OFF>
__device__ __forceinline__ s16x4 tr_asm(unsigned a) {
  s16x4 r;
  asm volatile("ds_read_b64_tr_b16 %0, %1 offset:%2" : "=&v"(r) : "v"(a), "i"(OFF) : "memory");
  return r;
}
__device__ __forceinline__ bf16x8 join8(s16x4 lo, s16x4 hi) {
  bf16x8 r;
  r[0] = lo[0]; r[1] = lo[1]; r[2] = lo[2]; r[3] = lo[3];
  r[4] = hi[0]; r[5] = hi[1]; r[6] = hi[2]; r[7] = hi[3];
  return r;
}
__device__ __forceinline__ unsigned lds_addr(const void* p) { return (unsigned)(uintptr_t)(LDS_AS const char*)p; }
#define WAIT_LGKM0() asm volatile("s_waitcnt lgkmcnt(0)" ::: "memory")
__device__ __forceinline__ void flag_wait_asm(unsigned faddr, unsigned target) {
  unsigned v, sv;
  asm volatile(".Lfw_%=:\n\tds_read_b32 %0, %2\n\ts_waitcnt lgkmcnt(0)\n\tv_readfirstlane_b32 %1, %0\n\ts_nop 0\n\t"
               "s_cmp_ge_u32 %1, %3\n\ts_cbranch_scc1 .Lfd_%=\n\ts_sleep 1\n\ts_branch .Lfw_%=\n\t.Lfd_%=:"
               : "=&v"(v), "=&s"(sv) : "v"(faddr), "s"(target) : "memory", "scc");
}
__device__ __forceinline__ void signal1(unsigned a, unsigned one = 1u) {
  asm volatile("s_mov_b64 exec, 1\n\tds_add_u32 %0, %1\n\ts_mov_b64 exec, -1" :: "v"(a), "v"(one) : "memory");
}
__device__ __forceinline__ void dma16(unsigned vo, const void* sb, unsigned lds) {
  asm volatile("s_mov_b32 m0, %2\n\ts_nop 0\n\tglobal_load_lds_dwordx4 %0, %1" :: "v"(vo), "s"(sb), "s"(lds) : "memory");
}
template <int LO, int GO> __device__ __forceinline__ void dma32s(unsigned vo0, unsigned vo1, const void* sb, unsigned lds) {
  asm volatile("s_add_u32 m0, %5, %6\n\ts_add_u32 vcc_lo, %2, %4\n\ts_addc_u32 vcc_hi, %3, 0\n\t"
               "global_load_lds_dwordx4 %0, vcc\n\tglobal_load_lds_dwordx4 %1, vcc offset:1024"
               :: "v"(vo0), "v"(vo1), "s"((unsigned)(uintptr_t)sb), "s"((unsigned)((uintptr_t)sb >> 32)), "i"(GO), "s"(lds), "i"(LO) : "memory", "scc", "vcc");
}
template <int LO, int GO, int WRAP, int LIM> __device__ __forceinline__ void dma32r(unsigned vo0, unsigned vo1, const void* sb, unsigned lds, int rot) {
  const char* s2 = (const char*)sb - WRAP;
  asm volatile("s_add_u32 m0, %5, %6\n\ts_cmp_ge_u32 %7, %8\n\ts_cselect_b64 vcc, %3, %2\n\ts_add_u32 vcc_lo, vcc_lo, %4\n\ts_addc_u32 vcc_hi, vcc_hi, 0\n\t"
               "global_load_lds_dwordx4 %0, vcc\n\tglobal_load_lds_dwordx4 %1, vcc offset:1024"
               :: "v"(vo0), "v"(vo1), "s"(sb), "s"(s2), "i"(GO), "s"(lds), "i"(LO), "s"(rot), "i"(LIM) : "memory", "scc", "vcc");
}
__device__ __forceinline__ void dma16o(unsigned vo, const void* sb, int go, unsigned lds, int lo) {
  asm volatile("s_add_u32 m0, %3, %4\n\ts_nop 0\n\tglobal_load_lds_dwordx4 %0, %1 offset:%2" :: "v"(vo), "s"(sb), "i"(go), "s"(lds), "i"(lo - go) : "memory", "scc");
}
__device__ __forceinline__ void st16(unsigned vo, const void* sb, u32x4 v) {
  asm volatile("global_store_dwordx4 %0, %1, %2 sc1\n\ts_nop 1" :: "v"(vo), "v"(v), "s"(sb) : "memory");
}
__device__ __forceinline__ void touch(unsigned vo, const void* sb, unsigned lds) {
  asm volatile("s_mov_b32 m0, %2\n\ts_nop 0\n\tglobal_load_lds_dword %0, %1" :: "v"(vo), "s"(sb), "s"(lds) : "memory");
}
__device__ __forceinline__ void st8(unsigned vo, const void* sb, u32x2 v) { asm volatile("global_store_dwordx2 %0, %1, %2 sc1" :: "v"(vo), "v"(v), "s"(sb) : "memory"); }
template <class A, class B> __device__ __forceinline__ void MFMA(f32x4& acc, const A& a, const B& b) {
  asm volatile("v_mfma_f32_16x16x32_bf16 %0, %1, %2, %0" : "+v"(acc) : "v"(a), "v"(b));
}
template <class A, class B> __device__ __forceinline__ void MFMA0(f32x4& acc, const A& a, const B& b) {
  asm volatile("v_mfma_f32_16x16x32_bf16 %0, %1, %2, 0" : "=&v"(acc) : "v"(a), "v"(b));
}
template <bool Z, class A, class B> __device__ __forceinline__ void MFMAW(int n, f32x4& acc, const A& a, const B& b) {
  if (n < 0) { if (Z) MFMA0(acc, a, b); else MFMA(acc, a, b); }
  else if (Z) asm volatile("s_waitcnt lgkmcnt(%3)\n\tv_mfma_f32_16x16x32_bf16 %0, %1, %2, 0" : "=&v"(acc) : "v"(a), "v"(b), "i"(n));
  else asm volatile("s_waitcnt lgkmcnt(%3)\n\tv_mfma_f32_16x16x32_bf16 %0, %1, %2, %0" : "+v"(acc) : "v"(a), "v"(b), "i"(n));
}
__device__ __forceinline__ float sel4(float a, float b, float c, float d, u64 m1, u64 m2) {
  float r, t;
  asm volatile("v_cndmask_b32_e64 %0, %2, %3, %6\n\tv_cndmask_b32_e64 %1, %4, %5, %6\n\tv_cndmask_b32_e64 %0, %0, %1, %7"
               : "=&v"(r), "=&v"(t) : "v"(a), "v"(b), "v"(c), "v"(d), "s"(m1), "s"(m2));
  return r;
}
template <class T> __device__ __forceinline__ void pin1(T& r) { asm volatile("" : "+v"(r)); }
template <class T> __device__ __forceinline__ T opq(T r) { asm volatile("" : "+v"(r)); return r; }
template <class... T> __device__ __forceinline__ void pin(T&... r) { (pin1(r), ...); }
template <int N, class... T> __device__ __forceinline__ void wait_vm(T&... r) { asm volatile("s_waitcnt vmcnt(%0)" :: "n"(N) : "memory"); (pin1(r), ...); }
template <int N, class... T> __device__ __forceinline__ void wait_lg(T&... r) { asm volatile("s_waitcnt lgkmcnt(%0)" :: "n"(N) : "memory"); (pin1(r), ...); }
template <int OFF, class T> __device__ __forceinline__ void ds16(T& d, unsigned a) {
  asm volatile("ds_read_b128 %0, %1 offset:%2" : "=&v"(d) : "v"(a), "i"(OFF) : "memory");
}
template <int OFF> __device__ __forceinline__ void dsw16(unsigned a, const f32x4& v) {
  asm volatile("ds_write_b128 %0, %1 offset:%2" :: "v"(a), "v"(v), "i"(OFF) : "memory");
}
__device__ __forceinline__ void dsw4(unsigned a, float v) { asm volatile("ds_write_b32 %0, %1" :: "v"(a), "v"(v) : "memory"); }
#define HSC ""
template <int OFF, class T> __device__ __forceinline__ void ld16h(T& d, unsigned vo, const void* sb) {
  asm volatile("global_load_dwordx4 %0, %1, %2 offset:%3 " HSC : "=&v"(d) : "v"(vo), "s"(sb), "i"(OFF) : "memory");
}
template <class T> __device__ __forceinline__ void reld6(T (&B)[6], unsigned vo, const void* sb) {
  asm volatile("global_load_dwordx4 %0, %6, %7 offset:-2048 sc1\n\tglobal_load_dwordx4 %1, %6, %7 offset:-1024 sc1\n\t"
               "global_load_dwordx4 %2, %6, %7 sc1\n\tglobal_load_dwordx4 %3, %6, %7 offset:1024 sc1\n\t"
               "global_load_dwordx4 %4, %6, %7 offset:2048 sc1\n\tglobal_load_dwordx4 %5, %6, %7 offset:3072 sc1"
               : "+v"(B[0]), "+v"(B[1]), "+v"(B[2]), "+v"(B[3]), "+v"(B[4]), "+v"(B[5]) : "v"(vo), "s"(sb) : "memory");
}

constexpr int L_FLAG = 0;
constexpr int L_DUM = 256;
constexpr int L_XM = 512;
constexpr int L_XR = 768;
constexpr int L_CS = 1024;
constexpr int L_ZS = 2048;
constexpr int L_CS2 = 3072;
constexpr int L_Z1 = 4096;
constexpr int L_RAW = L_Z1;
constexpr int L_SLOT = 16384;
constexpr int L_KV = 512, KV_P = 3088, L_WO = L_KV + 8 * KV_P, L_SW = L_WO + 4096;
static_assert(L_WO % 16 == 0 && L_SW + 1024 <= L_SLOT + 2 * 8192, "staging");
constexpr int L_P2 = L_SLOT + 12 * 8192;
constexpr int CV_C = 4608, CV_CS = 4160, CV_BUF = CV_C + 2 * CV_CS;
constexpr int M_CV = L_SLOT;
constexpr int M_RING = L_SLOT + 6 * 8192;
constexpr int P3_G = L_SLOT;
constexpr int P3_C = P3_G + 80 * 1024;
constexpr int yat(int j) { return M_CV + (j >> 1) * CV_BUF + 8192 + (j & 1) * 2048; }
constexpr int M_LDS = 163840;
static_assert(3 * CV_BUF + 8192 <= 6 * 8192 && P3_C + 60 * 1024 <= M_LDS && CV_BUF >= 8192 + 4096, "LDS map");

enum { UX, UA, UB, UV };
struct Unit { int kind, idx, need, free, slot; };
#define SCHED cBuuuuuuuwO_uuxuouu_uuuxuxuu_uuxuxuu_uuuuh_Evuumumuh_vuumumuhF_vuvumum_DRvuuUHmmmmSptt_VhhhttttttJItttttttt
#define STR_(x) #x
#define STR(x) STR_(x)
constexpr char SCH[] = STR(SCHED);
constexpr int NSCH = sizeof(SCH) - 1;
constexpr int XNEED[10] = {1, 2, 2, 3, 3, 4, 4, 5, 5, 6};
constexpr int VNEED[5] = {5, 6, 7, 7, 8};
constexpr int XSLOT[10] = {17, 16, 15, 13, 10, 14, 11, 17, 9, 16}, WSLOT[10] = {6, 2, 4, 0, 6, 12, 10, 4, 0, 8}, VSLOT[10] = {8, 15, 3, 2, 14, 17, 16, 15, 7, 6}, XW = 6;
constexpr int cnt(char c, int e = NSCH) { int n = 0; for (int i = 0; i < e; ++i) n += SCH[i] == c; return n; }
constexpr int pos(char c, int k = 0) { for (int i = 0; i < NSCH; ++i) if (SCH[i] == c && k-- == 0) return i; return NSCH; }
constexpr Unit unit(int n) {
  for (int b = 1; b < 11; ++b) {
    for (int c = 0; c < 10; ++c) if (XNEED[c] == b && n-- == 0) return {UX, c, b, b + 1, XSLOT[c]};
    for (int c = 0; c < 5; ++c) if (VNEED[c] == b && n-- == 0) return {UV, c, b, b + 1, VSLOT[c]};
    if (n < 2) return {n ? UB : UA, b - 1, b < 8 ? b : 8, b < 7 ? b + 2 : 9, WSLOT[b - 1] + n};
    n -= 2;
  }
  return {UV, n + 5, 10, 99, VSLOT[n + 5]};
}
constexpr int FLAGB = cnt('_', pos('D')), GZ = cnt('_', pos('E'));
constexpr int nvm(char c) { return c == 'u' ? 2 : (c == 'H' ? 5 : (c == 'U' ? 4 : (c == 'F' || c == 'h' || c == 't' || c == 'w' || c == 'O'))); }
constexpr int behind(int e, char w) {
  const int b = cnt('_', e) + 1;
  int n = 0;
  for (int i = 0; i < e; ++i) {
    const char c = SCH[i];
    n = (w == '_' ? (c == 'u' && unit(cnt('u', i)).need <= b) || (c == 'h' && b == FLAGB) || (c == 'o' && b == GZ) : c == w) ? 0 : n + nvm(c);
  }
  return n;
}
constexpr bool plan_ok() {
  if (cnt('u') != 40 || cnt('_') != 9 || cnt('x') != 5 || cnt('h') != 6 || cnt('t') != 16 || cnt('I') * cnt('J') * cnt('p') != 1 || cnt('v') != 5 || cnt('m') != 10) return false;
  if (pos('B') > pos('_') || pos('o') < pos('_') || pos('h') < pos('_') || pos('H') < pos('_') || pos('t') < pos('_') || cnt('B') * cnt('O') * cnt('D') * cnt('E') * cnt('F') * cnt('o') * cnt('H') * cnt('U') * cnt('V') * cnt('R') * cnt('S') != 1) return false;
  if (cnt('_', pos('h', 2)) >= FLAGB || GZ < 2 || GZ > 6 || FLAGB < GZ || FLAGB > 9 || pos('o') < pos('O') || cnt('_', pos('o')) >= GZ || pos('H') > pos('I') || pos('F') > pos('U')) return false;
  if (cnt('_', pos('R')) != 8 || cnt('_', pos('S')) != 8 || pos('S') < pos('R') || cnt('_', pos('I')) != 9 || pos('J') < pos('U') || cnt('_', pos('J')) != 9) return false;
  if (cnt('_', pos('V')) != 9 || cnt('u', pos('V')) < 40) return false;
  for (int c = 0; c < 5; ++c) if (cnt('_', pos('x', c)) != XNEED[c] || cnt('_', pos('v', c)) != VNEED[c] || pos('m', 2 * c) < pos('v', c)) return false;
  if (cnt('_', pos('p')) != 8 || pos('p') < pos('m', 9)) return false;
  int fr[18] = {1, cnt('_', pos('o')) + 1};
  for (int i = 0; i < NSCH; ++i) if (SCH[i] == 'u') {
    const Unit u = unit(cnt('u', i));
    const int p = cnt('_', i);
    if (p < fr[u.slot] || p >= u.need) return false;
    fr[u.slot] = u.free;
  }
  return true;
}
static_assert(plan_ok(), "phase A plan");
template <int I, int E, class F> __device__ __forceinline__ void sfor(F&& f) { if constexpr (I < E) { f(IC(I)); sfor<I + 1, E>(f); } }

__device__ __forceinline__ void conv_core(unsigned buf, int lane, unsigned Dw, f32x4 (&ac)[8]) {
  const int l15 = lane & 15, g4 = lane >> 4;
  const int c = (l15 + 1) & 1;
  unsigned aa[4];
#pragma unroll
  for (int k = 0; k < 4; ++k) aa[k] = buf + CV_C + c * CV_CS + (2031 - l15 + 8 * g4 - c) * 2 - 512 - 1024 * k;
  unsigned bb = buf + 512 + 32 * l15 - 32 + 16 * g4;
  u32x2 A[2][8][2];
  u32x4 B[2][8];
  constexpr int TT[36] = {7, 6, 5, 4, 7, 6, 5, 4, 7, 6, 5, 3, 7, 6, 5, 4, 7, 6, 3, 2, 7, 6, 5, 4, 7, 3, 2, 1, 7, 6, 5, 4, 3, 2, 1, 0};
  constexpr int AA[36] = {0, 0, 0, 0, 1, 1, 1, 1, 2, 2, 2, 0, 3, 3, 3, 2, 4, 4, 1, 0, 5, 5, 4, 3, 6, 2, 1, 0, 7, 6, 5, 4, 3, 2, 1, 0};
  auto ld1 = [&](int p, int n) {
    if (n < 16) {
      const int a = n >> 1, o = ((a & 1) ? 0 : 128) + 2 * (n & 1);
      asm volatile("ds_read2_b32 %0, %1 offset0:%2 offset1:%3" : "=&v"(A[p][(n >> 1) & 7][n & 1]) : "v"(aa[(a >> 1) & 3]), "i"(o), "i"(o + 1) : "memory");
    } else
      asm volatile("ds_read_b128 %0, %1 offset:%2" : "=&v"(B[p][n & 7]) : "v"(bb), "i"(512 * (n & 7)) : "memory");
  };
  auto next = [&](int dl) {
#pragma unroll
    for (int k = 0; k < 4; ++k) aa[k] += dl;
    bb += dl;
    SB();
  };
  auto land = [&](int p) {
    asm volatile("s_waitcnt lgkmcnt(0)" : "+v"(A[p][0][0]), "+v"(A[p][0][1]), "+v"(A[p][1][0]), "+v"(A[p][1][1]), "+v"(A[p][2][0]), "+v"(A[p][2][1]),
                 "+v"(A[p][3][0]), "+v"(A[p][3][1]), "+v"(A[p][4][0]), "+v"(A[p][4][1]), "+v"(A[p][5][0]), "+v"(A[p][5][1]), "+v"(A[p][6][0]), "+v"(A[p][6][1]),
                 "+v"(A[p][7][0]), "+v"(A[p][7][1]), "+v"(B[p][0]), "+v"(B[p][1]), "+v"(B[p][2]), "+v"(B[p][3]), "+v"(B[p][4]), "+v"(B[p][5]),
                 "+v"(B[p][6]), "+v"(B[p][7]) :: "memory");
    SB();
  };
  auto mm = [&](int p) {
#pragma unroll
    for (int n = 0; n < 36; ++n) {
      const int t = TT[n], a = AA[n];
      const u32x4 av = {A[p][a][0][0], A[p][a][0][1], A[p][a][1][0], A[p][a][1][1]};
      MFMA(ac[t], av, B[p][t - a]);
      if (n < 24) ld1(p ^ 1, n);
      SB();
    }
    land(p ^ 1);
  };
#pragma unroll
  for (int n = 0; n < 24; ++n) ld1(0, n);
  u32x4 ad;
#pragma unroll
  for (int e = 0; e < 4; ++e) ad[e] = lane == 32 + 2 * e + (lane & 1) + (lane & 8) * 3 ? Dw << (16 * (lane & 1)) : 0u;
  pin(ad);
  land(0);
#pragma unroll
  for (int t = 0; t < 8; ++t) MFMA0(ac[t], ad, B[0][t]);
#pragma unroll 1
  for (int r = 0; r < 8; r += 2) {
    next(-64);
    mm(0);
    next(r < 6 ? -64 : 0);
    mm(1);
  }
  asm volatile("s_nop 15" : "+v"(ac[0]), "+v"(ac[1]), "+v"(ac[2]), "+v"(ac[3]), "+v"(ac[4]), "+v"(ac[5]), "+v"(ac[6]), "+v"(ac[7]));
}

#define ROTA 3
#define RA1 1
#define RA2 0
#define RA3 0
#define RB1 0
#define RB2 4
#define RB3 0
#define G2P 0
#define RB4 2
#define THR 24
#define RB5 2
#define YS 8
#define WO3 0
#define C3P 0
#define FE3 9
#define KS0 1
#define KS1 3
#define CPR 1
#define STW 1
#define PN 10
#define P2P 2
#define SGS 11
#define ANUM {0, 0, 0, 0, 0, 3, 3, 2, 3, 3, 2, 0, 0, 0, 0, 0}
constexpr int cum16(const int (&a)[16], int n) { int c = 0; for (int i = 0; i < n; ++i) c += a[i]; return c; }

__global__ __launch_bounds__(512) void k_f(
    const u16* __restrict__ x, const u16* __restrict__ kin, const u16* __restrict__ vin, const u16* __restrict__ shift_k,
    const u16* __restrict__ ssm_k, const u16* __restrict__ ssm_D, const u16* __restrict__ w_in, const u16* __restrict__ w_sp,
    const u16* __restrict__ b_sp, const u16* __restrict__ w_out, u16* __restrict__ out, char* ws) {
  extern __shared__ __attribute__((aligned(16))) char smem[];
  const int tid = threadIdx.x, lane = tid & 63, wave = __builtin_amdgcn_readfirstlane(tid >> 6);
  const int b = blockIdx.x;
  const int xcd = b & 7, idx = b >> 3;
  const int rb = 8 * (xcd >> 1) + (idx >> 2), cb = 4 * (xcd & 1) + (idx & 3);
  const int t0 = rb * 64, d0 = cb * 80;
  const int g4 = lane >> 4, l15 = lane & 15;
  const unsigned lin = (unsigned)(lane * 16);
  const unsigned lb = lds_addr(smem), flg = lb + L_FLAG;
  AB_DECL;
  AB(0);
  asm volatile("" :: "s"(x), "s"(kin), "s"(vin), "s"(shift_k), "s"(ssm_k), "s"(ssm_D), "s"(w_in), "s"(w_sp), "s"(b_sp), "s"(w_out), "s"(out), "s"(ws));
  u64 tag = *(const __attribute__((address_space(4))) u64*)(uintptr_t)ws;
  int tmo = 1 << 16;
#define CWARM 1
  auto cwarm = [&]() {
    u64 pc;
    unsigned clen;
    asm volatile("s_getpc_b64 %0\n.Lpc_here_%=:\n\ts_mov_b32 %1, k_end-.Lpc_here_%=" : "=s"(pc), "=s"(clen));
    touch(min((unsigned)(idx * 1536 + min(lane, 11) * 128), clen - 4u) & ~3u, (const void*)pc, lb + L_DUM);
  };

  if (wave < 4) {
#pragma unroll
    for (int i = 0; i < 3; ++i) {
      const int id = min(i * 64 + lane, 189), r = id / 10, c = id % 10;
      const char* kp = (r < 11 ? (const char*)kin + (r - 3) * (D_ * 2) : (const char*)vin + (r - 11) * (D_ * 2)) + (size_t)(t0 + wave * 8) * (D_ * 2) + cb * 160 + c * 16;
#pragma unroll
      for (int h = 0; h < 2; ++h)
        asm volatile("s_mov_b32 m0, %1\n\ts_nop 0\n\tglobal_load_lds_dwordx4 %0, off" :: "v"(kp + (h ? 32 : (i == 0 && t0 + wave == 0 && lane < 30 ? 3 : 0)) * (D_ * 2)),
                     "s"(lb + L_KV + (wave + 4 * h) * KV_P + i * 1024) : "memory");
      SB();
    }
    if (tid < 64) *(unsigned*)(smem + L_FLAG + tid * 4) = 0u;
    dma16((unsigned)(min(lane, 39) * 16), shift_k + d0 * 4, lb + L_SW);
    asm volatile("s_waitcnt lgkmcnt(0)\n\ts_barrier" ::: "memory");
  }
  SB();
  const int cw = wave & 3, wm = wave & 1, wl = (wave >> 1) & 1, ksb = wm ^ wl, odd = cb & 1, pr = cb >> 1;
  const int r8 = lane >> 3, c8 = lane & 7;
  unsigned voX[2], voW[2];
#pragma unroll
  for (int h = 0; h < 2; ++h) {
    voX[h] = (unsigned)((h * 8 + r8) * (D_ * 2) + ((c8 ^ (h * 4 + (r8 & 3))) << 4) - h * 1024);
    voW[h] = (unsigned)((h * 8 + r8) * (F2_ * 2) + ((c8 ^ (h * 4 + (r8 & 2))) << 4) - h * 1024);
  }
  const int rot = (RA1 * ((rb & 7) * 5 >> 2) + ROTA * (cb & 3) + RA2 * ((rb >> 3) * 5 >> 1) + RA3 * (cb >> 2)) % 10, rot2 = (RB1 * (cb & 3) + RB2 * (cb >> 2) + RB3 * (rb & 7) + RB4 * (rb >> 3) + RB5 * (cb & 1)) & 7;
  const char* gX = (const char*)x + (size_t)(t0 + cw * 16) * (D_ * 2) + rot * 128;
  const char* gW = (const char*)w_in + (size_t)(cw * 16 + rot * 64) * (F2_ * 2) + pr * 384;
  const char *gV = gW + odd * 256, *gB = gW + (odd ? 128 : FH_ * 2 + 128);
  const char* gS = (const char*)w_sp + (size_t)t0 * (S_ * 2);
  const unsigned aoff[2] = {(unsigned)(r8 * 4096 + ((c8 ^ r8) << 4)), (unsigned)(r8 * 4096 + ((c8 ^ r8) << 4) + 32768)};
  const unsigned ldw = lb + cw * 2048;
  auto issue = [&](auto nc) {
    constexpr Unit u = unit(decltype(nc)::value);
    constexpr int lo = L_SLOT + u.slot * 8192;
    if (u.kind == UX) dma32r<lo, u.idx * 128, 1280, 10 - u.idx>(voX[0], voX[1], gX, ldw, rot);
    else dma32r<lo, u.idx * (64 * F2_ * 2) + (u.kind == UA ? FH_ * 2 : 0), 640 * F2_ * 2, 10 - u.idx>(voW[0], voW[1], u.kind == UB ? gB : gV, ldw, rot);
    SB();
  };
  const int wid = rb * 4 + cw;
  auto run0 = [&](auto ic) {
    constexpr int i = decltype(ic)::value;
    constexpr char c = SCH[i];
    if constexpr (c == 'u') issue(IC(cnt('u', i)));
    if constexpr (c == 'B') asm volatile("s_barrier" ::: "memory");
    if constexpr (c == 'c') { if (CWARM && wave == 5) cwarm(); }
    if constexpr (c == 'O') {
      const int wi = min(wid, 119);
      dma16((unsigned)((lane >> 1) * (D_ * 2) + (lane & 1) * 16), w_out + (size_t)(wi / 5) * 32 * D_ + d0 + (wi % 5) * 16, lb + L_WO + cw * 1024);
    }
    if constexpr (c == 'w')
      touch((unsigned)(min(lane, 19) * (F2_ * 2)), (const char*)w_in + (size_t)((rb & 7) * 80 + cw * 20) * (F2_ * 2) + pr * 384 + odd * 256, lb + L_DUM);
    SB();
  };
  if (wave >= 4) {
    __builtin_amdgcn_s_setprio(3);
    sfor<0, pos('_')>(run0);
  }
  SB();
  auto inctag = [&]() {
    asm volatile("" : "+s"(tag));
    tag += 1;
    asm volatile("" : "+s"(tag));
  };
  unsigned pX[2], pW[2];
#pragma unroll
  for (int k = 0; k < 2; ++k) {
    const int q = l15 >> 2, p = lane & 3, sz = (q >> 1) | ((g4 & 1) << 1);
    pX[k] = lb + (wm * 32 + q * 8 + p) * 128 + ((((k ^ ksb) * 4 + g4) ^ (l15 & 7)) << 4);
    pW[k] = lb + ((k ^ ksb) * 32 + g4 * 8 + q) * 128 + (((sz << 1) | (p >> 1)) << 4) + (p & 1) * 8;
  }
  bf16x8 ones;
#pragma unroll
  for (int e = 0; e < 8; ++e) ones[e] = (short)0x3F80;
  f32x4 xm[2], xr[2];
  auto ldxs = [&]() {
#pragma unroll
    for (int i = 0; i < 2; ++i) {
      xm[i] = *(const f32x4*)(smem + L_XM + (wm * 32 + g4 * 8 + i * 4) * 4);
      xr[i] = *(const f32x4*)(smem + L_XR + (wm * 32 + g4 * 8 + i * 4) * 4);
    }
  };
  auto act = [&](const f32x4& a, float c, int i) {
    const f32x2 nc = {-c, -c};
    const f32x2 lo = gelu2(__builtin_elementwise_fma((f32x2){xm[i][0], xm[i][1]}, nc, (f32x2){a[0], a[1]}) * (f32x2){xr[i][0], xr[i][1]});
    const f32x2 hi = gelu2(__builtin_elementwise_fma((f32x2){xm[i][2], xm[i][3]}, nc, (f32x2){a[2], a[3]}) * (f32x2){xr[i][2], xr[i][3]});
    return (f32x4){lo[0], lo[1], hi[0], hi[1]};
  };
  auto pack8 = [&](const f32x4& a, const f32x4& c) { return (u32x4){pack2(a[0], a[1]), pack2(a[2], a[3]), pack2(c[0], c[1]), pack2(c[2], c[3])}; };
  auto z1at = [&](int f0) { return (u32x4*)(smem + L_Z1 + (f0 + l15) * 128 + (((wm * 4 + g4) ^ (l15 & 7)) << 4)); };
  auto z1put = [&](int f0, const u32x4& v) { *z1at(f0) = v; };
  f32x4 ac2[2][2], cs2[2];
  s16x4 VF[10][2][2];
  unsigned aV[2][2];
  auto rdV = [&](auto hc_, auto nc, int r) {
    constexpr int n = decltype(nc)::value, c = 5 * decltype(hc_)::value + (n >> 1), so = (VSLOT[c] - 2) * 8192, po = n < 2 ? -1 : ((VSLOT[n < 2 ? c : c - 1] - 2) * 8192) >> 16;
    const int jj = r >> 1;
    if (!(r & 1)) {
      if ((so >> 16) != po) aV[n & 1][jj] = opq((pW[n & 1] ^ (unsigned)(wl * 64 + jj * 32)) + L_SLOT + 2 * 8192 + (so & ~0xFFFF));
      VF[n][jj][0] = tr_asm<(so & 0xFFFF)>(aV[n & 1][jj]);
    } else VF[n][jj][1] = tr_asm<(so & 0xFFFF) + 512>(aV[n & 1][jj]);
    SB();
  };
  auto mm2 = [&](auto hc_, auto nc, bf16x8 (*X)[2][2], int nw, auto&& nxt) {
    constexpr int n = decltype(nc)::value, j = n >> 1, k = n & 1;
    constexpr bool z = n == 0 && !decltype(hc_)::value;
    const bf16x8 w0 = join8(VF[n][0][0], VF[n][0][1]), w1 = join8(VF[n][1][0], VF[n][1][1]);
    if (k == 0) { MFMAW<n == 0>(nw, cs2[0], ones, w0); MFMAW<n == 0>(-1, cs2[1], ones, w1); }
    MFMAW<z>(k ? nw : -1, ac2[0][0], X[j][k][0], w0); nxt(0);
    MFMAW<z>(-1, ac2[1][0], X[j][k][1], w0); nxt(1);
    MFMAW<z>(-1, ac2[0][1], X[j][k][0], w1); nxt(2);
    MFMAW<z>(-1, ac2[1][1], X[j][k][1], w1); nxt(3);
    if constexpr (n == 9) {
      pin(cs2[0], cs2[1]);
      if (g4 < 2) *(float*)(smem + L_CS2 + (wave * 32 + lane) * 4) = g4 ? cs2[1][0] : cs2[0][0];
    }
    SB();
  };
  const unsigned p2a = lb + L_P2 + cw * 4096 + lin;
  auto sttag = [&](const char* p) {
    asm volatile("s_mov_b64 exec, 1\n\tglobal_store_dwordx2 %0, %1, %2 sc1\n\ts_mov_b64 exec, -1" :: "v"(lin), "v"((u32x2){opq((unsigned)tag), opq((unsigned)(tag >> 32))}), "s"(p) : "memory");
  };
  auto ldtag = [&](u32x2& f, unsigned vo, const char* p) { asm volatile("global_load_dwordx2 %0, %1, %2 sc1" : "=&v"(f) : "v"(vo), "s"(p) : "memory"); };
  auto notag = [&](const u32x2& f) { return __builtin_amdgcn_ballot_w64(f[0] != (unsigned)tag || f[1] != (unsigned)(tag >> 32)) != 0; };
  auto waittag = [&](u32x2& f, unsigned vo, const char* p) {
    while (__builtin_expect(notag(f) && tmo > 0, 0)) {
      --tmo;
      __builtin_amdgcn_s_sleep(4);
      ldtag(f, vo, p);
      wait_vm<0>(f);
    }
  };
  u32x2 ef;

  char* const hf = ws + WS_H;
  bf16x8 Wq[15];
  const char* wg = ws + WS_WO + (size_t)(wave * 120 + cb * 5) * 1024;
  auto ldW = [&]() {
    ldtag(ef, (unsigned)((lane & 3) * 8), ws + WS_LF + (rb * 8 + wave) * 32);
#pragma unroll
    for (int n = 0; n < 15; ++n)
      asm volatile("global_load_dwordx4 %0, %1, %2 offset:%3 sc1" : "=&v"(Wq[n]) : "v"(lin), "s"(wg + (n / 5) * 40960 + 2048), "i"((n % 5) * 1024 - 2048) : "memory");
    SB();
  };
  f32x4 ac3[5];
  auto gemm3 = [&](auto khc) {
    constexpr int kh = decltype(khc)::value;
    unsigned hoff[4];
#pragma unroll
    for (int it = 0; it < 4; ++it) hoff[it] = (unsigned)(((rb * 4 + ((cw + 1 + it) & 3)) * 24 + 3 * wave) * 1024) + lin;
    u32x4 Hq[4][3];
    auto ldh1 = [&](int it, int k) {
      asm volatile("global_load_dwordx4 %0, %1, %2 offset:%3 " HSC : "=&v"(Hq[it][k]) : "v"(hoff[it]), "s"(hf), "i"(k * 1024) : "memory");
    };
    waittag(ef, (unsigned)((lane & 3) * 8), ws + WS_LF + (rb * 8 + wave) * 32);
    AB(18);
    signal1(flg + 48);
    flag_wait_asm(flg + 48, 8u);
    AB(19);
    __builtin_amdgcn_s_setprio(3);
    ldh1(0, 0); ldh1(0, 1); ldh1(0, 2);
    __builtin_amdgcn_s_setprio(2);
    ldh1(1, 0); ldh1(1, 1); ldh1(1, 2);
    __builtin_amdgcn_s_setprio(kh * C3P);
    SB();
    const unsigned px = lb + (kh ? P3_C + cw * 15360 : P3_G + cw * 20480) + lin;
    f32x4 pa[5][5], ae[5];
    auto put = [&](int it, int j) { asm volatile("ds_write_b128 %0, %1 offset:%2" :: "v"(px), "v"((it & 1) ? ac3[j] : ae[j]), "i"((it * 5 + j) * 1024) : "memory"); };
    auto fetch = [&](int n, int g, int q) {
      const int ms = (cw - 1 - q) & 3;
      const unsigned pn = lb + (g ? P3_C + ms * 15360 : P3_G + ms * 20480) + lin;
#pragma unroll
      for (int j = 0; j < 5; ++j) asm volatile("ds_read_b128 %0, %1 offset:%2" : "=&v"(pa[n][j]) : "v"(pn), "i"((q * 5 + j) * 1024) : "memory");
      SB();
    };
    sfor<0, 4>([&](auto itc) {
      constexpr int it = decltype(itc)::value;
      wait_vm<(it < 3 ? 3 : 0)>(Hq[it][0], Hq[it][1], Hq[it][2]);
      if constexpr (it == 0) pin(Wq[0], Wq[1], Wq[2], Wq[3], Wq[4], Wq[5], Wq[6], Wq[7], Wq[8], Wq[9], Wq[10], Wq[11], Wq[12], Wq[13], Wq[14]);
      auto bad = [&]() { return __builtin_amdgcn_ballot_w64(umx(umx(Hq[it][0][3], Hq[it][1][3]), Hq[it][2][3]) == HOLE) != 0; };
      if (__builtin_expect(bad(), 0)) {
        do {
          --tmo;
          asm volatile("s_sleep 2\n\tglobal_load_dwordx4 %0, %3, %4 sc1\n\tglobal_load_dwordx4 %1, %3, %4 offset:1024 sc1\n\t"
                       "global_load_dwordx4 %2, %3, %4 offset:2048 sc1\n\ts_waitcnt vmcnt(0)"
                       : "+v"(Hq[it][0]), "+v"(Hq[it][1]), "+v"(Hq[it][2]) : "v"(hoff[it]), "s"(hf) : "memory");
        } while (bad() && tmo > 0);
      }
      SB();
#pragma unroll
      for (int k = 0; k < 3; ++k)
#pragma unroll
        for (int j = 0; j < 5; ++j) {
          f32x4& ac = (it & 1) ? ac3[j] : ae[j];
          if (k == 0) MFMA0(ac, Wq[j], Hq[it][0]); else MFMA(ac, Wq[k * 5 + j], Hq[it][k]);
          if constexpr (it + 2 < 4) { if (k == 0 && j < 3) ldh1(it + 2 < 4 ? it + 2 : 0, j); }
          const int m = k * 5 + j - WO3;
          if (it > 0 && m >= 0 && m < 10 && !(m & 1)) put(it - 1, m >> 1);
          if (it > 0 && m == 8) signal1(flg + 160 + (it < 3 ? kh : 2) * 16 + ((cw + it) & 3) * 4);
          if (it == 3 && m == FE3) { flag_wait_asm(flg + 160 + wave * 4, 2u); fetch(0, kh, 0); fetch(1, kh, 1); }
          SB();
        }
      if constexpr (it == 3) {
        if (kh) {
          flag_wait_asm(flg + 192 + cw * 4, 3u);
          fetch(2, 1, 2); fetch(3, 0, 2); fetch(4, 0, 3);
        } else asm volatile("s_waitcnt lgkmcnt(0)\n\ts_nop 4" ::: "memory");
        pin(pa[0][0], pa[0][1], pa[0][2], pa[0][3], pa[0][4], pa[1][0], pa[1][1], pa[1][2], pa[1][3], pa[1][4]);
#pragma unroll
        for (int j = 0; j < 5; ++j) {
          pa[0][j] += pa[1][j];
          pin(pa[0][j], ac3[j]);
          ac3[j] += pa[0][j];
          if (!kh) put(3, j);
        }
        if (kh) {
          wait_lg<5>(pa[2][0], pa[2][1], pa[2][2], pa[2][3], pa[2][4], pa[3][0], pa[3][1], pa[3][2], pa[3][3], pa[3][4]);
#pragma unroll
          for (int j = 0; j < 5; ++j) ac3[j] += pa[2][j] + pa[3][j];
          wait_lg<0>(pa[4][0], pa[4][1], pa[4][2], pa[4][3], pa[4][4]);
#pragma unroll
          for (int j = 0; j < 5; ++j) ac3[j] += pa[4][j];
        } else signal1(flg + 192 + cw * 4);
      }
      SB();
      if constexpr (it == 0) AB(20);
      if constexpr (it == 2) AB(21);
    });
    AB(22);
  };
  if (wave >= 4) {
    u32x4 hole = {HOLE, HOLE, HOLE, HOLE};
    bf16x8 XF[5][2][2];
    auto rdXF = [&](auto cc) {
      constexpr int c = decltype(cc)::value, so = L_SLOT + XSLOT[c] * 8192;
#pragma unroll
      for (int k = 0; k < 2; ++k) {
        const unsigned a = pX[k] + (so & ~0x7FFF);
        ds16<(so & 0x7FFF)>(XF[c][k][0], a);
        ds16<(so & 0x7FFF) + 512>(XF[c][k][1], a);
      }
      SB();
    };
    u32x4 TA[16];
    auto ldT = [&](u32x4& r, int d, int w, int i) {
      asm volatile("global_load_dwordx4 %0, %1, %2" : "=&v"(r) : "v"(aoff[i & 1] + (unsigned)(((d + rot2) & 7) * 128)), "s"(gS + ((w + 1 + (i >> 1)) & 3) * 65536 + w * 1024) : "memory");
    };
    auto ldx = [&](int c) {
      u32x4 xv;
      asm volatile("global_load_dwordx4 %0, %1, %2\n\ts_waitcnt vmcnt(0)" : "=&v"(xv) : "v"((unsigned)(lane * (D_ * 2) + c * 16)), "s"(x + (size_t)t0 * D_) : "memory");
      return xv;
    };
    const bool docv = cw < ((rb & 1) ? 2 : 3);
    const int d = d0 + (cw < 2 ? 2 * rb + cw : 64 + (rb >> 1));
    u32x4 hc[4], uc[4];
    unsigned Dw;
    auto ldhc = [&]() {
#pragma unroll
      for (int k = 0; k < 4; ++k) asm volatile("global_load_dwordx4 %0, %1, %2 offset:%3" : "=&v"(hc[k]) : "v"(lin), "s"(ssm_k + (size_t)d * S_), "i"(k * 1024) : "memory");
      asm volatile("global_load_ushort %0, %1, %2" : "=&v"(Dw) : "v"(0u), "s"(ssm_D + d) : "memory");
    };
    unsigned rwo;
    f32x2 ra, rc;
    auto rdraw = [&]() { asm volatile("ds_read_b64 %0, %2\n\tds_read_b64 %1, %2 offset:512" : "=&v"(ra), "=&v"(rc) : "v"(rwo) : "memory"); SB(); };
    const char* efp = ws + WS_EF + cb * 256;
    char* cb_ = smem + M_CV + cw * CV_BUF;
    auto run = [&](auto ic) {
      constexpr int i = decltype(ic)::value, p = cnt('_', i) + 1;
      constexpr char c = SCH[i];
      if constexpr (c == '_') {
        if constexpr (p == 1) {
          inctag();
          pin(ones, hole);
          AB(1);
        }
        CB(3 * (p - XBAR));
        asm volatile("s_waitcnt vmcnt(%0) lgkmcnt(0)" :: "n"(behind(i, '_')) : "memory");
        CB(3 * (p - XBAR) + 1);
        asm volatile("s_barrier" ::: "memory");
        CB(3 * (p - XBAR) + 2);
        SB();
#pragma unroll
        for (int q = 0; q < 5; ++q) if (XNEED[q] + 1 == p) pin(XF[q][0][0], XF[q][0][1], XF[q][1][0], XF[q][1][1]);
        if constexpr (p == 7) {
          rwo = lb + L_RAW + ((lane >> 3) * 4 + (lane & 3)) * 16 + (lane & 4) * 2;
          pin(rwo);
        }
        if constexpr (p == 9) __builtin_amdgcn_s_setprio(CPR);
        if constexpr (p == 2) AB(4);
        if constexpr (p == 6) AB(5);
        if constexpr (p == 9) AB(6);
      }
      run0(ic);
      if constexpr (c == 'x') rdXF(IC(cnt('x', i)));
      if constexpr (c == 'v') sfor<2 * cnt('v', i), 2 * cnt('v', i) + 2>([&](auto nc) { rdV(IC(0), nc, 0); rdV(IC(0), nc, 1); rdV(IC(0), nc, 2); rdV(IC(0), nc, 3); });
      if constexpr (c == 'm') {
        constexpr int n = cnt('m', i);
        mm2(IC(0), IC(n), XF, VNEED[n >> 1] < p - 1 || (n & 1) ? -1 : 0, [](int) {});
      }
      if constexpr (c == 'p') {
        asm volatile("s_nop 7" : "+v"(ac2[0][0]), "+v"(ac2[0][1]), "+v"(ac2[1][0]), "+v"(ac2[1][1]));
        dsw16<0>(p2a, ac2[0][0]); dsw16<1024>(p2a, ac2[1][0]); dsw16<2048>(p2a, ac2[0][1]); dsw16<3072>(p2a, ac2[1][1]);
      }
      if constexpr (c == 'E' || c == 'D') { if (wave == 5) sttag(ws + WS_EF + (c == 'D' ? 2048 : 0) + (cb * 32 + rb) * 8); }
      if constexpr (c == 'U') {
        wait_vm<behind(i, 'F')>(ef);
        waittag(ef, (unsigned)((lane & 31) * 8), efp);
#pragma unroll
        for (int k = 0; k < 4; ++k) asm volatile("global_load_dwordx4 %0, %1, %2 offset:%3 sc1" : "=&v"(uc[k]) : "v"(lin), "s"(ws + WS_KV + (size_t)d * (S_ * 2)), "i"(k * 1024) : "memory");
      }
      if constexpr (c == 'F') ldtag(ef, (unsigned)((lane & 31) * 8), efp);
      if constexpr (c == 'o') {
        wait_vm<behind(i, 'O')>();
        if (wid < 120) {
          const unsigned ra = lb + L_WO + cw * 1024 + (8 * g4 + (l15 >> 2)) * 32 + 8 * (lane & 3);
          s16x4 lo = tr_asm<0>(ra), hi = tr_asm<128>(ra);
          wait_lg<0>(lo, hi);
          st16(lin, ws + WS_WO + (size_t)((wid / 5) * 40 + cb * 5 + wid % 5) * 1024, __builtin_bit_cast(u32x4, join8(lo, hi)));
        }
      }
      if constexpr (c == 'h') {
        constexpr int n = cnt('h', i);
        st16(lin, ws + (n < 3 ? WS_Z2 + (size_t)((rb * 2 + wl) * 48 + cb * 6 + wm * 3 + n) * 1024 : WS_H + (size_t)((rb * 4 + cw) * 24 + cb * 3 + n - 3) * 1024), hole);
      }
      if constexpr (c == 't') { constexpr int n = cnt('t', i); ldT(TA[n], 3 + (n >> 3), cw, n & 7); }
      if constexpr (c == 'H') ldhc();
      if constexpr (c == 'R') rdraw();
      if constexpr (c == 'V') {
        wait_vm<behind(i, '_')>();
        signal1(flg + 60);
        AB(2);
      }
      if constexpr (c == 'I') {
        wait_vm<behind(i, 'H')>(hc[0], hc[1], hc[2], hc[3], Dw);
        if (docv) {
          if (lane < 36) *(u32x4*)(cb_ + (lane < 32 ? lane * 16 : CV_C + ((lane >> 1) & 1) * CV_CS + 4096 + (lane & 1) * 16)) = (u32x4){0u, 0u, 0u, 0u};
#pragma unroll
          for (int k = 0; k < 4; ++k) {
            const int a = k * 64 + lane;
            const int h0 = k ? __builtin_amdgcn_readlane((int)hc[k ? k - 1 : 0][3], 63) : 0;
            const unsigned hp = (unsigned)__builtin_amdgcn_update_dpp(h0, (int)hc[k][3], 0x138, 0xF, 0xF, false);
            const unsigned w[5] = {hp, hc[k][0], hc[k][1], hc[k][2], hc[k][3]};
            u32x4 sw, mx;
#pragma unroll
            for (int q = 0; q < 4; ++q) {
              sw[q] = (w[4 - q] >> 16) | (w[4 - q] << 16);
              mx[q] = (w[4 - q] & 0xFFFFu) | (w[3 - q] & 0xFFFF0000u);
            }
            char* cp = cb_ + CV_C + (2040 - 8 * a) * 2;
            *(u32x4*)(cp) = sw;
            *(u32x4*)(cp + CV_CS) = mx;
          }
        }
      }
      if constexpr (c == 'J') {
        wait_vm<behind(i, 'U')>(uc[0], uc[1], uc[2], uc[3]);
        if (docv) {
#pragma unroll
          for (int k = 0; k < 4; ++k) *(u32x4*)(cb_ + 512 + (k * 64 + lane) * 16) = uc[k];
        }
      }
      if constexpr (c == 'S') {
        wait_lg<0>(ra, rc);
        if (wave == 6) {
          const float s1 = nv(ra[0]) + rc[0], fm = s1 * (1.0f / 640.0f), sm = s1 * fm;
          float fq = (ra[1] - sm) + rc[1];
          if (__builtin_expect(__builtin_amdgcn_ballot_w64(!(fq >= 1e-3f * sm)) != 0, 0)) {
            const f32x2 mm = {fm, fm};
            f32x2 q2 = {0.f, 0.f};
#pragma unroll 1
            for (int c = 0; c < 80; ++c) {
              const u32x4 xv = ldx(c);
#pragma unroll
              for (int e = 0; e < 4; ++e) { const f32x2 d = unpk(xv[e]) - mm; q2 = __builtin_elementwise_fma(d, d, q2); }
            }
            fq = q2[0] + q2[1];
          }
          const float vbf = rbf(fmaxf(fq, 0.f) * (1.0f / 640.0f));
          *(float*)(smem + L_XM + lane * 4) = rbf(fm);
          *(float*)(smem + L_XR + lane * 4) = __builtin_amdgcn_rcpf(rbf(__builtin_amdgcn_sqrtf(rbf(vbf + 1e-5f))));
        }
      }
      SB();
    };
    sfor<pos('_'), NSCH>(run);
    __builtin_amdgcn_s_setprio(0);
    AB(9);
    auto ldS = [&](auto dc, auto wc) {
      constexpr int dd = decltype(dc)::value, w = decltype(wc)::value;
      const unsigned to = (unsigned)(((dd + rot2) & 7) * 128);
      if (THR < 60) wait_vm<THR>();
      dma32s<M_RING + w * 24576 + (2 - dd) * 8192, ((w + 1) & 3) * 65536 + w * 1024>(aoff[0] + to, aoff[1] - 1024 + to, gS, lb);
      if (THR < 60) wait_vm<THR>();
      dma32s<M_RING + w * 24576 + (2 - dd) * 8192 + 2048, ((w + 2) & 3) * 65536 + w * 1024>(aoff[0] + to, aoff[1] - 1024 + to, gS, lb);
      if (THR < 60) wait_vm<THR>();
      dma32s<M_RING + w * 24576 + (2 - dd) * 8192 + 4096, ((w + 3) & 3) * 65536 + w * 1024>(aoff[0] + to, aoff[1] - 1024 + to, gS, lb);
      if (THR < 60) wait_vm<THR>();
      dma32s<M_RING + w * 24576 + (2 - dd) * 8192 + 6144, w * 65536 + w * 1024>(aoff[0] + to, aoff[1] - 1024 + to, gS, lb);
      SB();
    };
    auto epi2 = [&]() {
      flag_wait_asm(flg + 132, 4u);
#pragma unroll
      for (int jj = 0; jj < 2; ++jj) {
        u32x4* zp = z1at(odd * 32 + wl * 32 + jj * 16);
        u32x4 o = *zp;
#pragma unroll
        for (int q = 0; q < 4; ++q) o[q] = pack2s(gelu1(nv(lo16(o[q]))), gelu1(hi16(o[q])));
        *zp = o;
      }
      signal1(flg + 56);
      SB();
    };
    auto putT = [&](const u32x4* T, unsigned a, unsigned f) {
#pragma unroll
      for (int i = 0; i < 8; ++i) asm volatile("ds_write_b128 %0, %1 offset:%2" :: "v"(a), "v"(T[i]), "i"(i * 1024) : "memory");
      signal1(f);
    };
    const unsigned tx = lb + M_CV + cw * CV_BUF + lin;
    auto landT = [&]() {
      sttag(ws + WS_LF + (rb * 8 + cb) * 32 + cw * 8);
#pragma unroll
      for (int n = 0; n < 16; ++n) pin(TA[n]);
      putT(TA, tx, flg + 112 + cw * 4);
    };
    auto putT4 = [&]() {
      flag_wait_asm(flg + 96 + cw * 4, 1u);
      putT(TA + 8, lb + M_RING + cw * 24576 + 16384 + lin, flg + 112 + cw * 4);
    };
    if (docv) {
      flag_wait_asm(flg + 36, 4u);
      AB(12);
      f32x4 ac[8];
      conv_core(lds_addr(cb_), lane, Dw, ac);
      AB(13);
      signal1(flg + 40);
      wait_vm<0>();
      landT();
#pragma unroll
      for (int k = 0; k < 4; ++k) {
        u32x2 ov[2];
#pragma unroll
        for (int t = 0; t < 2; ++t)
#pragma unroll
          for (int h = 0; h < 2; ++h) ov[t][h] = pack2(ac[2 * k + t][2 * h], ac[2 * k + t][2 * h + 1]);
        const auto s0 = __builtin_amdgcn_permlane16_swap(ov[0][0], ov[1][0], false, false);
        const auto s1 = __builtin_amdgcn_permlane16_swap(ov[0][1], ov[1][1], false, false);
        st16((unsigned)((256 * (g4 & 1) + 16 * l15 + 8 * (g4 >> 1)) * 2), ws + WS_Y + (size_t)d * (S_ * 2) + k * 1024, (u32x4){s0[0], s1[0], s0[1], s1[1]});
      }
      wait_vm<0>();
      sttag(ws + WS_YF + d * 8);
      putT4();
      epi2();
    } else if (cw != 3) {
      wait_vm<0>();
      landT();
      putT4();
      epi2();
    } else {
      ldS(IC(0), IC(0)); ldS(IC(0), IC(1)); ldS(IC(0), IC(2)); ldS(IC(1), IC(1)); ldS(IC(2), IC(1));
      wait_vm<40>();
      landT();
      AB(12);
      flag_wait_asm(flg + 16, 4u);
      AB(8);
      wait_vm<24>();
      ldS(IC(0), IC(3)); ldS(IC(1), IC(0)); ldS(IC(1), IC(2)); ldS(IC(1), IC(3));
      wait_vm<24>();
      signal1(flg + 32);
      ldS(IC(2), IC(0)); ldS(IC(2), IC(2)); ldS(IC(2), IC(3));
      AB(10);
      u32x4 TB[32];
#pragma unroll
      for (int n = 0; n < 32; ++n) {
        if (n == 8) wait_vm<32>();
        if (THR < 60 && !(n & 1)) wait_vm<THR>();
        if (THR < 32 && n == THR) signal1(flg + 32);
        ldT(TB[n], 5, n >> 3, n & 7);
      }
      wait_vm<32>();
      if (THR >= 32) signal1(flg + 32);
      AB(11);
      wait_vm<0>();
#pragma unroll
      for (int n = 0; n < 32; ++n) pin(TB[n]);
      putT4();
#pragma unroll
      for (int w = 0; w < 4; ++w) {
        flag_wait_asm(flg + 96 + w * 4, 2u);
        putT(TB + 8 * w, lb + M_RING + w * 24576 + 8192 + lin, flg + 144 + w * 4);
      }
      epi2();
      if (!odd) {
        u32x4 zx[4];
        ldtag(ef, (unsigned)((lane & 1) * 8), ws + WS_XF + (rb * 4 + pr) * 16);
        wait_vm<0>(ef);
        waittag(ef, (unsigned)((lane & 1) * 8), ws + WS_XF + (rb * 4 + pr) * 16);
#pragma unroll
        for (int c = 0; c < 4; ++c)
          asm volatile("global_load_dwordx4 %0, %1, %2 offset:%3 sc1" : "=&v"(zx[c]) : "v"(lin), "s"(ws + WS_ZX + (size_t)(rb * 4 + pr) * 4096), "i"(c * 1024) : "memory");
        wait_vm<0>(zx[0], zx[1], zx[2], zx[3]);
#pragma unroll
        for (int c = 0; c < 4; ++c) *(u32x4*)(smem + L_Z1 + (64 + (c & 1) * 16 + l15) * 128 + ((((c >> 1) * 4 + g4) ^ (l15 & 7)) << 4)) = zx[c];
      }
      signal1(flg + 56);
      AB(13);
    }
    SB();
    wait_vm<0>();
    AB(14);
    u32x2 xv[5];
    f32x2 sv[5][2];
    flag_wait_asm(flg + 40, (rb & 1) ? 2u : 3u);
    const int yc = min(cw * 24, 56);
    const unsigned yfo = (unsigned)(min(lane, 23) * 8);
    const char* yfp = ws + WS_YF + (d0 + yc) * 8;
    auto ldy = [&]() {
#pragma unroll
      for (int n = 0; n < 3; ++n) {
        const int q = (yc >> 3) + n;
        asm volatile("s_mov_b32 m0, %2\n\ts_nop 0\n\tglobal_load_lds_dwordx4 %0, %1 sc1" :: "v"((unsigned)(r8 * (S_ * 2) + ((c8 ^ (r8 & 6)) << 4))),
                     "s"(ws + WS_Y + ((size_t)(d0 + q * 8) * S_ + t0) * 2), "s"(lb + yat(0) + (q >> 2) * CV_BUF + (q & 3) * 1024) : "memory");
      }
    };
    flag_wait_asm(flg + 96 + cw * 4, 3u);
    ldtag(ef, yfo, yfp);
    wait_vm<0>(ef);
    flag_wait_asm(flg + 44, 4u);
    waittag(ef, yfo, yfp);
    AB(15);
    ldy();
#pragma unroll
    for (int j = 0; j < 5; ++j)
      asm volatile("global_load_dwordx2 %0, %1, %2 offset:%3" : "=&v"(xv[j]) : "v"((unsigned)(l15 * D_ * 2 + 8 * g4)), "s"(x + (size_t)(t0 + cw * 16) * D_ + d0), "i"(j * 32) : "memory");
    ldW();
    wait_vm<16>(xv[0], xv[1], xv[2], xv[3], xv[4]);
    AB(17);
    signal1(flg + 52);
    flag_wait_asm(flg + 52, 4u);
    {
      const int yr_ = 4 * g4 + (l15 >> 2);
      const unsigned ya = lb + yr_ * 128 + (((cw * 2 + ((lane >> 1) & 1)) ^ (yr_ & 6)) << 4) + (lane & 1) * 8;
      const float nm = -*(const float*)(smem + L_XM + (cw * 16 + l15) * 4), sr = *(const float*)(smem + L_XR + (cw * 16 + l15) * 4);
      s16x4 yr[5] = {tr_asm<yat(0)>(ya), tr_asm<yat(1)>(ya), tr_asm<yat(2)>(ya), tr_asm<yat(3)>(ya), tr_asm<yat(4)>(ya)};
      wait_lg<0>(yr[0], yr[1], yr[2], yr[3], yr[4]);
      AB(16);
      flag_wait_asm(flg + 96 + cw * 4, 4u);
#pragma unroll
      for (int j = 0; j < 5; ++j) {
        const u32x2 yw = __builtin_bit_cast(u32x2, yr[j]);
#pragma unroll
        for (int h = 0; h < 2; ++h) {
          sv[j][h] = ((unpk(xv[j][h]) + (f32x2){nm, nm}) * (f32x2){sr, sr}) * unpk(yw[h]);
          pin(sv[j][h]);
        }
      }
      SB();
    }
    wait_vm<15>(ef);
    gemm3(IC(1));
    unsigned pk[5][2];
#pragma unroll
    for (int j = 0; j < 5; ++j)
#pragma unroll
      for (int h = 0; h < 2; ++h) {
        const f32x2 o2 = {ac3[j][2 * h], ac3[j][2 * h + 1]};
        const f32x2 a = o2 * (f32x2){-1.4426950f, -1.4426950f};
        const f32x2 e = (f32x2){__builtin_amdgcn_exp2f(a[0]), __builtin_amdgcn_exp2f(a[1])} + (f32x2){1.0f, 1.0f};
        const f32x2 r = {__builtin_amdgcn_rcpf(e[0]), __builtin_amdgcn_rcpf(e[1])};
        pk[j][h] = pk2(__builtin_elementwise_fma(o2 * r, sv[j][h], unpk(xv[j][h])));
      }
    u16* op = out + (size_t)(t0 + cw * 16 + l15) * D_ + d0;
#pragma unroll
    for (int q = 0; q < 2; ++q) {
      const auto s0 = __builtin_amdgcn_permlane16_swap(pk[2 * q][0], pk[2 * q + 1][0], false, false);
      const auto s1 = __builtin_amdgcn_permlane16_swap(pk[2 * q][1], pk[2 * q + 1][1], false, false);
      *(u32x4*)(op + 32 * q + 16 * (g4 & 1) + 8 * (g4 >> 1)) = (u32x4){s0[0], s1[0], s0[1], s1[1]};
    }
    *(u32x2*)(op + 64 + 4 * g4) = (u32x2){pk[4][0], pk[4][1]};
  } else {
    u32x4 ko[2];
    unsigned k4o[2];
    {
      inctag();
      pin(ones);
      AB(1);
      wait_vm<0>();
      signal1(flg + 224);
      flag_wait_asm(flg + 224, 4u);
      AB(29);
      unsigned kk[16], vv[10];
      const char* kl = smem + L_KV + c8 * KV_P + wave * 32 + r8 * 4;
      const char* k4 = smem + L_KV + 128 + wave * 8 + ((lane >> 2) & 7) * KV_P + (lane >> 5) * 4 + (lane & 3) * 320;
#pragma unroll
      for (int i = 0; i < 16; ++i) kk[i] = *(const unsigned*)(i < 11 ? kl + i * 160 : k4 + (i - 11) * 160);
#pragma unroll
      for (int i = 0; i < 10; ++i) vv[i] = *(const unsigned*)(i < 8 ? kl + (11 + i) * 160 : k4 + (3 + i) * 160);
      const u32x4 sw[2] = {*(const u32x4*)(smem + L_SW + wave * 128 + r8 * 16), *(const u32x4*)(smem + L_SW + 512 + wave * 32 + (lane >> 5) * 16)};
      f32x2 kf[16], rr[10];
#pragma unroll
      for (int i = 0; i < 16; ++i) kf[i] = unpk((i < 3 ? t0 + c8 == 0 : (i >= 11 && i < 14 && t0 + (lane & 31) * 2 + i < 14)) ? 0u : kk[i]);
#pragma unroll
      for (int i = 0; i < 10; ++i) {
        const u32x4& w = sw[i >> 3];
        const int k0 = i < 8 ? i : i + 3;
        f32x2 a0 = kf[k0] * (f32x2){hi16(w[1]), hi16(w[3])};
        a0 = __builtin_elementwise_fma(kf[k0 + 1], (f32x2){lo16(w[1]), lo16(w[3])}, a0);
        a0 = __builtin_elementwise_fma(kf[k0 + 2], (f32x2){hi16(w[0]), hi16(w[2])}, a0);
        a0 = __builtin_elementwise_fma(kf[k0 + 3], (f32x2){lo16(w[0]), lo16(w[2])}, a0);
        rr[i] = a0 * unpk(vv[i]);
      }
#pragma unroll
      for (int c = 0; c < 2; ++c) {
        ko[c] = (u32x4){pack2(rr[0][c], rr[1][c]), pack2(rr[2][c], rr[3][c]), pack2(rr[4][c], rr[5][c]), pack2(rr[6][c], rr[7][c])};
        k4o[c] = pack2(rr[8][c], rr[9][c]);
        pin(ko[c], k4o[c]);
      }
      SB();
      AB(30);
    }
    unsigned kso[2] = {(unsigned)((r8 * 2 * S_ + c8 * 8) * 2), (unsigned)((lane >> 5) * 4 * S_ + (lane & 31) * 4)};
    pin(kso[0], kso[1]);
    const bool isz2 = !(odd && wl);
    const char* stp = ws + WS_ST + (size_t)((rb * 12 + pr * 3 + odd * 2 + wl) * 1024 + wm * 512);
    auto kvst = [&](int c) {
      st16(kso[0], ws + WS_KV + ((size_t)(d0 + wave * 16 + c) * S_ + t0) * 2, ko[c]);
      asm volatile("global_store_dword %0, %1, %2 sc1" :: "v"(kso[1]), "v"(k4o[c]), "s"(ws + WS_KV + ((size_t)(d0 + 64 + wave * 4 + c) * S_ + t0) * 2) : "memory");
      if (c && STW && isz2) st8(lin >> 1, stp, (u32x2){0u, ~(unsigned)tag});
      SB();
    };
    const char* Ab[4];
#pragma unroll
    for (int i = 0; i < 4; ++i) Ab[i] = (const char*)w_sp + (size_t)(t0 + ((wave + 1 + i) & 3) * 16) * (S_ * 2) + wave * 1024;
    const unsigned ringl = lb + M_RING + wave * 24576;
    auto ldA1 = [&](int d, int i) {
      dma16o(aoff[i & 1] + (unsigned)(((d + rot2) & 7) * 128), Ab[i >> 1], 0, d == 7 ? lb + M_CV + wave * CV_BUF : ringl, i * 1024);
    };
    unsigned bsv;
    {
      f32x4 zv[2][4], cs[4];
      s16x4 WB[20][4][2];
      bf16x8 XG[10][2][2];
      unsigned aX[2][2], aW[2][2][4];
#pragma unroll
      for (int k = 0; k < 2; ++k)
#pragma unroll
        for (int w = 0; w < 2; ++w) {
          aX[w][k] = pX[k] + L_SLOT + XW * 8192 + w * 65536;
          pin(aX[w][k]);
#pragma unroll
          for (int jj = 0; jj < 4; ++jj) { aW[w][k][jj] = (pW[k] ^ (unsigned)(jj * 32)) + L_SLOT + wl * 8192 + w * 65536; pin(aW[w][k][jj]); }
        }
      auto rd1 = [&](auto nc, int r) {
        constexpr int n = decltype(nc)::value, j = n >> 1, k = n & 1, c = n > 0 && n < 11 ? n - 1 : 0, sx = (XSLOT[c] - XW) * 8192, sa = WSLOT[j] * 8192;
        if (r < 0) { if (r & 1) ds16<(sx & 0xFFFF) + 512>(XG[c][(r + 4) >> 1][1], aX[sx >> 16][(r + 4) >> 1]); else ds16<(sx & 0xFFFF)>(XG[c][(r + 4) >> 1][0], aX[sx >> 16][(r + 4) >> 1]); }
        else if (r & 1) WB[n][r >> 1][1] = tr_asm<(sa & 0xFFFF) + 512>(aW[sa >> 16][k][r >> 1]);
        else WB[n][r >> 1][0] = tr_asm<(sa & 0xFFFF)>(aW[sa >> 16][k][r >> 1]);
        SB();
      };
      auto bar = [&](auto bc) {
        SB();
        constexpr int p = decltype(bc)::value;
        CB(3 * (p - XBAR));
        if constexpr (p == 1 || p >= 8) asm volatile("s_waitcnt lgkmcnt(0)\n\ts_barrier" ::: "memory");
        else if constexpr (p == GZ) asm volatile("s_waitcnt vmcnt(0) lgkmcnt(8)\n\ts_barrier" ::: "memory");
        else if constexpr (p < 7) asm volatile("s_waitcnt lgkmcnt(8)\n\ts_barrier" ::: "memory");
        else asm volatile("s_barrier" ::: "memory");
        CB(3 * (p - XBAR) + 2);
        SB();
      };
      float cp[4];
      f32x4 st[2][2], rv;
      const unsigned rwa = lb + L_RAW + (ksb * 2 + wm) * 256 + l15 * 16 + ((l15 >> 2) == g4 ? 0 : 1024);
      const unsigned csa = lb + L_CS + (wave * 64 + lane) * 4;
      auto kstep = [&](auto nc) {
        constexpr int n = decltype(nc)::value, j = n >> 1, k = n & 1, nn = n + 1, r0 = nn >= 2 && nn <= 10 ? -4 : 0;
        constexpr int sc = n == 1 ? 0 : n - 2, ns = !k ? 0 : (n == 1 || n == 11 ? 1 : 2);
        if constexpr (n == KS0 || n == KS1) kvst(n == KS1);
        if constexpr (k) bar(IC(nn / 2 + 1));
        int r = r0, c = 0;
        auto nxt = [&]() { if (r < 8) { rd1(IC(nn), r++); ++c; } };
        auto sel = [&](int q) {
          const f32x4& sq = st[q & 1][q >> 1];
          rv[q] = sel4(sq[0], sq[1], sq[2], sq[3], 0xAAAAAAAAAAAAAAAAull, 0xCCCCCCCCCCCCCCCCull);
          SB();
        };
#pragma unroll
        for (int jj = 0; jj < 4; ++jj) {
          const bf16x8 wf = join8(WB[n][jj][0], WB[n][jj][1]);
          MFMAW<n == 0>(jj == 0 ? 4 : (jj == 2 ? c : -1), zv[0][jj], XG[j][k][0], wf);
          SB();
          nxt();
          if (n == 12) nxt();
          if (n == 12 && jj == 2) sel(2);
          MFMAW<n == 0>(-1, zv[1][jj], XG[j][k][1], wf);
          SB();
          nxt();
          if (n == 12) nxt();
          if (n == 12 && (jj == 1 || jj == 2)) sel(jj == 1 ? 0 : 3);
          if (n == 12 && jj == 2) { dsw16<0>(rwa, rv); SB(); }
          if (k == 0) {
            MFMAW<n == 0>(-1, cs[jj], ones, wf);
            SB();
            nxt();
            if (n == 12) nxt();
            if (n == 12 && jj == 1) sel(1);
          }
#pragma unroll
          for (int q = 0; q < ns; ++q) {
            const bf16x8& xs = XG[sc + q][1][jj >> 1];
            MFMAW<n == 1>(-1, st[jj & 1][jj >> 1], xs, (jj & 1) ? xs : ones);
            SB();
            nxt();
          }
        }
      };
      auto lstep = [&](auto nc, auto ac, auto bc, int w) {
        constexpr int n = decltype(nc)::value, j = n >> 1, k = n & 1, na = decltype(ac)::value, nb = decltype(bc)::value;
        int r = 0;
        auto nxt = [&]() { if (na > 0 && r < 16) { if (r < 8) rd1(ac, r); else rd1(bc, r - 8); ++r; } };
#pragma unroll
        for (int jj = 0; jj < 4; ++jj) {
          const bf16x8 wf = join8(WB[n][jj][0], WB[n][jj][1]);
          if (k == 0) { MFMAW<false>(jj == 0 ? w : -1, cs[jj], ones, wf); SB(); nxt(); nxt(); }
          MFMAW<false>(jj == 0 && k ? w : -1, zv[0][jj], XG[j][k][0], wf);
          SB();
          nxt();
          if (k) nxt();
          MFMAW<false>(-1, zv[1][jj], XG[j][k][1], wf);
          SB();
          nxt();
          if (k) nxt();
          if (n == 15 && jj == 2) {
            dsw4(csa, sel4(cs[0][0], cs[1][0], cs[2][0], cs[3][0], 0xFFFF0000FFFF0000ull, 0xFFFFFFFF00000000ull));
            SB();
          }
        }
      };
      bar(IC(1));
      AB(2);
#pragma unroll
      for (int r = -4; r < 8; ++r) rd1(IC(0), r);
      kstep(IC(0)); AB(4); kstep(IC(1)); kstep(IC(2)); kstep(IC(3)); kstep(IC(4)); kstep(IC(5)); kstep(IC(6)); kstep(IC(7)); kstep(IC(8)); AB(5); kstep(IC(9));
      kstep(IC(10)); kstep(IC(11)); kstep(IC(12));
      bar(IC(8));
      lstep(IC(13), IC(14), IC(16), -1);
      lstep(IC(14), IC(18), IC(15), 8);
      lstep(IC(16), IC(17), IC(19), 15);
      lstep(IC(18), IC(0), IC(0), 15);
      lstep(IC(15), IC(0), IC(0), -1);
      lstep(IC(17), IC(0), IC(0), 9);
      bar(IC(9));
      __builtin_amdgcn_s_setprio(2);
#pragma unroll
      for (int q = 0; q < 4; ++q) cp[q] = *(const float*)(smem + L_CS + ((wave ^ 1) * 64 + q * 16 + l15) * 4);
      ldxs();
      SB();
      lstep(IC(19), IC(0), IC(0), -1);
      AB(6);
      float ct[4];
#pragma unroll
      for (int jj = 0; jj < 4; ++jj) ct[jj] = cs[jj][0] + cp[jj];
#pragma unroll
      for (int jj = 0; jj < 4; ++jj) {
        if (jj == 0) asm volatile("s_nop 3");
        if (jj == 2) SB();
        if (!(jj & 1)) pin(zv[0][jj], zv[1][jj], zv[0][jj + 1], zv[1][jj + 1]);
#pragma unroll
        for (int i = 0; i < 2; ++i) zv[i][jj] = act(zv[i][jj], ct[jj], i);
      }
      if (isz2) {
        f32x4 pv[2], s1[2], s2[2];
#pragma unroll
        for (int i = 0; i < 2; ++i) {
#pragma unroll
          for (int e = 0; e < 4; ++e) {
            const float z0 = zv[i][0][e];
            pv[i][e] = __int_as_float(__builtin_amdgcn_update_dpp(0, __float_as_int(z0), 0x150, 0xF, 0xF, true));
          }
          const f32x4 a0 = zv[i][0] - pv[i], a1 = zv[i][1] - pv[i], a2 = zv[i][2] - pv[i], a3 = zv[i][3] - pv[i];
          s1[i] = (a0 + a1) + (a2 + a3);
          s2[i] = __builtin_elementwise_fma(a3, a3, __builtin_elementwise_fma(a2, a2, __builtin_elementwise_fma(a1, a1, a0 * a0)));
        }
        float r[8], q[4];
#define DA(d, s, ctl, bm) "v_add_f32_dpp " d ", " s ", " s " " ctl " row_mask:0xf bank_mask:" bm "\n\t"
        asm volatile("s_nop 1\n\t"
                     DA("%0", "%12", "row_ror:8", "0x3") DA("%1", "%13", "row_ror:8", "0x3") DA("%2", "%14", "row_ror:8", "0x3") DA("%3", "%15", "row_ror:8", "0x3")
                     DA("%4", "%16", "row_ror:8", "0x3") DA("%5", "%17", "row_ror:8", "0x3") DA("%6", "%18", "row_ror:8", "0x3") DA("%7", "%19", "row_ror:8", "0x3")
                     DA("%0", "%20", "row_ror:8", "0xc") DA("%1", "%21", "row_ror:8", "0xc") DA("%2", "%22", "row_ror:8", "0xc") DA("%3", "%23", "row_ror:8", "0xc")
                     DA("%4", "%24", "row_ror:8", "0xc") DA("%5", "%25", "row_ror:8", "0xc") DA("%6", "%26", "row_ror:8", "0xc") DA("%7", "%27", "row_ror:8", "0xc")
                     DA("%8", "%0", "row_half_mirror", "0x5") DA("%9", "%1", "row_half_mirror", "0x5") DA("%10", "%2", "row_half_mirror", "0x5") DA("%11", "%3", "row_half_mirror", "0x5")
                     DA("%8", "%4", "row_half_mirror", "0xa") DA("%9", "%5", "row_half_mirror", "0xa") DA("%10", "%6", "row_half_mirror", "0xa") DA("%11", "%7", "row_half_mirror", "0xa")
                     DA("%8", "%8", "quad_perm:[1,0,3,2]", "0xf") DA("%9", "%9", "quad_perm:[1,0,3,2]", "0xf") DA("%10", "%10", "quad_perm:[1,0,3,2]", "0xf") DA("%11", "%11", "quad_perm:[1,0,3,2]", "0xf")
                     DA("%8", "%8", "quad_perm:[2,3,0,1]", "0xf") DA("%9", "%9", "quad_perm:[2,3,0,1]", "0xf") DA("%10", "%10", "quad_perm:[2,3,0,1]", "0xf") DA("%11", "%11", "quad_perm:[2,3,0,1]", "0xf")
                     : "=&v"(r[0]), "=&v"(r[1]), "=&v"(r[2]), "=&v"(r[3]), "=&v"(r[4]), "=&v"(r[5]), "=&v"(r[6]), "=&v"(r[7]),
                       "=&v"(q[0]), "=&v"(q[1]), "=&v"(q[2]), "=&v"(q[3])
                     : "v"(s1[0][0]), "v"(s2[0][0]), "v"(s1[0][1]), "v"(s2[0][1]), "v"(s1[0][2]), "v"(s2[0][2]), "v"(s1[0][3]), "v"(s2[0][3]),
                       "v"(s1[1][0]), "v"(s2[1][0]), "v"(s1[1][1]), "v"(s2[1][1]), "v"(s1[1][2]), "v"(s2[1][2]), "v"(s1[1][3]), "v"(s2[1][3]));
#undef DA
        const f32x4 p4 = (lane & 8) ? pv[1] : pv[0];
        const float pa = (lane & 4) ? p4[2] : p4[0], pb = (lane & 4) ? p4[3] : p4[1];
        const float pp = (lane & 2) ? pb : pa, S1 = (lane & 2) ? q[2] : q[0], S2 = (lane & 2) ? q[3] : q[1];
        const float dl = S1 * (1.0f / 64.0f);
        st8(lin >> 1, stp, (u32x2){__float_as_uint((lane & 1) ? fmaxf(S2 - S1 * dl, 0.f) : pp + dl), (unsigned)tag});
      } else {
#pragma unroll
        for (int jj = 0; jj < 4; ++jj) {
          u32x4 o = pack8(zv[0][jj], zv[1][jj]);
          if (jj >= 2) z1put(jj * 16 - 32, o);
          else st16(lin, ws + WS_ZX + (size_t)((rb * 4 + pr) * 4 + wm * 2 + jj) * 1024, o);
        }
      }
      signal1(flg + 36);
      SB();
      AB(7);
      __builtin_amdgcn_s_setprio(P2P);
      unsigned po = (unsigned)((lane >> 5) * 6144 + (lane & 31) * 16);
      const char* pb = ws + WS_ST + (size_t)rb * 12288 + wm * 512 + 2048;
      u32x4 sg[6];
      auto ldsg = [&]() {
#pragma unroll
        for (int c = 0; c < 6; ++c) asm volatile("global_load_dwordx4 %0, %1, %2 offset:%3 sc1" : "=&v"(sg[c]) : "v"(po), "s"(pb), "i"(c * 1024 - 2048) : "memory");
      };
      auto look = [&]() {
        wait_vm<0>();
        ldsg();
        asm volatile("global_load_ushort %0, %1, %2" : "=&v"(bsv) : "v"((unsigned)(l15 * 2)), "s"(b_sp + t0 + wave * 16) : "memory");
        if (!isz2) sttag(ws + WS_XF + (rb * 4 + pr) * 16 + wm * 8);
        SB();
      };
      flag_wait_asm(flg + 60, 4u);
      ds16<0>(ac2[0][0], p2a); ds16<1024>(ac2[1][0], p2a); ds16<2048>(ac2[0][1], p2a); ds16<3072>(ac2[1][1], p2a);
      sfor<0, 4>([&](auto nc) { rdV(IC(1), nc, 0); rdV(IC(1), nc, 1); rdV(IC(1), nc, 2); rdV(IC(1), nc, 3); });
      sfor<0, 10>([&](auto nc) {
        constexpr int n = decltype(nc)::value;
        mm2(IC(1), nc, XG + 5, n == 0 ? 12 : (n < 6 ? 15 : 4 * (9 - n)), [&](int r) {
          SB();
          if constexpr (n < 3) { if (r < 2) { rdV(IC(1), IC(4 + 2 * n), 2 * r); rdV(IC(1), IC(4 + 2 * n), 2 * r + 1); } else { rdV(IC(1), IC(5 + 2 * n), 2 * r - 4); rdV(IC(1), IC(5 + 2 * n), 2 * r - 3); } }
        });
        if constexpr (n == 2) signal1(flg + 16);
        if constexpr (n == PN) look();
      });
      signal1(flg + 20 + wl * 4);
      AB(3);
      if (PN > 9) look();
      AB(8);
      wait_vm<1>(sg[0], sg[1], sg[2], sg[3], sg[4], sg[5]);
      if (isz2) {
        const unsigned zw = lb + L_ZS + wave * 256 + lane * 4, zr_ = lb + L_ZS + wave * 256 + g4 * 32;
        const char* zb = ws + WS_Z2 + (size_t)((rb * 2 + wm) * 48 + pr * 12 + odd * 8 + wl * 4) * 1024;
        auto bad = [&]() {
          unsigned an = sg[0][1] & sg[0][3], on = sg[0][1] | sg[0][3];
#pragma unroll
          for (int c = 1; c < 6; ++c) { an &= sg[c][1] & sg[c][3]; on |= sg[c][1] | sg[c][3]; }
          return __builtin_amdgcn_ballot_w64(((an ^ (unsigned)tag) | (on ^ (unsigned)tag)) != 0) != 0;
        };
        if (__builtin_expect(bad(), 0)) {
          do {
            --tmo;
            reld6(sg, po, pb);
            wait_vm<0>(sg[0], sg[1], sg[2], sg[3], sg[4], sg[5]);
          } while (bad() && tmo > 0);
        }
        AB(9);
        auto f = [&](int c, int e) { return __uint_as_float(sg[c][e]); };
        auto sum6 = [&](int e) { return add1(add1(add1(f(0, e), f(1, e)), add1(f(2, e), f(3, e))), add1(f(4, e), f(5, e))); };
        const float nmh = sum6(0) * (-1.0f / 6.0f), mh = -nmh;
        float qa = sum6(2) * (1.0f / 64.0f), qb;
#pragma unroll
        for (int c = 0; c < 6; c += 2) { const float u = add1(f(c, 0), nmh), v = add1(f(c + 1, 0), nmh); qa = fma1(u, u, qa); qb = c ? fma1(v, v, qb) : v * v; }
        const float Mh = add1(qa, qb);
        const auto sm = __builtin_amdgcn_permlane32_swap(__float_as_uint(mh), __float_as_uint(mh), false, false);
        const auto sM = __builtin_amdgcn_permlane32_swap(__float_as_uint(Mh), __float_as_uint(Mh), false, false);
        const float dm = __uint_as_float(sm[0]) - __uint_as_float(sm[1]), mean = rbf(add1(__uint_as_float(sm[0]), __uint_as_float(sm[1])) * 0.5f);
        const float vbf = rbf(__builtin_fmaf(dm * dm, 3.0f, add1(__uint_as_float(sM[0]), __uint_as_float(sM[1]))) * (1.0f / 12.0f));
        const float rz = __builtin_amdgcn_rcpf(rbf(__builtin_amdgcn_sqrtf(rbf(vbf + 1e-5f))));
        dsw4(zw, lane < 32 ? -mean * rz : rz);
        f32x4 zm[2], zr[2];
        ds16<0>(zm[0], zr_); ds16<128>(zr[0], zr_); ds16<16>(zm[1], zr_); ds16<144>(zr[1], zr_);
        wait_lg<0>(zm[0], zm[1], zr[0], zr[1]);
#pragma unroll
        for (int jj = 0; jj < 4; ++jj) {
          u32x4 o;
#pragma unroll
          for (int q = 0; q < 4; ++q) {
            const int i = q >> 1, e = (q & 1) * 2;
            o[q] = pack2(fma1(zv[i][jj][e], zr[i][e], zm[i][e]), fma1(zv[i][jj][e + 1], zr[i][e + 1], zm[i][e + 1]));
          }
          o[3] = nohole(o[3]);
          asm volatile("global_store_dwordx4 %0, %1, %2 offset:%3 sc1\n\ts_nop 1" :: "v"(lin), "v"(o), "s"(zb), "i"(jj * 1024) : "memory");
          SB();
        }
      }
      SB();
      AB(10);
      ldtag(ef, (unsigned)((lane & 31) * 8), ws + WS_EF + 2048 + cb * 256);
      signal1(flg + 28);
    }
    const char* Bs = ws + WS_Z2 + (size_t)(wave * 16 * 48 + cb * 6) * 1024 + 2048;
    bf16x8 Bf[3][6];
    auto ldB1 = [&](int s, int j) {
      asm volatile("global_load_dwordx4 %0, %1, %2 offset:%3 " HSC : "=&v"(Bf[s % 3][j]) : "v"(lin + (unsigned)(((s + 2 * rot2) & 15) * 49152)), "s"(Bs), "i"(j * 1024 - 2048) : "memory");
    };
    f32x4 acc[4][6];
    unsigned ara[2][2];
#pragma unroll
    for (int ks = 0; ks < 2; ++ks) {
      ara[0][ks] = ringl + l15 * 128 + (((ks * 4 + g4) ^ (l15 & 7)) << 4);
      ara[1][ks] = ara[0][ks] + (M_CV - M_RING) + wave * (CV_BUF - 24576);
    }
    u32x4 af[2][4];
    auto rdA = [&](int s, int i) {
      const int q = (0x30123012 >> (4 * (s >> 1))) & 3;
      asm volatile("ds_read_b128 %0, %1 offset:%2" : "=&v"(af[s & 1][i]) : "v"(ara[q == 3][s & 1]), "i"((q % 3) * 8192 + i * 2048) : "memory");
    };
    flag_wait_asm(flg + 28, 4u);
    AB(11);
    wait_vm<0>(ef, bsv);
    waittag(ef, (unsigned)((lane & 31) * 8), ws + WS_EF + 2048 + cb * 256);
    AB(12); SS(1 - 3 * XS);
#pragma unroll
    for (int j = 0; j < 6; ++j) ldB1(0, j);
#pragma unroll
    for (int j = 0; j < 6; ++j) ldB1(1, j);
    SS(2 - 3 * XS);
    {
      flag_wait_asm(flg + 20 + wl * 4, 2u);
      const float* cp2 = (const float*)(smem + L_CS2 + (wl * 64 + l15) * 4);
      pin(ac2[0][0], ac2[0][1], ac2[1][0], ac2[1][1]);
#pragma unroll
      for (int jj = 0; jj < 2; ++jj) {
        const float nct = -add1(add1(cp2[jj * 16], cp2[32 + jj * 16]), add1(cp2[128 + jj * 16], cp2[160 + jj * 16]));
        u32x4 o;
#pragma unroll
        for (int q = 0; q < 4; ++q) {
          const int i = q >> 1, e = (q & 1) * 2;
          o[q] = pack2(mul1(fma1(xm[i][e], nct, ac2[i][jj][e]), xr[i][e]), mul1(fma1(xm[i][e + 1], nct, ac2[i][jj][e + 1]), xr[i][e + 1]));
        }
        z1put(odd * 32 + wl * 32 + jj * 16, o);
      }
      signal1(flg + 132);
      SB();
    }
    __builtin_amdgcn_s_setprio(G2P);
    f32x4 bs4 = {lo16(bsv), lo16(bsv), lo16(bsv), lo16(bsv)};
    pin(bs4);
    AB(25);
    flag_wait_asm(flg + 32, 1u);
    AB(26);
#pragma unroll
    for (int i = 0; i < 4; ++i) rdA(0, i);
    SB();
    const unsigned xa = ringl + lane * 16;
    auto putp = [&](int i, int j) { asm volatile("ds_write_b128 %0, %1 offset:%2" :: "v"(xa), "v"(acc[i][j]), "i"(i * 6144 + j * 1024) : "memory"); };
    unsigned mx[2], fv;
    u64 hm;
    auto chk = [&](auto sc, int nw, int k) {
      constexpr int s = decltype(sc)::value, sl = s % 3;
      auto d3 = [&](int j) { return __builtin_bit_cast(u32x4, Bf[sl][j])[3]; };
      if (k == 0) {
        asm volatile("s_waitcnt vmcnt(%0)" :: "i"(nw) : "memory");
        pin(Bf[sl][0], Bf[sl][1], Bf[sl][2], Bf[sl][3], Bf[sl][4], Bf[sl][5]);
        mx[0] = opq(umx(umx(d3(0), d3(1)), d3(2)));
      }
      if (k == 1) mx[1] = opq(umx(umx(d3(3), d3(4)), mx[0]));
      if (k == 2) { hm = __builtin_amdgcn_ballot_w64(umx(mx[1], d3(5)) == HOLE); asm volatile("" : "+s"(hm)); }
      if (k == 3 && __builtin_expect(hm != 0, 0)) {
        do {
          --tmo;
          asm volatile("s_waitcnt vmcnt(0)\n\ts_sleep 2" ::: "memory");
#pragma unroll
          for (int q = 0; q < (s ? 3 : 2); ++q)
            if (s + q < 16) reld6(Bf[(s + q) % 3], opq(lin) + (unsigned)(((s + q + 2 * rot2) & 15) * 49152), Bs);
          wait_vm<0>(Bf[sl][0], Bf[sl][1], Bf[sl][2], Bf[sl][3], Bf[sl][4], Bf[sl][5]);
        } while (__builtin_amdgcn_ballot_w64(umx(umx(umx(d3(0), d3(1)), d3(2)), umx(umx(d3(3), d3(4)), d3(5))) == HOLE) != 0 && tmo > 0);
      }
      SB();
    };
    chk(IC(0), 6, 0); chk(IC(0), 0, 1); chk(IC(0), 0, 2); chk(IC(0), 0, 3);
    auto mm = [&](int s, int i, int j) {
      if (s == 0 && i == 3) asm volatile("v_mfma_f32_16x16x32_bf16 %0, %1, %2, %3" : "=&v"(acc[i][j]) : "v"(Bf[0][j]), "v"(af[0][i]), "v"(bs4));
      else if (s == 0) MFMA0(acc[i][j], Bf[0][j], af[0][i]);
      else MFMA(acc[i][j], Bf[s % 3][j], af[s & 1][i]);
      SB();
    };
    auto step = [&](auto sc) {
      constexpr int s = decltype(sc)::value;
      constexpr int NA[16] = ANUM, a0 = s == 0 ? 0 : cum16(NA, s);
      static_assert(cum16(NA, 5) == 0 && cum16(NA, 7) <= 8 && cum16(NA, 9) >= 8 && cum16(NA, 11) == 16 && NA[s] <= 5, "");
      constexpr int NV = (s == 0 ? 5 : (s == 1 ? 1 : 6 + NA[s ? s - 1 : 0])) + (s < 13 ? 5 : 0) + NA[s];
      auto fad = [&](int q) { return flg + (q == 1 ? 32 : (q == 9 ? 144 : 112) + wave * 4); };
      wait_lg<0>(af[s & 1][0], af[s & 1][1], af[s & 1][2], af[s & 1][3]);
      if constexpr (s == 1 || s == 5 || s == 7 || s == 9) {
        pin(fv);
        constexpr unsigned tg = s == 1 || s == 7 ? 2u : 1u;
        if (__builtin_expect((unsigned)__builtin_amdgcn_readfirstlane((int)fv) < tg, 0)) flag_wait_asm(fad(s), tg);
      }
      SS(3 + 2 * (s - XS));
      SB();
#pragma unroll
      for (int j = 0; j < 6; ++j) {
        mm(s, 0, j);
        if (s == 0) ldB1(2, j);
        if (j == 5) chk(IC(s + 1), NV + (s == 0), 0);
        SB();
        mm(s, 1, j);
        if (j < NA[s]) ldA1(6 + ((a0 + j) >> 3), (a0 + j) & 7);
        if (j == 5) chk(IC(s + 1), 0, 1);
        SB();
        mm(s, 2, j);
        if (j < 4) rdA(s + 1, j);
        if (j == 4 && (s == 0 || s == 2 || s == YS)) signal1(flg + 96 + wave * 4);
        if (j == 4 && (s == 0 || s == 4 || s == 6 || s == 8)) asm volatile("ds_read_b32 %0, %1" : "=&v"(fv) : "v"(fad(s + 1)) : "memory");
        if (j == 4 && s == SGS) signal1(flg + 44);
        if (j == 5) chk(IC(s + 1), 0, 2);
        SB();
        mm(s, 3, j);
        if (s + 3 < 16) ldB1(s + 3, j);
        SB();
      }
      SS(4 + 2 * (s - XS));
      chk(IC(s + 1), 0, 3);
    };
    step(IC(0)); AB(13); step(IC(1)); step(IC(2)); step(IC(3)); AB(14); step(IC(4)); step(IC(5)); step(IC(6)); step(IC(7));
    step(IC(8)); step(IC(9)); step(IC(10)); step(IC(11)); step(IC(12)); step(IC(13));
    wait_lg<0>(af[0][0], af[0][1], af[0][2], af[0][3]);
    SS(3 + 2 * (14 - XS));
#pragma unroll
    for (int i = 0; i < 4; ++i) {
#pragma unroll
      for (int n = 0; n < 12; ++n) {
        if (i == 0 && n == 6) { chk(IC(15), 0, 3); wait_lg<0>(af[1][0], af[1][1], af[1][2], af[1][3]); }
        mm(14 + n / 6, i, n % 6);
        if (i == 0 && n < 2) { rdA(15, 2 * n); rdA(15, 2 * n + 1); }
        if (i == 0 && n >= 2 && n < 5) chk(IC(15), 0, n - 2);
        if (i > 0 && (n & 1)) putp(i - 1, n >> 1);
        SB();
      }
      if (i > 0) signal1(flg + ((wave + i) & 3) * 4);
      SB();
    }
    SS(4 + 2 * (14 - XS));
    AB(15);
    flag_wait_asm(flg + 56, 5u);
    s16x4 z1r[6];
    {
      const int fr = 4 * g4 + (l15 >> 2), p = lane & 3;
      const unsigned za = lb + L_Z1 + fr * 128 + (((wave * 2 + (p >> 1)) ^ (fr & 7)) << 4) + (p & 1) * 8;
      z1r[0] = tr_asm<0>(za); z1r[1] = tr_asm<2048>(za); z1r[2] = tr_asm<4096>(za);
      z1r[3] = tr_asm<6144>(za); z1r[4] = tr_asm<8192>(za); z1r[5] = tr_asm<10240>(za);
    }
    flag_wait_asm(flg + wave * 4, 3u);
    asm volatile("" : "+v"(z1r[0]), "+v"(z1r[1]), "+v"(z1r[2]), "+v"(z1r[3]), "+v"(z1r[4]), "+v"(z1r[5]));
    AB(16);
    f32x4 own[6];
    {
      f32x4 pr_[3][6];
#pragma unroll
      for (int q = 0; q < 3; ++q) {
        const char* src = smem + M_RING + ((wave + 3 - q) & 3) * 24576;
#pragma unroll
        for (int j = 0; j < 6; ++j) pr_[q][j] = *(const f32x4*)(src + q * 6144 + j * 1024 + lane * 16);
      }
      asm volatile("s_nop 7" : "+v"(acc[3][0]), "+v"(acc[3][1]), "+v"(acc[3][2]), "+v"(acc[3][3]), "+v"(acc[3][4]), "+v"(acc[3][5]));
#pragma unroll
      for (int j = 0; j < 6; ++j) own[j] = (acc[3][j] + pr_[0][j]) + (pr_[1][j] + pr_[2][j]);
    }
    SB();
    {
      unsigned pk[6][2];
#pragma unroll
      for (int j = 0; j < 6; ++j) {
        const u32x2 z = __builtin_bit_cast(u32x2, z1r[j]);
#pragma unroll
        for (int h = 0; h < 2; ++h) pk[j][h] = pk2(unpk(z[h]) * (f32x2){own[j][2 * h], own[j][2 * h + 1]});
      }
      const unsigned eo = (unsigned)((l15 + 16 * (2 * (g4 & 1) + (g4 >> 1))) * 16);
#pragma unroll
      for (int q = 0; q < 3; ++q) {
        const auto s0 = __builtin_amdgcn_permlane16_swap(pk[2 * q][0], pk[2 * q + 1][0], false, false);
        const auto s1 = __builtin_amdgcn_permlane16_swap(pk[2 * q][1], pk[2 * q + 1][1], false, false);
        st16(eo, hf + (size_t)((rb * 4 + wave) * 24 + cb * 3 + q) * 1024, (u32x4){s0[0], s1[0], s0[1], nohole(s1[1])});
      }
    }
    signal1(flg + 96 + wave * 4);
    AB(17);
    ldW();
    wait_vm<15>(ef);
    gemm3(IC(0));
    if (b == 0 && tid == 0) *(u64*)(ws + WS_EP) = tag;
  }
  AB(24);
  AB_OUT();
}

extern "C" __global__ void k_end() {}

extern "C" void kernel_launch(void* const* d_in, const int* in_sizes, int n_in,
                              void* d_out, int out_size, void* d_ws, size_t ws_size,
                              hipStream_t stream) {
  (void)hipFuncSetAttribute((const void*)k_f, hipFuncAttributeMaxDynamicSharedMemorySize, M_LDS);
  k_f<<<256, 512, M_LDS, stream>>>((const u16*)d_in[0], (const u16*)d_in[1], (const u16*)d_in[2], (const u16*)d_in[3], (const u16*)d_in[4],
                                   (const u16*)d_in[5], (const u16*)d_in[7], (const u16*)d_in[8], (const u16*)d_in[9], (const u16*)d_in[10],
                                   (u16*)d_out, (char*)d_ws);
}
```

```cpp
#define OSC ""
#include <hip/hip_runtime.h>
#include <hip/hip_bf16.h>
#include <stdint.h>
#include <type_traits>

typedef unsigned short u16;
typedef unsigned long long u64;
typedef __attribute__((ext_vector_type(8))) short bf16x8;
typedef __attribute__((ext_vector_type(4))) short s16x4;
typedef __attribute__((ext_vector_type(4))) float f32x4;
typedef __attribute__((ext_vector_type(2))) float f32x2;
typedef __attribute__((ext_vector_type(4))) unsigned int u32x4;
typedef __attribute__((ext_vector_type(2))) unsigned int u32x2;
typedef __attribute__((ext_vector_type(2))) __bf16 bf16x2n;

#define LDS_AS __attribute__((address_space(3)))
#define IC(n) std::integral_constant<int, (n)>{}
#define SB() __builtin_amdgcn_sched_barrier(0)

#define AB_DECL do {} while (0)
#define AB(i) do {} while (0)
#define AB_OUT() do {} while (0)
#define CB(i) do {} while (0)
#define SS(i) do {} while (0)
constexpr int S_ = 2048, D_ = 640, FH_ = 768, F2_ = 1536;
constexpr unsigned HOLE = 0xFFFFFFFFu;

constexpr size_t WS_EP = 0;
constexpr size_t WS_EF = 4096;
constexpr size_t WS_LF = 22528;
constexpr size_t WS_YF = 14336;
constexpr size_t WS_XF = 20480;
constexpr size_t WS_ST = 32768;
constexpr size_t WS_Z2 = WS_ST + 32 * 12 * 1024;
constexpr size_t WS_H = WS_Z2 + 64 * 48 * 1024;
constexpr size_t WS_ZX = WS_H + 128 * 24 * 1024;
constexpr size_t WS_KV = WS_ZX + 32 * 4 * 4096;
constexpr size_t WS_Y = WS_KV + D_ * S_ * 2;
constexpr size_t WS_WO = WS_Y + D_ * S_ * 2;

__device__ __forceinline__ float bf2f(u16 v) { return __uint_as_float(((unsigned)v) << 16); }
__device__ __forceinline__ unsigned pack2(float lo, float hi) {
  f32x2 v = {lo, hi};
  bf16x2n r = __builtin_convertvector(v, bf16x2n);
  return __builtin_bit_cast(unsigned, r);
}
__device__ __forceinline__ float nv(float v) { asm("" : "+v"(v)); return v; }
__device__ __forceinline__ float fma1(float a, float b, float c) { float r; asm("v_fma_f32 %0, %1, %2, %3" : "=v"(r) : "v"(a), "v"(b), "v"(c)); return r; }
__device__ __forceinline__ float add1(float a, float b) { float r; asm("v_add_f32 %0, %1, %2" : "=v"(r) : "v"(a), "v"(b)); return r; }
__device__ __forceinline__ float mul1(float a, float b) { float r; asm("v_mul_f32 %0, %1, %2" : "=v"(r) : "v"(a), "v"(b)); return r; }
__device__ __forceinline__ unsigned pack2s(float lo, float hi) { return pack2(nv(lo), nv(hi)); }
__device__ __forceinline__ float lo16(unsigned w) { return __uint_as_float(w << 16); }
__device__ __forceinline__ float hi16(unsigned w) { return __uint_as_float(w & 0xFFFF0000u); }
__device__ __forceinline__ float rbf(float f) { return lo16(pack2(f, f)); }
__device__ __forceinline__ f32x2 unpk(unsigned w) { f32x2 r = {lo16(w), hi16(w)}; return r; }
__device__ __forceinline__ unsigned pk2(f32x2 v) { return pack2(v[0], v[1]); }
__device__ __forceinline__ unsigned umx(unsigned a, unsigned b) { return a > b ? a : b; }
__device__ __forceinline__ unsigned nohole(unsigned a) { return a < HOLE - 1 ? a : HOLE - 1; }
__device__ __forceinline__ f32x2 gelu2(f32x2 x) {
  const f32x2 c1 = {-2.3022082f, -2.3022082f}, c2 = {-0.10294324f, -0.10294324f};
  const f32x2 a = x * __builtin_elementwise_fma(x * x, c2, c1);
  f32x2 e = {__builtin_amdgcn_exp2f(a[0]), __builtin_amdgcn_exp2f(a[1])};
  e = e + (f32x2){1.0f, 1.0f};
  const f32x2 r = {__builtin_amdgcn_rcpf(e[0]), __builtin_amdgcn_rcpf(e[1])};
  return x * r;
}
__device__ __forceinline__ float gelu1(float x) {
  const float a = x * __builtin_fmaf(x * x, -0.10294324f, -2.3022082f);
  return x * __builtin_amdgcn_rcpf(__builtin_amdgcn_exp2f(a) + 1.0f);
}
template <int OFF>
__device__ __forceinline__ s16x4 tr_asm(unsigned a) {
  s16x4 r;
  asm volatile("ds_read_b64_tr_b16 %0, %1 offset:%2" : "=&v"(r) : "v"(a), "i"(OFF) : "memory");
  return r;
}
__device__ __forceinline__ bf16x8 join8(s16x4 lo, s16x4 hi) {
  bf16x8 r;
  r[0] = lo[0]; r[1] = lo[1]; r[2] = lo[2]; r[3] = lo[3];
  r[4] = hi[0]; r[5] = hi[1]; r[6] = hi[2]; r[7] = hi[3];
  return r;
}
__device__ __forceinline__ unsigned lds_addr(const void* p) { return (unsigned)(uintptr_t)(LDS_AS const char*)p; }
#define WAIT_LGKM0() asm volatile("s_waitcnt lgkmcnt(0)" ::: "memory")
__device__ __forceinline__ void flag_wait_asm(unsigned faddr, unsigned target) {
  unsigned v, sv;
  asm volatile(".Lfw_%=:\n\tds_read_b32 %0, %2\n\ts_waitcnt lgkmcnt(0)\n\tv_readfirstlane_b32 %1, %0\n\ts_nop 0\n\t"
               "s_cmp_ge_u32 %1, %3\n\ts_cbranch_scc1 .Lfd_%=\n\ts_sleep 1\n\ts_branch .Lfw_%=\n\t.Lfd_%=:"
               : "=&v"(v), "=&s"(sv) : "v"(faddr), "s"(target) : "memory", "scc");
}
__device__ __forceinline__ void signal1(unsigned a, unsigned one = 1u) {
  asm volatile("s_mov_b64 exec, 1\n\tds_add_u32 %0, %1\n\ts_mov_b64 exec, -1" :: "v"(a), "v"(one) : "memory");
}
__device__ __forceinline__ void dma16(unsigned vo, const void* sb, unsigned lds) {
  asm volatile("s_mov_b32 m0, %2\n\ts_nop 0\n\tglobal_load_lds_dwordx4 %0, %1" :: "v"(vo), "s"(sb), "s"(lds) : "memory");
}
template <int LO, int GO> __device__ __forceinline__ void dma32s(unsigned vo0, unsigned vo1, const void* sb, unsigned lds) {
  asm volatile("s_add_u32 m0, %5, %6\n\ts_add_u32 vcc_lo, %2, %4\n\ts_addc_u32 vcc_hi, %3, 0\n\t"
               "global_load_lds_dwordx4 %0, vcc\n\tglobal_load_lds_dwordx4 %1, vcc offset:1024"
               :: "v"(vo0), "v"(vo1), "s"((unsigned)(uintptr_t)sb), "s"((unsigned)((uintptr_t)sb >> 32)), "i"(GO), "s"(lds), "i"(LO) : "memory", "scc", "vcc");
}
template <int LO, int GO, int WRAP, int LIM> __device__ __forceinline__ void dma32r(unsigned vo0, unsigned vo1, const void* sb, unsigned lds, int rot) {
  const char* s2 = (const char*)sb - WRAP;
  asm volatile("s_add_u32 m0, %5, %6\n\ts_cmp_ge_u32 %7, %8\n\ts_cselect_b64 vcc, %3, %2\n\ts_add_u32 vcc_lo, vcc_lo, %4\n\ts_addc_u32 vcc_hi, vcc_hi, 0\n\t"
               "global_load_lds_dwordx4 %0, vcc\n\tglobal_load_lds_dwordx4 %1, vcc offset:1024"
               :: "v"(vo0), "v"(vo1), "s"(sb), "s"(s2), "i"(GO), "s"(lds), "i"(LO), "s"(rot), "i"(LIM) : "memory", "scc", "vcc");
}
__device__ __forceinline__ void dma16o(unsigned vo, const void* sb, int go, unsigned lds, int lo) {
  asm volatile("s_add_u32 m0, %3, %4\n\ts_nop 0\n\tglobal_load_lds_dwordx4 %0, %1 offset:%2" :: "v"(vo), "s"(sb), "i"(go), "s"(lds), "i"(lo - go) : "memory", "scc");
}
__device__ __forceinline__ void st16(unsigned vo, const void* sb, u32x4 v) {
  asm volatile("global_store_dwordx4 %0, %1, %2 sc1\n\ts_nop 1" :: "v"(vo), "v"(v), "s"(sb) : "memory");
}
__device__ __forceinline__ void touch(unsigned vo, const void* sb, unsigned lds) {
  asm volatile("s_mov_b32 m0, %2\n\ts_nop 0\n\tglobal_load_lds_dword %0, %1" :: "v"(vo), "s"(sb), "s"(lds) : "memory");
}
__device__ __forceinline__ void st8(unsigned vo, const void* sb, u32x2 v) { asm volatile("global_store_dwordx2 %0, %1, %2 sc1" :: "v"(vo), "v"(v), "s"(sb) : "memory"); }
template <class A, class B> __device__ __forceinline__ void MFMA(f32x4& acc, const A& a, const B& b) {
  asm volatile("v_mfma_f32_16x16x32_bf16 %0, %1, %2, %0" : "+v"(acc) : "v"(a), "v"(b));
}
template <class A, class B> __device__ __forceinline__ void MFMA0(f32x4& acc, const A& a, const B& b) {
  asm volatile("v_mfma_f32_16x16x32_bf16 %0, %1, %2, 0" : "=&v"(acc) : "v"(a), "v"(b));
}
template <bool Z, class A, class B> __device__ __forceinline__ void MFMAW(int n, f32x4& acc, const A& a, const B& b) {
  if (n < 0) { if (Z) MFMA0(acc, a, b); else MFMA(acc, a, b); }
  else if (Z) asm volatile("s_waitcnt lgkmcnt(%3)\n\tv_mfma_f32_16x16x32_bf16 %0, %1, %2, 0" : "=&v"(acc) : "v"(a), "v"(b), "i"(n));
  else asm volatile("s_waitcnt lgkmcnt(%3)\n\tv_mfma_f32_16x16x32_bf16 %0, %1, %2, %0" : "+v"(acc) : "v"(a), "v"(b), "i"(n));
}
__device__ __forceinline__ float sel4(float a, float b, float c, float d, u64 m1, u64 m2) {
  float r, t;
  asm volatile("v_cndmask_b32_e64 %0, %2, %3, %6\n\tv_cndmask_b32_e64 %1, %4, %5, %6\n\tv_cndmask_b32_e64 %0, %0, %1, %7"
               : "=&v"(r), "=&v"(t) : "v"(a), "v"(b), "v"(c), "v"(d), "s"(m1), "s"(m2));
  return r;
}
template <class T> __device__ __forceinline__ void pin1(T& r) { asm volatile("" : "+v"(r)); }
template <class T> __device__ __forceinline__ T opq(T r) { asm volatile("" : "+v"(r)); return r; }
template <class... T> __device__ __forceinline__ void pin(T&... r) { (pin1(r), ...); }
template <int N, class... T> __device__ __forceinline__ void wait_vm(T&... r) { asm volatile("s_waitcnt vmcnt(%0)" :: "n"(N) : "memory"); (pin1(r), ...); }
template <int N, class... T> __device__ __forceinline__ void wait_lg(T&... r) { asm volatile("s_waitcnt lgkmcnt(%0)" :: "n"(N) : "memory"); (pin1(r), ...); }
template <int OFF, class T> __device__ __forceinline__ void ds16(T& d, unsigned a) {
  asm volatile("ds_read_b128 %0, %1 offset:%2" : "=&v"(d) : "v"(a), "i"(OFF) : "memory");
}
template <int OFF> __device__ __forceinline__ void dsw16(unsigned a, const f32x4& v) {
  asm volatile("ds_write_b128 %0, %1 offset:%2" :: "v"(a), "v"(v), "i"(OFF) : "memory");
}
__device__ __forceinline__ void dsw4(unsigned a, float v) { asm volatile("ds_write_b32 %0, %1" :: "v"(a), "v"(v) : "memory"); }
#define HSC ""
template <int OFF, class T> __device__ __forceinline__ void ld16h(T& d, unsigned vo, const void* sb) {
  asm volatile("global_load_dwordx4 %0, %1, %2 offset:%3 " HSC : "=&v"(d) : "v"(vo), "s"(sb), "i"(OFF) : "memory");
}
template <class T> __device__ __forceinline__ void reld6(T (&B)[6], unsigned vo, const void* sb) {
  asm volatile("global_load_dwordx4 %0, %6, %7 offset:-2048 sc1\n\tglobal_load_dwordx4 %1, %6, %7 offset:-1024 sc1\n\t"
               "global_load_dwordx4 %2, %6, %7 sc1\n\tglobal_load_dwordx4 %3, %6, %7 offset:1024 sc1\n\t"
               "global_load_dwordx4 %4, %6, %7 offset:2048 sc1\n\tglobal_load_dwordx4 %5, %6, %7 offset:3072 sc1"
               : "+v"(B[0]), "+v"(B[1]), "+v"(B[2]), "+v"(B[3]), "+v"(B[4]), "+v"(B[5]) : "v"(vo), "s"(sb) : "memory");
}

constexpr int L_FLAG = 0;
constexpr int L_DUM = 256;
constexpr int L_XM = 512;
constexpr int L_XR = 768;
constexpr int L_CS = 1024;
constexpr int L_ZS = 2048;
constexpr int L_CS2 = 3072;
constexpr int L_Z1 = 4096;
constexpr int L_RAW = L_Z1;
constexpr int L_SLOT = 16384;
constexpr int L_KV = 512, KV_P = 3088, L_WO = L_KV + 8 * KV_P;
static_assert(L_WO % 16 == 0 && L_WO + 4 * 1024 <= L_SLOT + 2 * 8192, "staging");
constexpr int L_P2 = L_SLOT + 12 * 8192;
constexpr int CV_C = 4608, CV_CS = 4160, CV_BUF = CV_C + 2 * CV_CS;
constexpr int M_CV = L_SLOT;
constexpr int M_RING = L_SLOT + 6 * 8192;
constexpr int P3_G = L_SLOT;
constexpr int P3_C = P3_G + 80 * 1024;
constexpr int yat(int j) { return M_CV + (j >> 1) * CV_BUF + 8192 + (j & 1) * 2048; }
constexpr int M_LDS = 163840;
static_assert(3 * CV_BUF + 8192 <= 6 * 8192 && P3_C + 60 * 1024 <= M_LDS && CV_BUF >= 8192 + 4096, "LDS map");

enum { UX, UA, UB, UV };
struct Unit { int kind, idx, need, free, slot; };
#define SCHED gugugugwBuluouk_uuxuu_uuxuxu_uuxuxuu_Euuuuh_vuumumuh_vuumumuhF_vuvumum_DRvuuUHmmmmSptt_VhhhttttttJItttttttt
#define STR_(x) #x
#define STR(x) STR_(x)
constexpr char SCH[] = STR(SCHED);
constexpr int NSCH = sizeof(SCH) - 1;
constexpr int XNEED[10] = {1, 2, 2, 3, 3, 4, 4, 5, 5, 6};
constexpr int VNEED[5] = {5, 6, 7, 7, 8};
constexpr int XSLOT[10] = {17, 16, 15, 13, 10, 14, 11, 17, 9, 16}, WSLOT[10] = {6, 2, 4, 0, 6, 12, 10, 4, 0, 8}, VSLOT[10] = {8, 15, 3, 2, 14, 17, 16, 15, 7, 6}, XW = 6;
constexpr int cnt(char c, int e = NSCH) { int n = 0; for (int i = 0; i < e; ++i) n += SCH[i] == c || (c == 'u' && SCH[i] == 'g'); return n; }
constexpr int pos(char c, int k = 0) { for (int i = 0; i < NSCH; ++i) if (SCH[i] == c && k-- == 0) return i; return NSCH; }
constexpr Unit unit(int n) {
  for (int b = 1; b < 11; ++b) {
    for (int c = 0; c < 10; ++c) if (XNEED[c] == b && n-- == 0) return {UX, c, b, b + 1, XSLOT[c]};
    for (int c = 0; c < 5; ++c) if (VNEED[c] == b && n-- == 0) return {UV, c, b, b + 1, VSLOT[c]};
    if (n < 2) return {n ? UB : UA, b - 1, b < 8 ? b : 8, b < 7 ? b + 2 : 9, WSLOT[b - 1] + n};
    n -= 2;
  }
  return {UV, n + 5, 10, 99, VSLOT[n + 5]};
}
constexpr int FLAGB = cnt('_', pos('D')), GZ = cnt('_', pos('E'));
constexpr int nvm(char c) { return c == 'u' ? 2 : (c == 'H' ? 5 : (c == 'U' ? 4 : (c == 'F' || c == 'h' || c == 't' || c == 'w'))); }
constexpr int behind(int g, int e, char w) {
  const int b = cnt('_', e) + 1;
  int n = 0;
  for (int i = 0; i < e; ++i) {
    const char c = SCH[i];
    if (g == 0) { if (c == 'g') n = w == '_' && unit(cnt('u', i)).need <= b ? 0 : n + 2; if (c == 'B') n += 2; continue; }
    n = (w == '_' ? (c == 'u' && unit(cnt('u', i)).need <= b) || (c == 'h' && b == FLAGB) || (c == 'k' && b == GZ) : c == w) ? 0 : n + nvm(c);
  }
  return g == 0 && b >= GZ ? 0 : n;
}
constexpr bool plan_ok() {
  if (cnt('u') != 40 || cnt('_') != 9 || cnt('x') != 5 || cnt('h') != 6 || cnt('t') != 16 || cnt('I') * cnt('J') * cnt('p') != 1 || cnt('v') != 5 || cnt('m') != 10) return false;
  if (cnt('B') * cnt('D') * cnt('E') * cnt('F') * cnt('l') * cnt('o') * cnt('k') * cnt('H') * cnt('U') * cnt('V') * cnt('R') * cnt('S') != 1) return false;
  if (pos('l') < pos('B') || pos('o') < pos('B') || pos('k') < pos('l') || pos('k') > pos('_') || pos('o') > pos('_') || cnt('g', pos('B')) != cnt('g')) return false;
  if (cnt('_', pos('h', 2)) >= FLAGB || GZ < 3 || GZ > 6 || FLAGB < GZ || FLAGB > 9 || pos('o') > pos('k') || pos('H') > pos('I') || pos('F') > pos('U')) return false;
  if (cnt('_', pos('R')) != 8 || cnt('_', pos('S')) != 8 || pos('S') < pos('R') || cnt('_', pos('I')) != 9 || pos('J') < pos('U') || cnt('_', pos('J')) != 9) return false;
  if (cnt('_', pos('V')) != 9 || cnt('u', pos('V')) < 40) return false;
  for (int c = 0; c < 5; ++c) if (cnt('_', pos('x', c)) != XNEED[c] || cnt('_', pos('v', c)) != VNEED[c] || pos('m', 2 * c) < pos('v', c)) return false;
  if (cnt('_', pos('p')) != 8 || pos('p') < pos('m', 9)) return false;
  int fr[18] = {1, 1};
  for (int i = 0; i < NSCH; ++i) if (SCH[i] == 'u' || SCH[i] == 'g') {
    const Unit u = unit(cnt('u', i));
    const int p = cnt('_', i);
    if (p < fr[u.slot] || p >= u.need) return false;
    fr[u.slot] = u.free;
  }
  return true;
}
static_assert(plan_ok(), "phase A plan");
template <int I, int E, class F> __device__ __forceinline__ void sfor(F&& f) { if constexpr (I < E) { f(IC(I)); sfor<I + 1, E>(f); } }

__device__ __forceinline__ void conv_core(unsigned buf, int lane, unsigned Dw, f32x4 (&ac)[8]) {
  const int l15 = lane & 15, g4 = lane >> 4;
  const int c = (l15 + 1) & 1;
  unsigned aa[4];
#pragma unroll
  for (int k = 0; k < 4; ++k) aa[k] = buf + CV_C + c * CV_CS + (2031 - l15 + 8 * g4 - c) * 2 - 512 - 1024 * k;
  unsigned bb = buf + 512 + 32 * l15 - 32 + 16 * g4;
  u32x2 A[2][8][2];
  u32x4 B[2][8];
  constexpr int TT[36] = {7, 6, 5, 4, 7, 6, 5, 4, 7, 6, 5, 3, 7, 6, 5, 4, 7, 6, 3, 2, 7, 6, 5, 4, 7, 3, 2, 1, 7, 6, 5, 4, 3, 2, 1, 0};
  constexpr int AA[36] = {0, 0, 0, 0, 1, 1, 1, 1, 2, 2, 2, 0, 3, 3, 3, 2, 4, 4, 1, 0, 5, 5, 4, 3, 6, 2, 1, 0, 7, 6, 5, 4, 3, 2, 1, 0};
  auto ld1 = [&](int p, int n) {
    if (n < 16) {
      const int a = n >> 1, o = ((a & 1) ? 0 : 128) + 2 * (n & 1);
      asm volatile("ds_read2_b32 %0, %1 offset0:%2 offset1:%3" : "=&v"(A[p][(n >> 1) & 7][n & 1]) : "v"(aa[(a >> 1) & 3]), "i"(o), "i"(o + 1) : "memory");
    } else
      asm volatile("ds_read_b128 %0, %1 offset:%2" : "=&v"(B[p][n & 7]) : "v"(bb), "i"(512 * (n & 7)) : "memory");
  };
  auto next = [&](int dl) {
#pragma unroll
    for (int k = 0; k < 4; ++k) aa[k] += dl;
    bb += dl;
    SB();
  };
  auto land = [&](int p) {
    asm volatile("s_waitcnt lgkmcnt(0)" : "+v"(A[p][0][0]), "+v"(A[p][0][1]), "+v"(A[p][1][0]), "+v"(A[p][1][1]), "+v"(A[p][2][0]), "+v"(A[p][2][1]),
                 "+v"(A[p][3][0]), "+v"(A[p][3][1]), "+v"(A[p][4][0]), "+v"(A[p][4][1]), "+v"(A[p][5][0]), "+v"(A[p][5][1]), "+v"(A[p][6][0]), "+v"(A[p][6][1]),
                 "+v"(A[p][7][0]), "+v"(A[p][7][1]), "+v"(B[p][0]), "+v"(B[p][1]), "+v"(B[p][2]), "+v"(B[p][3]), "+v"(B[p][4]), "+v"(B[p][5]),
                 "+v"(B[p][6]), "+v"(B[p][7]) :: "memory");
    SB();
  };
  auto mm = [&](int p) {
#pragma unroll
    for (int n = 0; n < 36; ++n) {
      const int t = TT[n], a = AA[n];
      const u32x4 av = {A[p][a][0][0], A[p][a][0][1], A[p][a][1][0], A[p][a][1][1]};
      MFMA(ac[t], av, B[p][t - a]);
      if (n < 24) ld1(p ^ 1, n);
      SB();
    }
    land(p ^ 1);
  };
#pragma unroll
  for (int n = 0; n < 24; ++n) ld1(0, n);
  u32x4 ad;
#pragma unroll
  for (int e = 0; e < 4; ++e) ad[e] = lane == 32 + 2 * e + (lane & 1) + (lane & 8) * 3 ? Dw << (16 * (lane & 1)) : 0u;
  pin(ad);
  land(0);
#pragma unroll
  for (int t = 0; t < 8; ++t) MFMA0(ac[t], ad, B[0][t]);
#pragma unroll 1
  for (int r = 0; r < 8; r += 2) {
    next(-64);
    mm(0);
    next(r < 6 ? -64 : 0);
    mm(1);
  }
  asm volatile("s_nop 15" : "+v"(ac[0]), "+v"(ac[1]), "+v"(ac[2]), "+v"(ac[3]), "+v"(ac[4]), "+v"(ac[5]), "+v"(ac[6]), "+v"(ac[7]));
}

#define ROTA 3
#define RA1 1
#define RA2 0
#define RA3 0
#define RB1 0
#define RB2 4
#define RB3 0
#define G2P 0
#define RB4 2
#define THR 24
#define RB5 2
#define YS 8
#define WO3 0
#define C3P 0
#define FE3 9
#define WSH 1
#define CPR 1
#define STW 1
#define PN 10
#define P2P 2
#define SGS 11
#define ANUM {0, 0, 0, 0, 0, 3, 3, 2, 3, 3, 2, 0, 0, 0, 0, 0}
constexpr int cum16(const int (&a)[16], int n) { int c = 0; for (int i = 0; i < n; ++i) c += a[i]; return c; }

__global__ __launch_bounds__(512) void k_f(
    const u16* __restrict__ x, const u16* __restrict__ kin, const u16* __restrict__ vin, const u16* __restrict__ shift_k,
    const u16* __restrict__ ssm_k, const u16* __restrict__ ssm_D, const u16* __restrict__ w_in, const u16* __restrict__ w_sp,
    const u16* __restrict__ b_sp, const u16* __restrict__ w_out, u16* __restrict__ out, char* ws) {
  extern __shared__ __attribute__((aligned(16))) char smem[];
  const int tid = threadIdx.x, lane = tid & 63, wave = __builtin_amdgcn_readfirstlane(tid >> 6);
  const int b = blockIdx.x;
  const int xcd = b & 7, idx = b >> 3;
  const int rb = 8 * (xcd >> 1) + (idx >> 2), cb = 4 * (xcd & 1) + (idx & 3);
  const int t0 = rb * 64, d0 = cb * 80;
  const int g4 = lane >> 4, l15 = lane & 15;
  const unsigned lin = (unsigned)(lane * 16);
  const unsigned lb = lds_addr(smem), flg = lb + L_FLAG;
  AB_DECL;
  AB(0);
  asm volatile("" :: "s"(x), "s"(kin), "s"(vin), "s"(shift_k), "s"(ssm_k), "s"(ssm_D), "s"(w_in), "s"(w_sp), "s"(b_sp), "s"(w_out), "s"(out), "s"(ws));
  u64 tag = *(const __attribute__((address_space(4))) u64*)(uintptr_t)ws;
  int tmo = 1 << 16;
#define CWARM 1
  auto cwarm = [&]() {
    u64 pc;
    unsigned clen;
    asm volatile("s_getpc_b64 %0\n.Lpc_here_%=:\n\ts_mov_b32 %1, k_end-.Lpc_here_%=" : "=s"(pc), "=s"(clen));
    touch(min((unsigned)(idx * 1536 + min(lane, 11) * 128), clen - 4u) & ~3u, (const void*)pc, lb + L_DUM);
  };

  const int cw = wave & 3, wm = wave & 1, wl = (wave >> 1) & 1, ksb = wm ^ wl, odd = cb & 1, pr = cb >> 1;
  const int r8 = lane >> 3, c8 = lane & 7;
  unsigned voX[2], voW[2];
#pragma unroll
  for (int h = 0; h < 2; ++h) {
    voX[h] = (unsigned)((h * 8 + r8) * (D_ * 2) + ((c8 ^ (h * 4 + (r8 & 3))) << 4) - h * 1024);
    voW[h] = (unsigned)((h * 8 + r8) * (F2_ * 2) + ((c8 ^ (h * 4 + (r8 & 2))) << 4) - h * 1024);
  }
  const int rot = (RA1 * ((rb & 7) * 5 >> 2) + ROTA * (cb & 3) + RA2 * ((rb >> 3) * 5 >> 1) + RA3 * (cb >> 2)) % 10, rot2 = (RB1 * (cb & 3) + RB2 * (cb >> 2) + RB3 * (rb & 7) + RB4 * (rb >> 3) + RB5 * (cb & 1)) & 7;
  const char* gX = (const char*)x + (size_t)(t0 + cw * 16) * (D_ * 2) + rot * 128;
  const char* gW = (const char*)w_in + (size_t)(cw * 16 + rot * 64) * (F2_ * 2) + pr * 384;
  const char *gV = gW + odd * 256, *gB = gW + (odd ? 128 : FH_ * 2 + 128);
  const char* gS = (const char*)w_sp + (size_t)t0 * (S_ * 2);
  const unsigned aoff[2] = {(unsigned)(r8 * 4096 + ((c8 ^ r8) << 4)), (unsigned)(r8 * 4096 + ((c8 ^ r8) << 4) + 32768)};
  const unsigned ldw = lb + cw * 2048;
  auto issue = [&](auto nc) {
    constexpr Unit u = unit(decltype(nc)::value);
    constexpr int lo = L_SLOT + u.slot * 8192;
    if (u.kind == UX) dma32r<lo, u.idx * 128, 1280, 10 - u.idx>(voX[0], voX[1], gX, ldw, rot);
    else dma32r<lo, u.idx * (64 * F2_ * 2) + (u.kind == UA ? FH_ * 2 : 0), 640 * F2_ * 2, 10 - u.idx>(voW[0], voW[1], u.kind == UB ? gB : gV, ldw, rot);
    SB();
  };
  const int kd = d0 + min(wave, 4) * 16 + r8 * 2;
  u32x4 sw;
  {
    const int ks0 = t0 + wave * 8;
#pragma unroll
    for (int i = 0; i < 3; ++i) {
      const int id = min(i * 64 + lane, 189), r = id / 10, c = id % 10;
      const char* kp = (r < 11 ? (const char*)kin + (size_t)max(ks0 - 3 + r, 0) * (D_ * 2) : (const char*)vin + (size_t)(ks0 + r - 11) * (D_ * 2)) + cb * 160 + c * 16;
      asm volatile("s_mov_b32 m0, %1\n\ts_nop 0\n\tglobal_load_lds_dwordx4 %0, off" :: "v"(kp), "s"(lb + L_KV + wave * KV_P + i * 1024) : "memory");
    }
    if (CWARM && wave == 5) cwarm();
    SB();
  }
  auto ldsw = [&]() { asm volatile("global_load_dwordx4 %0, %1, %2" : "=&v"(sw) : "v"(kd * 8), "s"(shift_k) : "memory"); SB(); };
  unsigned kk[11], vv[8];
  u32x4 oa, ob;
  auto kvld = [&]() {
    const char* kl = smem + L_KV + c8 * KV_P + min(wave, 4) * 32 + r8 * 4;
#pragma unroll
    for (int i = 0; i < 11; ++i) kk[i] = *(const unsigned*)(kl + i * 160);
#pragma unroll
    for (int i = 0; i < 8; ++i) vv[i] = *(const unsigned*)(kl + (11 + i) * 160);
  };
  auto kvm = [&](int e0, int e1) {
    const f32x2 w0 = {lo16(sw[0]), lo16(sw[2])}, w1 = {hi16(sw[0]), hi16(sw[2])};
    const f32x2 w2 = {lo16(sw[1]), lo16(sw[3])}, w3 = {hi16(sw[1]), hi16(sw[3])};
    f32x2 kf[11], rr[8];
#pragma unroll
    for (int i = 2 * e0; i < 2 * e1 + 3; ++i) kf[i] = unpk((i < 3 && t0 + c8 == 0) ? 0u : kk[i]);
#pragma unroll
    for (int i = 2 * e0; i < 2 * e1; ++i) {
      f32x2 a0 = kf[i] * w3;
      a0 = __builtin_elementwise_fma(kf[i + 1], w2, a0);
      a0 = __builtin_elementwise_fma(kf[i + 2], w1, a0);
      a0 = __builtin_elementwise_fma(kf[i + 3], w0, a0);
      rr[i] = a0 * unpk(vv[i]);
    }
#pragma unroll
    for (int e = e0; e < e1; ++e) { oa[e] = pack2(rr[2 * e][0], rr[2 * e + 1][0]); ob[e] = pack2(rr[2 * e][1], rr[2 * e + 1][1]); }
  };
  auto kvst = [&]() {
    const unsigned ko = (unsigned)((kd * S_ + t0 + c8 * 8) * 2);
    st16(ko, ws + WS_KV, oa);
    st16(ko, ws + WS_KV + S_ * 2, ob);
    SB();
  };
  auto barrier0 = [&](int nv) {
    SB();
    asm volatile("s_waitcnt vmcnt(%0) lgkmcnt(0)\n\ts_barrier" :: "n"(nv) : "memory");
    SB();
    asm volatile("" : "+v"(sw), "+s"(tag));
    tag += 1;
    asm volatile("" : "+s"(tag));
  };
  unsigned pX[2], pW[2];
#pragma unroll
  for (int k = 0; k < 2; ++k) {
    const int q = l15 >> 2, p = lane & 3, sz = (q >> 1) | ((g4 & 1) << 1);
    pX[k] = lb + (wm * 32 + q * 8 + p) * 128 + ((((k ^ ksb) * 4 + g4) ^ (l15 & 7)) << 4);
    pW[k] = lb + ((k ^ ksb) * 32 + g4 * 8 + q) * 128 + (((sz << 1) | (p >> 1)) << 4) + (p & 1) * 8;
  }
  bf16x8 ones;
#pragma unroll
  for (int e = 0; e < 8; ++e) ones[e] = (short)0x3F80;
  f32x4 xm[2], xr[2];
  auto ldxs = [&]() {
#pragma unroll
    for (int i = 0; i < 2; ++i) {
      xm[i] = *(const f32x4*)(smem + L_XM + (wm * 32 + g4 * 8 + i * 4) * 4);
      xr[i] = *(const f32x4*)(smem + L_XR + (wm * 32 + g4 * 8 + i * 4) * 4);
    }
  };
  auto act = [&](const f32x4& a, float c, int i) {
    const f32x2 nc = {-c, -c};
    const f32x2 lo = gelu2(__builtin_elementwise_fma((f32x2){xm[i][0], xm[i][1]}, nc, (f32x2){a[0], a[1]}) * (f32x2){xr[i][0], xr[i][1]});
    const f32x2 hi = gelu2(__builtin_elementwise_fma((f32x2){xm[i][2], xm[i][3]}, nc, (f32x2){a[2], a[3]}) * (f32x2){xr[i][2], xr[i][3]});
    return (f32x4){lo[0], lo[1], hi[0], hi[1]};
  };
  auto pack8 = [&](const f32x4& a, const f32x4& c) { return (u32x4){pack2(a[0], a[1]), pack2(a[2], a[3]), pack2(c[0], c[1]), pack2(c[2], c[3])}; };
  auto z1at = [&](int f0) { return (u32x4*)(smem + L_Z1 + (f0 + l15) * 128 + (((wm * 4 + g4) ^ (l15 & 7)) << 4)); };
  auto z1put = [&](int f0, const u32x4& v) { *z1at(f0) = v; };
  f32x4 ac2[2][2], cs2[2];
  s16x4 VF[10][2][2];
  unsigned aV[2][2];
  auto rdV = [&](auto hc_, auto nc, int r) {
    constexpr int n = decltype(nc)::value, c = 5 * decltype(hc_)::value + (n >> 1), so = (VSLOT[c] - 2) * 8192, po = n < 2 ? -1 : ((VSLOT[n < 2 ? c : c - 1] - 2) * 8192) >> 16;
    const int jj = r >> 1;
    if (!(r & 1)) {
      if ((so >> 16) != po) aV[n & 1][jj] = opq((pW[n & 1] ^ (unsigned)(wl * 64 + jj * 32)) + L_SLOT + 2 * 8192 + (so & ~0xFFFF));
      VF[n][jj][0] = tr_asm<(so & 0xFFFF)>(aV[n & 1][jj]);
    } else VF[n][jj][1] = tr_asm<(so & 0xFFFF) + 512>(aV[n & 1][jj]);
    SB();
  };
  auto mm2 = [&](auto hc_, auto nc, bf16x8 (*X)[2][2], int nw, auto&& nxt) {
    constexpr int n = decltype(nc)::value, j = n >> 1, k = n & 1;
    constexpr bool z = n == 0 && !decltype(hc_)::value;
    const bf16x8 w0 = join8(VF[n][0][0], VF[n][0][1]), w1 = join8(VF[n][1][0], VF[n][1][1]);
    if (k == 0) { MFMAW<n == 0>(nw, cs2[0], ones, w0); MFMAW<n == 0>(-1, cs2[1], ones, w1); }
    MFMAW<z>(k ? nw : -1, ac2[0][0], X[j][k][0], w0); nxt(0);
    MFMAW<z>(-1, ac2[1][0], X[j][k][1], w0); nxt(1);
    MFMAW<z>(-1, ac2[0][1], X[j][k][0], w1); nxt(2);
    MFMAW<z>(-1, ac2[1][1], X[j][k][1], w1); nxt(3);
    if constexpr (n == 9) {
      pin(cs2[0], cs2[1]);
      if (g4 < 2) *(float*)(smem + L_CS2 + (wave * 32 + lane) * 4) = g4 ? cs2[1][0] : cs2[0][0];
    }
    SB();
  };
  const unsigned p2a = lb + L_P2 + cw * 4096 + lin;
  auto sttag = [&](const char* p) {
    asm volatile("s_mov_b64 exec, 1\n\tglobal_store_dwordx2 %0, %1, %2 sc1\n\ts_mov_b64 exec, -1" :: "v"(lin), "v"((u32x2){opq((unsigned)tag), opq((unsigned)(tag >> 32))}), "s"(p) : "memory");
  };
  auto ldtag = [&](u32x2& f, unsigned vo, const char* p) { asm volatile("global_load_dwordx2 %0, %1, %2 sc1" : "=&v"(f) : "v"(vo), "s"(p) : "memory"); };
  auto notag = [&](const u32x2& f) { return __builtin_amdgcn_ballot_w64(f[0] != (unsigned)tag || f[1] != (unsigned)(tag >> 32)) != 0; };
  auto waittag = [&](u32x2& f, unsigned vo, const char* p) {
    while (__builtin_expect(notag(f) && tmo > 0, 0)) {
      --tmo;
      __builtin_amdgcn_s_sleep(4);
      ldtag(f, vo, p);
      wait_vm<0>(f);
    }
  };
  u32x2 ef;

  char* const hf = ws + WS_H;
  bf16x8 Wq[15];
  const char* wg = ws + WS_WO + (size_t)(wave * 120 + cb * 5) * 1024;
  auto ldW = [&]() {
    ldtag(ef, (unsigned)((lane & 3) * 8), ws + WS_LF + (rb * 8 + wave) * 32);
#pragma unroll
    for (int n = 0; n < 15; ++n)
      asm volatile("global_load_dwordx4 %0, %1, %2 offset:%3 sc1" : "=&v"(Wq[n]) : "v"(lin), "s"(wg + (n / 5) * 40960 + 2048), "i"((n % 5) * 1024 - 2048) : "memory");
    SB();
  };
  f32x4 ac3[5];
  auto gemm3 = [&](auto khc) {
    constexpr int kh = decltype(khc)::value;
    unsigned hoff[4];
#pragma unroll
    for (int it = 0; it < 4; ++it) hoff[it] = (unsigned)(((rb * 4 + ((cw + 1 + it) & 3)) * 24 + 3 * wave) * 1024) + lin;
    u32x4 Hq[4][3];
    auto ldh1 = [&](int it, int k) {
      asm volatile("global_load_dwordx4 %0, %1, %2 offset:%3 " HSC : "=&v"(Hq[it][k]) : "v"(hoff[it]), "s"(hf), "i"(k * 1024) : "memory");
    };
    waittag(ef, (unsigned)((lane & 3) * 8), ws + WS_LF + (rb * 8 + wave) * 32);
    AB(18);
    signal1(flg + 48);
    flag_wait_asm(flg + 48, 8u);
    AB(19);
    __builtin_amdgcn_s_setprio(3);
    ldh1(0, 0); ldh1(0, 1); ldh1(0, 2);
    __builtin_amdgcn_s_setprio(2);
    ldh1(1, 0); ldh1(1, 1); ldh1(1, 2);
    __builtin_amdgcn_s_setprio(kh * C3P);
    SB();
    const unsigned px = lb + (kh ? P3_C + cw * 15360 : P3_G + cw * 20480) + lin;
    f32x4 pa[5][5], ae[5];
    auto put = [&](int it, int j) { asm volatile("ds_write_b128 %0, %1 offset:%2" :: "v"(px), "v"((it & 1) ? ac3[j] : ae[j]), "i"((it * 5 + j) * 1024) : "memory"); };
    auto fetch = [&](int n, int g, int q) {
      const int ms = (cw - 1 - q) & 3;
      const unsigned pn = lb + (g ? P3_C + ms * 15360 : P3_G + ms * 20480) + lin;
#pragma unroll
      for (int j = 0; j < 5; ++j) asm volatile("ds_read_b128 %0, %1 offset:%2" : "=&v"(pa[n][j]) : "v"(pn), "i"((q * 5 + j) * 1024) : "memory");
      SB();
    };
    sfor<0, 4>([&](auto itc) {
      constexpr int it = decltype(itc)::value;
      wait_vm<(it < 3 ? 3 : 0)>(Hq[it][0], Hq[it][1], Hq[it][2]);
      if constexpr (it == 0) pin(Wq[0], Wq[1], Wq[2], Wq[3], Wq[4], Wq[5], Wq[6], Wq[7], Wq[8], Wq[9], Wq[10], Wq[11], Wq[12], Wq[13], Wq[14]);
      auto bad = [&]() { return __builtin_amdgcn_ballot_w64(umx(umx(Hq[it][0][3], Hq[it][1][3]), Hq[it][2][3]) == HOLE) != 0; };
      if (__builtin_expect(bad(), 0)) {
        do {
          --tmo;
          asm volatile("s_sleep 2\n\tglobal_load_dwordx4 %0, %3, %4 sc1\n\tglobal_load_dwordx4 %1, %3, %4 offset:1024 sc1\n\t"
                       "global_load_dwordx4 %2, %3, %4 offset:2048 sc1\n\ts_waitcnt vmcnt(0)"
                       : "+v"(Hq[it][0]), "+v"(Hq[it][1]), "+v"(Hq[it][2]) : "v"(hoff[it]), "s"(hf) : "memory");
        } while (bad() && tmo > 0);
      }
      SB();
#pragma unroll
      for (int k = 0; k < 3; ++k)
#pragma unroll
        for (int j = 0; j < 5; ++j) {
          f32x4& ac = (it & 1) ? ac3[j] : ae[j];
          if (k == 0) MFMA0(ac, Wq[j], Hq[it][0]); else MFMA(ac, Wq[k * 5 + j], Hq[it][k]);
          if constexpr (it + 2 < 4) { if (k == 0 && j < 3) ldh1(it + 2 < 4 ? it + 2 : 0, j); }
          const int m = k * 5 + j - WO3;
          if (it > 0 && m >= 0 && m < 10 && !(m & 1)) put(it - 1, m >> 1);
          if (it > 0 && m == 8) signal1(flg + 160 + (it < 3 ? kh : 2) * 16 + ((cw + it) & 3) * 4);
          if (it == 3 && m == FE3) { flag_wait_asm(flg + 160 + wave * 4, 2u); fetch(0, kh, 0); fetch(1, kh, 1); }
          SB();
        }
      if constexpr (it == 3) {
        if (kh) {
          flag_wait_asm(flg + 192 + cw * 4, 3u);
          fetch(2, 1, 2); fetch(3, 0, 2); fetch(4, 0, 3);
        } else asm volatile("s_waitcnt lgkmcnt(0)\n\ts_nop 4" ::: "memory");
        pin(pa[0][0], pa[0][1], pa[0][2], pa[0][3], pa[0][4], pa[1][0], pa[1][1], pa[1][2], pa[1][3], pa[1][4]);
#pragma unroll
        for (int j = 0; j < 5; ++j) {
          pa[0][j] += pa[1][j];
          pin(pa[0][j], ac3[j]);
          ac3[j] += pa[0][j];
          if (!kh) put(3, j);
        }
        if (kh) {
          wait_lg<5>(pa[2][0], pa[2][1], pa[2][2], pa[2][3], pa[2][4], pa[3][0], pa[3][1], pa[3][2], pa[3][3], pa[3][4]);
#pragma unroll
          for (int j = 0; j < 5; ++j) ac3[j] += pa[2][j] + pa[3][j];
          wait_lg<0>(pa[4][0], pa[4][1], pa[4][2], pa[4][3], pa[4][4]);
#pragma unroll
          for (int j = 0; j < 5; ++j) ac3[j] += pa[4][j];
        } else signal1(flg + 192 + cw * 4);
      }
      SB();
      if constexpr (it == 0) AB(20);
      if constexpr (it == 2) AB(21);
    });
    AB(22);
  };
  if (wave >= 4) {
    const int wid = rb * 4 + cw;
    __builtin_amdgcn_s_setprio(3);
    ldsw();
    if (wid < 120) dma16((unsigned)((lane >> 1) * (D_ * 2) + (lane & 1) * 16), w_out + (size_t)(wid / 5) * 32 * D_ + d0 + (wid % 5) * 16, lb + L_WO + cw * 1024);
    SB();
    u32x4 hole = {HOLE, HOLE, HOLE, HOLE};
    bf16x8 XF[5][2][2];
    auto rdXF = [&](auto cc) {
      constexpr int c = decltype(cc)::value, so = L_SLOT + XSLOT[c] * 8192;
#pragma unroll
      for (int k = 0; k < 2; ++k) {
        const unsigned a = pX[k] + (so & ~0x7FFF);
        ds16<(so & 0x7FFF)>(XF[c][k][0], a);
        ds16<(so & 0x7FFF) + 512>(XF[c][k][1], a);
      }
      SB();
    };
    u32x4 TA[16];
    auto ldT = [&](u32x4& r, int d, int w, int i) {
      asm volatile("global_load_dwordx4 %0, %1, %2" : "=&v"(r) : "v"(aoff[i & 1] + (unsigned)(((d + rot2) & 7) * 128)), "s"(gS + ((w + 1 + (i >> 1)) & 3) * 65536 + w * 1024) : "memory");
    };
    auto ldx = [&](int c) {
      u32x4 xv;
      asm volatile("global_load_dwordx4 %0, %1, %2\n\ts_waitcnt vmcnt(0)" : "=&v"(xv) : "v"((unsigned)(lane * (D_ * 2) + c * 16)), "s"(x + (size_t)t0 * D_) : "memory");
      return xv;
    };
    const bool docv = cw < ((rb & 1) ? 2 : 3);
    const int d = d0 + (cw < 2 ? 2 * rb + cw : 64 + (rb >> 1));
    u32x4 hc[4], uc[4];
    unsigned Dw;
    auto ldhc = [&]() {
#pragma unroll
      for (int k = 0; k < 4; ++k) asm volatile("global_load_dwordx4 %0, %1, %2 offset:%3" : "=&v"(hc[k]) : "v"(lin), "s"(ssm_k + (size_t)d * S_), "i"(k * 1024) : "memory");
      asm volatile("global_load_ushort %0, %1, %2" : "=&v"(Dw) : "v"(0u), "s"(ssm_D + d) : "memory");
    };
    unsigned rwo;
    f32x2 ra, rc;
    auto rdraw = [&]() { asm volatile("ds_read_b64 %0, %2\n\tds_read_b64 %1, %2 offset:512" : "=&v"(ra), "=&v"(rc) : "v"(rwo) : "memory"); SB(); };
    const char* efp = ws + WS_EF + cb * 256;
    char* cb_ = smem + M_CV + cw * CV_BUF;
    auto run = [&](auto ic) {
      constexpr int i = decltype(ic)::value, p = cnt('_', i) + 1;
      constexpr char c = SCH[i];
      if constexpr (c == '_') {
        CB(3 * (p - XBAR));
        asm volatile("s_waitcnt vmcnt(%0) lgkmcnt(0)" :: "n"(behind(1, i, '_')) : "memory");
        CB(3 * (p - XBAR) + 1);
        asm volatile("s_barrier" ::: "memory");
        CB(3 * (p - XBAR) + 2);
        SB();
#pragma unroll
        for (int q = 0; q < 5; ++q) if (XNEED[q] + 1 == p) pin(XF[q][0][0], XF[q][0][1], XF[q][1][0], XF[q][1][1]);
        if constexpr (p == 7) {
          rwo = lb + L_RAW + ((lane >> 3) * 4 + (lane & 3)) * 16 + (lane & 4) * 2;
          pin(rwo);
        }
        if constexpr (p == 9) __builtin_amdgcn_s_setprio(CPR);
        if constexpr (p == 2) AB(4);
        if constexpr (p == 6) AB(5);
        if constexpr (p == 9) AB(6);
      }
      if constexpr (c == 'u') issue(IC(cnt('u', i)));
      if constexpr (c == 'x') rdXF(IC(cnt('x', i)));
      if constexpr (c == 'B') {
        AB(1);
        barrier0(behind(1, i, 'B'));
        pin(ones, hole);
      }
      if constexpr (c == 'v') sfor<2 * cnt('v', i), 2 * cnt('v', i) + 2>([&](auto nc) { rdV(IC(0), nc, 0); rdV(IC(0), nc, 1); rdV(IC(0), nc, 2); rdV(IC(0), nc, 3); });
      if constexpr (c == 'm') {
        constexpr int n = cnt('m', i);
        mm2(IC(0), IC(n), XF, VNEED[n >> 1] < p - 1 || (n & 1) ? -1 : 0, [](int) {});
      }
      if constexpr (c == 'p') {
        asm volatile("s_nop 7" : "+v"(ac2[0][0]), "+v"(ac2[0][1]), "+v"(ac2[1][0]), "+v"(ac2[1][1]));
        dsw16<0>(p2a, ac2[0][0]); dsw16<1024>(p2a, ac2[1][0]); dsw16<2048>(p2a, ac2[0][1]); dsw16<3072>(p2a, ac2[1][1]);
      }
      if constexpr (c == 'w')
        touch((unsigned)(min(lane, 19) * (F2_ * 2)), (const char*)w_in + (size_t)(((rb + WSH) & 7) * 80 + cw * 20) * (F2_ * 2) + pr * 384 + odd * 256, lb + L_DUM);
      if constexpr (c == 'l') { if (wave == 7) kvld(); }
      if constexpr (c == 'k') { if (wave == 7) { kvm(0, 4); kvst(); } }
      if constexpr (c == 'E' || c == 'D') { if (wave == 5) sttag(ws + WS_EF + (c == 'D' ? 2048 : 0) + (cb * 32 + rb) * 8); }
      if constexpr (c == 'U') {
        wait_vm<behind(1, i, 'F')>(ef);
        waittag(ef, (unsigned)((lane & 31) * 8), efp);
#pragma unroll
        for (int k = 0; k < 4; ++k) asm volatile("global_load_dwordx4 %0, %1, %2 offset:%3 sc1" : "=&v"(uc[k]) : "v"(lin), "s"(ws + WS_KV + (size_t)d * (S_ * 2)), "i"(k * 1024) : "memory");
      }
      if constexpr (c == 'F') ldtag(ef, (unsigned)((lane & 31) * 8), efp);
      if constexpr (c == 'o') {
        if (wid < 120) {
          const unsigned ra = lb + L_WO + cw * 1024 + (8 * g4 + (l15 >> 2)) * 32 + 8 * (lane & 3);
          s16x4 lo = tr_asm<0>(ra), hi = tr_asm<128>(ra);
          wait_lg<0>(lo, hi);
          st16(lin, ws + WS_WO + (size_t)((wid / 5) * 40 + cb * 5 + wid % 5) * 1024, __builtin_bit_cast(u32x4, join8(lo, hi)));
        }
      }
      if constexpr (c == 'h') {
        constexpr int n = cnt('h', i);
        st16(lin, ws + (n < 3 ? WS_Z2 + (size_t)((rb * 2 + wl) * 48 + cb * 6 + wm * 3 + n) * 1024 : WS_H + (size_t)((rb * 4 + cw) * 24 + cb * 3 + n - 3) * 1024), hole);
      }
      if constexpr (c == 't') { constexpr int n = cnt('t', i); ldT(TA[n], 3 + (n >> 3), cw, n & 7); }
      if constexpr (c == 'H') ldhc();
      if constexpr (c == 'R') rdraw();
      if constexpr (c == 'V') {
        wait_vm<behind(1, i, '_')>();
        signal1(flg + 60);
        AB(2);
      }
      if constexpr (c == 'I') {
        wait_vm<behind(1, i, 'H')>(hc[0], hc[1], hc[2], hc[3], Dw);
        if (docv) {
          if (lane < 36) *(u32x4*)(cb_ + (lane < 32 ? lane * 16 : CV_C + ((lane >> 1) & 1) * CV_CS + 4096 + (lane & 1) * 16)) = (u32x4){0u, 0u, 0u, 0u};
#pragma unroll
          for (int k = 0; k < 4; ++k) {
            const int a = k * 64 + lane;
            const int h0 = k ? __builtin_amdgcn_readlane((int)hc[k ? k - 1 : 0][3], 63) : 0;
            const unsigned hp = (unsigned)__builtin_amdgcn_update_dpp(h0, (int)hc[k][3], 0x138, 0xF, 0xF, false);
            const unsigned w[5] = {hp, hc[k][0], hc[k][1], hc[k][2], hc[k][3]};
            u32x4 sw, mx;
#pragma unroll
            for (int q = 0; q < 4; ++q) {
              sw[q] = (w[4 - q] >> 16) | (w[4 - q] << 16);
              mx[q] = (w[4 - q] & 0xFFFFu) | (w[3 - q] & 0xFFFF0000u);
            }
            char* cp = cb_ + CV_C + (2040 - 8 * a) * 2;
            *(u32x4*)(cp) = sw;
            *(u32x4*)(cp + CV_CS) = mx;
          }
        }
      }
      if constexpr (c == 'J') {
        wait_vm<behind(1, i, 'U')>(uc[0], uc[1], uc[2], uc[3]);
        if (docv) {
#pragma unroll
          for (int k = 0; k < 4; ++k) *(u32x4*)(cb_ + 512 + (k * 64 + lane) * 16) = uc[k];
        }
      }
      if constexpr (c == 'S') {
        wait_lg<0>(ra, rc);
        if (wave == 6) {
          const float s1 = nv(ra[0]) + rc[0], fm = s1 * (1.0f / 640.0f), sm = s1 * fm;
          float fq = (ra[1] - sm) + rc[1];
          if (__builtin_expect(__builtin_amdgcn_ballot_w64(!(fq >= 1e-3f * sm)) != 0, 0)) {
            const f32x2 mm = {fm, fm};
            f32x2 q2 = {0.f, 0.f};
#pragma unroll 1
            for (int c = 0; c < 80; ++c) {
              const u32x4 xv = ldx(c);
#pragma unroll
              for (int e = 0; e < 4; ++e) { const f32x2 d = unpk(xv[e]) - mm; q2 = __builtin_elementwise_fma(d, d, q2); }
            }
            fq = q2[0] + q2[1];
          }
          const float vbf = rbf(fmaxf(fq, 0.f) * (1.0f / 640.0f));
          *(float*)(smem + L_XM + lane * 4) = rbf(fm);
          *(float*)(smem + L_XR + lane * 4) = __builtin_amdgcn_rcpf(rbf(__builtin_amdgcn_sqrtf(rbf(vbf + 1e-5f))));
        }
      }
      SB();
    };
    sfor<0, NSCH>(run);
    __builtin_amdgcn_s_setprio(0);
    AB(9);
    auto ldS = [&](auto dc, auto wc) {
      constexpr int dd = decltype(dc)::value, w = decltype(wc)::value;
      const unsigned to = (unsigned)(((dd + rot2) & 7) * 128);
      if (THR < 60) wait_vm<THR>();
      dma32s<M_RING + w * 24576 + (2 - dd) * 8192, ((w + 1) & 3) * 65536 + w * 1024>(aoff[0] + to, aoff[1] - 1024 + to, gS, lb);
      if (THR < 60) wait_vm<THR>();
      dma32s<M_RING + w * 24576 + (2 - dd) * 8192 + 2048, ((w + 2) & 3) * 65536 + w * 1024>(aoff[0] + to, aoff[1] - 1024 + to, gS, lb);
      if (THR < 60) wait_vm<THR>();
      dma32s<M_RING + w * 24576 + (2 - dd) * 8192 + 4096, ((w + 3) & 3) * 65536 + w * 1024>(aoff[0] + to, aoff[1] - 1024 + to, gS, lb);
      if (THR < 60) wait_vm<THR>();
      dma32s<M_RING + w * 24576 + (2 - dd) * 8192 + 6144, w * 65536 + w * 1024>(aoff[0] + to, aoff[1] - 1024 + to, gS, lb);
      SB();
    };
    auto epi2 = [&]() {
      flag_wait_asm(flg + 132, 4u);
#pragma unroll
      for (int jj = 0; jj < 2; ++jj) {
        u32x4* zp = z1at(odd * 32 + wl * 32 + jj * 16);
        u32x4 o = *zp;
#pragma unroll
        for (int q = 0; q < 4; ++q) o[q] = pack2s(gelu1(nv(lo16(o[q]))), gelu1(hi16(o[q])));
        *zp = o;
      }
      signal1(flg + 56);
      SB();
    };
    auto putT = [&](const u32x4* T, unsigned a, unsigned f) {
#pragma unroll
      for (int i = 0; i < 8; ++i) asm volatile("ds_write_b128 %0, %1 offset:%2" :: "v"(a), "v"(T[i]), "i"(i * 1024) : "memory");
      signal1(f);
    };
    const unsigned tx = lb + M_CV + cw * CV_BUF + lin;
    auto landT = [&]() {
      sttag(ws + WS_LF + (rb * 8 + cb) * 32 + cw * 8);
#pragma unroll
      for (int n = 0; n < 16; ++n) pin(TA[n]);
      putT(TA, tx, flg + 112 + cw * 4);
    };
    auto putT4 = [&]() {
      flag_wait_asm(flg + 96 + cw * 4, 1u);
      putT(TA + 8, lb + M_RING + cw * 24576 + 16384 + lin, flg + 112 + cw * 4);
    };
    if (docv) {
      flag_wait_asm(flg + 36, 4u);
      AB(12);
      f32x4 ac[8];
      conv_core(lds_addr(cb_), lane, Dw, ac);
      AB(13);
      signal1(flg + 40);
      wait_vm<0>();
      landT();
#pragma unroll
      for (int k = 0; k < 4; ++k) {
        u32x2 ov[2];
#pragma unroll
        for (int t = 0; t < 2; ++t)
#pragma unroll
          for (int h = 0; h < 2; ++h) ov[t][h] = pack2(ac[2 * k + t][2 * h], ac[2 * k + t][2 * h + 1]);
        const auto s0 = __builtin_amdgcn_permlane16_swap(ov[0][0], ov[1][0], false, false);
        const auto s1 = __builtin_amdgcn_permlane16_swap(ov[0][1], ov[1][1], false, false);
        st16((unsigned)((256 * (g4 & 1) + 16 * l15 + 8 * (g4 >> 1)) * 2), ws + WS_Y + (size_t)d * (S_ * 2) + k * 1024, (u32x4){s0[0], s1[0], s0[1], s1[1]});
      }
      wait_vm<0>();
      sttag(ws + WS_YF + d * 8);
      putT4();
      epi2();
    } else if (cw != 3) {
      wait_vm<0>();
      landT();
      putT4();
      epi2();
    } else {
      ldS(IC(0), IC(0)); ldS(IC(0), IC(1)); ldS(IC(0), IC(2)); ldS(IC(1), IC(1)); ldS(IC(2), IC(1));
      wait_vm<40>();
      landT();
      AB(12);
      flag_wait_asm(flg + 16, 4u);
      AB(8);
      wait_vm<24>();
      ldS(IC(0), IC(3)); ldS(IC(1), IC(0)); ldS(IC(1), IC(2)); ldS(IC(1), IC(3));
      wait_vm<24>();
      signal1(flg + 32);
      ldS(IC(2), IC(0)); ldS(IC(2), IC(2)); ldS(IC(2), IC(3));
      AB(10);
      u32x4 TB[32];
#pragma unroll
      for (int n = 0; n < 32; ++n) {
        if (n == 8) wait_vm<32>();
        if (THR < 60 && !(n & 1)) wait_vm<THR>();
        if (THR < 32 && n == THR) signal1(flg + 32);
        ldT(TB[n], 5, n >> 3, n & 7);
      }
      wait_vm<32>();
      if (THR >= 32) signal1(flg + 32);
      AB(11);
      wait_vm<0>();
#pragma unroll
      for (int n = 0; n < 32; ++n) pin(TB[n]);
      putT4();
#pragma unroll
      for (int w = 0; w < 4; ++w) {
        flag_wait_asm(flg + 96 + w * 4, 2u);
        putT(TB + 8 * w, lb + M_RING + w * 24576 + 8192 + lin, flg + 144 + w * 4);
      }
      epi2();
      if (!odd) {
        u32x4 zx[4];
        ldtag(ef, (unsigned)((lane & 1) * 8), ws + WS_XF + (rb * 4 + pr) * 16);
        wait_vm<0>(ef);
        waittag(ef, (unsigned)((lane & 1) * 8), ws + WS_XF + (rb * 4 + pr) * 16);
#pragma unroll
        for (int c = 0; c < 4; ++c)
          asm volatile("global_load_dwordx4 %0, %1, %2 offset:%3 sc1" : "=&v"(zx[c]) : "v"(lin), "s"(ws + WS_ZX + (size_t)(rb * 4 + pr) * 4096), "i"(c * 1024) : "memory");
        wait_vm<0>(zx[0], zx[1], zx[2], zx[3]);
#pragma unroll
        for (int c = 0; c < 4; ++c) *(u32x4*)(smem + L_Z1 + (64 + (c & 1) * 16 + l15) * 128 + ((((c >> 1) * 4 + g4) ^ (l15 & 7)) << 4)) = zx[c];
      }
      signal1(flg + 56);
      AB(13);
    }
    SB();
    wait_vm<0>();
    AB(14);
    u32x2 xv[5];
    f32x2 sv[5][2];
    flag_wait_asm(flg + 40, (rb & 1) ? 2u : 3u);
    const int yc = min(cw * 24, 56);
    const unsigned yfo = (unsigned)(min(lane, 23) * 8);
    const char* yfp = ws + WS_YF + (d0 + yc) * 8;
    auto ldy = [&]() {
#pragma unroll
      for (int n = 0; n < 3; ++n) {
        const int q = (yc >> 3) + n;
        asm volatile("s_mov_b32 m0, %2\n\ts_nop 0\n\tglobal_load_lds_dwordx4 %0, %1 sc1" :: "v"((unsigned)(r8 * (S_ * 2) + ((c8 ^ (r8 & 6)) << 4))),
                     "s"(ws + WS_Y + ((size_t)(d0 + q * 8) * S_ + t0) * 2), "s"(lb + yat(0) + (q >> 2) * CV_BUF + (q & 3) * 1024) : "memory");
      }
    };
    flag_wait_asm(flg + 96 + cw * 4, 3u);
    ldtag(ef, yfo, yfp);
    wait_vm<0>(ef);
    flag_wait_asm(flg + 44, 4u);
    waittag(ef, yfo, yfp);
    AB(15);
    ldy();
#pragma unroll
    for (int j = 0; j < 5; ++j)
      asm volatile("global_load_dwordx2 %0, %1, %2 offset:%3" : "=&v"(xv[j]) : "v"((unsigned)(l15 * D_ * 2 + 8 * g4)), "s"(x + (size_t)(t0 + cw * 16) * D_ + d0), "i"(j * 32) : "memory");
    ldW();
    wait_vm<16>(xv[0], xv[1], xv[2], xv[3], xv[4]);
    AB(17);
    signal1(flg + 52);
    flag_wait_asm(flg + 52, 4u);
    {
      const int yr_ = 4 * g4 + (l15 >> 2);
      const unsigned ya = lb + yr_ * 128 + (((cw * 2 + ((lane >> 1) & 1)) ^ (yr_ & 6)) << 4) + (lane & 1) * 8;
      const float nm = -*(const float*)(smem + L_XM + (cw * 16 + l15) * 4), sr = *(const float*)(smem + L_XR + (cw * 16 + l15) * 4);
      s16x4 yr[5] = {tr_asm<yat(0)>(ya), tr_asm<yat(1)>(ya), tr_asm<yat(2)>(ya), tr_asm<yat(3)>(ya), tr_asm<yat(4)>(ya)};
      wait_lg<0>(yr[0], yr[1], yr[2], yr[3], yr[4]);
      AB(16);
      flag_wait_asm(flg + 96 + cw * 4, 4u);
#pragma unroll
      for (int j = 0; j < 5; ++j) {
        const u32x2 yw = __builtin_bit_cast(u32x2, yr[j]);
#pragma unroll
        for (int h = 0; h < 2; ++h) {
          sv[j][h] = ((unpk(xv[j][h]) + (f32x2){nm, nm}) * (f32x2){sr, sr}) * unpk(yw[h]);
          pin(sv[j][h]);
        }
      }
      SB();
    }
    wait_vm<15>(ef);
    gemm3(IC(1));
    unsigned pk[5][2];
#pragma unroll
    for (int j = 0; j < 5; ++j)
#pragma unroll
      for (int h = 0; h < 2; ++h) {
        const f32x2 o2 = {ac3[j][2 * h], ac3[j][2 * h + 1]};
        const f32x2 a = o2 * (f32x2){-1.4426950f, -1.4426950f};
        const f32x2 e = (f32x2){__builtin_amdgcn_exp2f(a[0]), __builtin_amdgcn_exp2f(a[1])} + (f32x2){1.0f, 1.0f};
        const f32x2 r = {__builtin_amdgcn_rcpf(e[0]), __builtin_amdgcn_rcpf(e[1])};
        pk[j][h] = pk2(__builtin_elementwise_fma(o2 * r, sv[j][h], unpk(xv[j][h])));
      }
    const u16* op = out + (size_t)(t0 + cw * 16) * D_ + d0;
#pragma unroll
    for (int q = 0; q < 2; ++q) {
      const auto s0 = __builtin_amdgcn_permlane16_swap(pk[2 * q][0], pk[2 * q + 1][0], false, false);
      const auto s1 = __builtin_amdgcn_permlane16_swap(pk[2 * q][1], pk[2 * q + 1][1], false, false);
      asm volatile("global_store_dwordx4 %0, %1, %2 offset:%3 " OSC "\n\ts_nop 1" :: "v"((unsigned)(l15 * D_ * 2 + 32 * (g4 & 1) + 16 * (g4 >> 1))), "v"((u32x4){s0[0], s1[0], s0[1], s1[1]}),
                   "s"(op), "i"(64 * q) : "memory");
    }
    asm volatile("global_store_dwordx2 %0, %1, %2 offset:128 " OSC :: "v"((unsigned)(l15 * D_ * 2 + 8 * g4)), "v"((u32x2){pk[4][0], pk[4][1]}), "s"(op) : "memory");
  } else {
    ldsw();
    sfor<0, pos('B')>([&](auto ic) { if constexpr (SCH[ic] == 'g') issue(IC(cnt('u', ic))); });
    if (tid < 64) *(unsigned*)(smem + L_FLAG + tid * 4) = 0u;
    pin(ones);
    AB(1);
    barrier0(behind(0, pos('B'), 'B'));
    kvld();
    kvm(0, 4);
    kvst();
    const bool isz2 = !(odd && wl);
    const char* stp = ws + WS_ST + (size_t)((rb * 12 + pr * 3 + odd * 2 + wl) * 1024 + wm * 512);
    if (STW && isz2) st8(lin >> 1, stp, (u32x2){0u, ~(unsigned)tag});
    const char* Ab[4];
#pragma unroll
    for (int i = 0; i < 4; ++i) Ab[i] = (const char*)w_sp + (size_t)(t0 + ((wave + 1 + i) & 3) * 16) * (S_ * 2) + wave * 1024;
    const unsigned ringl = lb + M_RING + wave * 24576;
    auto ldA1 = [&](int d, int i) {
      dma16o(aoff[i & 1] + (unsigned)(((d + rot2) & 7) * 128), Ab[i >> 1], 0, d == 7 ? lb + M_CV + wave * CV_BUF : ringl, i * 1024);
    };
    unsigned bsv;
    {
      f32x4 zv[2][4], cs[4];
      s16x4 WB[20][4][2];
      bf16x8 XG[10][2][2];
      unsigned aX[2][2], aW[2][2][4];
#pragma unroll
      for (int k = 0; k < 2; ++k)
#pragma unroll
        for (int w = 0; w < 2; ++w) {
          aX[w][k] = pX[k] + L_SLOT + XW * 8192 + w * 65536;
          pin(aX[w][k]);
#pragma unroll
          for (int jj = 0; jj < 4; ++jj) { aW[w][k][jj] = (pW[k] ^ (unsigned)(jj * 32)) + L_SLOT + wl * 8192 + w * 65536; pin(aW[w][k][jj]); }
        }
      auto rd1 = [&](auto nc, int r) {
        constexpr int n = decltype(nc)::value, j = n >> 1, k = n & 1, c = n > 0 && n < 11 ? n - 1 : 0, sx = (XSLOT[c] - XW) * 8192, sa = WSLOT[j] * 8192;
        if (r < 0) { if (r & 1) ds16<(sx & 0xFFFF) + 512>(XG[c][(r + 4) >> 1][1], aX[sx >> 16][(r + 4) >> 1]); else ds16<(sx & 0xFFFF)>(XG[c][(r + 4) >> 1][0], aX[sx >> 16][(r + 4) >> 1]); }
        else if (r & 1) WB[n][r >> 1][1] = tr_asm<(sa & 0xFFFF) + 512>(aW[sa >> 16][k][r >> 1]);
        else WB[n][r >> 1][0] = tr_asm<(sa & 0xFFFF)>(aW[sa >> 16][k][r >> 1]);
        SB();
      };
      auto bar = [&](auto bc) {
        SB();
        constexpr int p = decltype(bc)::value;
        CB(3 * (p - XBAR));
        if constexpr (p == 1) asm volatile("s_waitcnt vmcnt(%0) lgkmcnt(0)\n\ts_barrier" :: "n"(behind(0, pos('_', p - 1), '_')) : "memory");
        else if constexpr (p <= GZ) asm volatile("s_waitcnt vmcnt(%0) lgkmcnt(8)\n\ts_barrier" :: "n"(behind(0, pos('_', p - 1), '_')) : "memory");
        else if constexpr (p >= 8) asm volatile("s_waitcnt lgkmcnt(0)\n\ts_barrier" ::: "memory");
        else if constexpr (p < 7) asm volatile("s_waitcnt lgkmcnt(8)\n\ts_barrier" ::: "memory");
        else asm volatile("s_barrier" ::: "memory");
        CB(3 * (p - XBAR) + 2);
        SB();
      };
      float cp[4];
      f32x4 st[2][2], rv;
      const unsigned rwa = lb + L_RAW + (ksb * 2 + wm) * 256 + l15 * 16 + ((l15 >> 2) == g4 ? 0 : 1024);
      const unsigned csa = lb + L_CS + (wave * 64 + lane) * 4;
      auto kstep = [&](auto nc) {
        constexpr int n = decltype(nc)::value, j = n >> 1, k = n & 1, nn = n + 1, r0 = nn >= 2 && nn <= 10 ? -4 : 0;
        constexpr int sc = n == 1 ? 0 : n - 2, ns = !k ? 0 : (n == 1 || n == 11 ? 1 : 2);
        if constexpr (k) bar(IC(nn / 2 + 1));
        int r = r0, c = 0;
        auto nxt = [&]() { if (r < 8) { rd1(IC(nn), r++); ++c; } };
        auto sel = [&](int q) {
          const f32x4& sq = st[q & 1][q >> 1];
          rv[q] = sel4(sq[0], sq[1], sq[2], sq[3], 0xAAAAAAAAAAAAAAAAull, 0xCCCCCCCCCCCCCCCCull);
          SB();
        };
#pragma unroll
        for (int jj = 0; jj < 4; ++jj) {
          const bf16x8 wf = join8(WB[n][jj][0], WB[n][jj][1]);
          MFMAW<n == 0>(jj == 0 ? 4 : (jj == 2 ? c : -1), zv[0][jj], XG[j][k][0], wf);
          SB();
          nxt();
          if (n == 12) nxt();
          if (n == 12 && jj == 2) sel(2);
          MFMAW<n == 0>(-1, zv[1][jj], XG[j][k][1], wf);
          SB();
          nxt();
          if (n == 12) nxt();
          if (n == 12 && (jj == 1 || jj == 2)) sel(jj == 1 ? 0 : 3);
          if (n == 12 && jj == 2) { dsw16<0>(rwa, rv); SB(); }
          if (k == 0) {
            MFMAW<n == 0>(-1, cs[jj], ones, wf);
            SB();
            nxt();
            if (n == 12) nxt();
            if (n == 12 && jj == 1) sel(1);
          }
#pragma unroll
          for (int q = 0; q < ns; ++q) {
            const bf16x8& xs = XG[sc + q][1][jj >> 1];
            MFMAW<n == 1>(-1, st[jj & 1][jj >> 1], xs, (jj & 1) ? xs : ones);
            SB();
            nxt();
          }
        }
      };
      auto lstep = [&](auto nc, auto ac, auto bc, int w) {
        constexpr int n = decltype(nc)::value, j = n >> 1, k = n & 1, na = decltype(ac)::value, nb = decltype(bc)::value;
        int r = 0;
        auto nxt = [&]() { if (na > 0 && r < 16) { if (r < 8) rd1(ac, r); else rd1(bc, r - 8); ++r; } };
#pragma unroll
        for (int jj = 0; jj < 4; ++jj) {
          const bf16x8 wf = join8(WB[n][jj][0], WB[n][jj][1]);
          if (k == 0) { MFMAW<false>(jj == 0 ? w : -1, cs[jj], ones, wf); SB(); nxt(); nxt(); }
          MFMAW<false>(jj == 0 && k ? w : -1, zv[0][jj], XG[j][k][0], wf);
          SB();
          nxt();
          if (k) nxt();
          MFMAW<false>(-1, zv[1][jj], XG[j][k][1], wf);
          SB();
          nxt();
          if (k) nxt();
          if (n == 15 && jj == 2) {
            dsw4(csa, sel4(cs[0][0], cs[1][0], cs[2][0], cs[3][0], 0xFFFF0000FFFF0000ull, 0xFFFFFFFF00000000ull));
            SB();
          }
        }
      };
      bar(IC(1));
      AB(2);
#pragma unroll
      for (int r = -4; r < 8; ++r) rd1(IC(0), r);
      kstep(IC(0)); AB(4); kstep(IC(1)); kstep(IC(2)); kstep(IC(3)); kstep(IC(4)); kstep(IC(5)); kstep(IC(6)); kstep(IC(7)); kstep(IC(8)); AB(5); kstep(IC(9));
      kstep(IC(10)); kstep(IC(11)); kstep(IC(12));
      bar(IC(8));
      lstep(IC(13), IC(14), IC(16), -1);
      lstep(IC(14), IC(18), IC(15), 8);
      lstep(IC(16), IC(17), IC(19), 15);
      lstep(IC(18), IC(0), IC(0), 15);
      lstep(IC(15), IC(0), IC(0), -1);
      lstep(IC(17), IC(0), IC(0), 9);
      bar(IC(9));
      __builtin_amdgcn_s_setprio(2);
#pragma unroll
      for (int q = 0; q < 4; ++q) cp[q] = *(const float*)(smem + L_CS + ((wave ^ 1) * 64 + q * 16 + l15) * 4);
      ldxs();
      SB();
      lstep(IC(19), IC(0), IC(0), -1);
      AB(6);
      float ct[4];
#pragma unroll
      for (int jj = 0; jj < 4; ++jj) ct[jj] = cs[jj][0] + cp[jj];
#pragma unroll
      for (int jj = 0; jj < 4; ++jj) {
        if (jj == 0) asm volatile("s_nop 3");
        if (jj == 2) SB();
        if (!(jj & 1)) pin(zv[0][jj], zv[1][jj], zv[0][jj + 1], zv[1][jj + 1]);
#pragma unroll
        for (int i = 0; i < 2; ++i) zv[i][jj] = act(zv[i][jj], ct[jj], i);
      }
      if (isz2) {
        f32x4 pv[2], s1[2], s2[2];
#pragma unroll
        for (int i = 0; i < 2; ++i) {
#pragma unroll
          for (int e = 0; e < 4; ++e) {
            const float z0 = zv[i][0][e];
            pv[i][e] = __int_as_float(__builtin_amdgcn_update_dpp(0, __float_as_int(z0), 0x150, 0xF, 0xF, true));
          }
          const f32x4 a0 = zv[i][0] - pv[i], a1 = zv[i][1] - pv[i], a2 = zv[i][2] - pv[i], a3 = zv[i][3] - pv[i];
          s1[i] = (a0 + a1) + (a2 + a3);
          s2[i] = __builtin_elementwise_fma(a3, a3, __builtin_elementwise_fma(a2, a2, __builtin_elementwise_fma(a1, a1, a0 * a0)));
        }
        float r[8], q[4];
#define DA(d, s, ctl, bm) "v_add_f32_dpp " d ", " s ", " s " " ctl " row_mask:0xf bank_mask:" bm "\n\t"
        asm volatile("s_nop 1\n\t"
                     DA("%0", "%12", "row_ror:8", "0x3") DA("%1", "%13", "row_ror:8", "0x3") DA("%2", "%14", "row_ror:8", "0x3") DA("%3", "%15", "row_ror:8", "0x3")
                     DA("%4", "%16", "row_ror:8", "0x3") DA("%5", "%17", "row_ror:8", "0x3") DA("%6", "%18", "row_ror:8", "0x3") DA("%7", "%19", "row_ror:8", "0x3")
                     DA("%0", "%20", "row_ror:8", "0xc") DA("%1", "%21", "row_ror:8", "0xc") DA("%2", "%22", "row_ror:8", "0xc") DA("%3", "%23", "row_ror:8", "0xc")
                     DA("%4", "%24", "row_ror:8", "0xc") DA("%5", "%25", "row_ror:8", "0xc") DA("%6", "%26", "row_ror:8", "0xc") DA("%7", "%27", "row_ror:8", "0xc")
                     DA("%8", "%0", "row_half_mirror", "0x5") DA("%9", "%1", "row_half_mirror", "0x5") DA("%10", "%2", "row_half_mirror", "0x5") DA("%11", "%3", "row_half_mirror", "0x5")
                     DA("%8", "%4", "row_half_mirror", "0xa") DA("%9", "%5", "row_half_mirror", "0xa") DA("%10", "%6", "row_half_mirror", "0xa") DA("%11", "%7", "row_half_mirror", "0xa")
                     DA("%8", "%8", "quad_perm:[1,0,3,2]", "0xf") DA("%9", "%9", "quad_perm:[1,0,3,2]", "0xf") DA("%10", "%10", "quad_perm:[1,0,3,2]", "0xf") DA("%11", "%11", "quad_perm:[1,0,3,2]", "0xf")
                     DA("%8", "%8", "quad_perm:[2,3,0,1]", "0xf") DA("%9", "%9", "quad_perm:[2,3,0,1]", "0xf") DA("%10", "%10", "quad_perm:[2,3,0,1]", "0xf") DA("%11", "%11", "quad_perm:[2,3,0,1]", "0xf")
                     : "=&v"(r[0]), "=&v"(r[1]), "=&v"(r[2]), "=&v"(r[3]), "=&v"(r[4]), "=&v"(r[5]), "=&v"(r[6]), "=&v"(r[7]),
                       "=&v"(q[0]), "=&v"(q[1]), "=&v"(q[2]), "=&v"(q[3])
                     : "v"(s1[0][0]), "v"(s2[0][0]), "v"(s1[0][1]), "v"(s2[0][1]), "v"(s1[0][2]), "v"(s2[0][2]), "v"(s1[0][3]), "v"(s2[0][3]),
                       "v"(s1[1][0]), "v"(s2[1][0]), "v"(s1[1][1]), "v"(s2[1][1]), "v"(s1[1][2]), "v"(s2[1][2]), "v"(s1[1][3]), "v"(s2[1][3]));
#undef DA
        const f32x4 p4 = (lane & 8) ? pv[1] : pv[0];
        const float pa = (lane & 4) ? p4[2] : p4[0], pb = (lane & 4) ? p4[3] : p4[1];
        const float pp = (lane & 2) ? pb : pa, S1 = (lane & 2) ? q[2] : q[0], S2 = (lane & 2) ? q[3] : q[1];
        const float dl = S1 * (1.0f / 64.0f);
        st8(lin >> 1, stp, (u32x2){__float_as_uint((lane & 1) ? fmaxf(S2 - S1 * dl, 0.f) : pp + dl), (unsigned)tag});
      } else {
#pragma unroll
        for (int jj = 0; jj < 4; ++jj) {
          u32x4 o = pack8(zv[0][jj], zv[1][jj]);
          if (jj >= 2) z1put(jj * 16 - 32, o);
          else st16(lin, ws + WS_ZX + (size_t)((rb * 4 + pr) * 4 + wm * 2 + jj) * 1024, o);
        }
      }
      signal1(flg + 36);
      SB();
      AB(7);
      __builtin_amdgcn_s_setprio(P2P);
      unsigned po = (unsigned)((lane >> 5) * 6144 + (lane & 31) * 16);
      const char* pb = ws + WS_ST + (size_t)rb * 12288 + wm * 512 + 2048;
      u32x4 sg[6];
      auto ldsg = [&]() {
#pragma unroll
        for (int c = 0; c < 6; ++c) asm volatile("global_load_dwordx4 %0, %1, %2 offset:%3 sc1" : "=&v"(sg[c]) : "v"(po), "s"(pb), "i"(c * 1024 - 2048) : "memory");
      };
      auto look = [&]() {
        wait_vm<0>();
        ldsg();
        asm volatile("global_load_ushort %0, %1, %2" : "=&v"(bsv) : "v"((unsigned)(l15 * 2)), "s"(b_sp + t0 + wave * 16) : "memory");
        if (!isz2) sttag(ws + WS_XF + (rb * 4 + pr) * 16 + wm * 8);
        SB();
      };
      flag_wait_asm(flg + 60, 4u);
      ds16<0>(ac2[0][0], p2a); ds16<1024>(ac2[1][0], p2a); ds16<2048>(ac2[0][1], p2a); ds16<3072>(ac2[1][1], p2a);
      sfor<0, 4>([&](auto nc) { rdV(IC(1), nc, 0); rdV(IC(1), nc, 1); rdV(IC(1), nc, 2); rdV(IC(1), nc, 3); });
      sfor<0, 10>([&](auto nc) {
        constexpr int n = decltype(nc)::value;
        mm2(IC(1), nc, XG + 5, n == 0 ? 12 : (n < 6 ? 15 : 4 * (9 - n)), [&](int r) {
          SB();
          if constexpr (n < 3) { if (r < 2) { rdV(IC(1), IC(4 + 2 * n), 2 * r); rdV(IC(1), IC(4 + 2 * n), 2 * r + 1); } else { rdV(IC(1), IC(5 + 2 * n), 2 * r - 4); rdV(IC(1), IC(5 + 2 * n), 2 * r - 3); } }
        });
        if constexpr (n == 2) signal1(flg + 16);
        if constexpr (n == PN) look();
      });
      signal1(flg + 20 + wl * 4);
      AB(3);
      if (PN > 9) look();
      AB(8);
      wait_vm<1>(sg[0], sg[1], sg[2], sg[3], sg[4], sg[5]);
      if (isz2) {
        const unsigned zw = lb + L_ZS + wave * 256 + lane * 4, zr_ = lb + L_ZS + wave * 256 + g4 * 32;
        const char* zb = ws + WS_Z2 + (size_t)((rb * 2 + wm) * 48 + pr * 12 + odd * 8 + wl * 4) * 1024;
        auto bad = [&]() {
          unsigned an = sg[0][1] & sg[0][3], on = sg[0][1] | sg[0][3];
#pragma unroll
          for (int c = 1; c < 6; ++c) { an &= sg[c][1] & sg[c][3]; on |= sg[c][1] | sg[c][3]; }
          return __builtin_amdgcn_ballot_w64(((an ^ (unsigned)tag) | (on ^ (unsigned)tag)) != 0) != 0;
        };
        if (__builtin_expect(bad(), 0)) {
          do {
            --tmo;
            reld6(sg, po, pb);
            wait_vm<0>(sg[0], sg[1], sg[2], sg[3], sg[4], sg[5]);
          } while (bad() && tmo > 0);
        }
        AB(9);
        auto f = [&](int c, int e) { return __uint_as_float(sg[c][e]); };
        auto sum6 = [&](int e) { return add1(add1(add1(f(0, e), f(1, e)), add1(f(2, e), f(3, e))), add1(f(4, e), f(5, e))); };
        const float nmh = sum6(0) * (-1.0f / 6.0f), mh = -nmh;
        float qa = sum6(2) * (1.0f / 64.0f), qb;
#pragma unroll
        for (int c = 0; c < 6; c += 2) { const float u = add1(f(c, 0), nmh), v = add1(f(c + 1, 0), nmh); qa = fma1(u, u, qa); qb = c ? fma1(v, v, qb) : v * v; }
        const float Mh = add1(qa, qb);
        const auto sm = __builtin_amdgcn_permlane32_swap(__float_as_uint(mh), __float_as_uint(mh), false, false);
        const auto sM = __builtin_amdgcn_permlane32_swap(__float_as_uint(Mh), __float_as_uint(Mh), false, false);
        const float dm = __uint_as_float(sm[0]) - __uint_as_float(sm[1]), mean = rbf(add1(__uint_as_float(sm[0]), __uint_as_float(sm[1])) * 0.5f);
        const float vbf = rbf(__builtin_fmaf(dm * dm, 3.0f, add1(__uint_as_float(sM[0]), __uint_as_float(sM[1]))) * (1.0f / 12.0f));
        const float rz = __builtin_amdgcn_rcpf(rbf(__builtin_amdgcn_sqrtf(rbf(vbf + 1e-5f))));
        dsw4(zw, lane < 32 ? -mean * rz : rz);
        f32x4 zm[2], zr[2];
        ds16<0>(zm[0], zr_); ds16<128>(zr[0], zr_); ds16<16>(zm[1], zr_); ds16<144>(zr[1], zr_);
        wait_lg<0>(zm[0], zm[1], zr[0], zr[1]);
#pragma unroll
        for (int jj = 0; jj < 4; ++jj) {
          u32x4 o;
#pragma unroll
          for (int q = 0; q < 4; ++q) {
            const int i = q >> 1, e = (q & 1) * 2;
            o[q] = pack2(fma1(zv[i][jj][e], zr[i][e], zm[i][e]), fma1(zv[i][jj][e + 1], zr[i][e + 1], zm[i][e + 1]));
          }
          o[3] = nohole(o[3]);
          asm volatile("global_store_dwordx4 %0, %1, %2 offset:%3 sc1\n\ts_nop 1" :: "v"(lin), "v"(o), "s"(zb), "i"(jj * 1024) : "memory");
          SB();
        }
      }
      SB();
      AB(10);
      ldtag(ef, (unsigned)((lane & 31) * 8), ws + WS_EF + 2048 + cb * 256);
      signal1(flg + 28);
    }
    const char* Bs = ws + WS_Z2 + (size_t)(wave * 16 * 48 + cb * 6) * 1024 + 2048;
    bf16x8 Bf[3][6];
    auto ldB1 = [&](int s, int j) {
      asm volatile("global_load_dwordx4 %0, %1, %2 offset:%3 " HSC : "=&v"(Bf[s % 3][j]) : "v"(lin + (unsigned)(((s + 2 * rot2) & 15) * 49152)), "s"(Bs), "i"(j * 1024 - 2048) : "memory");
    };
    f32x4 acc[4][6];
    unsigned ara[2][2];
#pragma unroll
    for (int ks = 0; ks < 2; ++ks) {
      ara[0][ks] = ringl + l15 * 128 + (((ks * 4 + g4) ^ (l15 & 7)) << 4);
      ara[1][ks] = ara[0][ks] + (M_CV - M_RING) + wave * (CV_BUF - 24576);
    }
    u32x4 af[2][4];
    auto rdA = [&](int s, int i) {
      const int q = (0x30123012 >> (4 * (s >> 1))) & 3;
      asm volatile("ds_read_b128 %0, %1 offset:%2" : "=&v"(af[s & 1][i]) : "v"(ara[q == 3][s & 1]), "i"((q % 3) * 8192 + i * 2048) : "memory");
    };
    flag_wait_asm(flg + 28, 4u);
    AB(11);
    wait_vm<0>(ef, bsv);
    waittag(ef, (unsigned)((lane & 31) * 8), ws + WS_EF + 2048 + cb * 256);
    AB(12); SS(1 - 3 * XS);
#pragma unroll
    for (int j = 0; j < 6; ++j) ldB1(0, j);
#pragma unroll
    for (int j = 0; j < 6; ++j) ldB1(1, j);
    SS(2 - 3 * XS);
    {
      flag_wait_asm(flg + 20 + wl * 4, 2u);
      const float* cp2 = (const float*)(smem + L_CS2 + (wl * 64 + l15) * 4);
      pin(ac2[0][0], ac2[0][1], ac2[1][0], ac2[1][1]);
#pragma unroll
      for (int jj = 0; jj < 2; ++jj) {
        const float nct = -add1(add1(cp2[jj * 16], cp2[32 + jj * 16]), add1(cp2[128 + jj * 16], cp2[160 + jj * 16]));
        u32x4 o;
#pragma unroll
        for (int q = 0; q < 4; ++q) {
          const int i = q >> 1, e = (q & 1) * 2;
          o[q] = pack2(mul1(fma1(xm[i][e], nct, ac2[i][jj][e]), xr[i][e]), mul1(fma1(xm[i][e + 1], nct, ac2[i][jj][e + 1]), xr[i][e + 1]));
        }
        z1put(odd * 32 + wl * 32 + jj * 16, o);
      }
      signal1(flg + 132);
      SB();
    }
    __builtin_amdgcn_s_setprio(G2P);
    f32x4 bs4 = {lo16(bsv), lo16(bsv), lo16(bsv), lo16(bsv)};
    pin(bs4);
    AB(25);
    flag_wait_asm(flg + 32, 1u);
    AB(26);
#pragma unroll
    for (int i = 0; i < 4; ++i) rdA(0, i);
    SB();
    const unsigned xa = ringl + lane * 16;
    auto putp = [&](int i, int j) { asm volatile("ds_write_b128 %0, %1 offset:%2" :: "v"(xa), "v"(acc[i][j]), "i"(i * 6144 + j * 1024) : "memory"); };
    unsigned mx[2], fv;
    u64 hm;
    auto chk = [&](auto sc, int nw, int k) {
      constexpr int s = decltype(sc)::value, sl = s % 3;
      auto d3 = [&](int j) { return __builtin_bit_cast(u32x4, Bf[sl][j])[3]; };
      if (k == 0) {
        asm volatile("s_waitcnt vmcnt(%0)" :: "i"(nw) : "memory");
        pin(Bf[sl][0], Bf[sl][1], Bf[sl][2], Bf[sl][3], Bf[sl][4], Bf[sl][5]);
        mx[0] = opq(umx(umx(d3(0), d3(1)), d3(2)));
      }
      if (k == 1) mx[1] = opq(umx(umx(d3(3), d3(4)), mx[0]));
      if (k == 2) { hm = __builtin_amdgcn_ballot_w64(umx(mx[1], d3(5)) == HOLE); asm volatile("" : "+s"(hm)); }
      if (k == 3 && __builtin_expect(hm != 0, 0)) {
        do {
          --tmo;
          asm volatile("s_waitcnt vmcnt(0)\n\ts_sleep 2" ::: "memory");
#pragma unroll
          for (int q = 0; q < (s ? 3 : 2); ++q)
            if (s + q < 16) reld6(Bf[(s + q) % 3], opq(lin) + (unsigned)(((s + q + 2 * rot2) & 15) * 49152), Bs);
          wait_vm<0>(Bf[sl][0], Bf[sl][1], Bf[sl][2], Bf[sl][3], Bf[sl][4], Bf[sl][5]);
        } while (__builtin_amdgcn_ballot_w64(umx(umx(umx(d3(0), d3(1)), d3(2)), umx(umx(d3(3), d3(4)), d3(5))) == HOLE) != 0 && tmo > 0);
      }
      SB();
    };
    chk(IC(0), 6, 0); chk(IC(0), 0, 1); chk(IC(0), 0, 2); chk(IC(0), 0, 3);
    auto mm = [&](int s, int i, int j) {
      if (s == 0 && i == 3) asm volatile("v_mfma_f32_16x16x32_bf16 %0, %1, %2, %3" : "=&v"(acc[i][j]) : "v"(Bf[0][j]), "v"(af[0][i]), "v"(bs4));
      else if (s == 0) MFMA0(acc[i][j], Bf[0][j], af[0][i]);
      else MFMA(acc[i][j], Bf[s % 3][j], af[s & 1][i]);
      SB();
    };
    auto step = [&](auto sc) {
      constexpr int s = decltype(sc)::value;
      constexpr int NA[16] = ANUM, a0 = s == 0 ? 0 : cum16(NA, s);
      static_assert(cum16(NA, 5) == 0 && cum16(NA, 7) <= 8 && cum16(NA, 9) >= 8 && cum16(NA, 11) == 16 && NA[s] <= 5, "");
      constexpr int NV = (s == 0 ? 5 : (s == 1 ? 1 : 6 + NA[s ? s - 1 : 0])) + (s < 13 ? 5 : 0) + NA[s];
      auto fad = [&](int q) { return flg + (q == 1 ? 32 : (q == 9 ? 144 : 112) + wave * 4); };
      wait_lg<0>(af[s & 1][0], af[s & 1][1], af[s & 1][2], af[s & 1][3]);
      if constexpr (s == 1 || s == 5 || s == 7 || s == 9) {
        pin(fv);
        constexpr unsigned tg = s == 1 || s == 7 ? 2u : 1u;
        if (__builtin_expect((unsigned)__builtin_amdgcn_readfirstlane((int)fv) < tg, 0)) flag_wait_asm(fad(s), tg);
      }
      SS(3 + 2 * (s - XS));
      SB();
#pragma unroll
      for (int j = 0; j < 6; ++j) {
        mm(s, 0, j);
        if (s == 0) ldB1(2, j);
        if (j == 5) chk(IC(s + 1), NV + (s == 0), 0);
        SB();
        mm(s, 1, j);
        if (j < NA[s]) ldA1(6 + ((a0 + j) >> 3), (a0 + j) & 7);
        if (j == 5) chk(IC(s + 1), 0, 1);
        SB();
        mm(s, 2, j);
        if (j < 4) rdA(s + 1, j);
        if (j == 4 && (s == 0 || s == 2 || s == YS)) signal1(flg + 96 + wave * 4);
        if (j == 4 && (s == 0 || s == 4 || s == 6 || s == 8)) asm volatile("ds_read_b32 %0, %1" : "=&v"(fv) : "v"(fad(s + 1)) : "memory");
        if (j == 4 && s == SGS) signal1(flg + 44);
        if (j == 5) chk(IC(s + 1), 0, 2);
        SB();
        mm(s, 3, j);
        if (s + 3 < 16) ldB1(s + 3, j);
        SB();
      }
      SS(4 + 2 * (s - XS));
      chk(IC(s + 1), 0, 3);
    };
    step(IC(0)); AB(13); step(IC(1)); step(IC(2)); step(IC(3)); AB(14); step(IC(4)); step(IC(5)); step(IC(6)); step(IC(7));
    step(IC(8)); step(IC(9)); step(IC(10)); step(IC(11)); step(IC(12)); step(IC(13));
    wait_lg<0>(af[0][0], af[0][1], af[0][2], af[0][3]);
    SS(3 + 2 * (14 - XS));
#pragma unroll
    for (int i = 0; i < 4; ++i) {
#pragma unroll
      for (int n = 0; n < 12; ++n) {
        if (i == 0 && n == 6) { chk(IC(15), 0, 3); wait_lg<0>(af[1][0], af[1][1], af[1][2], af[1][3]); }
        mm(14 + n / 6, i, n % 6);
        if (i == 0 && n < 2) { rdA(15, 2 * n); rdA(15, 2 * n + 1); }
        if (i == 0 && n >= 2 && n < 5) chk(IC(15), 0, n - 2);
        if (i > 0 && (n & 1)) putp(i - 1, n >> 1);
        SB();
      }
      if (i > 0) signal1(flg + ((wave + i) & 3) * 4);
      SB();
    }
    SS(4 + 2 * (14 - XS));
    AB(15);
    flag_wait_asm(flg + 56, 5u);
    s16x4 z1r[6];
    {
      const int fr = 4 * g4 + (l15 >> 2), p = lane & 3;
      const unsigned za = lb + L_Z1 + fr * 128 + (((wave * 2 + (p >> 1)) ^ (fr & 7)) << 4) + (p & 1) * 8;
      z1r[0] = tr_asm<0>(za); z1r[1] = tr_asm<2048>(za); z1r[2] = tr_asm<4096>(za);
      z1r[3] = tr_asm<6144>(za); z1r[4] = tr_asm<8192>(za); z1r[5] = tr_asm<10240>(za);
    }
    flag_wait_asm(flg + wave * 4, 3u);
    asm volatile("" : "+v"(z1r[0]), "+v"(z1r[1]), "+v"(z1r[2]), "+v"(z1r[3]), "+v"(z1r[4]), "+v"(z1r[5]));
    AB(16);
    f32x4 own[6];
    {
      f32x4 pr_[3][6];
#pragma unroll
      for (int q = 0; q < 3; ++q) {
        const char* src = smem + M_RING + ((wave + 3 - q) & 3) * 24576;
#pragma unroll
        for (int j = 0; j < 6; ++j) pr_[q][j] = *(const f32x4*)(src + q * 6144 + j * 1024 + lane * 16);
      }
      asm volatile("s_nop 7" : "+v"(acc[3][0]), "+v"(acc[3][1]), "+v"(acc[3][2]), "+v"(acc[3][3]), "+v"(acc[3][4]), "+v"(acc[3][5]));
#pragma unroll
      for (int j = 0; j < 6; ++j) own[j] = (acc[3][j] + pr_[0][j]) + (pr_[1][j] + pr_[2][j]);
    }
    SB();
    {
      unsigned pk[6][2];
#pragma unroll
      for (int j = 0; j < 6; ++j) {
        const u32x2 z = __builtin_bit_cast(u32x2, z1r[j]);
#pragma unroll
        for (int h = 0; h < 2; ++h) pk[j][h] = pk2(unpk(z[h]) * (f32x2){own[j][2 * h], own[j][2 * h + 1]});
      }
      const unsigned eo = (unsigned)((l15 + 16 * (2 * (g4 & 1) + (g4 >> 1))) * 16);
#pragma unroll
      for (int q = 0; q < 3; ++q) {
        const auto s0 = __builtin_amdgcn_permlane16_swap(pk[2 * q][0], pk[2 * q + 1][0], false, false);
        const auto s1 = __builtin_amdgcn_permlane16_swap(pk[2 * q][1], pk[2 * q + 1][1], false, false);
        st16(eo, hf + (size_t)((rb * 4 + wave) * 24 + cb * 3 + q) * 1024, (u32x4){s0[0], s1[0], s0[1], nohole(s1[1])});
      }
    }
    signal1(flg + 96 + wave * 4);
    AB(17);
    ldW();
    wait_vm<15>(ef);
    gemm3(IC(0));
    if (b == 0 && tid == 0) *(u64*)(ws + WS_EP) = tag;
  }
  AB(24);
  AB_OUT();
}

extern "C" __global__ void k_end() {}

extern "C" void kernel_launch(void* const* d_in, const int* in_sizes, int n_in,
                              void* d_out, int out_size, void* d_ws, size_t ws_size,
                              hipStream_t stream) {
  (void)hipFuncSetAttribute((const void*)k_f, hipFuncAttributeMaxDynamicSharedMemorySize, M_LDS);
  k_f<<<256, 512, M_LDS, stream>>>((const u16*)d_in[0], (const u16*)d_in[1], (const u16*)d_in[2], (const u16*)d_in[3], (const u16*)d_in[4],
                                   (const u16*)d_in[5], (const u16*)d_in[7], (const u16*)d_in[8], (const u16*)d_in[9], (const u16*)d_in[10],
                                   (u16*)d_out, (char*)d_ws);
}
```

```cpp
#define PROF_AB
#define ABM (1u | (1u << 24))
#define HQ 24
#define HM 'z'
#define ABV 0
#include <hip/hip_runtime.h>
#include <hip/hip_bf16.h>
#include <stdint.h>
#include <type_traits>

typedef unsigned short u16;
typedef unsigned long long u64;
typedef __attribute__((ext_vector_type(8))) short bf16x8;
typedef __attribute__((ext_vector_type(4))) short s16x4;
typedef __attribute__((ext_vector_type(4))) float f32x4;
typedef __attribute__((ext_vector_type(2))) float f32x2;
typedef __attribute__((ext_vector_type(4))) unsigned int u32x4;
typedef __attribute__((ext_vector_type(2))) unsigned int u32x2;
typedef __attribute__((ext_vector_type(2))) __bf16 bf16x2n;

#define LDS_AS __attribute__((address_space(3)))
#define IC(n) std::integral_constant<int, (n)>{}
#define SB() __builtin_amdgcn_sched_barrier(0)

__device__ unsigned g_ab[256][8][32];
#define AB_DECL unsigned ab_[32] = {}
#define AB(i) do { SB(); if ((ABM >> (i)) & 1) { ab_[i] = (unsigned)__builtin_amdgcn_s_memrealtime(); if ((ABV >> (i)) & 1) asm volatile("" : "+v"(ab_[i])); } SB(); } while (0)
#define AB_OUT() do { if ((threadIdx.x & 63) == 0) for (int i_ = 0; i_ < 32; ++i_) g_ab[blockIdx.x][threadIdx.x >> 6][i_] = ab_[i_] ? ab_[i_] : ab_[0]; } while (0)
#define CB(i) do {} while (0)
#define SS(i) do {} while (0)
constexpr int S_ = 2048, D_ = 640, FH_ = 768, F2_ = 1536;
constexpr unsigned HOLE = 0xFFFFFFFFu;

constexpr size_t WS_EP = 0;
constexpr size_t WS_EF = 4096;
constexpr size_t WS_LF = 22528;
constexpr size_t WS_YF = 14336;
constexpr size_t WS_XF = 20480;
constexpr size_t WS_ST = 32768;
constexpr size_t WS_Z2 = WS_ST + 32 * 12 * 1024;
constexpr size_t WS_H = WS_Z2 + 64 * 48 * 1024;
constexpr size_t WS_ZX = WS_H + 128 * 24 * 1024;
constexpr size_t WS_KV = WS_ZX + 32 * 4 * 4096;
constexpr size_t WS_Y = WS_KV + D_ * S_ * 2;
constexpr size_t WS_WO = WS_Y + D_ * S_ * 2;

__device__ __forceinline__ float bf2f(u16 v) { return __uint_as_float(((unsigned)v) << 16); }
__device__ __forceinline__ unsigned pack2(float lo, float hi) {
  f32x2 v = {lo, hi};
  bf16x2n r = __builtin_convertvector(v, bf16x2n);
  return __builtin_bit_cast(unsigned, r);
}
__device__ __forceinline__ float nv(float v) { asm("" : "+v"(v)); return v; }
__device__ __forceinline__ float fma1(float a, float b, float c) { float r; asm("v_fma_f32 %0, %1, %2, %3" : "=v"(r) : "v"(a), "v"(b), "v"(c)); return r; }
__device__ __forceinline__ float add1(float a, float b) { float r; asm("v_add_f32 %0, %1, %2" : "=v"(r) : "v"(a), "v"(b)); return r; }
__device__ __forceinline__ float mul1(float a, float b) { float r; asm("v_mul_f32 %0, %1, %2" : "=v"(r) : "v"(a), "v"(b)); return r; }
__device__ __forceinline__ unsigned pack2s(float lo, float hi) { return pack2(nv(lo), nv(hi)); }
__device__ __forceinline__ float lo16(unsigned w) { return __uint_as_float(w << 16); }
__device__ __forceinline__ float hi16(unsigned w) { return __uint_as_float(w & 0xFFFF0000u); }
__device__ __forceinline__ float rbf(float f) { return lo16(pack2(f, f)); }
__device__ __forceinline__ f32x2 unpk(unsigned w) { f32x2 r = {lo16(w), hi16(w)}; return r; }
__device__ __forceinline__ unsigned pk2(f32x2 v) { return pack2(v[0], v[1]); }
__device__ __forceinline__ unsigned umx(unsigned a, unsigned b) { return a > b ? a : b; }
__device__ __forceinline__ unsigned nohole(unsigned a) { return a < HOLE - 1 ? a : HOLE - 1; }
__device__ __forceinline__ f32x2 gelu2(f32x2 x) {
  const f32x2 c1 = {-2.3022082f, -2.3022082f}, c2 = {-0.10294324f, -0.10294324f};
  const f32x2 a = x * __builtin_elementwise_fma(x * x, c2, c1);
  f32x2 e = {__builtin_amdgcn_exp2f(a[0]), __builtin_amdgcn_exp2f(a[1])};
  e = e + (f32x2){1.0f, 1.0f};
  const f32x2 r = {__builtin_amdgcn_rcpf(e[0]), __builtin_amdgcn_rcpf(e[1])};
  return x * r;
}
__device__ __forceinline__ float gelu1(float x) {
  const float a = x * __builtin_fmaf(x * x, -0.10294324f, -2.3022082f);
  return x * __builtin_amdgcn_rcpf(__builtin_amdgcn_exp2f(a) + 1.0f);
}
template <int OFF>
__device__ __forceinline__ s16x4 tr_asm(unsigned a) {
  s16x4 r;
  asm volatile("ds_read_b64_tr_b16 %0, %1 offset:%2" : "=&v"(r) : "v"(a), "i"(OFF) : "memory");
  return r;
}
__device__ __forceinline__ bf16x8 join8(s16x4 lo, s16x4 hi) {
  bf16x8 r;
  r[0] = lo[0]; r[1] = lo[1]; r[2] = lo[2]; r[3] = lo[3];
  r[4] = hi[0]; r[5] = hi[1]; r[6] = hi[2]; r[7] = hi[3];
  return r;
}
__device__ __forceinline__ unsigned lds_addr(const void* p) { return (unsigned)(uintptr_t)(LDS_AS const char*)p; }
#define WAIT_LGKM0() asm volatile("s_waitcnt lgkmcnt(0)" ::: "memory")
__device__ __forceinline__ void flag_wait_asm(unsigned faddr, unsigned target) {
  unsigned v, sv;
  asm volatile(".Lfw_%=:\n\tds_read_b32 %0, %2\n\ts_waitcnt lgkmcnt(0)\n\tv_readfirstlane_b32 %1, %0\n\ts_nop 0\n\t"
               "s_cmp_ge_u32 %1, %3\n\ts_cbranch_scc1 .Lfd_%=\n\ts_sleep 1\n\ts_branch .Lfw_%=\n\t.Lfd_%=:"
               : "=&v"(v), "=&s"(sv) : "v"(faddr), "s"(target) : "memory", "scc");
}
__device__ __forceinline__ void signal1(unsigned a, unsigned one = 1u) {
  asm volatile("s_mov_b64 exec, 1\n\tds_add_u32 %0, %1\n\ts_mov_b64 exec, -1" :: "v"(a), "v"(one) : "memory");
}
__device__ __forceinline__ void dma16(unsigned vo, const void* sb, unsigned lds) {
  asm volatile("s_mov_b32 m0, %2\n\ts_nop 0\n\tglobal_load_lds_dwordx4 %0, %1" :: "v"(vo), "s"(sb), "s"(lds) : "memory");
}
template <int LO, int GO> __device__ __forceinline__ void dma32s(unsigned vo0, unsigned vo1, const void* sb, unsigned lds) {
  asm volatile("s_add_u32 m0, %5, %6\n\ts_add_u32 vcc_lo, %2, %4\n\ts_addc_u32 vcc_hi, %3, 0\n\t"
               "global_load_lds_dwordx4 %0, vcc\n\tglobal_load_lds_dwordx4 %1, vcc offset:1024"
               :: "v"(vo0), "v"(vo1), "s"((unsigned)(uintptr_t)sb), "s"((unsigned)((uintptr_t)sb >> 32)), "i"(GO), "s"(lds), "i"(LO) : "memory", "scc", "vcc");
}
template <int LO, int GO, int WRAP, int LIM> __device__ __forceinline__ void dma32r(unsigned vo0, unsigned vo1, const void* sb, unsigned lds, int rot) {
  const char* s2 = (const char*)sb - WRAP;
  asm volatile("s_add_u32 m0, %5, %6\n\ts_cmp_ge_u32 %7, %8\n\ts_cselect_b64 vcc, %3, %2\n\ts_add_u32 vcc_lo, vcc_lo, %4\n\ts_addc_u32 vcc_hi, vcc_hi, 0\n\t"
               "global_load_lds_dwordx4 %0, vcc\n\tglobal_load_lds_dwordx4 %1, vcc offset:1024"
               :: "v"(vo0), "v"(vo1), "s"(sb), "s"(s2), "i"(GO), "s"(lds), "i"(LO), "s"(rot), "i"(LIM) : "memory", "scc", "vcc");
}
__device__ __forceinline__ void dma16o(unsigned vo, const void* sb, int go, unsigned lds, int lo) {
  asm volatile("s_add_u32 m0, %3, %4\n\ts_nop 0\n\tglobal_load_lds_dwordx4 %0, %1 offset:%2" :: "v"(vo), "s"(sb), "i"(go), "s"(lds), "i"(lo - go) : "memory", "scc");
}
__device__ __forceinline__ void st16(unsigned vo, const void* sb, u32x4 v) {
  asm volatile("global_store_dwordx4 %0, %1, %2 sc1\n\ts_nop 1" :: "v"(vo), "v"(v), "s"(sb) : "memory");
}
__device__ __forceinline__ void touch(unsigned vo, const void* sb, unsigned lds) {
  asm volatile("s_mov_b32 m0, %2\n\ts_nop 0\n\tglobal_load_lds_dword %0, %1" :: "v"(vo), "s"(sb), "s"(lds) : "memory");
}
__device__ __forceinline__ void st8(unsigned vo, const void* sb, u32x2 v) { asm volatile("global_store_dwordx2 %0, %1, %2 sc1" :: "v"(vo), "v"(v), "s"(sb) : "memory"); }
template <class A, class B> __device__ __forceinline__ void MFMA(f32x4& acc, const A& a, const B& b) {
  asm volatile("v_mfma_f32_16x16x32_bf16 %0, %1, %2, %0" : "+v"(acc) : "v"(a), "v"(b));
}
template <class A, class B> __device__ __forceinline__ void MFMA0(f32x4& acc, const A& a, const B& b) {
  asm volatile("v_mfma_f32_16x16x32_bf16 %0, %1, %2, 0" : "=&v"(acc) : "v"(a), "v"(b));
}
template <bool Z, class A, class B> __device__ __forceinline__ void MFMAW(int n, f32x4& acc, const A& a, const B& b) {
  if (n < 0) { if (Z) MFMA0(acc, a, b); else MFMA(acc, a, b); }
  else if (Z) asm volatile("s_waitcnt lgkmcnt(%3)\n\tv_mfma_f32_16x16x32_bf16 %0, %1, %2, 0" : "=&v"(acc) : "v"(a), "v"(b), "i"(n));
  else asm volatile("s_waitcnt lgkmcnt(%3)\n\tv_mfma_f32_16x16x32_bf16 %0, %1, %2, %0" : "+v"(acc) : "v"(a), "v"(b), "i"(n));
}
__device__ __forceinline__ float sel4(float a, float b, float c, float d, u64 m1, u64 m2) {
  float r, t;
  asm volatile("v_cndmask_b32_e64 %0, %2, %3, %6\n\tv_cndmask_b32_e64 %1, %4, %5, %6\n\tv_cndmask_b32_e64 %0, %0, %1, %7"
               : "=&v"(r), "=&v"(t) : "v"(a), "v"(b), "v"(c), "v"(d), "s"(m1), "s"(m2));
  return r;
}
template <class T> __device__ __forceinline__ void pin1(T& r) { asm volatile("" : "+v"(r)); }
template <class T> __device__ __forceinline__ T opq(T r) { asm volatile("" : "+v"(r)); return r; }
template <class... T> __device__ __forceinline__ void pin(T&... r) { (pin1(r), ...); }
template <int N, class... T> __device__ __forceinline__ void wait_vm(T&... r) { asm volatile("s_waitcnt vmcnt(%0)" :: "n"(N) : "memory"); (pin1(r), ...); }
template <int N, class... T> __device__ __forceinline__ void wait_lg(T&... r) { asm volatile("s_waitcnt lgkmcnt(%0)" :: "n"(N) : "memory"); (pin1(r), ...); }
template <int OFF, class T> __device__ __forceinline__ void ds16(T& d, unsigned a) {
  asm volatile("ds_read_b128 %0, %1 offset:%2" : "=&v"(d) : "v"(a), "i"(OFF) : "memory");
}
template <int OFF> __device__ __forceinline__ void dsw16(unsigned a, const f32x4& v) {
  asm volatile("ds_write_b128 %0, %1 offset:%2" :: "v"(a), "v"(v), "i"(OFF) : "memory");
}
__device__ __forceinline__ void dsw4(unsigned a, float v) { asm volatile("ds_write_b32 %0, %1" :: "v"(a), "v"(v) : "memory"); }
#define OSC ""
#define HSC ""
template <int OFF, class T> __device__ __forceinline__ void ld16h(T& d, unsigned vo, const void* sb) {
  asm volatile("global_load_dwordx4 %0, %1, %2 offset:%3 " HSC : "=&v"(d) : "v"(vo), "s"(sb), "i"(OFF) : "memory");
}
template <class T> __device__ __forceinline__ void reld6(T (&B)[6], unsigned vo, const void* sb) {
  asm volatile("global_load_dwordx4 %0, %6, %7 offset:-2048 sc1\n\tglobal_load_dwordx4 %1, %6, %7 offset:-1024 sc1\n\t"
               "global_load_dwordx4 %2, %6, %7 sc1\n\tglobal_load_dwordx4 %3, %6, %7 offset:1024 sc1\n\t"
               "global_load_dwordx4 %4, %6, %7 offset:2048 sc1\n\tglobal_load_dwordx4 %5, %6, %7 offset:3072 sc1"
               : "+v"(B[0]), "+v"(B[1]), "+v"(B[2]), "+v"(B[3]), "+v"(B[4]), "+v"(B[5]) : "v"(vo), "s"(sb) : "memory");
}

constexpr int L_FLAG = 0;
constexpr int L_DUM = 256;
constexpr int L_XM = 512;
constexpr int L_XR = 768;
constexpr int L_CS = 1024;
constexpr int L_ZS = 2048;
constexpr int L_CS2 = 3072;
constexpr int L_Z1 = 4096;
constexpr int L_RAW = L_Z1;
constexpr int L_SLOT = 16384;
constexpr int L_KV = 512, KV_P = 3088, L_WO = L_KV + 8 * KV_P;
static_assert(L_WO % 16 == 0 && L_WO + 4 * 1024 <= L_SLOT + 2 * 8192, "staging");
constexpr int L_P2 = L_SLOT + 12 * 8192;
constexpr int CV_C = 4608, CV_CS = 4160, CV_BUF = CV_C + 2 * CV_CS;
constexpr int M_CV = L_SLOT;
constexpr int M_RING = L_SLOT + 6 * 8192;
constexpr int P3_G = L_SLOT;
constexpr int P3_C = P3_G + 80 * 1024;
constexpr int yat(int j) { return M_CV + (j >> 1) * CV_BUF + 8192 + (j & 1) * 2048; }
constexpr int M_LDS = 163840;
static_assert(3 * CV_BUF + 8192 <= 6 * 8192 && P3_C + 60 * 1024 <= M_LDS && CV_BUF >= 8192 + 4096, "LDS map");

enum { UX, UA, UB, UV };
struct Unit { int kind, idx, need, free, slot; };
#define SCHED gugugugwBuluouk_uuxuu_uuxuxu_uuxuxuu_Euuuuh_vuumumuh_vuumumuhF_vuvumum_DRvuuUHmmmmSptt_VhhhttttttJItttttttt
#define STR_(x) #x
#define STR(x) STR_(x)
constexpr char SCH[] = STR(SCHED);
constexpr int NSCH = sizeof(SCH) - 1;
constexpr int XNEED[10] = {1, 2, 2, 3, 3, 4, 4, 5, 5, 6};
constexpr int VNEED[5] = {5, 6, 7, 7, 8};
constexpr int XSLOT[10] = {17, 16, 15, 13, 10, 14, 11, 17, 9, 16}, WSLOT[10] = {6, 2, 4, 0, 6, 12, 10, 4, 0, 8}, VSLOT[10] = {8, 15, 3, 2, 14, 17, 16, 15, 7, 6}, XW = 6;
constexpr int cnt(char c, int e = NSCH) { int n = 0; for (int i = 0; i < e; ++i) n += SCH[i] == c || (c == 'u' && SCH[i] == 'g'); return n; }
constexpr int pos(char c, int k = 0) { for (int i = 0; i < NSCH; ++i) if (SCH[i] == c && k-- == 0) return i; return NSCH; }
constexpr Unit unit(int n) {
  for (int b = 1; b < 11; ++b) {
    for (int c = 0; c < 10; ++c) if (XNEED[c] == b && n-- == 0) return {UX, c, b, b + 1, XSLOT[c]};
    for (int c = 0; c < 5; ++c) if (VNEED[c] == b && n-- == 0) return {UV, c, b, b + 1, VSLOT[c]};
    if (n < 2) return {n ? UB : UA, b - 1, b < 8 ? b : 8, b < 7 ? b + 2 : 9, WSLOT[b - 1] + n};
    n -= 2;
  }
  return {UV, n + 5, 10, 99, VSLOT[n + 5]};
}
constexpr int FLAGB = cnt('_', pos('D')), GZ = cnt('_', pos('E'));
constexpr int nvm(char c) { return c == 'u' ? 2 : (c == 'H' ? 5 : (c == 'U' ? 4 : (c == 'F' || c == 'h' || c == 't' || c == 'w'))); }
constexpr int behind(int g, int e, char w) {
  const int b = cnt('_', e) + 1;
  int n = 0;
  for (int i = 0; i < e; ++i) {
    const char c = SCH[i];
    if (g == 0) { if (c == 'g') n = w == '_' && unit(cnt('u', i)).need <= b ? 0 : n + 2; if (c == 'B') n += 2; continue; }
    n = (w == '_' ? (c == 'u' && unit(cnt('u', i)).need <= b) || (c == 'h' && b == FLAGB) || (c == 'k' && b == GZ) : c == w) ? 0 : n + nvm(c);
  }
  return g == 0 && b >= GZ ? 0 : n;
}
constexpr bool plan_ok() {
  if (cnt('u') != 40 || cnt('_') != 9 || cnt('x') != 5 || cnt('h') != 6 || cnt('t') != 16 || cnt('I') * cnt('J') * cnt('p') != 1 || cnt('v') != 5 || cnt('m') != 10) return false;
  if (cnt('B') * cnt('D') * cnt('E') * cnt('F') * cnt('l') * cnt('o') * cnt('k') * cnt('H') * cnt('U') * cnt('V') * cnt('R') * cnt('S') != 1) return false;
  if (pos('l') < pos('B') || pos('o') < pos('B') || pos('k') < pos('l') || pos('k') > pos('_') || pos('o') > pos('_') || cnt('g', pos('B')) != cnt('g')) return false;
  if (cnt('_', pos('h', 2)) >= FLAGB || GZ < 3 || GZ > 6 || FLAGB < GZ || FLAGB > 9 || pos('o') > pos('k') || pos('H') > pos('I') || pos('F') > pos('U')) return false;
  if (cnt('_', pos('R')) != 8 || cnt('_', pos('S')) != 8 || pos('S') < pos('R') || cnt('_', pos('I')) != 9 || pos('J') < pos('U') || cnt('_', pos('J')) != 9) return false;
  if (cnt('_', pos('V')) != 9 || cnt('u', pos('V')) < 40) return false;
  for (int c = 0; c < 5; ++c) if (cnt('_', pos('x', c)) != XNEED[c] || cnt('_', pos('v', c)) != VNEED[c] || pos('m', 2 * c) < pos('v', c)) return false;
  if (cnt('_', pos('p')) != 8 || pos('p') < pos('m', 9)) return false;
  int fr[18] = {1, 1};
  for (int i = 0; i < NSCH; ++i) if (SCH[i] == 'u' || SCH[i] == 'g') {
    const Unit u = unit(cnt('u', i));
    const int p = cnt('_', i);
    if (p < fr[u.slot] || p >= u.need) return false;
    fr[u.slot] = u.free;
  }
  return true;
}
static_assert(plan_ok(), "phase A plan");
template <int I, int E, class F> __device__ __forceinline__ void sfor(F&& f) { if constexpr (I < E) { f(IC(I)); sfor<I + 1, E>(f); } }

__device__ __forceinline__ void conv_core(unsigned buf, int lane, unsigned Dw, f32x4 (&ac)[8]) {
  const int l15 = lane & 15, g4 = lane >> 4;
  const int c = (l15 + 1) & 1;
  unsigned aa[4];
#pragma unroll
  for (int k = 0; k < 4; ++k) aa[k] = buf + CV_C + c * CV_CS + (2031 - l15 + 8 * g4 - c) * 2 - 512 - 1024 * k;
  unsigned bb = buf + 512 + 32 * l15 - 32 + 16 * g4;
  u32x2 A[2][8][2];
  u32x4 B[2][8];
  constexpr int TT[36] = {7, 6, 5, 4, 7, 6, 5, 4, 7, 6, 5, 3, 7, 6, 5, 4, 7, 6, 3, 2, 7, 6, 5, 4, 7, 3, 2, 1, 7, 6, 5, 4, 3, 2, 1, 0};
  constexpr int AA[36] = {0, 0, 0, 0, 1, 1, 1, 1, 2, 2, 2, 0, 3, 3, 3, 2, 4, 4, 1, 0, 5, 5, 4, 3, 6, 2, 1, 0, 7, 6, 5, 4, 3, 2, 1, 0};
  auto ld1 = [&](int p, int n) {
    if (n < 16) {
      const int a = n >> 1, o = ((a & 1) ? 0 : 128) + 2 * (n & 1);
      asm volatile("ds_read2_b32 %0, %1 offset0:%2 offset1:%3" : "=&v"(A[p][(n >> 1) & 7][n & 1]) : "v"(aa[(a >> 1) & 3]), "i"(o), "i"(o + 1) : "memory");
    } else
      asm volatile("ds_read_b128 %0, %1 offset:%2" : "=&v"(B[p][n & 7]) : "v"(bb), "i"(512 * (n & 7)) : "memory");
  };
  auto next = [&](int dl) {
#pragma unroll
    for (int k = 0; k < 4; ++k) aa[k] += dl;
    bb += dl;
    SB();
  };
  auto land = [&](int p) {
    asm volatile("s_waitcnt lgkmcnt(0)" : "+v"(A[p][0][0]), "+v"(A[p][0][1]), "+v"(A[p][1][0]), "+v"(A[p][1][1]), "+v"(A[p][2][0]), "+v"(A[p][2][1]),
                 "+v"(A[p][3][0]), "+v"(A[p][3][1]), "+v"(A[p][4][0]), "+v"(A[p][4][1]), "+v"(A[p][5][0]), "+v"(A[p][5][1]), "+v"(A[p][6][0]), "+v"(A[p][6][1]),
                 "+v"(A[p][7][0]), "+v"(A[p][7][1]), "+v"(B[p][0]), "+v"(B[p][1]), "+v"(B[p][2]), "+v"(B[p][3]), "+v"(B[p][4]), "+v"(B[p][5]),
                 "+v"(B[p][6]), "+v"(B[p][7]) :: "memory");
    SB();
  };
  auto mm = [&](int p) {
#pragma unroll
    for (int n = 0; n < 36; ++n) {
      const int t = TT[n], a = AA[n];
      const u32x4 av = {A[p][a][0][0], A[p][a][0][1], A[p][a][1][0], A[p][a][1][1]};
      MFMA(ac[t], av, B[p][t - a]);
      if (n < 24) ld1(p ^ 1, n);
      SB();
    }
    land(p ^ 1);
  };
#pragma unroll
  for (int n = 0; n < 24; ++n) ld1(0, n);
  u32x4 ad;
#pragma unroll
  for (int e = 0; e < 4; ++e) ad[e] = lane == 32 + 2 * e + (lane & 1) + (lane & 8) * 3 ? Dw << (16 * (lane & 1)) : 0u;
  pin(ad);
  land(0);
#pragma unroll
  for (int t = 0; t < 8; ++t) MFMA0(ac[t], ad, B[0][t]);
#pragma unroll 1
  for (int r = 0; r < 8; r += 2) {
    next(-64);
    mm(0);
    next(r < 6 ? -64 : 0);
    mm(1);
  }
  asm volatile("s_nop 15" : "+v"(ac[0]), "+v"(ac[1]), "+v"(ac[2]), "+v"(ac[3]), "+v"(ac[4]), "+v"(ac[5]), "+v"(ac[6]), "+v"(ac[7]));
}

#define ROTA 3
#define RA1 1
#define RA2 0
#define RA3 0
#define RB1 0
#define RB2 4
#define RB3 0
#define G2P 0
#define RB4 2
#define THR 24
#define RB5 2
#define YS 8
#define WO3 0
#define C3P 0
#define FE3 9
#define WSH 1
#define CPR 1
#define STW 1
#define PN 10
#define P2P 2
#define SGS 11
#define ANUM {0, 0, 0, 0, 0, 3, 3, 2, 3, 3, 2, 0, 0, 0, 0, 0}
constexpr int cum16(const int (&a)[16], int n) { int c = 0; for (int i = 0; i < n; ++i) c += a[i]; return c; }

__global__ __launch_bounds__(512) void k_f(
    const u16* __restrict__ x, const u16* __restrict__ kin, const u16* __restrict__ vin, const u16* __restrict__ shift_k,
    const u16* __restrict__ ssm_k, const u16* __restrict__ ssm_D, const u16* __restrict__ w_in, const u16* __restrict__ w_sp,
    const u16* __restrict__ b_sp, const u16* __restrict__ w_out, u16* __restrict__ out, char* ws) {
  extern __shared__ __attribute__((aligned(16))) char smem[];
  const int tid = threadIdx.x, lane = tid & 63, wave = __builtin_amdgcn_readfirstlane(tid >> 6);
  const int b = blockIdx.x;
  const int xcd = b & 7, idx = b >> 3;
  const int rb = 8 * (xcd >> 1) + (idx >> 2), cb = 4 * (xcd & 1) + (idx & 3);
  const int t0 = rb * 64, d0 = cb * 80;
  const int g4 = lane >> 4, l15 = lane & 15;
  const unsigned lin = (unsigned)(lane * 16);
  const unsigned lb = lds_addr(smem), flg = lb + L_FLAG;
  AB_DECL;
  AB(0);
  asm volatile("" :: "s"(x), "s"(kin), "s"(vin), "s"(shift_k), "s"(ssm_k), "s"(ssm_D), "s"(w_in), "s"(w_sp), "s"(b_sp), "s"(w_out), "s"(out), "s"(ws));
  u64 tag = *(const __attribute__((address_space(4))) u64*)(uintptr_t)ws;
  int tmo = 1 << 16;
#define CWARM 1
  auto cwarm = [&]() {
    u64 pc;
    unsigned clen;
    asm volatile("s_getpc_b64 %0\n.Lpc_here_%=:\n\ts_mov_b32 %1, k_end-.Lpc_here_%=" : "=s"(pc), "=s"(clen));
    touch(min((unsigned)(idx * 1536 + min(lane, 11) * 128), clen - 4u) & ~3u, (const void*)pc, lb + L_DUM);
  };

  const int cw = wave & 3, wm = wave & 1, wl = (wave >> 1) & 1, ksb = wm ^ wl, odd = cb & 1, pr = cb >> 1;
  const int r8 = lane >> 3, c8 = lane & 7;
  unsigned voX[2], voW[2];
#pragma unroll
  for (int h = 0; h < 2; ++h) {
    voX[h] = (unsigned)((h * 8 + r8) * (D_ * 2) + ((c8 ^ (h * 4 + (r8 & 3))) << 4) - h * 1024);
    voW[h] = (unsigned)((h * 8 + r8) * (F2_ * 2) + ((c8 ^ (h * 4 + (r8 & 2))) << 4) - h * 1024);
  }
  const int rot = (RA1 * ((rb & 7) * 5 >> 2) + ROTA * (cb & 3) + RA2 * ((rb >> 3) * 5 >> 1) + RA3 * (cb >> 2)) % 10, rot2 = (RB1 * (cb & 3) + RB2 * (cb >> 2) + RB3 * (rb & 7) + RB4 * (rb >> 3) + RB5 * (cb & 1)) & 7;
  const char* gX = (const char*)x + (size_t)(t0 + cw * 16) * (D_ * 2) + rot * 128;
  const char* gW = (const char*)w_in + (size_t)(cw * 16 + rot * 64) * (F2_ * 2) + pr * 384;
  const char *gV = gW + odd * 256, *gB = gW + (odd ? 128 : FH_ * 2 + 128);
  const char* gS = (const char*)w_sp + (size_t)t0 * (S_ * 2);
  const unsigned aoff[2] = {(unsigned)(r8 * 4096 + ((c8 ^ r8) << 4)), (unsigned)(r8 * 4096 + ((c8 ^ r8) << 4) + 32768)};
  const unsigned ldw = lb + cw * 2048;
  auto issue = [&](auto nc) {
    constexpr Unit u = unit(decltype(nc)::value);
    constexpr int lo = L_SLOT + u.slot * 8192;
    if (u.kind == UX) dma32r<lo, u.idx * 128, 1280, 10 - u.idx>(voX[0], voX[1], gX, ldw, rot);
    else dma32r<lo, u.idx * (64 * F2_ * 2) + (u.kind == UA ? FH_ * 2 : 0), 640 * F2_ * 2, 10 - u.idx>(voW[0], voW[1], u.kind == UB ? gB : gV, ldw, rot);
    SB();
  };
  const int kd = d0 + min(wave, 4) * 16 + r8 * 2;
  u32x4 sw;
  {
    const int ks0 = t0 + wave * 8;
#pragma unroll
    for (int i = 0; i < 3; ++i) {
      const int id = min(i * 64 + lane, 189), r = id / 10, c = id % 10;
      const char* kp = (r < 11 ? (const char*)kin + (size_t)max(ks0 - 3 + r, 0) * (D_ * 2) : (const char*)vin + (size_t)(ks0 + r - 11) * (D_ * 2)) + cb * 160 + c * 16;
      asm volatile("s_mov_b32 m0, %1\n\ts_nop 0\n\tglobal_load_lds_dwordx4 %0, off" :: "v"(kp), "s"(lb + L_KV + wave * KV_P + i * 1024) : "memory");
    }
    if (CWARM && wave == 5) cwarm();
    SB();
  }
  auto ldsw = [&]() { asm volatile("global_load_dwordx4 %0, %1, %2" : "=&v"(sw) : "v"(kd * 8), "s"(shift_k) : "memory"); SB(); };
  unsigned kk[11], vv[8];
  u32x4 oa, ob;
  auto kvld = [&]() {
    const char* kl = smem + L_KV + c8 * KV_P + min(wave, 4) * 32 + r8 * 4;
#pragma unroll
    for (int i = 0; i < 11; ++i) kk[i] = *(const unsigned*)(kl + i * 160);
#pragma unroll
    for (int i = 0; i < 8; ++i) vv[i] = *(const unsigned*)(kl + (11 + i) * 160);
  };
  auto kvm = [&](int e0, int e1) {
    const f32x2 w0 = {lo16(sw[0]), lo16(sw[2])}, w1 = {hi16(sw[0]), hi16(sw[2])};
    const f32x2 w2 = {lo16(sw[1]), lo16(sw[3])}, w3 = {hi16(sw[1]), hi16(sw[3])};
    f32x2 kf[11], rr[8];
#pragma unroll
    for (int i = 2 * e0; i < 2 * e1 + 3; ++i) kf[i] = unpk((i < 3 && t0 + c8 == 0) ? 0u : kk[i]);
#pragma unroll
    for (int i = 2 * e0; i < 2 * e1; ++i) {
      f32x2 a0 = kf[i] * w3;
      a0 = __builtin_elementwise_fma(kf[i + 1], w2, a0);
      a0 = __builtin_elementwise_fma(kf[i + 2], w1, a0);
      a0 = __builtin_elementwise_fma(kf[i + 3], w0, a0);
      rr[i] = a0 * unpk(vv[i]);
    }
#pragma unroll
    for (int e = e0; e < e1; ++e) { oa[e] = pack2(rr[2 * e][0], rr[2 * e + 1][0]); ob[e] = pack2(rr[2 * e][1], rr[2 * e + 1][1]); }
  };
  auto kvst = [&]() {
    const unsigned ko = (unsigned)((kd * S_ + t0 + c8 * 8) * 2);
    st16(ko, ws + WS_KV, oa);
    st16(ko, ws + WS_KV + S_ * 2, ob);
    SB();
  };
  auto barrier0 = [&](int nv) {
    SB();
    asm volatile("s_waitcnt vmcnt(%0) lgkmcnt(0)\n\ts_barrier" :: "n"(nv) : "memory");
    SB();
    asm volatile("" : "+v"(sw), "+s"(tag));
    tag += 1;
    asm volatile("" : "+s"(tag));
  };
  unsigned pX[2], pW[2];
#pragma unroll
  for (int k = 0; k < 2; ++k) {
    const int q = l15 >> 2, p = lane & 3, sz = (q >> 1) | ((g4 & 1) << 1);
    pX[k] = lb + (wm * 32 + q * 8 + p) * 128 + ((((k ^ ksb) * 4 + g4) ^ (l15 & 7)) << 4);
    pW[k] = lb + ((k ^ ksb) * 32 + g4 * 8 + q) * 128 + (((sz << 1) | (p >> 1)) << 4) + (p & 1) * 8;
  }
  bf16x8 ones;
#pragma unroll
  for (int e = 0; e < 8; ++e) ones[e] = (short)0x3F80;
  f32x4 xm[2], xr[2];
  auto ldxs = [&]() {
#pragma unroll
    for (int i = 0; i < 2; ++i) {
      xm[i] = *(const f32x4*)(smem + L_XM + (wm * 32 + g4 * 8 + i * 4) * 4);
      xr[i] = *(const f32x4*)(smem + L_XR + (wm * 32 + g4 * 8 + i * 4) * 4);
    }
  };
  auto act = [&](const f32x4& a, float c, int i) {
    const f32x2 nc = {-c, -c};
    const f32x2 lo = gelu2(__builtin_elementwise_fma((f32x2){xm[i][0], xm[i][1]}, nc, (f32x2){a[0], a[1]}) * (f32x2){xr[i][0], xr[i][1]});
    const f32x2 hi = gelu2(__builtin_elementwise_fma((f32x2){xm[i][2], xm[i][3]}, nc, (f32x2){a[2], a[3]}) * (f32x2){xr[i][2], xr[i][3]});
    return (f32x4){lo[0], lo[1], hi[0], hi[1]};
  };
  auto pack8 = [&](const f32x4& a, const f32x4& c) { return (u32x4){pack2(a[0], a[1]), pack2(a[2], a[3]), pack2(c[0], c[1]), pack2(c[2], c[3])}; };
  auto z1at = [&](int f0) { return (u32x4*)(smem + L_Z1 + (f0 + l15) * 128 + (((wm * 4 + g4) ^ (l15 & 7)) << 4)); };
  auto z1put = [&](int f0, const u32x4& v) { *z1at(f0) = v; };
  f32x4 ac2[2][2], cs2[2];
  s16x4 VF[10][2][2];
  unsigned aV[2][2];
  auto rdV = [&](auto hc_, auto nc, int r) {
    constexpr int n = decltype(nc)::value, c = 5 * decltype(hc_)::value + (n >> 1), so = (VSLOT[c] - 2) * 8192, po = n < 2 ? -1 : ((VSLOT[n < 2 ? c : c - 1] - 2) * 8192) >> 16;
    const int jj = r >> 1;
    if (!(r & 1)) {
      if ((so >> 16) != po) aV[n & 1][jj] = opq((pW[n & 1] ^ (unsigned)(wl * 64 + jj * 32)) + L_SLOT + 2 * 8192 + (so & ~0xFFFF));
      VF[n][jj][0] = tr_asm<(so & 0xFFFF)>(aV[n & 1][jj]);
    } else VF[n][jj][1] = tr_asm<(so & 0xFFFF) + 512>(aV[n & 1][jj]);
    SB();
  };
  auto mm2 = [&](auto hc_, auto nc, bf16x8 (*X)[2][2], int nw, auto&& nxt) {
    constexpr int n = decltype(nc)::value, j = n >> 1, k = n & 1;
    constexpr bool z = n == 0 && !decltype(hc_)::value;
    const bf16x8 w0 = join8(VF[n][0][0], VF[n][0][1]), w1 = join8(VF[n][1][0], VF[n][1][1]);
    if (k == 0) { MFMAW<n == 0>(nw, cs2[0], ones, w0); MFMAW<n == 0>(-1, cs2[1], ones, w1); }
    MFMAW<z>(k ? nw : -1, ac2[0][0], X[j][k][0], w0); nxt(0);
    MFMAW<z>(-1, ac2[1][0], X[j][k][1], w0); nxt(1);
    MFMAW<z>(-1, ac2[0][1], X[j][k][0], w1); nxt(2);
    MFMAW<z>(-1, ac2[1][1], X[j][k][1], w1); nxt(3);
    if constexpr (n == 9) {
      pin(cs2[0], cs2[1]);
      if (g4 < 2) *(float*)(smem + L_CS2 + (wave * 32 + lane) * 4) = g4 ? cs2[1][0] : cs2[0][0];
    }
    SB();
  };
  const unsigned p2a = lb + L_P2 + cw * 4096 + lin;
  auto sttag = [&](const char* p) {
    asm volatile("s_mov_b64 exec, 1\n\tglobal_store_dwordx2 %0, %1, %2 sc1\n\ts_mov_b64 exec, -1" :: "v"(lin), "v"((u32x2){opq((unsigned)tag), opq((unsigned)(tag >> 32))}), "s"(p) : "memory");
  };
  auto ldtag = [&](u32x2& f, unsigned vo, const char* p) { asm volatile("global_load_dwordx2 %0, %1, %2 sc1" : "=&v"(f) : "v"(vo), "s"(p) : "memory"); };
  auto notag = [&](const u32x2& f) { return __builtin_amdgcn_ballot_w64(f[0] != (unsigned)tag || f[1] != (unsigned)(tag >> 32)) != 0; };
  auto waittag = [&](u32x2& f, unsigned vo, const char* p) {
    while (__builtin_expect(notag(f) && tmo > 0, 0)) {
      --tmo;
      __builtin_amdgcn_s_sleep(4);
      ldtag(f, vo, p);
      wait_vm<0>(f);
    }
  };
  u32x2 ef;

  char* const hf = ws + WS_H;
  bf16x8 Wq[15];
  const char* wg = ws + WS_WO + (size_t)(wave * 120 + cb * 5) * 1024;
  auto ldW = [&]() {
    ldtag(ef, (unsigned)((lane & 3) * 8), ws + WS_LF + (rb * 8 + wave) * 32);
#pragma unroll
    for (int n = 0; n < 15; ++n)
      asm volatile("global_load_dwordx4 %0, %1, %2 offset:%3 sc1" : "=&v"(Wq[n]) : "v"(lin), "s"(wg + (n / 5) * 40960 + 2048), "i"((n % 5) * 1024 - 2048) : "memory");
    SB();
  };
  f32x4 ac3[5];
  auto gemm3 = [&](auto khc) {
    constexpr int kh = decltype(khc)::value;
    unsigned hoff[4];
#pragma unroll
    for (int it = 0; it < 4; ++it) hoff[it] = (unsigned)(((rb * 4 + ((cw + 1 + it) & 3)) * 24 + 3 * wave) * 1024) + lin;
    u32x4 Hq[4][3];
    auto ldh1 = [&](int it, int k) {
      asm volatile("global_load_dwordx4 %0, %1, %2 offset:%3 " HSC : "=&v"(Hq[it][k]) : "v"(hoff[it]), "s"(hf), "i"(k * 1024) : "memory");
    };
    waittag(ef, (unsigned)((lane & 3) * 8), ws + WS_LF + (rb * 8 + wave) * 32);
    AB(18);
    signal1(flg + 48);
    flag_wait_asm(flg + 48, 8u);
    AB(19);
    __builtin_amdgcn_s_setprio(3);
    ldh1(0, 0); ldh1(0, 1); ldh1(0, 2);
    __builtin_amdgcn_s_setprio(2);
    ldh1(1, 0); ldh1(1, 1); ldh1(1, 2);
    __builtin_amdgcn_s_setprio(kh * C3P);
    SB();
    const unsigned px = lb + (kh ? P3_C + cw * 15360 : P3_G + cw * 20480) + lin;
    f32x4 pa[5][5], ae[5];
    auto put = [&](int it, int j) { asm volatile("ds_write_b128 %0, %1 offset:%2" :: "v"(px), "v"((it & 1) ? ac3[j] : ae[j]), "i"((it * 5 + j) * 1024) : "memory"); };
    auto fetch = [&](int n, int g, int q) {
      const int ms = (cw - 1 - q) & 3;
      const unsigned pn = lb + (g ? P3_C + ms * 15360 : P3_G + ms * 20480) + lin;
#pragma unroll
      for (int j = 0; j < 5; ++j) asm volatile("ds_read_b128 %0, %1 offset:%2" : "=&v"(pa[n][j]) : "v"(pn), "i"((q * 5 + j) * 1024) : "memory");
      SB();
    };
    sfor<0, 4>([&](auto itc) {
      constexpr int it = decltype(itc)::value;
      wait_vm<(it < 3 ? 3 : 0)>(Hq[it][0], Hq[it][1], Hq[it][2]);
      if constexpr (it == 0) pin(Wq[0], Wq[1], Wq[2], Wq[3], Wq[4], Wq[5], Wq[6], Wq[7], Wq[8], Wq[9], Wq[10], Wq[11], Wq[12], Wq[13], Wq[14]);
      auto bad = [&]() { return __builtin_amdgcn_ballot_w64(umx(umx(Hq[it][0][3], Hq[it][1][3]), Hq[it][2][3]) == HOLE) != 0; };
      if (__builtin_expect(bad(), 0)) {
        do {
          --tmo;
          asm volatile("s_sleep 2\n\tglobal_load_dwordx4 %0, %3, %4 sc1\n\tglobal_load_dwordx4 %1, %3, %4 offset:1024 sc1\n\t"
                       "global_load_dwordx4 %2, %3, %4 offset:2048 sc1\n\ts_waitcnt vmcnt(0)"
                       : "+v"(Hq[it][0]), "+v"(Hq[it][1]), "+v"(Hq[it][2]) : "v"(hoff[it]), "s"(hf) : "memory");
        } while (bad() && tmo > 0);
      }
      SB();
#pragma unroll
      for (int k = 0; k < 3; ++k)
#pragma unroll
        for (int j = 0; j < 5; ++j) {
          f32x4& ac = (it & 1) ? ac3[j] : ae[j];
          if (k == 0) MFMA0(ac, Wq[j], Hq[it][0]); else MFMA(ac, Wq[k * 5 + j], Hq[it][k]);
          if constexpr (it + 2 < 4) { if (k == 0 && j < 3) ldh1(it + 2 < 4 ? it + 2 : 0, j); }
          const int m = k * 5 + j - WO3;
          if (it > 0 && m >= 0 && m < 10 && !(m & 1)) put(it - 1, m >> 1);
          if (it > 0 && m == 8) signal1(flg + 160 + (it < 3 ? kh : 2) * 16 + ((cw + it) & 3) * 4);
          if (it == 3 && m == FE3) { flag_wait_asm(flg + 160 + wave * 4, 2u); fetch(0, kh, 0); fetch(1, kh, 1); }
          SB();
        }
      if constexpr (it == 3) {
        if (kh) {
          flag_wait_asm(flg + 192 + cw * 4, 3u);
          fetch(2, 1, 2); fetch(3, 0, 2); fetch(4, 0, 3);
        } else asm volatile("s_waitcnt lgkmcnt(0)\n\ts_nop 4" ::: "memory");
        pin(pa[0][0], pa[0][1], pa[0][2], pa[0][3], pa[0][4], pa[1][0], pa[1][1], pa[1][2], pa[1][3], pa[1][4]);
#pragma unroll
        for (int j = 0; j < 5; ++j) {
          pa[0][j] += pa[1][j];
          pin(pa[0][j], ac3[j]);
          ac3[j] += pa[0][j];
          if (!kh) put(3, j);
        }
        if (kh) {
          wait_lg<5>(pa[2][0], pa[2][1], pa[2][2], pa[2][3], pa[2][4], pa[3][0], pa[3][1], pa[3][2], pa[3][3], pa[3][4]);
#pragma unroll
          for (int j = 0; j < 5; ++j) ac3[j] += pa[2][j] + pa[3][j];
          wait_lg<0>(pa[4][0], pa[4][1], pa[4][2], pa[4][3], pa[4][4]);
#pragma unroll
          for (int j = 0; j < 5; ++j) ac3[j] += pa[4][j];
        } else signal1(flg + 192 + cw * 4);
      }
      SB();
      if constexpr (it == 0) AB(20);
      if constexpr (it == 2) AB(21);
    });
    AB(22);
  };
  if (wave >= 4) {
    const int wid = rb * 4 + cw;
    __builtin_amdgcn_s_setprio(3);
    ldsw();
    if (wid < 120) dma16((unsigned)((lane >> 1) * (D_ * 2) + (lane & 1) * 16), w_out + (size_t)(wid / 5) * 32 * D_ + d0 + (wid % 5) * 16, lb + L_WO + cw * 1024);
    SB();
    u32x4 hole = {HOLE, HOLE, HOLE, HOLE};
    bf16x8 XF[5][2][2];
    auto rdXF = [&](auto cc) {
      constexpr int c = decltype(cc)::value, so = L_SLOT + XSLOT[c] * 8192;
#pragma unroll
      for (int k = 0; k < 2; ++k) {
        const unsigned a = pX[k] + (so & ~0x7FFF);
        ds16<(so & 0x7FFF)>(XF[c][k][0], a);
        ds16<(so & 0x7FFF) + 512>(XF[c][k][1], a);
      }
      SB();
    };
    u32x4 TA[16];
    auto ldT = [&](u32x4& r, int d, int w, int i) {
      asm volatile("global_load_dwordx4 %0, %1, %2" : "=&v"(r) : "v"(aoff[i & 1] + (unsigned)(((d + rot2) & 7) * 128)), "s"(gS + ((w + 1 + (i >> 1)) & 3) * 65536 + w * 1024) : "memory");
    };
    auto ldx = [&](int c) {
      u32x4 xv;
      asm volatile("global_load_dwordx4 %0, %1, %2\n\ts_waitcnt vmcnt(0)" : "=&v"(xv) : "v"((unsigned)(lane * (D_ * 2) + c * 16)), "s"(x + (size_t)t0 * D_) : "memory");
      return xv;
    };
    const bool docv = cw < ((rb & 1) ? 2 : 3);
    const int d = d0 + (cw < 2 ? 2 * rb + cw : 64 + (rb >> 1));
    u32x4 hc[4], uc[4];
    unsigned Dw;
    auto ldhc = [&]() {
#pragma unroll
      for (int k = 0; k < 4; ++k) asm volatile("global_load_dwordx4 %0, %1, %2 offset:%3" : "=&v"(hc[k]) : "v"(lin), "s"(ssm_k + (size_t)d * S_), "i"(k * 1024) : "memory");
      asm volatile("global_load_ushort %0, %1, %2" : "=&v"(Dw) : "v"(0u), "s"(ssm_D + d) : "memory");
    };
    unsigned rwo;
    f32x2 ra, rc;
    auto rdraw = [&]() { asm volatile("ds_read_b64 %0, %2\n\tds_read_b64 %1, %2 offset:512" : "=&v"(ra), "=&v"(rc) : "v"(rwo) : "memory"); SB(); };
    const char* efp = ws + WS_EF + cb * 256;
    char* cb_ = smem + M_CV + cw * CV_BUF;
    auto run = [&](auto ic) {
      constexpr int i = decltype(ic)::value, p = cnt('_', i) + 1;
      constexpr char c = SCH[i];
      if constexpr (c == '_') {
        CB(3 * (p - XBAR));
        asm volatile("s_waitcnt vmcnt(%0) lgkmcnt(0)" :: "n"(behind(1, i, '_')) : "memory");
        CB(3 * (p - XBAR) + 1);
        asm volatile("s_barrier" ::: "memory");
        CB(3 * (p - XBAR) + 2);
        SB();
#pragma unroll
        for (int q = 0; q < 5; ++q) if (XNEED[q] + 1 == p) pin(XF[q][0][0], XF[q][0][1], XF[q][1][0], XF[q][1][1]);
        if constexpr (p == 7) {
          rwo = lb + L_RAW + ((lane >> 3) * 4 + (lane & 3)) * 16 + (lane & 4) * 2;
          pin(rwo);
        }
        if constexpr (p == 9) __builtin_amdgcn_s_setprio(CPR);
        if constexpr (p == 2) AB(4);
        if constexpr (p == 6) AB(5);
        if constexpr (p == 9) AB(6);
      }
      if constexpr (c == 'u') issue(IC(cnt('u', i)));
      if constexpr (c == 'x') rdXF(IC(cnt('x', i)));
      if constexpr (c == 'B') {
        AB(1);
        barrier0(behind(1, i, 'B'));
        pin(ones, hole);
      }
      if constexpr (c == 'v') sfor<2 * cnt('v', i), 2 * cnt('v', i) + 2>([&](auto nc) { rdV(IC(0), nc, 0); rdV(IC(0), nc, 1); rdV(IC(0), nc, 2); rdV(IC(0), nc, 3); });
      if constexpr (c == 'm') {
        constexpr int n = cnt('m', i);
        mm2(IC(0), IC(n), XF, VNEED[n >> 1] < p - 1 || (n & 1) ? -1 : 0, [](int) {});
      }
      if constexpr (c == 'p') {
        asm volatile("s_nop 7" : "+v"(ac2[0][0]), "+v"(ac2[0][1]), "+v"(ac2[1][0]), "+v"(ac2[1][1]));
        dsw16<0>(p2a, ac2[0][0]); dsw16<1024>(p2a, ac2[1][0]); dsw16<2048>(p2a, ac2[0][1]); dsw16<3072>(p2a, ac2[1][1]);
      }
      if constexpr (c == 'w')
        touch((unsigned)(min(lane, 19) * (F2_ * 2)), (const char*)w_in + (size_t)(((rb + WSH) & 7) * 80 + cw * 20) * (F2_ * 2) + pr * 384 + odd * 256, lb + L_DUM);
      if constexpr (c == 'l') { if (wave == 7) kvld(); }
      if constexpr (c == 'k') { if (wave == 7) { kvm(0, 4); kvst(); } }
      if constexpr (c == 'E' || c == 'D') { if (wave == 5) sttag(ws + WS_EF + (c == 'D' ? 2048 : 0) + (cb * 32 + rb) * 8); }
      if constexpr (c == 'U') {
        wait_vm<behind(1, i, 'F')>(ef);
        waittag(ef, (unsigned)((lane & 31) * 8), efp);
#pragma unroll
        for (int k = 0; k < 4; ++k) asm volatile("global_load_dwordx4 %0, %1, %2 offset:%3 sc1" : "=&v"(uc[k]) : "v"(lin), "s"(ws + WS_KV + (size_t)d * (S_ * 2)), "i"(k * 1024) : "memory");
      }
      if constexpr (c == 'F') ldtag(ef, (unsigned)((lane & 31) * 8), efp);
      if constexpr (c == 'o') {
        if (wid < 120) {
          const unsigned ra = lb + L_WO + cw * 1024 + (8 * g4 + (l15 >> 2)) * 32 + 8 * (lane & 3);
          s16x4 lo = tr_asm<0>(ra), hi = tr_asm<128>(ra);
          wait_lg<0>(lo, hi);
          st16(lin, ws + WS_WO + (size_t)((wid / 5) * 40 + cb * 5 + wid % 5) * 1024, __builtin_bit_cast(u32x4, join8(lo, hi)));
        }
      }
      if constexpr (c == 'h') {
        constexpr int n = cnt('h', i);
        st16(lin, ws + (n < 3 ? WS_Z2 + (size_t)((rb * 2 + wl) * 48 + cb * 6 + wm * 3 + n) * 1024 : WS_H + (size_t)((rb * 4 + cw) * 24 + cb * 3 + n - 3) * 1024), hole);
      }
      if constexpr (c == 't') { constexpr int n = cnt('t', i); ldT(TA[n], 3 + (n >> 3), cw, n & 7); }
      if constexpr (c == 'H') ldhc();
      if constexpr (c == 'R') rdraw();
      if constexpr (c == 'V') {
        wait_vm<behind(1, i, '_')>();
        signal1(flg + 60);
        AB(2);
      }
      if constexpr (c == 'I') {
        wait_vm<behind(1, i, 'H')>(hc[0], hc[1], hc[2], hc[3], Dw);
        if (docv) {
          if (lane < 36) *(u32x4*)(cb_ + (lane < 32 ? lane * 16 : CV_C + ((lane >> 1) & 1) * CV_CS + 4096 + (lane & 1) * 16)) = (u32x4){0u, 0u, 0u, 0u};
#pragma unroll
          for (int k = 0; k < 4; ++k) {
            const int a = k * 64 + lane;
            const int h0 = k ? __builtin_amdgcn_readlane((int)hc[k ? k - 1 : 0][3], 63) : 0;
            const unsigned hp = (unsigned)__builtin_amdgcn_update_dpp(h0, (int)hc[k][3], 0x138, 0xF, 0xF, false);
            const unsigned w[5] = {hp, hc[k][0], hc[k][1], hc[k][2], hc[k][3]};
            u32x4 sw, mx;
#pragma unroll
            for (int q = 0; q < 4; ++q) {
              sw[q] = (w[4 - q] >> 16) | (w[4 - q] << 16);
              mx[q] = (w[4 - q] & 0xFFFFu) | (w[3 - q] & 0xFFFF0000u);
            }
            char* cp = cb_ + CV_C + (2040 - 8 * a) * 2;
            *(u32x4*)(cp) = sw;
            *(u32x4*)(cp + CV_CS) = mx;
          }
        }
      }
      if constexpr (c == 'J') {
        wait_vm<behind(1, i, 'U')>(uc[0], uc[1], uc[2], uc[3]);
        if (docv) {
#pragma unroll
          for (int k = 0; k < 4; ++k) *(u32x4*)(cb_ + 512 + (k * 64 + lane) * 16) = uc[k];
        }
      }
      if constexpr (c == 'S') {
        wait_lg<0>(ra, rc);
        if (wave == 6) {
          const float s1 = nv(ra[0]) + rc[0], fm = s1 * (1.0f / 640.0f), sm = s1 * fm;
          float fq = (ra[1] - sm) + rc[1];
          if (__builtin_expect(__builtin_amdgcn_ballot_w64(!(fq >= 1e-3f * sm)) != 0, 0)) {
            const f32x2 mm = {fm, fm};
            f32x2 q2 = {0.f, 0.f};
#pragma unroll 1
            for (int c = 0; c < 80; ++c) {
              const u32x4 xv = ldx(c);
#pragma unroll
              for (int e = 0; e < 4; ++e) { const f32x2 d = unpk(xv[e]) - mm; q2 = __builtin_elementwise_fma(d, d, q2); }
            }
            fq = q2[0] + q2[1];
          }
          const float vbf = rbf(fmaxf(fq, 0.f) * (1.0f / 640.0f));
          *(float*)(smem + L_XM + lane * 4) = rbf(fm);
          *(float*)(smem + L_XR + lane * 4) = __builtin_amdgcn_rcpf(rbf(__builtin_amdgcn_sqrtf(rbf(vbf + 1e-5f))));
        }
      }
      SB();
    };
    sfor<0, NSCH>(run);
    __builtin_amdgcn_s_setprio(0);
    AB(9);
    auto ldS = [&](auto dc, auto wc) {
      constexpr int dd = decltype(dc)::value, w = decltype(wc)::value;
      const unsigned to = (unsigned)(((dd + rot2) & 7) * 128);
      if (THR < 60) wait_vm<THR>();
      dma32s<M_RING + w * 24576 + (2 - dd) * 8192, ((w + 1) & 3) * 65536 + w * 1024>(aoff[0] + to, aoff[1] - 1024 + to, gS, lb);
      if (THR < 60) wait_vm<THR>();
      dma32s<M_RING + w * 24576 + (2 - dd) * 8192 + 2048, ((w + 2) & 3) * 65536 + w * 1024>(aoff[0] + to, aoff[1] - 1024 + to, gS, lb);
      if (THR < 60) wait_vm<THR>();
      dma32s<M_RING + w * 24576 + (2 - dd) * 8192 + 4096, ((w + 3) & 3) * 65536 + w * 1024>(aoff[0] + to, aoff[1] - 1024 + to, gS, lb);
      if (THR < 60) wait_vm<THR>();
      dma32s<M_RING + w * 24576 + (2 - dd) * 8192 + 6144, w * 65536 + w * 1024>(aoff[0] + to, aoff[1] - 1024 + to, gS, lb);
      SB();
    };
    auto epi2 = [&]() {
      flag_wait_asm(flg + 132, 4u);
#pragma unroll
      for (int jj = 0; jj < 2; ++jj) {
        u32x4* zp = z1at(odd * 32 + wl * 32 + jj * 16);
        u32x4 o = *zp;
#pragma unroll
        for (int q = 0; q < 4; ++q) o[q] = pack2s(gelu1(nv(lo16(o[q]))), gelu1(hi16(o[q])));
        *zp = o;
      }
      signal1(flg + 56);
      SB();
    };
    auto putT = [&](const u32x4* T, unsigned a, unsigned f) {
#pragma unroll
      for (int i = 0; i < 8; ++i) asm volatile("ds_write_b128 %0, %1 offset:%2" :: "v"(a), "v"(T[i]), "i"(i * 1024) : "memory");
      signal1(f);
    };
    const unsigned tx = lb + M_CV + cw * CV_BUF + lin;
    auto landT = [&]() {
      sttag(ws + WS_LF + (rb * 8 + cb) * 32 + cw * 8);
#pragma unroll
      for (int n = 0; n < 16; ++n) pin(TA[n]);
      putT(TA, tx, flg + 112 + cw * 4);
    };
    auto putT4 = [&]() {
      flag_wait_asm(flg + 96 + cw * 4, 1u);
      putT(TA + 8, lb + M_RING + cw * 24576 + 16384 + lin, flg + 112 + cw * 4);
    };
    if (docv) {
      flag_wait_asm(flg + 36, 4u);
      AB(12);
      f32x4 ac[8];
      conv_core(lds_addr(cb_), lane, Dw, ac);
      AB(13);
      signal1(flg + 40);
      wait_vm<0>();
      landT();
#pragma unroll
      for (int k = 0; k < 4; ++k) {
        u32x2 ov[2];
#pragma unroll
        for (int t = 0; t < 2; ++t)
#pragma unroll
          for (int h = 0; h < 2; ++h) ov[t][h] = pack2(ac[2 * k + t][2 * h], ac[2 * k + t][2 * h + 1]);
        const auto s0 = __builtin_amdgcn_permlane16_swap(ov[0][0], ov[1][0], false, false);
        const auto s1 = __builtin_amdgcn_permlane16_swap(ov[0][1], ov[1][1], false, false);
        st16((unsigned)((256 * (g4 & 1) + 16 * l15 + 8 * (g4 >> 1)) * 2), ws + WS_Y + (size_t)d * (S_ * 2) + k * 1024, (u32x4){s0[0], s1[0], s0[1], s1[1]});
      }
      wait_vm<0>();
      sttag(ws + WS_YF + d * 8);
      putT4();
      epi2();
    } else if (cw != 3) {
      wait_vm<0>();
      landT();
      putT4();
      epi2();
    } else {
      ldS(IC(0), IC(0)); ldS(IC(0), IC(1)); ldS(IC(0), IC(2)); ldS(IC(1), IC(1)); ldS(IC(2), IC(1));
      wait_vm<40>();
      landT();
      AB(12);
      flag_wait_asm(flg + 16, 4u);
      AB(8);
      wait_vm<24>();
      ldS(IC(0), IC(3)); ldS(IC(1), IC(0)); ldS(IC(1), IC(2)); ldS(IC(1), IC(3));
      wait_vm<24>();
      signal1(flg + 32);
      ldS(IC(2), IC(0)); ldS(IC(2), IC(2)); ldS(IC(2), IC(3));
      AB(10);
      u32x4 TB[32];
#pragma unroll
      for (int n = 0; n < 32; ++n) {
        if (n == 8) wait_vm<32>();
        if (THR < 60 && !(n & 1)) wait_vm<THR>();
        if (THR < 32 && n == THR) signal1(flg + 32);
        ldT(TB[n], 5, n >> 3, n & 7);
      }
      wait_vm<32>();
      if (THR >= 32) signal1(flg + 32);
      AB(11);
      wait_vm<0>();
#pragma unroll
      for (int n = 0; n < 32; ++n) pin(TB[n]);
      putT4();
#pragma unroll
      for (int w = 0; w < 4; ++w) {
        flag_wait_asm(flg + 96 + w * 4, 2u);
        putT(TB + 8 * w, lb + M_RING + w * 24576 + 8192 + lin, flg + 144 + w * 4);
      }
      epi2();
      if (!odd) {
        u32x4 zx[4];
        ldtag(ef, (unsigned)((lane & 1) * 8), ws + WS_XF + (rb * 4 + pr) * 16);
        wait_vm<0>(ef);
        waittag(ef, (unsigned)((lane & 1) * 8), ws + WS_XF + (rb * 4 + pr) * 16);
#pragma unroll
        for (int c = 0; c < 4; ++c)
          asm volatile("global_load_dwordx4 %0, %1, %2 offset:%3 sc1" : "=&v"(zx[c]) : "v"(lin), "s"(ws + WS_ZX + (size_t)(rb * 4 + pr) * 4096), "i"(c * 1024) : "memory");
        wait_vm<0>(zx[0], zx[1], zx[2], zx[3]);
#pragma unroll
        for (int c = 0; c < 4; ++c) *(u32x4*)(smem + L_Z1 + (64 + (c & 1) * 16 + l15) * 128 + ((((c >> 1) * 4 + g4) ^ (l15 & 7)) << 4)) = zx[c];
      }
      signal1(flg + 56);
      AB(13);
    }
    SB();
    wait_vm<0>();
    AB(14);
    u32x2 xv[5];
    f32x2 sv[5][2];
    flag_wait_asm(flg + 40, (rb & 1) ? 2u : 3u);
    const int yc = min(cw * 24, 56);
    const unsigned yfo = (unsigned)(min(lane, 23) * 8);
    const char* yfp = ws + WS_YF + (d0 + yc) * 8;
    auto ldy = [&]() {
#pragma unroll
      for (int n = 0; n < 3; ++n) {
        const int q = (yc >> 3) + n;
        asm volatile("s_mov_b32 m0, %2\n\ts_nop 0\n\tglobal_load_lds_dwordx4 %0, %1 sc1" :: "v"((unsigned)(r8 * (S_ * 2) + ((c8 ^ (r8 & 6)) << 4))),
                     "s"(ws + WS_Y + ((size_t)(d0 + q * 8) * S_ + t0) * 2), "s"(lb + yat(0) + (q >> 2) * CV_BUF + (q & 3) * 1024) : "memory");
      }
    };
    flag_wait_asm(flg + 96 + cw * 4, 3u);
    ldtag(ef, yfo, yfp);
    wait_vm<0>(ef);
    flag_wait_asm(flg + 44, 4u);
    waittag(ef, yfo, yfp);
    AB(15);
    ldy();
#pragma unroll
    for (int j = 0; j < 5; ++j)
      asm volatile("global_load_dwordx2 %0, %1, %2 offset:%3" : "=&v"(xv[j]) : "v"((unsigned)(l15 * D_ * 2 + 8 * g4)), "s"(x + (size_t)(t0 + cw * 16) * D_ + d0), "i"(j * 32) : "memory");
    ldW();
    wait_vm<16>(xv[0], xv[1], xv[2], xv[3], xv[4]);
    AB(17);
    signal1(flg + 52);
    flag_wait_asm(flg + 52, 4u);
    {
      const int yr_ = 4 * g4 + (l15 >> 2);
      const unsigned ya = lb + yr_ * 128 + (((cw * 2 + ((lane >> 1) & 1)) ^ (yr_ & 6)) << 4) + (lane & 1) * 8;
      const float nm = -*(const float*)(smem + L_XM + (cw * 16 + l15) * 4), sr = *(const float*)(smem + L_XR + (cw * 16 + l15) * 4);
      s16x4 yr[5] = {tr_asm<yat(0)>(ya), tr_asm<yat(1)>(ya), tr_asm<yat(2)>(ya), tr_asm<yat(3)>(ya), tr_asm<yat(4)>(ya)};
      wait_lg<0>(yr[0], yr[1], yr[2], yr[3], yr[4]);
      AB(16);
      flag_wait_asm(flg + 96 + cw * 4, 4u);
#pragma unroll
      for (int j = 0; j < 5; ++j) {
        const u32x2 yw = __builtin_bit_cast(u32x2, yr[j]);
#pragma unroll
        for (int h = 0; h < 2; ++h) {
          sv[j][h] = ((unpk(xv[j][h]) + (f32x2){nm, nm}) * (f32x2){sr, sr}) * unpk(yw[h]);
          pin(sv[j][h]);
        }
      }
      SB();
    }
    wait_vm<15>(ef);
    gemm3(IC(1));
    unsigned pk[5][2];
#pragma unroll
    for (int j = 0; j < 5; ++j)
#pragma unroll
      for (int h = 0; h < 2; ++h) {
        const f32x2 o2 = {ac3[j][2 * h], ac3[j][2 * h + 1]};
        const f32x2 a = o2 * (f32x2){-1.4426950f, -1.4426950f};
        const f32x2 e = (f32x2){__builtin_amdgcn_exp2f(a[0]), __builtin_amdgcn_exp2f(a[1])} + (f32x2){1.0f, 1.0f};
        const f32x2 r = {__builtin_amdgcn_rcpf(e[0]), __builtin_amdgcn_rcpf(e[1])};
        pk[j][h] = pk2(__builtin_elementwise_fma(o2 * r, sv[j][h], unpk(xv[j][h])));
      }
    const u16* op = out + (size_t)(t0 + cw * 16) * D_ + d0;
#pragma unroll
    for (int q = 0; q < 2; ++q) {
      const auto s0 = __builtin_amdgcn_permlane16_swap(pk[2 * q][0], pk[2 * q + 1][0], false, false);
      const auto s1 = __builtin_amdgcn_permlane16_swap(pk[2 * q][1], pk[2 * q + 1][1], false, false);
      asm volatile("global_store_dwordx4 %0, %1, %2 offset:%3 " OSC "\n\ts_nop 1" :: "v"((unsigned)(l15 * D_ * 2 + 32 * (g4 & 1) + 16 * (g4 >> 1))), "v"((u32x4){s0[0], s1[0], s0[1], s1[1]}),
                   "s"(op), "i"(64 * q) : "memory");
    }
    asm volatile("global_store_dwordx2 %0, %1, %2 offset:128 " OSC :: "v"((unsigned)(l15 * D_ * 2 + 8 * g4)), "v"((u32x2){pk[4][0], pk[4][1]}), "s"(op) : "memory");
  } else {
    ldsw();
    sfor<0, pos('B')>([&](auto ic) { if constexpr (SCH[ic] == 'g') issue(IC(cnt('u', ic))); });
    if (tid < 64) *(unsigned*)(smem + L_FLAG + tid * 4) = 0u;
    pin(ones);
    AB(1);
    barrier0(behind(0, pos('B'), 'B'));
    kvld();
    kvm(0, 4);
    kvst();
    const bool isz2 = !(odd && wl);
    const char* stp = ws + WS_ST + (size_t)((rb * 12 + pr * 3 + odd * 2 + wl) * 1024 + wm * 512);
    if (STW && isz2) st8(lin >> 1, stp, (u32x2){0u, ~(unsigned)tag});
    const char* Ab[4];
#pragma unroll
    for (int i = 0; i < 4; ++i) Ab[i] = (const char*)w_sp + (size_t)(t0 + ((wave + 1 + i) & 3) * 16) * (S_ * 2) + wave * 1024;
    const unsigned ringl = lb + M_RING + wave * 24576;
    auto ldA1 = [&](int d, int i) {
      dma16o(aoff[i & 1] + (unsigned)(((d + rot2) & 7) * 128), Ab[i >> 1], 0, d == 7 ? lb + M_CV + wave * CV_BUF : ringl, i * 1024);
    };
    unsigned bsv;
    {
      f32x4 zv[2][4], cs[4];
      s16x4 WB[20][4][2];
      bf16x8 XG[10][2][2];
      unsigned aX[2][2], aW[2][2][4];
#pragma unroll
      for (int k = 0; k < 2; ++k)
#pragma unroll
        for (int w = 0; w < 2; ++w) {
          aX[w][k] = pX[k] + L_SLOT + XW * 8192 + w * 65536;
          pin(aX[w][k]);
#pragma unroll
          for (int jj = 0; jj < 4; ++jj) { aW[w][k][jj] = (pW[k] ^ (unsigned)(jj * 32)) + L_SLOT + wl * 8192 + w * 65536; pin(aW[w][k][jj]); }
        }
      auto rd1 = [&](auto nc, int r) {
        constexpr int n = decltype(nc)::value, j = n >> 1, k = n & 1, c = n > 0 && n < 11 ? n - 1 : 0, sx = (XSLOT[c] - XW) * 8192, sa = WSLOT[j] * 8192;
        if (r < 0) { if (r & 1) ds16<(sx & 0xFFFF) + 512>(XG[c][(r + 4) >> 1][1], aX[sx >> 16][(r + 4) >> 1]); else ds16<(sx & 0xFFFF)>(XG[c][(r + 4) >> 1][0], aX[sx >> 16][(r + 4) >> 1]); }
        else if (r & 1) WB[n][r >> 1][1] = tr_asm<(sa & 0xFFFF) + 512>(aW[sa >> 16][k][r >> 1]);
        else WB[n][r >> 1][0] = tr_asm<(sa & 0xFFFF)>(aW[sa >> 16][k][r >> 1]);
        SB();
      };
      auto bar = [&](auto bc) {
        SB();
        constexpr int p = decltype(bc)::value;
        CB(3 * (p - XBAR));
        if constexpr (p == 1) asm volatile("s_waitcnt vmcnt(%0) lgkmcnt(0)\n\ts_barrier" :: "n"(behind(0, pos('_', p - 1), '_')) : "memory");
        else if constexpr (p <= GZ) asm volatile("s_waitcnt vmcnt(%0) lgkmcnt(8)\n\ts_barrier" :: "n"(behind(0, pos('_', p - 1), '_')) : "memory");
        else if constexpr (p >= 8) asm volatile("s_waitcnt lgkmcnt(0)\n\ts_barrier" ::: "memory");
        else if constexpr (p < 7) asm volatile("s_waitcnt lgkmcnt(8)\n\ts_barrier" ::: "memory");
        else asm volatile("s_barrier" ::: "memory");
        CB(3 * (p - XBAR) + 2);
        SB();
      };
      float cp[4];
      f32x4 st[2][2], rv;
      const unsigned rwa = lb + L_RAW + (ksb * 2 + wm) * 256 + l15 * 16 + ((l15 >> 2) == g4 ? 0 : 1024);
      const unsigned csa = lb + L_CS + (wave * 64 + lane) * 4;
      auto kstep = [&](auto nc) {
        constexpr int n = decltype(nc)::value, j = n >> 1, k = n & 1, nn = n + 1, r0 = nn >= 2 && nn <= 10 ? -4 : 0;
        constexpr int sc = n == 1 ? 0 : n - 2, ns = !k ? 0 : (n == 1 || n == 11 ? 1 : 2);
        if constexpr (k) bar(IC(nn / 2 + 1));
        int r = r0, c = 0;
        auto nxt = [&]() { if (r < 8) { rd1(IC(nn), r++); ++c; } };
        auto sel = [&](int q) {
          const f32x4& sq = st[q & 1][q >> 1];
          rv[q] = sel4(sq[0], sq[1], sq[2], sq[3], 0xAAAAAAAAAAAAAAAAull, 0xCCCCCCCCCCCCCCCCull);
          SB();
        };
#pragma unroll
        for (int jj = 0; jj < 4; ++jj) {
          const bf16x8 wf = join8(WB[n][jj][0], WB[n][jj][1]);
          MFMAW<n == 0>(jj == 0 ? 4 : (jj == 2 ? c : -1), zv[0][jj], XG[j][k][0], wf);
          SB();
          nxt();
          if (n == 12) nxt();
          if (n == 12 && jj == 2) sel(2);
          MFMAW<n == 0>(-1, zv[1][jj], XG[j][k][1], wf);
          SB();
          nxt();
          if (n == 12) nxt();
          if (n == 12 && (jj == 1 || jj == 2)) sel(jj == 1 ? 0 : 3);
          if (n == 12 && jj == 2) { dsw16<0>(rwa, rv); SB(); }
          if (k == 0) {
            MFMAW<n == 0>(-1, cs[jj], ones, wf);
            SB();
            nxt();
            if (n == 12) nxt();
            if (n == 12 && jj == 1) sel(1);
          }
#pragma unroll
          for (int q = 0; q < ns; ++q) {
            const bf16x8& xs = XG[sc + q][1][jj >> 1];
            MFMAW<n == 1>(-1, st[jj & 1][jj >> 1], xs, (jj & 1) ? xs : ones);
            SB();
            nxt();
          }
        }
      };
      auto lstep = [&](auto nc, auto ac, auto bc, int w) {
        constexpr int n = decltype(nc)::value, j = n >> 1, k = n & 1, na = decltype(ac)::value, nb = decltype(bc)::value;
        int r = 0;
        auto nxt = [&]() { if (na > 0 && r < 16) { if (r < 8) rd1(ac, r); else rd1(bc, r - 8); ++r; } };
#pragma unroll
        for (int jj = 0; jj < 4; ++jj) {
          const bf16x8 wf = join8(WB[n][jj][0], WB[n][jj][1]);
          if (k == 0) { MFMAW<false>(jj == 0 ? w : -1, cs[jj], ones, wf); SB(); nxt(); nxt(); }
          MFMAW<false>(jj == 0 && k ? w : -1, zv[0][jj], XG[j][k][0], wf);
          SB();
          nxt();
          if (k) nxt();
          MFMAW<false>(-1, zv[1][jj], XG[j][k][1], wf);
          SB();
          nxt();
          if (k) nxt();
          if (n == 15 && jj == 2) {
            dsw4(csa, sel4(cs[0][0], cs[1][0], cs[2][0], cs[3][0], 0xFFFF0000FFFF0000ull, 0xFFFFFFFF00000000ull));
            SB();
          }
        }
      };
      bar(IC(1));
      AB(2);
#pragma unroll
      for (int r = -4; r < 8; ++r) rd1(IC(0), r);
      kstep(IC(0)); AB(4); kstep(IC(1)); kstep(IC(2)); kstep(IC(3)); kstep(IC(4)); kstep(IC(5)); kstep(IC(6)); kstep(IC(7)); kstep(IC(8)); AB(5); kstep(IC(9));
      kstep(IC(10)); kstep(IC(11)); kstep(IC(12));
      bar(IC(8));
      lstep(IC(13), IC(14), IC(16), -1);
      lstep(IC(14), IC(18), IC(15), 8);
      lstep(IC(16), IC(17), IC(19), 15);
      lstep(IC(18), IC(0), IC(0), 15);
      lstep(IC(15), IC(0), IC(0), -1);
      lstep(IC(17), IC(0), IC(0), 9);
      bar(IC(9));
      __builtin_amdgcn_s_setprio(2);
#pragma unroll
      for (int q = 0; q < 4; ++q) cp[q] = *(const float*)(smem + L_CS + ((wave ^ 1) * 64 + q * 16 + l15) * 4);
      ldxs();
      SB();
      lstep(IC(19), IC(0), IC(0), -1);
      AB(6);
      float ct[4];
#pragma unroll
      for (int jj = 0; jj < 4; ++jj) ct[jj] = cs[jj][0] + cp[jj];
#pragma unroll
      for (int jj = 0; jj < 4; ++jj) {
        if (jj == 0) asm volatile("s_nop 3");
        if (jj == 2) SB();
        if (!(jj & 1)) pin(zv[0][jj], zv[1][jj], zv[0][jj + 1], zv[1][jj + 1]);
#pragma unroll
        for (int i = 0; i < 2; ++i) zv[i][jj] = act(zv[i][jj], ct[jj], i);
      }
      if (isz2) {
        f32x4 pv[2], s1[2], s2[2];
#pragma unroll
        for (int i = 0; i < 2; ++i) {
#pragma unroll
          for (int e = 0; e < 4; ++e) {
            const float z0 = zv[i][0][e];
            pv[i][e] = __int_as_float(__builtin_amdgcn_update_dpp(0, __float_as_int(z0), 0x150, 0xF, 0xF, true));
          }
          const f32x4 a0 = zv[i][0] - pv[i], a1 = zv[i][1] - pv[i], a2 = zv[i][2] - pv[i], a3 = zv[i][3] - pv[i];
          s1[i] = (a0 + a1) + (a2 + a3);
          s2[i] = __builtin_elementwise_fma(a3, a3, __builtin_elementwise_fma(a2, a2, __builtin_elementwise_fma(a1, a1, a0 * a0)));
        }
        float r[8], q[4];
#define DA(d, s, ctl, bm) "v_add_f32_dpp " d ", " s ", " s " " ctl " row_mask:0xf bank_mask:" bm "\n\t"
        asm volatile("s_nop 1\n\t"
                     DA("%0", "%12", "row_ror:8", "0x3") DA("%1", "%13", "row_ror:8", "0x3") DA("%2", "%14", "row_ror:8", "0x3") DA("%3", "%15", "row_ror:8", "0x3")
                     DA("%4", "%16", "row_ror:8", "0x3") DA("%5", "%17", "row_ror:8", "0x3") DA("%6", "%18", "row_ror:8", "0x3") DA("%7", "%19", "row_ror:8", "0x3")
                     DA("%0", "%20", "row_ror:8", "0xc") DA("%1", "%21", "row_ror:8", "0xc") DA("%2", "%22", "row_ror:8", "0xc") DA("%3", "%23", "row_ror:8", "0xc")
                     DA("%4", "%24", "row_ror:8", "0xc") DA("%5", "%25", "row_ror:8", "0xc") DA("%6", "%26", "row_ror:8", "0xc") DA("%7", "%27", "row_ror:8", "0xc")
                     DA("%8", "%0", "row_half_mirror", "0x5") DA("%9", "%1", "row_half_mirror", "0x5") DA("%10", "%2", "row_half_mirror", "0x5") DA("%11", "%3", "row_half_mirror", "0x5")
                     DA("%8", "%4", "row_half_mirror", "0xa") DA("%9", "%5", "row_half_mirror", "0xa") DA("%10", "%6", "row_half_mirror", "0xa") DA("%11", "%7", "row_half_mirror", "0xa")
                     DA("%8", "%8", "quad_perm:[1,0,3,2]", "0xf") DA("%9", "%9", "quad_perm:[1,0,3,2]", "0xf") DA("%10", "%10", "quad_perm:[1,0,3,2]", "0xf") DA("%11", "%11", "quad_perm:[1,0,3,2]", "0xf")
                     DA("%8", "%8", "quad_perm:[2,3,0,1]", "0xf") DA("%9", "%9", "quad_perm:[2,3,0,1]", "0xf") DA("%10", "%10", "quad_perm:[2,3,0,1]", "0xf") DA("%11", "%11", "quad_perm:[2,3,0,1]", "0xf")
                     : "=&v"(r[0]), "=&v"(r[1]), "=&v"(r[2]), "=&v"(r[3]), "=&v"(r[4]), "=&v"(r[5]), "=&v"(r[6]), "=&v"(r[7]),
                       "=&v"(q[0]), "=&v"(q[1]), "=&v"(q[2]), "=&v"(q[3])
                     : "v"(s1[0][0]), "v"(s2[0][0]), "v"(s1[0][1]), "v"(s2[0][1]), "v"(s1[0][2]), "v"(s2[0][2]), "v"(s1[0][3]), "v"(s2[0][3]),
                       "v"(s1[1][0]), "v"(s2[1][0]), "v"(s1[1][1]), "v"(s2[1][1]), "v"(s1[1][2]), "v"(s2[1][2]), "v"(s1[1][3]), "v"(s2[1][3]));
#undef DA
        const f32x4 p4 = (lane & 8) ? pv[1] : pv[0];
        const float pa = (lane & 4) ? p4[2] : p4[0], pb = (lane & 4) ? p4[3] : p4[1];
        const float pp = (lane & 2) ? pb : pa, S1 = (lane & 2) ? q[2] : q[0], S2 = (lane & 2) ? q[3] : q[1];
        const float dl = S1 * (1.0f / 64.0f);
        st8(lin >> 1, stp, (u32x2){__float_as_uint((lane & 1) ? fmaxf(S2 - S1 * dl, 0.f) : pp + dl), (unsigned)tag});
      } else {
#pragma unroll
        for (int jj = 0; jj < 4; ++jj) {
          u32x4 o = pack8(zv[0][jj], zv[1][jj]);
          if (jj >= 2) z1put(jj * 16 - 32, o);
          else st16(lin, ws + WS_ZX + (size_t)((rb * 4 + pr) * 4 + wm * 2 + jj) * 1024, o);
        }
      }
      signal1(flg + 36);
      SB();
      AB(7);
      __builtin_amdgcn_s_setprio(P2P);
      unsigned po = (unsigned)((lane >> 5) * 6144 + (lane & 31) * 16);
      const char* pb = ws + WS_ST + (size_t)rb * 12288 + wm * 512 + 2048;
      u32x4 sg[6];
      auto ldsg = [&]() {
#pragma unroll
        for (int c = 0; c < 6; ++c) asm volatile("global_load_dwordx4 %0, %1, %2 offset:%3 sc1" : "=&v"(sg[c]) : "v"(po), "s"(pb), "i"(c * 1024 - 2048) : "memory");
      };
      auto look = [&]() {
        wait_vm<0>();
        ldsg();
        asm volatile("global_load_ushort %0, %1, %2" : "=&v"(bsv) : "v"((unsigned)(l15 * 2)), "s"(b_sp + t0 + wave * 16) : "memory");
        if (!isz2) sttag(ws + WS_XF + (rb * 4 + pr) * 16 + wm * 8);
        SB();
      };
      flag_wait_asm(flg + 60, 4u);
      ds16<0>(ac2[0][0], p2a); ds16<1024>(ac2[1][0], p2a); ds16<2048>(ac2[0][1], p2a); ds16<3072>(ac2[1][1], p2a);
      sfor<0, 4>([&](auto nc) { rdV(IC(1), nc, 0); rdV(IC(1), nc, 1); rdV(IC(1), nc, 2); rdV(IC(1), nc, 3); });
      sfor<0, 10>([&](auto nc) {
        constexpr int n = decltype(nc)::value;
        mm2(IC(1), nc, XG + 5, n == 0 ? 12 : (n < 6 ? 15 : 4 * (9 - n)), [&](int r) {
          SB();
          if constexpr (n < 3) { if (r < 2) { rdV(IC(1), IC(4 + 2 * n), 2 * r); rdV(IC(1), IC(4 + 2 * n), 2 * r + 1); } else { rdV(IC(1), IC(5 + 2 * n), 2 * r - 4); rdV(IC(1), IC(5 + 2 * n), 2 * r - 3); } }
        });
        if constexpr (n == 2) signal1(flg + 16);
        if constexpr (n == PN) look();
      });
      signal1(flg + 20 + wl * 4);
      AB(3);
      if (PN > 9) look();
      AB(8);
      wait_vm<1>(sg[0], sg[1], sg[2], sg[3], sg[4], sg[5]);
      if (isz2) {
        const unsigned zw = lb + L_ZS + wave * 256 + lane * 4, zr_ = lb + L_ZS + wave * 256 + g4 * 32;
        const char* zb = ws + WS_Z2 + (size_t)((rb * 2 + wm) * 48 + pr * 12 + odd * 8 + wl * 4) * 1024;
        auto bad = [&]() {
          unsigned an = sg[0][1] & sg[0][3], on = sg[0][1] | sg[0][3];
#pragma unroll
          for (int c = 1; c < 6; ++c) { an &= sg[c][1] & sg[c][3]; on |= sg[c][1] | sg[c][3]; }
          return __builtin_amdgcn_ballot_w64(((an ^ (unsigned)tag) | (on ^ (unsigned)tag)) != 0) != 0;
        };
        if (__builtin_expect(bad(), 0)) {
          do {
            --tmo;
            reld6(sg, po, pb);
            wait_vm<0>(sg[0], sg[1], sg[2], sg[3], sg[4], sg[5]);
          } while (bad() && tmo > 0);
        }
        AB(9);
        auto f = [&](int c, int e) { return __uint_as_float(sg[c][e]); };
        auto sum6 = [&](int e) { return add1(add1(add1(f(0, e), f(1, e)), add1(f(2, e), f(3, e))), add1(f(4, e), f(5, e))); };
        const float nmh = sum6(0) * (-1.0f / 6.0f), mh = -nmh;
        float qa = sum6(2) * (1.0f / 64.0f), qb;
#pragma unroll
        for (int c = 0; c < 6; c += 2) { const float u = add1(f(c, 0), nmh), v = add1(f(c + 1, 0), nmh); qa = fma1(u, u, qa); qb = c ? fma1(v, v, qb) : v * v; }
        const float Mh = add1(qa, qb);
        const auto sm = __builtin_amdgcn_permlane32_swap(__float_as_uint(mh), __float_as_uint(mh), false, false);
        const auto sM = __builtin_amdgcn_permlane32_swap(__float_as_uint(Mh), __float_as_uint(Mh), false, false);
        const float dm = __uint_as_float(sm[0]) - __uint_as_float(sm[1]), mean = rbf(add1(__uint_as_float(sm[0]), __uint_as_float(sm[1])) * 0.5f);
        const float vbf = rbf(__builtin_fmaf(dm * dm, 3.0f, add1(__uint_as_float(sM[0]), __uint_as_float(sM[1]))) * (1.0f / 12.0f));
        const float rz = __builtin_amdgcn_rcpf(rbf(__builtin_amdgcn_sqrtf(rbf(vbf + 1e-5f))));
        dsw4(zw, lane < 32 ? -mean * rz : rz);
        f32x4 zm[2], zr[2];
        ds16<0>(zm[0], zr_); ds16<128>(zr[0], zr_); ds16<16>(zm[1], zr_); ds16<144>(zr[1], zr_);
        wait_lg<0>(zm[0], zm[1], zr[0], zr[1]);
#pragma unroll
        for (int jj = 0; jj < 4; ++jj) {
          u32x4 o;
#pragma unroll
          for (int q = 0; q < 4; ++q) {
            const int i = q >> 1, e = (q & 1) * 2;
            o[q] = pack2(fma1(zv[i][jj][e], zr[i][e], zm[i][e]), fma1(zv[i][jj][e + 1], zr[i][e + 1], zm[i][e + 1]));
          }
          o[3] = nohole(o[3]);
          asm volatile("global_store_dwordx4 %0, %1, %2 offset:%3 sc1\n\ts_nop 1" :: "v"(lin), "v"(o), "s"(zb), "i"(jj * 1024) : "memory");
          SB();
        }
      }
      SB();
      AB(10);
      ldtag(ef, (unsigned)((lane & 31) * 8), ws + WS_EF + 2048 + cb * 256);
      signal1(flg + 28);
    }
    const char* Bs = ws + WS_Z2 + (size_t)(wave * 16 * 48 + cb * 6) * 1024 + 2048;
    bf16x8 Bf[3][6];
    auto ldB1 = [&](int s, int j) {
      asm volatile("global_load_dwordx4 %0, %1, %2 offset:%3 " HSC : "=&v"(Bf[s % 3][j]) : "v"(lin + (unsigned)(((s + 2 * rot2) & 15) * 49152)), "s"(Bs), "i"(j * 1024 - 2048) : "memory");
    };
    f32x4 acc[4][6];
    unsigned ara[2][2];
#pragma unroll
    for (int ks = 0; ks < 2; ++ks) {
      ara[0][ks] = ringl + l15 * 128 + (((ks * 4 + g4) ^ (l15 & 7)) << 4);
      ara[1][ks] = ara[0][ks] + (M_CV - M_RING) + wave * (CV_BUF - 24576);
    }
    u32x4 af[2][4];
    auto rdA = [&](int s, int i) {
      const int q = (0x30123012 >> (4 * (s >> 1))) & 3;
      asm volatile("ds_read_b128 %0, %1 offset:%2" : "=&v"(af[s & 1][i]) : "v"(ara[q == 3][s & 1]), "i"((q % 3) * 8192 + i * 2048) : "memory");
    };
    flag_wait_asm(flg + 28, 4u);
    AB(11);
    wait_vm<0>(ef, bsv);
    waittag(ef, (unsigned)((lane & 31) * 8), ws + WS_EF + 2048 + cb * 256);
    AB(12); SS(1 - 3 * XS);
#pragma unroll
    for (int j = 0; j < 6; ++j) ldB1(0, j);
#pragma unroll
    for (int j = 0; j < 6; ++j) ldB1(1, j);
    SS(2 - 3 * XS);
    {
      flag_wait_asm(flg + 20 + wl * 4, 2u);
      const float* cp2 = (const float*)(smem + L_CS2 + (wl * 64 + l15) * 4);
      pin(ac2[0][0], ac2[0][1], ac2[1][0], ac2[1][1]);
#pragma unroll
      for (int jj = 0; jj < 2; ++jj) {
        const float nct = -add1(add1(cp2[jj * 16], cp2[32 + jj * 16]), add1(cp2[128 + jj * 16], cp2[160 + jj * 16]));
        u32x4 o;
#pragma unroll
        for (int q = 0; q < 4; ++q) {
          const int i = q >> 1, e = (q & 1) * 2;
          o[q] = pack2(mul1(fma1(xm[i][e], nct, ac2[i][jj][e]), xr[i][e]), mul1(fma1(xm[i][e + 1], nct, ac2[i][jj][e + 1]), xr[i][e + 1]));
        }
        z1put(odd * 32 + wl * 32 + jj * 16, o);
      }
      signal1(flg + 132);
      SB();
    }
    __builtin_amdgcn_s_setprio(G2P);
    f32x4 bs4 = {lo16(bsv), lo16(bsv), lo16(bsv), lo16(bsv)};
    pin(bs4);
    AB(25);
    flag_wait_asm(flg + 32, 1u);
    AB(26);
#pragma unroll
    for (int i = 0; i < 4; ++i) rdA(0, i);
    SB();
    const unsigned xa = ringl + lane * 16;
    auto putp = [&](int i, int j) { asm volatile("ds_write_b128 %0, %1 offset:%2" :: "v"(xa), "v"(acc[i][j]), "i"(i * 6144 + j * 1024) : "memory"); };
    unsigned mx[2], fv;
    u64 hm;
    auto chk = [&](auto sc, int nw, int k) {
      constexpr int s = decltype(sc)::value, sl = s % 3;
      auto d3 = [&](int j) { return __builtin_bit_cast(u32x4, Bf[sl][j])[3]; };
      if (k == 0) {
        asm volatile("s_waitcnt vmcnt(%0)" :: "i"(nw) : "memory");
        pin(Bf[sl][0], Bf[sl][1], Bf[sl][2], Bf[sl][3], Bf[sl][4], Bf[sl][5]);
        mx[0] = opq(umx(umx(d3(0), d3(1)), d3(2)));
      }
      if (k == 1) mx[1] = opq(umx(umx(d3(3), d3(4)), mx[0]));
      if (k == 2) { hm = __builtin_amdgcn_ballot_w64(umx(mx[1], d3(5)) == HOLE); asm volatile("" : "+s"(hm)); }
      if (k == 3 && __builtin_expect(hm != 0, 0)) {
        do {
          --tmo;
          asm volatile("s_waitcnt vmcnt(0)\n\ts_sleep 2" ::: "memory");
#pragma unroll
          for (int q = 0; q < (s ? 3 : 2); ++q)
            if (s + q < 16) reld6(Bf[(s + q) % 3], opq(lin) + (unsigned)(((s + q + 2 * rot2) & 15) * 49152), Bs);
          wait_vm<0>(Bf[sl][0], Bf[sl][1], Bf[sl][2], Bf[sl][3], Bf[sl][4], Bf[sl][5]);
        } while (__builtin_amdgcn_ballot_w64(umx(umx(umx(d3(0), d3(1)), d3(2)), umx(umx(d3(3), d3(4)), d3(5))) == HOLE) != 0 && tmo > 0);
      }
      SB();
    };
    chk(IC(0), 6, 0); chk(IC(0), 0, 1); chk(IC(0), 0, 2); chk(IC(0), 0, 3);
    auto mm = [&](int s, int i, int j) {
      if (s == 0 && i == 3) asm volatile("v_mfma_f32_16x16x32_bf16 %0, %1, %2, %3" : "=&v"(acc[i][j]) : "v"(Bf[0][j]), "v"(af[0][i]), "v"(bs4));
      else if (s == 0) MFMA0(acc[i][j], Bf[0][j], af[0][i]);
      else MFMA(acc[i][j], Bf[s % 3][j], af[s & 1][i]);
      SB();
    };
    auto step = [&](auto sc) {
      constexpr int s = decltype(sc)::value;
      constexpr int NA[16] = ANUM, a0 = s == 0 ? 0 : cum16(NA, s);
      static_assert(cum16(NA, 5) == 0 && cum16(NA, 7) <= 8 && cum16(NA, 9) >= 8 && cum16(NA, 11) == 16 && NA[s] <= 5, "");
      constexpr int NV = (s == 0 ? 5 : (s == 1 ? 1 : 6 + NA[s ? s - 1 : 0])) + (s < 13 ? 5 : 0) + NA[s];
      auto fad = [&](int q) { return flg + (q == 1 ? 32 : (q == 9 ? 144 : 112) + wave * 4); };
      wait_lg<0>(af[s & 1][0], af[s & 1][1], af[s & 1][2], af[s & 1][3]);
      if constexpr (s == 1 || s == 5 || s == 7 || s == 9) {
        pin(fv);
        constexpr unsigned tg = s == 1 || s == 7 ? 2u : 1u;
        if (__builtin_expect((unsigned)__builtin_amdgcn_readfirstlane((int)fv) < tg, 0)) flag_wait_asm(fad(s), tg);
      }
      SS(3 + 2 * (s - XS));
      SB();
#pragma unroll
      for (int j = 0; j < 6; ++j) {
        mm(s, 0, j);
        if (s == 0) ldB1(2, j);
        if (j == 5) chk(IC(s + 1), NV + (s == 0), 0);
        SB();
        mm(s, 1, j);
        if (j < NA[s]) ldA1(6 + ((a0 + j) >> 3), (a0 + j) & 7);
        if (j == 5) chk(IC(s + 1), 0, 1);
        SB();
        mm(s, 2, j);
        if (j < 4) rdA(s + 1, j);
        if (j == 4 && (s == 0 || s == 2 || s == YS)) signal1(flg + 96 + wave * 4);
        if (j == 4 && (s == 0 || s == 4 || s == 6 || s == 8)) asm volatile("ds_read_b32 %0, %1" : "=&v"(fv) : "v"(fad(s + 1)) : "memory");
        if (j == 4 && s == SGS) signal1(flg + 44);
        if (j == 5) chk(IC(s + 1), 0, 2);
        SB();
        mm(s, 3, j);
        if (s + 3 < 16) ldB1(s + 3, j);
        SB();
      }
      SS(4 + 2 * (s - XS));
      chk(IC(s + 1), 0, 3);
    };
    step(IC(0)); AB(13); step(IC(1)); step(IC(2)); step(IC(3)); AB(14); step(IC(4)); step(IC(5)); step(IC(6)); step(IC(7));
    step(IC(8)); step(IC(9)); step(IC(10)); step(IC(11)); step(IC(12)); step(IC(13));
    wait_lg<0>(af[0][0], af[0][1], af[0][2], af[0][3]);
    SS(3 + 2 * (14 - XS));
#pragma unroll
    for (int i = 0; i < 4; ++i) {
#pragma unroll
      for (int n = 0; n < 12; ++n) {
        if (i == 0 && n == 6) { chk(IC(15), 0, 3); wait_lg<0>(af[1][0], af[1][1], af[1][2], af[1][3]); }
        mm(14 + n / 6, i, n % 6);
        if (i == 0 && n < 2) { rdA(15, 2 * n); rdA(15, 2 * n + 1); }
        if (i == 0 && n >= 2 && n < 5) chk(IC(15), 0, n - 2);
        if (i > 0 && (n & 1)) putp(i - 1, n >> 1);
        SB();
      }
      if (i > 0) signal1(flg + ((wave + i) & 3) * 4);
      SB();
    }
    SS(4 + 2 * (14 - XS));
    AB(15);
    flag_wait_asm(flg + 56, 5u);
    s16x4 z1r[6];
    {
      const int fr = 4 * g4 + (l15 >> 2), p = lane & 3;
      const unsigned za = lb + L_Z1 + fr * 128 + (((wave * 2 + (p >> 1)) ^ (fr & 7)) << 4) + (p & 1) * 8;
      z1r[0] = tr_asm<0>(za); z1r[1] = tr_asm<2048>(za); z1r[2] = tr_asm<4096>(za);
      z1r[3] = tr_asm<6144>(za); z1r[4] = tr_asm<8192>(za); z1r[5] = tr_asm<10240>(za);
    }
    flag_wait_asm(flg + wave * 4, 3u);
    asm volatile("" : "+v"(z1r[0]), "+v"(z1r[1]), "+v"(z1r[2]), "+v"(z1r[3]), "+v"(z1r[4]), "+v"(z1r[5]));
    AB(16);
    f32x4 own[6];
    {
      f32x4 pr_[3][6];
#pragma unroll
      for (int q = 0; q < 3; ++q) {
        const char* src = smem + M_RING + ((wave + 3 - q) & 3) * 24576;
#pragma unroll
        for (int j = 0; j < 6; ++j) pr_[q][j] = *(const f32x4*)(src + q * 6144 + j * 1024 + lane * 16);
      }
      asm volatile("s_nop 7" : "+v"(acc[3][0]), "+v"(acc[3][1]), "+v"(acc[3][2]), "+v"(acc[3][3]), "+v"(acc[3][4]), "+v"(acc[3][5]));
#pragma unroll
      for (int j = 0; j < 6; ++j) own[j] = (acc[3][j] + pr_[0][j]) + (pr_[1][j] + pr_[2][j]);
    }
    SB();
    {
      unsigned pk[6][2];
#pragma unroll
      for (int j = 0; j < 6; ++j) {
        const u32x2 z = __builtin_bit_cast(u32x2, z1r[j]);
#pragma unroll
        for (int h = 0; h < 2; ++h) pk[j][h] = pk2(unpk(z[h]) * (f32x2){own[j][2 * h], own[j][2 * h + 1]});
      }
      const unsigned eo = (unsigned)((l15 + 16 * (2 * (g4 & 1) + (g4 >> 1))) * 16);
#pragma unroll
      for (int q = 0; q < 3; ++q) {
        const auto s0 = __builtin_amdgcn_permlane16_swap(pk[2 * q][0], pk[2 * q + 1][0], false, false);
        const auto s1 = __builtin_amdgcn_permlane16_swap(pk[2 * q][1], pk[2 * q + 1][1], false, false);
        st16(eo, hf + (size_t)((rb * 4 + wave) * 24 + cb * 3 + q) * 1024, (u32x4){s0[0], s1[0], s0[1], nohole(s1[1])});
      }
    }
    signal1(flg + 96 + wave * 4);
    AB(17);
    ldW();
    wait_vm<15>(ef);
    gemm3(IC(0));
    if (b == 0 && tid == 0) *(u64*)(ws + WS_EP) = tag;
  }
  AB(24);
  ab_[31] = ab_[0] + 100 * ((1 << 16) - tmo);
  AB_OUT();
}

extern "C" __global__ void k_end() {}

__global__ __launch_bounds__(256) void k_dly() {
  const unsigned long long t0 = __builtin_amdgcn_s_memrealtime();
  __shared__ unsigned sm[3][256];
  const int b = threadIdx.x;
  const unsigned ref = g_ab[0][0][0];
  int mn = 1 << 30, mx = -(1 << 30), su = 0;
  for (int w = 0; w < 8; ++w) {
    const int a = (int)(g_ab[b][w][0] - ref), e = (int)(g_ab[b][w][HQ] - ref);
    mn = a < mn ? a : mn; mx = e > mx ? e : mx;
    if ((HM == 'g' && w < 4) || (HM == 'c' && w >= 4)) su += e;
  }
  sm[0][b] = mn; sm[1][b] = mx; sm[2][b] = su;
  __syncthreads();
  if (b == 0) {
    int MN = 1 << 30, MX = -(1 << 30), SU = 0;
    for (int i = 0; i < 256; ++i) { MN = (int)sm[0][i] < MN ? (int)sm[0][i] : MN; MX = (int)sm[1][i] > MX ? (int)sm[1][i] : MX; SU += (int)sm[2][i]; }
    int q = HM == 'z' ? 0 : (HM == 'x' ? MX - MN : SU / 1024 - MN);
    q = q < 0 ? 0 : (q > 5000 ? 5000 : q);
    const unsigned long long te = t0 + 2000 + 10 * q;
    while (__builtin_amdgcn_s_memrealtime() < te) __builtin_amdgcn_s_sleep(1);
  }
}
extern "C" void kernel_launch(void* const* d_in, const int* in_sizes, int n_in,
                              void* d_out, int out_size, void* d_ws, size_t ws_size,
                              hipStream_t stream) {
  (void)hipFuncSetAttribute((const void*)k_f, hipFuncAttributeMaxDynamicSharedMemorySize, M_LDS);
  k_f<<<256, 512, M_LDS, stream>>>((const u16*)d_in[0], (const u16*)d_in[1], (const u16*)d_in[2], (const u16*)d_in[3], (const u16*)d_in[4],
                                   (const u16*)d_in[5], (const u16*)d_in[7], (const u16*)d_in[8], (const u16*)d_in[9], (const u16*)d_in[10],
                                   (u16*)d_out, (char*)d_ws);
  k_dly<<<1, 256, 0, stream>>>();
}
```

```cpp
#include <hip/hip_runtime.h>
#include <hip/hip_bf16.h>
#include <stdint.h>
#include <type_traits>

typedef unsigned short u16;
typedef unsigned long long u64;
typedef __attribute__((ext_vector_type(8))) short bf16x8;
typedef __attribute__((ext_vector_type(4))) short s16x4;
typedef __attribute__((ext_vector_type(4))) float f32x4;
typedef __attribute__((ext_vector_type(2))) float f32x2;
typedef __attribute__((ext_vector_type(4))) unsigned int u32x4;
typedef __attribute__((ext_vector_type(2))) unsigned int u32x2;
typedef __attribute__((ext_vector_type(2))) __bf16 bf16x2n;

#define LDS_AS __attribute__((address_space(3)))
#define IC(n) std::integral_constant<int, (n)>{}
#define SB() __builtin_amdgcn_sched_barrier(0)

#define AB_DECL do {} while (0)
#define AB(i) do {} while (0)
#define AB_OUT() do {} while (0)
#define CB(i) do {} while (0)
#define SS(i) do {} while (0)
constexpr int S_ = 2048, D_ = 640, FH_ = 768, F2_ = 1536;
constexpr unsigned HOLE = 0xFFFFFFFFu;

constexpr size_t WS_EP = 0;
constexpr size_t WS_EF = 4096;
constexpr size_t WS_LF = 22528;
constexpr size_t WS_YF = 14336;
constexpr size_t WS_XF = 20480;
constexpr size_t WS_ST = 32768;
constexpr size_t WS_Z2 = WS_ST + 32 * 12 * 1024;
constexpr size_t WS_H = WS_Z2 + 64 * 48 * 1024;
constexpr size_t WS_ZX = WS_H + 128 * 24 * 1024;
constexpr size_t WS_KV = WS_ZX + 32 * 4 * 4096;
constexpr size_t WS_Y = WS_KV + D_ * S_ * 2;
constexpr size_t WS_WO = WS_Y + D_ * S_ * 2;

__device__ __forceinline__ float bf2f(u16 v) { return __uint_as_float(((unsigned)v) << 16); }
__device__ __forceinline__ unsigned pack2(float lo, float hi) {
  f32x2 v = {lo, hi};
  bf16x2n r = __builtin_convertvector(v, bf16x2n);
  return __builtin_bit_cast(unsigned, r);
}
__device__ __forceinline__ float nv(float v) { asm("" : "+v"(v)); return v; }
__device__ __forceinline__ float fma1(float a, float b, float c) { float r; asm("v_fma_f32 %0, %1, %2, %3" : "=v"(r) : "v"(a), "v"(b), "v"(c)); return r; }
__device__ __forceinline__ float add1(float a, float b) { float r; asm("v_add_f32 %0, %1, %2" : "=v"(r) : "v"(a), "v"(b)); return r; }
__device__ __forceinline__ float mul1(float a, float b) { float r; asm("v_mul_f32 %0, %1, %2" : "=v"(r) : "v"(a), "v"(b)); return r; }
__device__ __forceinline__ unsigned pack2s(float lo, float hi) { return pack2(nv(lo), nv(hi)); }
__device__ __forceinline__ float lo16(unsigned w) { return __uint_as_float(w << 16); }
__device__ __forceinline__ float hi16(unsigned w) { return __uint_as_float(w & 0xFFFF0000u); }
__device__ __forceinline__ float rbf(float f) { return lo16(pack2(f, f)); }
__device__ __forceinline__ f32x2 unpk(unsigned w) { f32x2 r = {lo16(w), hi16(w)}; return r; }
__device__ __forceinline__ unsigned pk2(f32x2 v) { return pack2(v[0], v[1]); }
__device__ __forceinline__ unsigned umx(unsigned a, unsigned b) { return a > b ? a : b; }
__device__ __forceinline__ unsigned nohole(unsigned a) { return a < HOLE - 1 ? a : HOLE - 1; }
__device__ __forceinline__ f32x2 gelu2(f32x2 x) {
  const f32x2 c1 = {-2.3022082f, -2.3022082f}, c2 = {-0.10294324f, -0.10294324f};
  const f32x2 a = x * __builtin_elementwise_fma(x * x, c2, c1);
  f32x2 e = {__builtin_amdgcn_exp2f(a[0]), __builtin_amdgcn_exp2f(a[1])};
  e = e + (f32x2){1.0f, 1.0f};
  const f32x2 r = {__builtin_amdgcn_rcpf(e[0]), __builtin_amdgcn_rcpf(e[1])};
  return x * r;
}
__device__ __forceinline__ float gelu1(float x) {
  const float a = x * __builtin_fmaf(x * x, -0.10294324f, -2.3022082f);
  return x * __builtin_amdgcn_rcpf(__builtin_amdgcn_exp2f(a) + 1.0f);
}
template <int OFF>
__device__ __forceinline__ s16x4 tr_asm(unsigned a) {
  s16x4 r;
  asm volatile("ds_read_b64_tr_b16 %0, %1 offset:%2" : "=&v"(r) : "v"(a), "i"(OFF) : "memory");
  return r;
}
__device__ __forceinline__ bf16x8 join8(s16x4 lo, s16x4 hi) {
  bf16x8 r;
  r[0] = lo[0]; r[1] = lo[1]; r[2] = lo[2]; r[3] = lo[3];
  r[4] = hi[0]; r[5] = hi[1]; r[6] = hi[2]; r[7] = hi[3];
  return r;
}
__device__ __forceinline__ unsigned lds_addr(const void* p) { return (unsigned)(uintptr_t)(LDS_AS const char*)p; }
#define WAIT_LGKM0() asm volatile("s_waitcnt lgkmcnt(0)" ::: "memory")
__device__ __forceinline__ void flag_wait_asm(unsigned faddr, unsigned target) {
  unsigned v, sv;
  asm volatile(".Lfw_%=:\n\tds_read_b32 %0, %2\n\ts_waitcnt lgkmcnt(0)\n\tv_readfirstlane_b32 %1, %0\n\ts_nop 0\n\t"
               "s_cmp_ge_u32 %1, %3\n\ts_cbranch_scc1 .Lfd_%=\n\ts_sleep 1\n\ts_branch .Lfw_%=\n\t.Lfd_%=:"
               : "=&v"(v), "=&s"(sv) : "v"(faddr), "s"(target) : "memory", "scc");
}
__device__ __forceinline__ void signal1(unsigned a, unsigned one = 1u) {
  asm volatile("s_mov_b64 exec, 1\n\tds_add_u32 %0, %1\n\ts_mov_b64 exec, -1" :: "v"(a), "v"(one) : "memory");
}
__device__ __forceinline__ void dma16(unsigned vo, const void* sb, unsigned lds) {
  asm volatile("s_mov_b32 m0, %2\n\ts_nop 0\n\tglobal_load_lds_dwordx4 %0, %1" :: "v"(vo), "s"(sb), "s"(lds) : "memory");
}
template <int LO, int GO> __device__ __forceinline__ void dma32s(unsigned vo0, unsigned vo1, const void* sb, unsigned lds) {
  asm volatile("s_add_u32 m0, %5, %6\n\ts_add_u32 vcc_lo, %2, %4\n\ts_addc_u32 vcc_hi, %3, 0\n\t"
               "global_load_lds_dwordx4 %0, vcc\n\tglobal_load_lds_dwordx4 %1, vcc offset:1024"
               :: "v"(vo0), "v"(vo1), "s"((unsigned)(uintptr_t)sb), "s"((unsigned)((uintptr_t)sb >> 32)), "i"(GO), "s"(lds), "i"(LO) : "memory", "scc", "vcc");
}
template <int LO, int GO, int WRAP, int LIM> __device__ __forceinline__ void dma32r(unsigned vo0, unsigned vo1, const void* sb, unsigned lds, int rot) {
  const char* s2 = (const char*)sb - WRAP;
  asm volatile("s_add_u32 m0, %5, %6\n\ts_cmp_ge_u32 %7, %8\n\ts_cselect_b64 vcc, %3, %2\n\ts_add_u32 vcc_lo, vcc_lo, %4\n\ts_addc_u32 vcc_hi, vcc_hi, 0\n\t"
               "global_load_lds_dwordx4 %0, vcc\n\tglobal_load_lds_dwordx4 %1, vcc offset:1024"
               :: "v"(vo0), "v"(vo1), "s"(sb), "s"(s2), "i"(GO), "s"(lds), "i"(LO), "s"(rot), "i"(LIM) : "memory", "scc", "vcc");
}
__device__ __forceinline__ void dma16o(unsigned vo, const void* sb, int go, unsigned lds, int lo) {
  asm volatile("s_add_u32 m0, %3, %4\n\ts_nop 0\n\tglobal_load_lds_dwordx4 %0, %1 offset:%2" :: "v"(vo), "s"(sb), "i"(go), "s"(lds), "i"(lo - go) : "memory", "scc");
}
__device__ __forceinline__ void st16(unsigned vo, const void* sb, u32x4 v) {
  asm volatile("global_store_dwordx4 %0, %1, %2 sc1\n\ts_nop 1" :: "v"(vo), "v"(v), "s"(sb) : "memory");
}
__device__ __forceinline__ void touch(unsigned vo, const void* sb, unsigned lds) {
  asm volatile("s_mov_b32 m0, %2\n\ts_nop 0\n\tglobal_load_lds_dword %0, %1" :: "v"(vo), "s"(sb), "s"(lds) : "memory");
}
__device__ __forceinline__ void st8(unsigned vo, const void* sb, u32x2 v) { asm volatile("global_store_dwordx2 %0, %1, %2 sc1" :: "v"(vo), "v"(v), "s"(sb) : "memory"); }
template <class A, class B> __device__ __forceinline__ void MFMA(f32x4& acc, const A& a, const B& b) {
  asm volatile("v_mfma_f32_16x16x32_bf16 %0, %1, %2, %0" : "+v"(acc) : "v"(a), "v"(b));
}
template <class A, class B> __device__ __forceinline__ void MFMA0(f32x4& acc, const A& a, const B& b) {
  asm volatile("v_mfma_f32_16x16x32_bf16 %0, %1, %2, 0" : "=&v"(acc) : "v"(a), "v"(b));
}
template <bool Z, class A, class B> __device__ __forceinline__ void MFMAW(int n, f32x4& acc, const A& a, const B& b) {
  if (n < 0) { if (Z) MFMA0(acc, a, b); else MFMA(acc, a, b); }
  else if (Z) asm volatile("s_waitcnt lgkmcnt(%3)\n\tv_mfma_f32_16x16x32_bf16 %0, %1, %2, 0" : "=&v"(acc) : "v"(a), "v"(b), "i"(n));
  else asm volatile("s_waitcnt lgkmcnt(%3)\n\tv_mfma_f32_16x16x32_bf16 %0, %1, %2, %0" : "+v"(acc) : "v"(a), "v"(b), "i"(n));
}
__device__ __forceinline__ float sel4(float a, float b, float c, float d, u64 m1, u64 m2) {
  float r, t;
  asm volatile("v_cndmask_b32_e64 %0, %2, %3, %6\n\tv_cndmask_b32_e64 %1, %4, %5, %6\n\tv_cndmask_b32_e64 %0, %0, %1, %7"
               : "=&v"(r), "=&v"(t) : "v"(a), "v"(b), "v"(c), "v"(d), "s"(m1), "s"(m2));
  return r;
}
template <class T> __device__ __forceinline__ void pin1(T& r) { asm volatile("" : "+v"(r)); }
template <class T> __device__ __forceinline__ T opq(T r) { asm volatile("" : "+v"(r)); return r; }
template <class... T> __device__ __forceinline__ void pin(T&... r) { (pin1(r), ...); }
template <int N, class... T> __device__ __forceinline__ void wait_vm(T&... r) { asm volatile("s_waitcnt vmcnt(%0)" :: "n"(N) : "memory"); (pin1(r), ...); }
template <int N, class... T> __device__ __forceinline__ void wait_lg(T&... r) { asm volatile("s_waitcnt lgkmcnt(%0)" :: "n"(N) : "memory"); (pin1(r), ...); }
template <int OFF, class T> __device__ __forceinline__ void ds16(T& d, unsigned a) {
  asm volatile("ds_read_b128 %0, %1 offset:%2" : "=&v"(d) : "v"(a), "i"(OFF) : "memory");
}
template <int OFF> __device__ __forceinline__ void dsw16(unsigned a, const f32x4& v) {
  asm volatile("ds_write_b128 %0, %1 offset:%2" :: "v"(a), "v"(v), "i"(OFF) : "memory");
}
__device__ __forceinline__ void dsw4(unsigned a, float v) { asm volatile("ds_write_b32 %0, %1" :: "v"(a), "v"(v) : "memory"); }
#define OSC ""
#define HSC ""
template <int OFF, class T> __device__ __forceinline__ void ld16h(T& d, unsigned vo, const void* sb) {
  asm volatile("global_load_dwordx4 %0, %1, %2 offset:%3 " HSC : "=&v"(d) : "v"(vo), "s"(sb), "i"(OFF) : "memory");
}
template <class T> __device__ __forceinline__ void reld6(T (&B)[6], unsigned vo, const void* sb) {
  asm volatile("global_load_dwordx4 %0, %6, %7 offset:-2048 sc1\n\tglobal_load_dwordx4 %1, %6, %7 offset:-1024 sc1\n\t"
               "global_load_dwordx4 %2, %6, %7 sc1\n\tglobal_load_dwordx4 %3, %6, %7 offset:1024 sc1\n\t"
               "global_load_dwordx4 %4, %6, %7 offset:2048 sc1\n\tglobal_load_dwordx4 %5, %6, %7 offset:3072 sc1"
               : "+v"(B[0]), "+v"(B[1]), "+v"(B[2]), "+v"(B[3]), "+v"(B[4]), "+v"(B[5]) : "v"(vo), "s"(sb) : "memory");
}

constexpr int L_FLAG = 0;
constexpr int L_DUM = 256;
constexpr int L_XM = 512;
constexpr int L_XR = 768;
constexpr int L_CS = 1024;
constexpr int L_ZS = 2048;
constexpr int L_CS2 = 3072;
constexpr int L_Z1 = 4096;
constexpr int L_RAW = L_Z1;
constexpr int L_SLOT = 16384;
constexpr int L_KV = 512, KV_P = 3088, L_WO = L_KV + 8 * KV_P;
static_assert(L_WO % 16 == 0 && L_WO + 4 * 1024 <= L_SLOT + 2 * 8192, "staging");
constexpr int L_P2 = L_SLOT + 12 * 8192;
constexpr int CV_C = 4608, CV_CS = 4160, CV_BUF = CV_C + 2 * CV_CS;
constexpr int M_CV = L_SLOT;
constexpr int M_RING = L_SLOT + 6 * 8192;
constexpr int P3_G = L_SLOT;
constexpr int P3_C = P3_G + 80 * 1024;
constexpr int yat(int j) { return M_CV + (j >> 1) * CV_BUF + 8192 + (j & 1) * 2048; }
constexpr int M_LDS = 163840;
static_assert(3 * CV_BUF + 8192 <= 6 * 8192 && P3_C + 60 * 1024 <= M_LDS && CV_BUF >= 8192 + 4096, "LDS map");

enum { UX, UA, UB, UV };
struct Unit { int kind, idx, need, free, slot; };
#define SCHED gugugugwBuluouk_uuxuu_uuxuxu_uuxuxuu_Euuuuh_vuumumuh_vuumumuhF_vuvumum_DRvuuUHmmmmSptt_VhhhttttttJItttttttt
#define STR_(x) #x
#define STR(x) STR_(x)
constexpr char SCH[] = STR(SCHED);
constexpr int NSCH = sizeof(SCH) - 1;
constexpr int XNEED[10] = {1, 2, 2, 3, 3, 4, 4, 5, 5, 6};
constexpr int VNEED[5] = {5, 6, 7, 7, 8};
constexpr int XSLOT[10] = {17, 16, 15, 13, 10, 14, 11, 17, 9, 16}, WSLOT[10] = {6, 2, 4, 0, 6, 12, 10, 4, 0, 8}, VSLOT[10] = {8, 15, 3, 2, 14, 17, 16, 15, 7, 6}, XW = 6;
constexpr int cnt(char c, int e = NSCH) { int n = 0; for (int i = 0; i < e; ++i) n += SCH[i] == c || (c == 'u' && SCH[i] == 'g'); return n; }
constexpr int pos(char c, int k = 0) { for (int i = 0; i < NSCH; ++i) if (SCH[i] == c && k-- == 0) return i; return NSCH; }
constexpr Unit unit(int n) {
  for (int b = 1; b < 11; ++b) {
    for (int c = 0; c < 10; ++c) if (XNEED[c] == b && n-- == 0) return {UX, c, b, b + 1, XSLOT[c]};
    for (int c = 0; c < 5; ++c) if (VNEED[c] == b && n-- == 0) return {UV, c, b, b + 1, VSLOT[c]};
    if (n < 2) return {n ? UB : UA, b - 1, b < 8 ? b : 8, b < 7 ? b + 2 : 9, WSLOT[b - 1] + n};
    n -= 2;
  }
  return {UV, n + 5, 10, 99, VSLOT[n + 5]};
}
constexpr int FLAGB = cnt('_', pos('D')), GZ = cnt('_', pos('E'));
constexpr int nvm(char c) { return c == 'u' ? 2 : (c == 'H' ? 5 : (c == 'U' ? 4 : (c == 'F' || c == 'h' || c == 't' || c == 'w'))); }
constexpr int behind(int g, int e, char w) {
  const int b = cnt('_', e) + 1;
  int n = 0;
  for (int i = 0; i < e; ++i) {
    const char c = SCH[i];
    if (g == 0) { if (c == 'g') n = w == '_' && unit(cnt('u', i)).need <= b ? 0 : n + 2; if (c == 'B') n += 2; continue; }
    n = (w == '_' ? (c == 'u' && unit(cnt('u', i)).need <= b) || (c == 'h' && b == FLAGB) || (c == 'k' && b == GZ) : c == w) ? 0 : n + nvm(c);
  }
  return g == 0 && b >= GZ ? 0 : n;
}
constexpr bool plan_ok() {
  if (cnt('u') != 40 || cnt('_') != 9 || cnt('x') != 5 || cnt('h') != 6 || cnt('t') != 16 || cnt('I') * cnt('J') * cnt('p') != 1 || cnt('v') != 5 || cnt('m') != 10) return false;
  if (cnt('B') * cnt('D') * cnt('E') * cnt('F') * cnt('l') * cnt('o') * cnt('k') * cnt('H') * cnt('U') * cnt('V') * cnt('R') * cnt('S') != 1) return false;
  if (pos('l') < pos('B') || pos('o') < pos('B') || pos('k') < pos('l') || pos('k') > pos('_') || pos('o') > pos('_') || cnt('g', pos('B')) != cnt('g')) return false;
  if (cnt('_', pos('h', 2)) >= FLAGB || GZ < 3 || GZ > 6 || FLAGB < GZ || FLAGB > 9 || pos('o') > pos('k') || pos('H') > pos('I') || pos('F') > pos('U')) return false;
  if (cnt('_', pos('R')) != 8 || cnt('_', pos('S')) != 8 || pos('S') < pos('R') || cnt('_', pos('I')) != 9 || pos('J') < pos('U') || cnt('_', pos('J')) != 9) return false;
  if (cnt('_', pos('V')) != 9 || cnt('u', pos('V')) < 40) return false;
  for (int c = 0; c < 5; ++c) if (cnt('_', pos('x', c)) != XNEED[c] || cnt('_', pos('v', c)) != VNEED[c] || pos('m', 2 * c) < pos('v', c)) return false;
  if (cnt('_', pos('p')) != 8 || pos('p') < pos('m', 9)) return false;
  int fr[18] = {1, 1};
  for (int i = 0; i < NSCH; ++i) if (SCH[i] == 'u' || SCH[i] == 'g') {
    const Unit u = unit(cnt('u', i));
    const int p = cnt('_', i);
    if (p < fr[u.slot] || p >= u.need) return false;
    fr[u.slot] = u.free;
  }
  return true;
}
static_assert(plan_ok(), "phase A plan");
template <int I, int E, class F> __device__ __forceinline__ void sfor(F&& f) { if constexpr (I < E) { f(IC(I)); sfor<I + 1, E>(f); } }

__device__ __forceinline__ void conv_core(unsigned buf, int lane, unsigned Dw, f32x4 (&ac)[8]) {
  const int l15 = lane & 15, g4 = lane >> 4;
  const int c = (l15 + 1) & 1;
  unsigned aa[4];
#pragma unroll
  for (int k = 0; k < 4; ++k) aa[k] = buf + CV_C + c * CV_CS + (2031 - l15 + 8 * g4 - c) * 2 - 512 - 1024 * k;
  unsigned bb = buf + 512 + 32 * l15 - 32 + 16 * g4;
  u32x2 A[2][8][2];
  u32x4 B[2][8];
  constexpr int TT[36] = {7, 6, 5, 4, 7, 6, 5, 4, 7, 6, 5, 3, 7, 6, 5, 4, 7, 6, 3, 2, 7, 6, 5, 4, 7, 3, 2, 1, 7, 6, 5, 4, 3, 2, 1, 0};
  constexpr int AA[36] = {0, 0, 0, 0, 1, 1, 1, 1, 2, 2, 2, 0, 3, 3, 3, 2, 4, 4, 1, 0, 5, 5, 4, 3, 6, 2, 1, 0, 7, 6, 5, 4, 3, 2, 1, 0};
  auto ld1 = [&](int p, int n) {
    if (n < 16) {
      const int a = n >> 1, o = ((a & 1) ? 0 : 128) + 2 * (n & 1);
      asm volatile("ds_read2_b32 %0, %1 offset0:%2 offset1:%3" : "=&v"(A[p][(n >> 1) & 7][n & 1]) : "v"(aa[(a >> 1) & 3]), "i"(o), "i"(o + 1) : "memory");
    } else
      asm volatile("ds_read_b128 %0, %1 offset:%2" : "=&v"(B[p][n & 7]) : "v"(bb), "i"(512 * (n & 7)) : "memory");
  };
  auto next = [&](int dl) {
#pragma unroll
    for (int k = 0; k < 4; ++k) aa[k] += dl;
    bb += dl;
    SB();
  };
  auto land = [&](int p) {
    asm volatile("s_waitcnt lgkmcnt(0)" : "+v"(A[p][0][0]), "+v"(A[p][0][1]), "+v"(A[p][1][0]), "+v"(A[p][1][1]), "+v"(A[p][2][0]), "+v"(A[p][2][1]),
                 "+v"(A[p][3][0]), "+v"(A[p][3][1]), "+v"(A[p][4][0]), "+v"(A[p][4][1]), "+v"(A[p][5][0]), "+v"(A[p][5][1]), "+v"(A[p][6][0]), "+v"(A[p][6][1]),
                 "+v"(A[p][7][0]), "+v"(A[p][7][1]), "+v"(B[p][0]), "+v"(B[p][1]), "+v"(B[p][2]), "+v"(B[p][3]), "+v"(B[p][4]), "+v"(B[p][5]),
                 "+v"(B[p][6]), "+v"(B[p][7]) :: "memory");
    SB();
  };
  auto mm = [&](int p) {
#pragma unroll
    for (int n = 0; n < 36; ++n) {
      const int t = TT[n], a = AA[n];
      const u32x4 av = {A[p][a][0][0], A[p][a][0][1], A[p][a][1][0], A[p][a][1][1]};
      MFMA(ac[t], av, B[p][t - a]);
      if (n < 24) ld1(p ^ 1, n);
      SB();
    }
    land(p ^ 1);
  };
#pragma unroll
  for (int n = 0; n < 24; ++n) ld1(0, n);
  u32x4 ad;
#pragma unroll
  for (int e = 0; e < 4; ++e) ad[e] = lane == 32 + 2 * e + (lane & 1) + (lane & 8) * 3 ? Dw << (16 * (lane & 1)) : 0u;
  pin(ad);
  land(0);
#pragma unroll
  for (int t = 0; t < 8; ++t) MFMA0(ac[t], ad, B[0][t]);
#pragma unroll 1
  for (int r = 0; r < 8; r += 2) {
    next(-64);
    mm(0);
    next(r < 6 ? -64 : 0);
    mm(1);
  }
  asm volatile("s_nop 15" : "+v"(ac[0]), "+v"(ac[1]), "+v"(ac[2]), "+v"(ac[3]), "+v"(ac[4]), "+v"(ac[5]), "+v"(ac[6]), "+v"(ac[7]));
}

#define ROTA 3
#define RA1 1
#define RA2 0
#define RA3 0
#define RB1 0
#define RB2 4
#define RB3 0
#define G2P 0
#define RB4 2
#define THR 24
#define RB5 2
#define YS 8
#define WO3 0
#define C3P 0
#define FE3 9
#define WSH 1
#define CPR 1
#define STW 1
#define PN 10
#define P2P 2
#define SGS 11
#define ANUM {0, 0, 0, 0, 0, 3, 3, 2, 3, 3, 2, 0, 0, 0, 0, 0}
constexpr int cum16(const int (&a)[16], int n) { int c = 0; for (int i = 0; i < n; ++i) c += a[i]; return c; }

__global__ __launch_bounds__(512) void k_f(
    const u16* __restrict__ x, const u16* __restrict__ kin, const u16* __restrict__ vin, const u16* __restrict__ shift_k,
    const u16* __restrict__ ssm_k, const u16* __restrict__ ssm_D, const u16* __restrict__ w_in, const u16* __restrict__ w_sp,
    const u16* __restrict__ b_sp, const u16* __restrict__ w_out, u16* __restrict__ out, char* ws) {
  extern __shared__ __attribute__((aligned(16))) char smem[];
  const int tid = threadIdx.x, lane = tid & 63, wave = __builtin_amdgcn_readfirstlane(tid >> 6);
  const int b = blockIdx.x;
  const int xcd = b & 7, idx = b >> 3;
  const int rb = 8 * (xcd >> 1) + (idx >> 2), cb = 4 * (xcd & 1) + (idx & 3);
  const int t0 = rb * 64, d0 = cb * 80;
  const int g4 = lane >> 4, l15 = lane & 15;
  const unsigned lin = (unsigned)(lane * 16);
  const unsigned lb = lds_addr(smem), flg = lb + L_FLAG;
  AB_DECL;
  AB(0);
  asm volatile("" :: "s"(x), "s"(kin), "s"(vin), "s"(shift_k), "s"(ssm_k), "s"(ssm_D), "s"(w_in), "s"(w_sp), "s"(b_sp), "s"(w_out), "s"(out), "s"(ws));
  u64 tag = *(const __attribute__((address_space(4))) u64*)(uintptr_t)ws;
  int tmo = 1 << 16;
#define CWARM 1
  auto cwarm = [&]() {
    u64 pc;
    unsigned clen;
    asm volatile("s_getpc_b64 %0\n.Lpc_here_%=:\n\ts_mov_b32 %1, k_end-.Lpc_here_%=" : "=s"(pc), "=s"(clen));
    touch(min((unsigned)(idx * 1536 + min(lane, 11) * 128), clen - 4u) & ~3u, (const void*)pc, lb + L_DUM);
  };

  const int cw = wave & 3, wm = wave & 1, wl = (wave >> 1) & 1, ksb = wm ^ wl, odd = cb & 1, pr = cb >> 1;
  const int r8 = lane >> 3, c8 = lane & 7;
  unsigned voX[2], voW[2];
#pragma unroll
  for (int h = 0; h < 2; ++h) {
    voX[h] = (unsigned)((h * 8 + r8) * (D_ * 2) + ((c8 ^ (h * 4 + (r8 & 3))) << 4) - h * 1024);
    voW[h] = (unsigned)((h * 8 + r8) * (F2_ * 2) + ((c8 ^ (h * 4 + (r8 & 2))) << 4) - h * 1024);
  }
  const int rot = (RA1 * ((rb & 7) * 5 >> 2) + ROTA * (cb & 3) + RA2 * ((rb >> 3) * 5 >> 1) + RA3 * (cb >> 2)) % 10, rot2 = (RB1 * (cb & 3) + RB2 * (cb >> 2) + RB3 * (rb & 7) + RB4 * (rb >> 3) + RB5 * (cb & 1)) & 7;
  const char* gX = (const char*)x + (size_t)(t0 + cw * 16) * (D_ * 2) + rot * 128;
  const char* gW = (const char*)w_in + (size_t)(cw * 16 + rot * 64) * (F2_ * 2) + pr * 384;
  const char *gV = gW + odd * 256, *gB = gW + (odd ? 128 : FH_ * 2 + 128);
  const char* gS = (const char*)w_sp + (size_t)t0 * (S_ * 2);
  const unsigned aoff[2] = {(unsigned)(r8 * 4096 + ((c8 ^ r8) << 4)), (unsigned)(r8 * 4096 + ((c8 ^ r8) << 4) + 32768)};
  const unsigned ldw = lb + cw * 2048;
  auto issue = [&](auto nc) {
    constexpr Unit u = unit(decltype(nc)::value);
    constexpr int lo = L_SLOT + u.slot * 8192;
    if (u.kind == UX) dma32r<lo, u.idx * 128, 1280, 10 - u.idx>(voX[0], voX[1], gX, ldw, rot);
    else dma32r<lo, u.idx * (64 * F2_ * 2) + (u.kind == UA ? FH_ * 2 : 0), 640 * F2_ * 2, 10 - u.idx>(voW[0], voW[1], u.kind == UB ? gB : gV, ldw, rot);
    SB();
  };
  const int kd = d0 + min(wave, 4) * 16 + r8 * 2;
  u32x4 sw;
  {
    const int ks0 = t0 + wave * 8;
#pragma unroll
    for (int i = 0; i < 3; ++i) {
      const int id = min(i * 64 + lane, 189), r = id / 10, c = id % 10;
      const char* kp = (r < 11 ? (const char*)kin + (size_t)max(ks0 - 3 + r, 0) * (D_ * 2) : (const char*)vin + (size_t)(ks0 + r - 11) * (D_ * 2)) + cb * 160 + c * 16;
      asm volatile("s_mov_b32 m0, %1\n\ts_nop 0\n\tglobal_load_lds_dwordx4 %0, off" :: "v"(kp), "s"(lb + L_KV + wave * KV_P + i * 1024) : "memory");
    }
    if (CWARM && wave == 5) cwarm();
    SB();
  }
  auto ldsw = [&]() { asm volatile("global_load_dwordx4 %0, %1, %2" : "=&v"(sw) : "v"(kd * 8), "s"(shift_k) : "memory"); SB(); };
  unsigned kk[11], vv[8];
  u32x4 oa, ob;
  auto kvld = [&]() {
    const char* kl = smem + L_KV + c8 * KV_P + min(wave, 4) * 32 + r8 * 4;
#pragma unroll
    for (int i = 0; i < 11; ++i) kk[i] = *(const unsigned*)(kl + i * 160);
#pragma unroll
    for (int i = 0; i < 8; ++i) vv[i] = *(const unsigned*)(kl + (11 + i) * 160);
  };
  auto kvm = [&](int e0, int e1) {
    const f32x2 w0 = {lo16(sw[0]), lo16(sw[2])}, w1 = {hi16(sw[0]), hi16(sw[2])};
    const f32x2 w2 = {lo16(sw[1]), lo16(sw[3])}, w3 = {hi16(sw[1]), hi16(sw[3])};
    f32x2 kf[11], rr[8];
#pragma unroll
    for (int i = 2 * e0; i < 2 * e1 + 3; ++i) kf[i] = unpk((i < 3 && t0 + c8 == 0) ? 0u : kk[i]);
#pragma unroll
    for (int i = 2 * e0; i < 2 * e1; ++i) {
      f32x2 a0 = kf[i] * w3;
      a0 = __builtin_elementwise_fma(kf[i + 1], w2, a0);
      a0 = __builtin_elementwise_fma(kf[i + 2], w1, a0);
      a0 = __builtin_elementwise_fma(kf[i + 3], w0, a0);
      rr[i] = a0 * unpk(vv[i]);
    }
#pragma unroll
    for (int e = e0; e < e1; ++e) { oa[e] = pack2(rr[2 * e][0], rr[2 * e + 1][0]); ob[e] = pack2(rr[2 * e][1], rr[2 * e + 1][1]); }
  };
  auto kvst = [&]() {
    const unsigned ko = (unsigned)((kd * S_ + t0 + c8 * 8) * 2);
    st16(ko, ws + WS_KV, oa);
    st16(ko, ws + WS_KV + S_ * 2, ob);
    SB();
  };
  auto barrier0 = [&](int nv) {
    SB();
    asm volatile("s_waitcnt vmcnt(%0) lgkmcnt(0)\n\ts_barrier" :: "n"(nv) : "memory");
    SB();
    asm volatile("" : "+v"(sw), "+s"(tag));
    tag += 1;
    asm volatile("" : "+s"(tag));
  };
  unsigned pX[2], pW[2];
#pragma unroll
  for (int k = 0; k < 2; ++k) {
    const int q = l15 >> 2, p = lane & 3, sz = (q >> 1) | ((g4 & 1) << 1);
    pX[k] = lb + (wm * 32 + q * 8 + p) * 128 + ((((k ^ ksb) * 4 + g4) ^ (l15 & 7)) << 4);
    pW[k] = lb + ((k ^ ksb) * 32 + g4 * 8 + q) * 128 + (((sz << 1) | (p >> 1)) << 4) + (p & 1) * 8;
  }
  bf16x8 ones;
#pragma unroll
  for (int e = 0; e < 8; ++e) ones[e] = (short)0x3F80;
  f32x4 xm[2], xr[2];
  auto ldxs = [&]() {
#pragma unroll
    for (int i = 0; i < 2; ++i) {
      xm[i] = *(const f32x4*)(smem + L_XM + (wm * 32 + g4 * 8 + i * 4) * 4);
      xr[i] = *(const f32x4*)(smem + L_XR + (wm * 32 + g4 * 8 + i * 4) * 4);
    }
  };
  auto act = [&](const f32x4& a, float c, int i) {
    const f32x2 nc = {-c, -c};
    const f32x2 lo = gelu2(__builtin_elementwise_fma((f32x2){xm[i][0], xm[i][1]}, nc, (f32x2){a[0], a[1]}) * (f32x2){xr[i][0], xr[i][1]});
    const f32x2 hi = gelu2(__builtin_elementwise_fma((f32x2){xm[i][2], xm[i][3]}, nc, (f32x2){a[2], a[3]}) * (f32x2){xr[i][2], xr[i][3]});
    return (f32x4){lo[0], lo[1], hi[0], hi[1]};
  };
  auto pack8 = [&](const f32x4& a, const f32x4& c) { return (u32x4){pack2(a[0], a[1]), pack2(a[2], a[3]), pack2(c[0], c[1]), pack2(c[2], c[3])}; };
  auto z1at = [&](int f0) { return (u32x4*)(smem + L_Z1 + (f0 + l15) * 128 + (((wm * 4 + g4) ^ (l15 & 7)) << 4)); };
  auto z1put = [&](int f0, const u32x4& v) { *z1at(f0) = v; };
  f32x4 ac2[2][2], cs2[2];
  s16x4 VF[10][2][2];
  unsigned aV[2][2];
  auto rdV = [&](auto hc_, auto nc, int r) {
    constexpr int n = decltype(nc)::value, c = 5 * decltype(hc_)::value + (n >> 1), so = (VSLOT[c] - 2) * 8192, po = n < 2 ? -1 : ((VSLOT[n < 2 ? c : c - 1] - 2) * 8192) >> 16;
    const int jj = r >> 1;
    if (!(r & 1)) {
      if ((so >> 16) != po) aV[n & 1][jj] = opq((pW[n & 1] ^ (unsigned)(wl * 64 + jj * 32)) + L_SLOT + 2 * 8192 + (so & ~0xFFFF));
      VF[n][jj][0] = tr_asm<(so & 0xFFFF)>(aV[n & 1][jj]);
    } else VF[n][jj][1] = tr_asm<(so & 0xFFFF) + 512>(aV[n & 1][jj]);
    SB();
  };
  auto mm2 = [&](auto hc_, auto nc, bf16x8 (*X)[2][2], int nw, auto&& nxt) {
    constexpr int n = decltype(nc)::value, j = n >> 1, k = n & 1;
    constexpr bool z = n == 0 && !decltype(hc_)::value;
    const bf16x8 w0 = join8(VF[n][0][0], VF[n][0][1]), w1 = join8(VF[n][1][0], VF[n][1][1]);
    if (k == 0) { MFMAW<n == 0>(nw, cs2[0], ones, w0); MFMAW<n == 0>(-1, cs2[1], ones, w1); }
    MFMAW<z>(k ? nw : -1, ac2[0][0], X[j][k][0], w0); nxt(0);
    MFMAW<z>(-1, ac2[1][0], X[j][k][1], w0); nxt(1);
    MFMAW<z>(-1, ac2[0][1], X[j][k][0], w1); nxt(2);
    MFMAW<z>(-1, ac2[1][1], X[j][k][1], w1); nxt(3);
    if constexpr (n == 9) {
      pin(cs2[0], cs2[1]);
      if (g4 < 2) *(float*)(smem + L_CS2 + (wave * 32 + lane) * 4) = g4 ? cs2[1][0] : cs2[0][0];
    }
    SB();
  };
  const unsigned p2a = lb + L_P2 + cw * 4096 + lin;
  auto sttag = [&](const char* p) {
    asm volatile("s_mov_b64 exec, 1\n\tglobal_store_dwordx2 %0, %1, %2 sc1\n\ts_mov_b64 exec, -1" :: "v"(lin), "v"((u32x2){opq((unsigned)tag), opq((unsigned)(tag >> 32))}), "s"(p) : "memory");
  };
  auto ldtag = [&](u32x2& f, unsigned vo, const char* p) { asm volatile("global_load_dwordx2 %0, %1, %2 sc1" : "=&v"(f) : "v"(vo), "s"(p) : "memory"); };
  auto notag = [&](const u32x2& f) { return __builtin_amdgcn_ballot_w64(f[0] != (unsigned)tag || f[1] != (unsigned)(tag >> 32)) != 0; };
  auto waittag = [&](u32x2& f, unsigned vo, const char* p) {
    while (__builtin_expect(notag(f) && tmo > 0, 0)) {
      --tmo;
      __builtin_amdgcn_s_sleep(4);
      ldtag(f, vo, p);
      wait_vm<0>(f);
    }
  };
  u32x2 ef;

  char* const hf = ws + WS_H;
  bf16x8 Wq[15];
  const char* wg = ws + WS_WO + (size_t)(wave * 120 + cb * 5) * 1024;
  auto ldW = [&]() {
    ldtag(ef, (unsigned)((lane & 3) * 8), ws + WS_LF + (rb * 8 + wave) * 32);
#pragma unroll
    for (int n = 0; n < 15; ++n)
      asm volatile("global_load_dwordx4 %0, %1, %2 offset:%3 sc1" : "=&v"(Wq[n]) : "v"(lin), "s"(wg + (n / 5) * 40960 + 2048), "i"((n % 5) * 1024 - 2048) : "memory");
    SB();
  };
  f32x4 ac3[5];
  auto gemm3 = [&](auto khc) {
    constexpr int kh = decltype(khc)::value;
    unsigned hoff[4];
#pragma unroll
    for (int it = 0; it < 4; ++it) hoff[it] = (unsigned)(((rb * 4 + ((cw + 1 + it) & 3)) * 24 + 3 * wave) * 1024) + lin;
    u32x4 Hq[4][3];
    auto ldh1 = [&](int it, int k) {
      asm volatile("global_load_dwordx4 %0, %1, %2 offset:%3 " HSC : "=&v"(Hq[it][k]) : "v"(hoff[it]), "s"(hf), "i"(k * 1024) : "memory");
    };
    waittag(ef, (unsigned)((lane & 3) * 8), ws + WS_LF + (rb * 8 + wave) * 32);
    AB(18);
    signal1(flg + 48);
    flag_wait_asm(flg + 48, 8u);
    AB(19);
    __builtin_amdgcn_s_setprio(3);
    ldh1(0, 0); ldh1(0, 1); ldh1(0, 2);
    __builtin_amdgcn_s_setprio(2);
    ldh1(1, 0); ldh1(1, 1); ldh1(1, 2);
    __builtin_amdgcn_s_setprio(kh * C3P);
    SB();
    const unsigned px = lb + (kh ? P3_C + cw * 15360 : P3_G + cw * 20480) + lin;
    f32x4 pa[5][5], ae[5];
    auto put = [&](int it, int j) { asm volatile("ds_write_b128 %0, %1 offset:%2" :: "v"(px), "v"((it & 1) ? ac3[j] : ae[j]), "i"((it * 5 + j) * 1024) : "memory"); };
    auto fetch = [&](int n, int g, int q) {
      const int ms = (cw - 1 - q) & 3;
      const unsigned pn = lb + (g ? P3_C + ms * 15360 : P3_G + ms * 20480) + lin;
#pragma unroll
      for (int j = 0; j < 5; ++j) asm volatile("ds_read_b128 %0, %1 offset:%2" : "=&v"(pa[n][j]) : "v"(pn), "i"((q * 5 + j) * 1024) : "memory");
      SB();
    };
    sfor<0, 4>([&](auto itc) {
      constexpr int it = decltype(itc)::value;
      wait_vm<(it < 3 ? 3 : 0)>(Hq[it][0], Hq[it][1], Hq[it][2]);
      if constexpr (it == 0) pin(Wq[0], Wq[1], Wq[2], Wq[3], Wq[4], Wq[5], Wq[6], Wq[7], Wq[8], Wq[9], Wq[10], Wq[11], Wq[12], Wq[13], Wq[14]);
      auto bad = [&]() { return __builtin_amdgcn_ballot_w64(umx(umx(Hq[it][0][3], Hq[it][1][3]), Hq[it][2][3]) == HOLE) != 0; };
      if (__builtin_expect(bad(), 0)) {
        do {
          --tmo;
          asm volatile("s_sleep 2\n\tglobal_load_dwordx4 %0, %3, %4 sc1\n\tglobal_load_dwordx4 %1, %3, %4 offset:1024 sc1\n\t"
                       "global_load_dwordx4 %2, %3, %4 offset:2048 sc1\n\ts_waitcnt vmcnt(0)"
                       : "+v"(Hq[it][0]), "+v"(Hq[it][1]), "+v"(Hq[it][2]) : "v"(hoff[it]), "s"(hf) : "memory");
        } while (bad() && tmo > 0);
      }
      SB();
#pragma unroll
      for (int k = 0; k < 3; ++k)
#pragma unroll
        for (int j = 0; j < 5; ++j) {
          f32x4& ac = (it & 1) ? ac3[j] : ae[j];
          if (k == 0) MFMA0(ac, Wq[j], Hq[it][0]); else MFMA(ac, Wq[k * 5 + j], Hq[it][k]);
          if constexpr (it + 2 < 4) { if (k == 0 && j < 3) ldh1(it + 2 < 4 ? it + 2 : 0, j); }
          const int m = k * 5 + j - WO3;
          if (it > 0 && m >= 0 && m < 10 && !(m & 1)) put(it - 1, m >> 1);
          if (it > 0 && m == 8) signal1(flg + 160 + (it < 3 ? kh : 2) * 16 + ((cw + it) & 3) * 4);
          if (it == 3 && m == FE3) { flag_wait_asm(flg + 160 + wave * 4, 2u); fetch(0, kh, 0); fetch(1, kh, 1); }
          SB();
        }
      if constexpr (it == 3) {
        if (kh) {
          flag_wait_asm(flg + 192 + cw * 4, 3u);
          fetch(2, 1, 2); fetch(3, 0, 2); fetch(4, 0, 3);
        } else asm volatile("s_waitcnt lgkmcnt(0)\n\ts_nop 4" ::: "memory");
        pin(pa[0][0], pa[0][1], pa[0][2], pa[0][3], pa[0][4], pa[1][0], pa[1][1], pa[1][2], pa[1][3], pa[1][4]);
#pragma unroll
        for (int j = 0; j < 5; ++j) {
          pa[0][j] += pa[1][j];
          pin(pa[0][j], ac3[j]);
          ac3[j] += pa[0][j];
          if (!kh) put(3, j);
        }
        if (kh) {
          wait_lg<5>(pa[2][0], pa[2][1], pa[2][2], pa[2][3], pa[2][4], pa[3][0], pa[3][1], pa[3][2], pa[3][3], pa[3][4]);
#pragma unroll
          for (int j = 0; j < 5; ++j) ac3[j] += pa[2][j] + pa[3][j];
          wait_lg<0>(pa[4][0], pa[4][1], pa[4][2], pa[4][3], pa[4][4]);
#pragma unroll
          for (int j = 0; j < 5; ++j) ac3[j] += pa[4][j];
        } else signal1(flg + 192 + cw * 4);
      }
      SB();
      if constexpr (it == 0) AB(20);
      if constexpr (it == 2) AB(21);
    });
    AB(22);
  };
  if (wave >= 4) {
    const int wid = rb * 4 + cw;
    __builtin_amdgcn_s_setprio(3);
    ldsw();
    if (wid < 120) dma16((unsigned)((lane >> 1) * (D_ * 2) + (lane & 1) * 16), w_out + (size_t)(wid / 5) * 32 * D_ + d0 + (wid % 5) * 16, lb + L_WO + cw * 1024);
    SB();
    u32x4 hole = {HOLE, HOLE, HOLE, HOLE};
    bf16x8 XF[5][2][2];
    auto rdXF = [&](auto cc) {
      constexpr int c = decltype(cc)::value, so = L_SLOT + XSLOT[c] * 8192;
#pragma unroll
      for (int k = 0; k < 2; ++k) {
        const unsigned a = pX[k] + (so & ~0x7FFF);
        ds16<(so & 0x7FFF)>(XF[c][k][0], a);
        ds16<(so & 0x7FFF) + 512>(XF[c][k][1], a);
      }
      SB();
    };
    u32x4 TA[16];
    auto ldT = [&](u32x4& r, int d, int w, int i) {
      asm volatile("global_load_dwordx4 %0, %1, %2" : "=&v"(r) : "v"(aoff[i & 1] + (unsigned)(((d + rot2) & 7) * 128)), "s"(gS + ((w + 1 + (i >> 1)) & 3) * 65536 + w * 1024) : "memory");
    };
    auto ldx = [&](int c) {
      u32x4 xv;
      asm volatile("global_load_dwordx4 %0, %1, %2\n\ts_waitcnt vmcnt(0)" : "=&v"(xv) : "v"((unsigned)(lane * (D_ * 2) + c * 16)), "s"(x + (size_t)t0 * D_) : "memory");
      return xv;
    };
    const bool docv = cw < ((rb & 1) ? 2 : 3);
    const int d = d0 + (cw < 2 ? 2 * rb + cw : 64 + (rb >> 1));
    u32x4 hc[4], uc[4];
    unsigned Dw;
    auto ldhc = [&]() {
#pragma unroll
      for (int k = 0; k < 4; ++k) asm volatile("global_load_dwordx4 %0, %1, %2 offset:%3" : "=&v"(hc[k]) : "v"(lin), "s"(ssm_k + (size_t)d * S_), "i"(k * 1024) : "memory");
      asm volatile("global_load_ushort %0, %1, %2" : "=&v"(Dw) : "v"(0u), "s"(ssm_D + d) : "memory");
    };
    unsigned rwo;
    f32x2 ra, rc;
    auto rdraw = [&]() { asm volatile("ds_read_b64 %0, %2\n\tds_read_b64 %1, %2 offset:512" : "=&v"(ra), "=&v"(rc) : "v"(rwo) : "memory"); SB(); };
    const char* efp = ws + WS_EF + cb * 256;
    char* cb_ = smem + M_CV + cw * CV_BUF;
    auto run = [&](auto ic) {
      constexpr int i = decltype(ic)::value, p = cnt('_', i) + 1;
      constexpr char c = SCH[i];
      if constexpr (c == '_') {
        CB(3 * (p - XBAR));
        asm volatile("s_waitcnt vmcnt(%0) lgkmcnt(0)" :: "n"(behind(1, i, '_')) : "memory");
        CB(3 * (p - XBAR) + 1);
        asm volatile("s_barrier" ::: "memory");
        CB(3 * (p - XBAR) + 2);
        SB();
#pragma unroll
        for (int q = 0; q < 5; ++q) if (XNEED[q] + 1 == p) pin(XF[q][0][0], XF[q][0][1], XF[q][1][0], XF[q][1][1]);
        if constexpr (p == 7) {
          rwo = lb + L_RAW + ((lane >> 3) * 4 + (lane & 3)) * 16 + (lane & 4) * 2;
          pin(rwo);
        }
        if constexpr (p == 9) __builtin_amdgcn_s_setprio(CPR);
        if constexpr (p == 2) AB(4);
        if constexpr (p == 6) AB(5);
        if constexpr (p == 9) AB(6);
      }
      if constexpr (c == 'u') issue(IC(cnt('u', i)));
      if constexpr (c == 'x') rdXF(IC(cnt('x', i)));
      if constexpr (c == 'B') {
        AB(1);
        barrier0(behind(1, i, 'B'));
        pin(ones, hole);
      }
      if constexpr (c == 'v') sfor<2 * cnt('v', i), 2 * cnt('v', i) + 2>([&](auto nc) { rdV(IC(0), nc, 0); rdV(IC(0), nc, 1); rdV(IC(0), nc, 2); rdV(IC(0), nc, 3); });
      if constexpr (c == 'm') {
        constexpr int n = cnt('m', i);
        mm2(IC(0), IC(n), XF, VNEED[n >> 1] < p - 1 || (n & 1) ? -1 : 0, [](int) {});
      }
      if constexpr (c == 'p') {
        asm volatile("s_nop 7" : "+v"(ac2[0][0]), "+v"(ac2[0][1]), "+v"(ac2[1][0]), "+v"(ac2[1][1]));
        dsw16<0>(p2a, ac2[0][0]); dsw16<1024>(p2a, ac2[1][0]); dsw16<2048>(p2a, ac2[0][1]); dsw16<3072>(p2a, ac2[1][1]);
      }
      if constexpr (c == 'w')
        touch((unsigned)(min(lane, 19) * (F2_ * 2)), (const char*)w_in + (size_t)(((rb + WSH) & 7) * 80 + cw * 20) * (F2_ * 2) + pr * 384 + odd * 256, lb + L_DUM);
      if constexpr (c == 'l') { if (wave == 7) kvld(); }
      if constexpr (c == 'k') { if (wave == 7) { kvm(0, 4); kvst(); } }
      if constexpr (c == 'E' || c == 'D') { if (wave == 5) sttag(ws + WS_EF + (c == 'D' ? 2048 : 0) + (cb * 32 + rb) * 8); }
      if constexpr (c == 'U') {
        wait_vm<behind(1, i, 'F')>(ef);
        waittag(ef, (unsigned)((lane & 31) * 8), efp);
#pragma unroll
        for (int k = 0; k < 4; ++k) asm volatile("global_load_dwordx4 %0, %1, %2 offset:%3 sc1" : "=&v"(uc[k]) : "v"(lin), "s"(ws + WS_KV + (size_t)d * (S_ * 2)), "i"(k * 1024) : "memory");
      }
      if constexpr (c == 'F') ldtag(ef, (unsigned)((lane & 31) * 8), efp);
      if constexpr (c == 'o') {
        if (wid < 120) {
          const unsigned ra = lb + L_WO + cw * 1024 + (8 * g4 + (l15 >> 2)) * 32 + 8 * (lane & 3);
          s16x4 lo = tr_asm<0>(ra), hi = tr_asm<128>(ra);
          wait_lg<0>(lo, hi);
          st16(lin, ws + WS_WO + (size_t)((wid / 5) * 40 + cb * 5 + wid % 5) * 1024, __builtin_bit_cast(u32x4, join8(lo, hi)));
        }
      }
      if constexpr (c == 'h') {
        constexpr int n = cnt('h', i);
        st16(lin, ws + (n < 3 ? WS_Z2 + (size_t)((rb * 2 + wl) * 48 + cb * 6 + wm * 3 + n) * 1024 : WS_H + (size_t)((rb * 4 + cw) * 24 + cb * 3 + n - 3) * 1024), hole);
      }
      if constexpr (c == 't') { constexpr int n = cnt('t', i); ldT(TA[n], 3 + (n >> 3), cw, n & 7); }
      if constexpr (c == 'H') ldhc();
      if constexpr (c == 'R') rdraw();
      if constexpr (c == 'V') {
        wait_vm<behind(1, i, '_')>();
        signal1(flg + 60);
        AB(2);
      }
      if constexpr (c == 'I') {
        wait_vm<behind(1, i, 'H')>(hc[0], hc[1], hc[2], hc[3], Dw);
        if (docv) {
          if (lane < 36) *(u32x4*)(cb_ + (lane < 32 ? lane * 16 : CV_C + ((lane >> 1) & 1) * CV_CS + 4096 + (lane & 1) * 16)) = (u32x4){0u, 0u, 0u, 0u};
#pragma unroll
          for (int k = 0; k < 4; ++k) {
            const int a = k * 64 + lane;
            const int h0 = k ? __builtin_amdgcn_readlane((int)hc[k ? k - 1 : 0][3], 63) : 0;
            const unsigned hp = (unsigned)__builtin_amdgcn_update_dpp(h0, (int)hc[k][3], 0x138, 0xF, 0xF, false);
            const unsigned w[5] = {hp, hc[k][0], hc[k][1], hc[k][2], hc[k][3]};
            u32x4 sw, mx;
#pragma unroll
            for (int q = 0; q < 4; ++q) {
              sw[q] = (w[4 - q] >> 16) | (w[4 - q] << 16);
              mx[q] = (w[4 - q] & 0xFFFFu) | (w[3 - q] & 0xFFFF0000u);
            }
            char* cp = cb_ + CV_C + (2040 - 8 * a) * 2;
            *(u32x4*)(cp) = sw;
            *(u32x4*)(cp + CV_CS) = mx;
          }
        }
      }
      if constexpr (c == 'J') {
        wait_vm<behind(1, i, 'U')>(uc[0], uc[1], uc[2], uc[3]);
        if (docv) {
#pragma unroll
          for (int k = 0; k < 4; ++k) *(u32x4*)(cb_ + 512 + (k * 64 + lane) * 16) = uc[k];
        }
      }
      if constexpr (c == 'S') {
        wait_lg<0>(ra, rc);
        if (wave == 6) {
          const float s1 = nv(ra[0]) + rc[0], fm = s1 * (1.0f / 640.0f), sm = s1 * fm;
          float fq = (ra[1] - sm) + rc[1];
          if (__builtin_expect(__builtin_amdgcn_ballot_w64(!(fq >= 1e-3f * sm)) != 0, 0)) {
            const f32x2 mm = {fm, fm};
            f32x2 q2 = {0.f, 0.f};
#pragma unroll 1
            for (int c = 0; c < 80; ++c) {
              const u32x4 xv = ldx(c);
#pragma unroll
              for (int e = 0; e < 4; ++e) { const f32x2 d = unpk(xv[e]) - mm; q2 = __builtin_elementwise_fma(d, d, q2); }
            }
            fq = q2[0] + q2[1];
          }
          const float vbf = rbf(fmaxf(fq, 0.f) * (1.0f / 640.0f));
          *(float*)(smem + L_XM + lane * 4) = rbf(fm);
          *(float*)(smem + L_XR + lane * 4) = __builtin_amdgcn_rcpf(rbf(__builtin_amdgcn_sqrtf(rbf(vbf + 1e-5f))));
        }
      }
      SB();
    };
    sfor<0, NSCH>(run);
    __builtin_amdgcn_s_setprio(0);
    AB(9);
    auto ldS = [&](auto dc, auto wc) {
      constexpr int dd = decltype(dc)::value, w = decltype(wc)::value;
      const unsigned to = (unsigned)(((dd + rot2) & 7) * 128);
      if (THR < 60) wait_vm<THR>();
      dma32s<M_RING + w * 24576 + (2 - dd) * 8192, ((w + 1) & 3) * 65536 + w * 1024>(aoff[0] + to, aoff[1] - 1024 + to, gS, lb);
      if (THR < 60) wait_vm<THR>();
      dma32s<M_RING + w * 24576 + (2 - dd) * 8192 + 2048, ((w + 2) & 3) * 65536 + w * 1024>(aoff[0] + to, aoff[1] - 1024 + to, gS, lb);
      if (THR < 60) wait_vm<THR>();
      dma32s<M_RING + w * 24576 + (2 - dd) * 8192 + 4096, ((w + 3) & 3) * 65536 + w * 1024>(aoff[0] + to, aoff[1] - 1024 + to, gS, lb);
      if (THR < 60) wait_vm<THR>();
      dma32s<M_RING + w * 24576 + (2 - dd) * 8192 + 6144, w * 65536 + w * 1024>(aoff[0] + to, aoff[1] - 1024 + to, gS, lb);
      SB();
    };
    auto epi2 = [&]() {
      flag_wait_asm(flg + 132, 4u);
#pragma unroll
      for (int jj = 0; jj < 2; ++jj) {
        u32x4* zp = z1at(odd * 32 + wl * 32 + jj * 16);
        u32x4 o = *zp;
#pragma unroll
        for (int q = 0; q < 4; ++q) o[q] = pack2s(gelu1(nv(lo16(o[q]))), gelu1(hi16(o[q])));
        *zp = o;
      }
      signal1(flg + 56);
      SB();
    };
    auto putT = [&](const u32x4* T, unsigned a, unsigned f) {
#pragma unroll
      for (int i = 0; i < 8; ++i) asm volatile("ds_write_b128 %0, %1 offset:%2" :: "v"(a), "v"(T[i]), "i"(i * 1024) : "memory");
      signal1(f);
    };
    const unsigned tx = lb + M_CV + cw * CV_BUF + lin;
    auto landT = [&]() {
      sttag(ws + WS_LF + (rb * 8 + cb) * 32 + cw * 8);
#pragma unroll
      for (int n = 0; n < 16; ++n) pin(TA[n]);
      putT(TA, tx, flg + 112 + cw * 4);
    };
    auto putT4 = [&]() {
      flag_wait_asm(flg + 96 + cw * 4, 1u);
      putT(TA + 8, lb + M_RING + cw * 24576 + 16384 + lin, flg + 112 + cw * 4);
    };
    if (docv) {
      flag_wait_asm(flg + 36, 4u);
      AB(12);
      f32x4 ac[8];
      conv_core(lds_addr(cb_), lane, Dw, ac);
      AB(13);
      signal1(flg + 40);
      wait_vm<0>();
      landT();
#pragma unroll
      for (int k = 0; k < 4; ++k) {
        u32x2 ov[2];
#pragma unroll
        for (int t = 0; t < 2; ++t)
#pragma unroll
          for (int h = 0; h < 2; ++h) ov[t][h] = pack2(ac[2 * k + t][2 * h], ac[2 * k + t][2 * h + 1]);
        const auto s0 = __builtin_amdgcn_permlane16_swap(ov[0][0], ov[1][0], false, false);
        const auto s1 = __builtin_amdgcn_permlane16_swap(ov[0][1], ov[1][1], false, false);
        st16((unsigned)((256 * (g4 & 1) + 16 * l15 + 8 * (g4 >> 1)) * 2), ws + WS_Y + (size_t)d * (S_ * 2) + k * 1024, (u32x4){s0[0], s1[0], s0[1], s1[1]});
      }
      wait_vm<0>();
      sttag(ws + WS_YF + d * 8);
      putT4();
      epi2();
    } else if (cw != 3) {
      wait_vm<0>();
      landT();
      putT4();
      epi2();
    } else {
      ldS(IC(0), IC(0)); ldS(IC(0), IC(1)); ldS(IC(0), IC(2)); ldS(IC(1), IC(1)); ldS(IC(2), IC(1));
      wait_vm<40>();
      landT();
      AB(12);
      flag_wait_asm(flg + 16, 4u);
      AB(8);
      wait_vm<24>();
      ldS(IC(0), IC(3)); ldS(IC(1), IC(0)); ldS(IC(1), IC(2)); ldS(IC(1), IC(3));
      wait_vm<24>();
      signal1(flg + 32);
      ldS(IC(2), IC(0)); ldS(IC(2), IC(2)); ldS(IC(2), IC(3));
      AB(10);
      u32x4 TB[32];
#pragma unroll
      for (int n = 0; n < 32; ++n) {
        if (n == 8) wait_vm<32>();
        if (THR < 60 && !(n & 1)) wait_vm<THR>();
        if (THR < 32 && n == THR) signal1(flg + 32);
        ldT(TB[n], 5, n >> 3, n & 7);
      }
      wait_vm<32>();
      if (THR >= 32) signal1(flg + 32);
      AB(11);
      wait_vm<0>();
#pragma unroll
      for (int n = 0; n < 32; ++n) pin(TB[n]);
      putT4();
#pragma unroll
      for (int w = 0; w < 4; ++w) {
        flag_wait_asm(flg + 96 + w * 4, 2u);
        putT(TB + 8 * w, lb + M_RING + w * 24576 + 8192 + lin, flg + 144 + w * 4);
      }
      epi2();
      if (!odd) {
        u32x4 zx[4];
        ldtag(ef, (unsigned)((lane & 1) * 8), ws + WS_XF + (rb * 4 + pr) * 16);
        wait_vm<0>(ef);
        waittag(ef, (unsigned)((lane & 1) * 8), ws + WS_XF + (rb * 4 + pr) * 16);
#pragma unroll
        for (int c = 0; c < 4; ++c)
          asm volatile("global_load_dwordx4 %0, %1, %2 offset:%3 sc1" : "=&v"(zx[c]) : "v"(lin), "s"(ws + WS_ZX + (size_t)(rb * 4 + pr) * 4096), "i"(c * 1024) : "memory");
        wait_vm<0>(zx[0], zx[1], zx[2], zx[3]);
#pragma unroll
        for (int c = 0; c < 4; ++c) *(u32x4*)(smem + L_Z1 + (64 + (c & 1) * 16 + l15) * 128 + ((((c >> 1) * 4 + g4) ^ (l15 & 7)) << 4)) = zx[c];
      }
      signal1(flg + 56);
      AB(13);
    }
    SB();
    wait_vm<0>();
    AB(14);
    u32x2 xv[5];
    f32x2 sv[5][2];
    flag_wait_asm(flg + 40, (rb & 1) ? 2u : 3u);
    const int yc = min(cw * 24, 56);
    const unsigned yfo = (unsigned)(min(lane, 23) * 8);
    const char* yfp = ws + WS_YF + (d0 + yc) * 8;
    auto ldy = [&]() {
#pragma unroll
      for (int n = 0; n < 3; ++n) {
        const int q = (yc >> 3) + n;
        asm volatile("s_mov_b32 m0, %2\n\ts_nop 0\n\tglobal_load_lds_dwordx4 %0, %1 sc1" :: "v"((unsigned)(r8 * (S_ * 2) + ((c8 ^ (r8 & 6)) << 4))),
                     "s"(ws + WS_Y + ((size_t)(d0 + q * 8) * S_ + t0) * 2), "s"(lb + yat(0) + (q >> 2) * CV_BUF + (q & 3) * 1024) : "memory");
      }
    };
    flag_wait_asm(flg + 96 + cw * 4, 3u);
    ldtag(ef, yfo, yfp);
    wait_vm<0>(ef);
    flag_wait_asm(flg + 44, 4u);
    waittag(ef, yfo, yfp);
    AB(15);
    ldy();
#pragma unroll
    for (int j = 0; j < 5; ++j)
      asm volatile("global_load_dwordx2 %0, %1, %2 offset:%3" : "=&v"(xv[j]) : "v"((unsigned)(l15 * D_ * 2 + 8 * g4)), "s"(x + (size_t)(t0 + cw * 16) * D_ + d0), "i"(j * 32) : "memory");
    ldW();
    wait_vm<16>(xv[0], xv[1], xv[2], xv[3], xv[4]);
    AB(17);
    signal1(flg + 52);
    flag_wait_asm(flg + 52, 4u);
    {
      const int yr_ = 4 * g4 + (l15 >> 2);
      const unsigned ya = lb + yr_ * 128 + (((cw * 2 + ((lane >> 1) & 1)) ^ (yr_ & 6)) << 4) + (lane & 1) * 8;
      const float nm = -*(const float*)(smem + L_XM + (cw * 16 + l15) * 4), sr = *(const float*)(smem + L_XR + (cw * 16 + l15) * 4);
      s16x4 yr[5] = {tr_asm<yat(0)>(ya), tr_asm<yat(1)>(ya), tr_asm<yat(2)>(ya), tr_asm<yat(3)>(ya), tr_asm<yat(4)>(ya)};
      wait_lg<0>(yr[0], yr[1], yr[2], yr[3], yr[4]);
      AB(16);
      flag_wait_asm(flg + 96 + cw * 4, 4u);
#pragma unroll
      for (int j = 0; j < 5; ++j) {
        const u32x2 yw = __builtin_bit_cast(u32x2, yr[j]);
#pragma unroll
        for (int h = 0; h < 2; ++h) {
          sv[j][h] = ((unpk(xv[j][h]) + (f32x2){nm, nm}) * (f32x2){sr, sr}) * unpk(yw[h]);
          pin(sv[j][h]);
        }
      }
      SB();
    }
    wait_vm<15>(ef);
    gemm3(IC(1));
    unsigned pk[5][2];
#pragma unroll
    for (int j = 0; j < 5; ++j)
#pragma unroll
      for (int h = 0; h < 2; ++h) {
        const f32x2 o2 = {ac3[j][2 * h], ac3[j][2 * h + 1]};
        const f32x2 a = o2 * (f32x2){-1.4426950f, -1.4426950f};
        const f32x2 e = (f32x2){__builtin_amdgcn_exp2f(a[0]), __builtin_amdgcn_exp2f(a[1])} + (f32x2){1.0f, 1.0f};
        const f32x2 r = {__builtin_amdgcn_rcpf(e[0]), __builtin_amdgcn_rcpf(e[1])};
        pk[j][h] = pk2(__builtin_elementwise_fma(o2 * r, sv[j][h], unpk(xv[j][h])));
      }
    const u16* op = out + (size_t)(t0 + cw * 16) * D_ + d0;
#pragma unroll
    for (int q = 0; q < 2; ++q) {
      const auto s0 = __builtin_amdgcn_permlane16_swap(pk[2 * q][0], pk[2 * q + 1][0], false, false);
      const auto s1 = __builtin_amdgcn_permlane16_swap(pk[2 * q][1], pk[2 * q + 1][1], false, false);
      asm volatile("global_store_dwordx4 %0, %1, %2 offset:%3 " OSC "\n\ts_nop 1" :: "v"((unsigned)(l15 * D_ * 2 + 32 * (g4 & 1) + 16 * (g4 >> 1))), "v"((u32x4){s0[0], s1[0], s0[1], s1[1]}),
                   "s"(op), "i"(64 * q) : "memory");
    }
    asm volatile("global_store_dwordx2 %0, %1, %2 offset:128 " OSC :: "v"((unsigned)(l15 * D_ * 2 + 8 * g4)), "v"((u32x2){pk[4][0], pk[4][1]}), "s"(op) : "memory");
  } else {
    ldsw();
    sfor<0, pos('B')>([&](auto ic) { if constexpr (SCH[ic] == 'g') issue(IC(cnt('u', ic))); });
    if (tid < 64) *(unsigned*)(smem + L_FLAG + tid * 4) = 0u;
    pin(ones);
    AB(1);
    barrier0(behind(0, pos('B'), 'B'));
    kvld();
    kvm(0, 4);
    kvst();
    const bool isz2 = !(odd && wl);
    const char* stp = ws + WS_ST + (size_t)((rb * 12 + pr * 3 + odd * 2 + wl) * 1024 + wm * 512);
    if (STW && isz2) st8(lin >> 1, stp, (u32x2){0u, ~(unsigned)tag});
    const char* Ab[4];
#pragma unroll
    for (int i = 0; i < 4; ++i) Ab[i] = (const char*)w_sp + (size_t)(t0 + ((wave + 1 + i) & 3) * 16) * (S_ * 2) + wave * 1024;
    const unsigned ringl = lb + M_RING + wave * 24576;
    auto ldA1 = [&](int d, int i) {
      dma16o(aoff[i & 1] + (unsigned)(((d + rot2) & 7) * 128), Ab[i >> 1], 0, d == 7 ? lb + M_CV + wave * CV_BUF : ringl, i * 1024);
    };
    unsigned bsv;
    {
      f32x4 zv[2][4], cs[4];
      s16x4 WB[20][4][2];
      bf16x8 XG[10][2][2];
      unsigned aX[2][2], aW[2][2][4];
#pragma unroll
      for (int k = 0; k < 2; ++k)
#pragma unroll
        for (int w = 0; w < 2; ++w) {
          aX[w][k] = pX[k] + L_SLOT + XW * 8192 + w * 65536;
          pin(aX[w][k]);
#pragma unroll
          for (int jj = 0; jj < 4; ++jj) { aW[w][k][jj] = (pW[k] ^ (unsigned)(jj * 32)) + L_SLOT + wl * 8192 + w * 65536; pin(aW[w][k][jj]); }
        }
      auto rd1 = [&](auto nc, int r) {
        constexpr int n = decltype(nc)::value, j = n >> 1, k = n & 1, c = n > 0 && n < 11 ? n - 1 : 0, sx = (XSLOT[c] - XW) * 8192, sa = WSLOT[j] * 8192;
        if (r < 0) { if (r & 1) ds16<(sx & 0xFFFF) + 512>(XG[c][(r + 4) >> 1][1], aX[sx >> 16][(r + 4) >> 1]); else ds16<(sx & 0xFFFF)>(XG[c][(r + 4) >> 1][0], aX[sx >> 16][(r + 4) >> 1]); }
        else if (r & 1) WB[n][r >> 1][1] = tr_asm<(sa & 0xFFFF) + 512>(aW[sa >> 16][k][r >> 1]);
        else WB[n][r >> 1][0] = tr_asm<(sa & 0xFFFF)>(aW[sa >> 16][k][r >> 1]);
        SB();
      };
      auto bar = [&](auto bc) {
        SB();
        constexpr int p = decltype(bc)::value;
        CB(3 * (p - XBAR));
        if constexpr (p == 1) asm volatile("s_waitcnt vmcnt(%0) lgkmcnt(0)\n\ts_barrier" :: "n"(behind(0, pos('_', p - 1), '_')) : "memory");
        else if constexpr (p <= GZ) asm volatile("s_waitcnt vmcnt(%0) lgkmcnt(8)\n\ts_barrier" :: "n"(behind(0, pos('_', p - 1), '_')) : "memory");
        else if constexpr (p >= 8) asm volatile("s_waitcnt lgkmcnt(0)\n\ts_barrier" ::: "memory");
        else if constexpr (p < 7) asm volatile("s_waitcnt lgkmcnt(8)\n\ts_barrier" ::: "memory");
        else asm volatile("s_barrier" ::: "memory");
        CB(3 * (p - XBAR) + 2);
        SB();
      };
      float cp[4];
      f32x4 st[2][2], rv;
      const unsigned rwa = lb + L_RAW + (ksb * 2 + wm) * 256 + l15 * 16 + ((l15 >> 2) == g4 ? 0 : 1024);
      const unsigned csa = lb + L_CS + (wave * 64 + lane) * 4;
      auto kstep = [&](auto nc) {
        constexpr int n = decltype(nc)::value, j = n >> 1, k = n & 1, nn = n + 1, r0 = nn >= 2 && nn <= 10 ? -4 : 0;
        constexpr int sc = n == 1 ? 0 : n - 2, ns = !k ? 0 : (n == 1 || n == 11 ? 1 : 2);
        if constexpr (k) bar(IC(nn / 2 + 1));
        int r = r0, c = 0;
        auto nxt = [&]() { if (r < 8) { rd1(IC(nn), r++); ++c; } };
        auto sel = [&](int q) {
          const f32x4& sq = st[q & 1][q >> 1];
          rv[q] = sel4(sq[0], sq[1], sq[2], sq[3], 0xAAAAAAAAAAAAAAAAull, 0xCCCCCCCCCCCCCCCCull);
          SB();
        };
#pragma unroll
        for (int jj = 0; jj < 4; ++jj) {
          const bf16x8 wf = join8(WB[n][jj][0], WB[n][jj][1]);
          MFMAW<n == 0>(jj == 0 ? 4 : (jj == 2 ? c : -1), zv[0][jj], XG[j][k][0], wf);
          SB();
          nxt();
          if (n == 12) nxt();
          if (n == 12 && jj == 2) sel(2);
          MFMAW<n == 0>(-1, zv[1][jj], XG[j][k][1], wf);
          SB();
          nxt();
          if (n == 12) nxt();
          if (n == 12 && (jj == 1 || jj == 2)) sel(jj == 1 ? 0 : 3);
          if (n == 12 && jj == 2) { dsw16<0>(rwa, rv); SB(); }
          if (k == 0) {
            MFMAW<n == 0>(-1, cs[jj], ones, wf);
            SB();
            nxt();
            if (n == 12) nxt();
            if (n == 12 && jj == 1) sel(1);
          }
#pragma unroll
          for (int q = 0; q < ns; ++q) {
            const bf16x8& xs = XG[sc + q][1][jj >> 1];
            MFMAW<n == 1>(-1, st[jj & 1][jj >> 1], xs, (jj & 1) ? xs : ones);
            SB();
            nxt();
          }
        }
      };
      auto lstep = [&](auto nc, auto ac, auto bc, int w) {
        constexpr int n = decltype(nc)::value, j = n >> 1, k = n & 1, na = decltype(ac)::value, nb = decltype(bc)::value;
        int r = 0;
        auto nxt = [&]() { if (na > 0 && r < 16) { if (r < 8) rd1(ac, r); else rd1(bc, r - 8); ++r; } };
#pragma unroll
        for (int jj = 0; jj < 4; ++jj) {
          const bf16x8 wf = join8(WB[n][jj][0], WB[n][jj][1]);
          if (k == 0) { MFMAW<false>(jj == 0 ? w : -1, cs[jj], ones, wf); SB(); nxt(); nxt(); }
          MFMAW<false>(jj == 0 && k ? w : -1, zv[0][jj], XG[j][k][0], wf);
          SB();
          nxt();
          if (k) nxt();
          MFMAW<false>(-1, zv[1][jj], XG[j][k][1], wf);
          SB();
          nxt();
          if (k) nxt();
          if (n == 15 && jj == 2) {
            dsw4(csa, sel4(cs[0][0], cs[1][0], cs[2][0], cs[3][0], 0xFFFF0000FFFF0000ull, 0xFFFFFFFF00000000ull));
            SB();
          }
        }
      };
      bar(IC(1));
      AB(2);
#pragma unroll
      for (int r = -4; r < 8; ++r) rd1(IC(0), r);
      kstep(IC(0)); AB(4); kstep(IC(1)); kstep(IC(2)); kstep(IC(3)); kstep(IC(4)); kstep(IC(5)); kstep(IC(6)); kstep(IC(7)); kstep(IC(8)); AB(5); kstep(IC(9));
      kstep(IC(10)); kstep(IC(11)); kstep(IC(12));
      bar(IC(8));
      lstep(IC(13), IC(14), IC(16), -1);
      lstep(IC(14), IC(18), IC(15), 8);
      lstep(IC(16), IC(17), IC(19), 15);
      lstep(IC(18), IC(0), IC(0), 15);
      lstep(IC(15), IC(0), IC(0), -1);
      lstep(IC(17), IC(0), IC(0), 9);
      bar(IC(9));
      __builtin_amdgcn_s_setprio(2);
#pragma unroll
      for (int q = 0; q < 4; ++q) cp[q] = *(const float*)(smem + L_CS + ((wave ^ 1) * 64 + q * 16 + l15) * 4);
      ldxs();
      SB();
      lstep(IC(19), IC(0), IC(0), -1);
      AB(6);
      float ct[4];
#pragma unroll
      for (int jj = 0; jj < 4; ++jj) ct[jj] = cs[jj][0] + cp[jj];
#pragma unroll
      for (int jj = 0; jj < 4; ++jj) {
        if (jj == 0) asm volatile("s_nop 3");
        if (jj == 2) SB();
        if (!(jj & 1)) pin(zv[0][jj], zv[1][jj], zv[0][jj + 1], zv[1][jj + 1]);
#pragma unroll
        for (int i = 0; i < 2; ++i) zv[i][jj] = act(zv[i][jj], ct[jj], i);
      }
      if (isz2) {
        f32x4 pv[2], s1[2], s2[2];
#pragma unroll
        for (int i = 0; i < 2; ++i) {
#pragma unroll
          for (int e = 0; e < 4; ++e) {
            const float z0 = zv[i][0][e];
            pv[i][e] = __int_as_float(__builtin_amdgcn_update_dpp(0, __float_as_int(z0), 0x150, 0xF, 0xF, true));
          }
          const f32x4 a0 = zv[i][0] - pv[i], a1 = zv[i][1] - pv[i], a2 = zv[i][2] - pv[i], a3 = zv[i][3] - pv[i];
          s1[i] = (a0 + a1) + (a2 + a3);
          s2[i] = __builtin_elementwise_fma(a3, a3, __builtin_elementwise_fma(a2, a2, __builtin_elementwise_fma(a1, a1, a0 * a0)));
        }
        float r[8], q[4];
#define DA(d, s, ctl, bm) "v_add_f32_dpp " d ", " s ", " s " " ctl " row_mask:0xf bank_mask:" bm "\n\t"
        asm volatile("s_nop 1\n\t"
                     DA("%0", "%12", "row_ror:8", "0x3") DA("%1", "%13", "row_ror:8", "0x3") DA("%2", "%14", "row_ror:8", "0x3") DA("%3", "%15", "row_ror:8", "0x3")
                     DA("%4", "%16", "row_ror:8", "0x3") DA("%5", "%17", "row_ror:8", "0x3") DA("%6", "%18", "row_ror:8", "0x3") DA("%7", "%19", "row_ror:8", "0x3")
                     DA("%0", "%20", "row_ror:8", "0xc") DA("%1", "%21", "row_ror:8", "0xc") DA("%2", "%22", "row_ror:8", "0xc") DA("%3", "%23", "row_ror:8", "0xc")
                     DA("%4", "%24", "row_ror:8", "0xc") DA("%5", "%25", "row_ror:8", "0xc") DA("%6", "%26", "row_ror:8", "0xc") DA("%7", "%27", "row_ror:8", "0xc")
                     DA("%8", "%0", "row_half_mirror", "0x5") DA("%9", "%1", "row_half_mirror", "0x5") DA("%10", "%2", "row_half_mirror", "0x5") DA("%11", "%3", "row_half_mirror", "0x5")
                     DA("%8", "%4", "row_half_mirror", "0xa") DA("%9", "%5", "row_half_mirror", "0xa") DA("%10", "%6", "row_half_mirror", "0xa") DA("%11", "%7", "row_half_mirror", "0xa")
                     DA("%8", "%8", "quad_perm:[1,0,3,2]", "0xf") DA("%9", "%9", "quad_perm:[1,0,3,2]", "0xf") DA("%10", "%10", "quad_perm:[1,0,3,2]", "0xf") DA("%11", "%11", "quad_perm:[1,0,3,2]", "0xf")
                     DA("%8", "%8", "quad_perm:[2,3,0,1]", "0xf") DA("%9", "%9", "quad_perm:[2,3,0,1]", "0xf") DA("%10", "%10", "quad_perm:[2,3,0,1]", "0xf") DA("%11", "%11", "quad_perm:[2,3,0,1]", "0xf")
                     : "=&v"(r[0]), "=&v"(r[1]), "=&v"(r[2]), "=&v"(r[3]), "=&v"(r[4]), "=&v"(r[5]), "=&v"(r[6]), "=&v"(r[7]),
                       "=&v"(q[0]), "=&v"(q[1]), "=&v"(q[2]), "=&v"(q[3])
                     : "v"(s1[0][0]), "v"(s2[0][0]), "v"(s1[0][1]), "v"(s2[0][1]), "v"(s1[0][2]), "v"(s2[0][2]), "v"(s1[0][3]), "v"(s2[0][3]),
                       "v"(s1[1][0]), "v"(s2[1][0]), "v"(s1[1][1]), "v"(s2[1][1]), "v"(s1[1][2]), "v"(s2[1][2]), "v"(s1[1][3]), "v"(s2[1][3]));
#undef DA
        const f32x4 p4 = (lane & 8) ? pv[1] : pv[0];
        const float pa = (lane & 4) ? p4[2] : p4[0], pb = (lane & 4) ? p4[3] : p4[1];
        const float pp = (lane & 2) ? pb : pa, S1 = (lane & 2) ? q[2] : q[0], S2 = (lane & 2) ? q[3] : q[1];
        const float dl = S1 * (1.0f / 64.0f);
        st8(lin >> 1, stp, (u32x2){__float_as_uint((lane & 1) ? fmaxf(S2 - S1 * dl, 0.f) : pp + dl), (unsigned)tag});
      } else {
#pragma unroll
        for (int jj = 0; jj < 4; ++jj) {
          u32x4 o = pack8(zv[0][jj], zv[1][jj]);
          if (jj >= 2) z1put(jj * 16 - 32, o);
          else st16(lin, ws + WS_ZX + (size_t)((rb * 4 + pr) * 4 + wm * 2 + jj) * 1024, o);
        }
      }
      signal1(flg + 36);
      SB();
      AB(7);
      __builtin_amdgcn_s_setprio(P2P);
      unsigned po = (unsigned)((lane >> 5) * 6144 + (lane & 31) * 16);
      const char* pb = ws + WS_ST + (size_t)rb * 12288 + wm * 512 + 2048;
      u32x4 sg[6];
      auto ldsg = [&]() {
#pragma unroll
        for (int c = 0; c < 6; ++c) asm volatile("global_load_dwordx4 %0, %1, %2 offset:%3 sc1" : "=&v"(sg[c]) : "v"(po), "s"(pb), "i"(c * 1024 - 2048) : "memory");
      };
      auto look = [&]() {
        wait_vm<0>();
        ldsg();
        asm volatile("global_load_ushort %0, %1, %2" : "=&v"(bsv) : "v"((unsigned)(l15 * 2)), "s"(b_sp + t0 + wave * 16) : "memory");
        if (!isz2) sttag(ws + WS_XF + (rb * 4 + pr) * 16 + wm * 8);
        SB();
      };
      flag_wait_asm(flg + 60, 4u);
      ds16<0>(ac2[0][0], p2a); ds16<1024>(ac2[1][0], p2a); ds16<2048>(ac2[0][1], p2a); ds16<3072>(ac2[1][1], p2a);
      sfor<0, 4>([&](auto nc) { rdV(IC(1), nc, 0); rdV(IC(1), nc, 1); rdV(IC(1), nc, 2); rdV(IC(1), nc, 3); });
      sfor<0, 10>([&](auto nc) {
        constexpr int n = decltype(nc)::value;
        mm2(IC(1), nc, XG + 5, n == 0 ? 12 : (n < 6 ? 15 : 4 * (9 - n)), [&](int r) {
          SB();
          if constexpr (n < 3) { if (r < 2) { rdV(IC(1), IC(4 + 2 * n), 2 * r); rdV(IC(1), IC(4 + 2 * n), 2 * r + 1); } else { rdV(IC(1), IC(5 + 2 * n), 2 * r - 4); rdV(IC(1), IC(5 + 2 * n), 2 * r - 3); } }
        });
        if constexpr (n == 2) signal1(flg + 16);
        if constexpr (n == PN) look();
      });
      signal1(flg + 20 + wl * 4);
      AB(3);
      if (PN > 9) look();
      AB(8);
      wait_vm<1>(sg[0], sg[1], sg[2], sg[3], sg[4], sg[5]);
      if (isz2) {
        const unsigned zw = lb + L_ZS + wave * 256 + lane * 4, zr_ = lb + L_ZS + wave * 256 + g4 * 32;
        const char* zb = ws + WS_Z2 + (size_t)((rb * 2 + wm) * 48 + pr * 12 + odd * 8 + wl * 4) * 1024;
        auto bad = [&]() {
          unsigned an = sg[0][1] & sg[0][3], on = sg[0][1] | sg[0][3];
#pragma unroll
          for (int c = 1; c < 6; ++c) { an &= sg[c][1] & sg[c][3]; on |= sg[c][1] | sg[c][3]; }
          return __builtin_amdgcn_ballot_w64(((an ^ (unsigned)tag) | (on ^ (unsigned)tag)) != 0) != 0;
        };
        if (__builtin_expect(bad(), 0)) {
          do {
            --tmo;
            reld6(sg, po, pb);
            wait_vm<0>(sg[0], sg[1], sg[2], sg[3], sg[4], sg[5]);
          } while (bad() && tmo > 0);
        }
        AB(9);
        auto f = [&](int c, int e) { return __uint_as_float(sg[c][e]); };
        auto sum6 = [&](int e) { return add1(add1(add1(f(0, e), f(1, e)), add1(f(2, e), f(3, e))), add1(f(4, e), f(5, e))); };
        const float nmh = sum6(0) * (-1.0f / 6.0f), mh = -nmh;
        float qa = sum6(2) * (1.0f / 64.0f), qb;
#pragma unroll
        for (int c = 0; c < 6; c += 2) { const float u = add1(f(c, 0), nmh), v = add1(f(c + 1, 0), nmh); qa = fma1(u, u, qa); qb = c ? fma1(v, v, qb) : v * v; }
        const float Mh = add1(qa, qb);
        const auto sm = __builtin_amdgcn_permlane32_swap(__float_as_uint(mh), __float_as_uint(mh), false, false);
        const auto sM = __builtin_amdgcn_permlane32_swap(__float_as_uint(Mh), __float_as_uint(Mh), false, false);
        const float dm = __uint_as_float(sm[0]) - __uint_as_float(sm[1]), mean = rbf(add1(__uint_as_float(sm[0]), __uint_as_float(sm[1])) * 0.5f);
        const float vbf = rbf(__builtin_fmaf(dm * dm, 3.0f, add1(__uint_as_float(sM[0]), __uint_as_float(sM[1]))) * (1.0f / 12.0f));
        const float rz = __builtin_amdgcn_rcpf(rbf(__builtin_amdgcn_sqrtf(rbf(vbf + 1e-5f))));
        dsw4(zw, lane < 32 ? -mean * rz : rz);
        f32x4 zm[2], zr[2];
        ds16<0>(zm[0], zr_); ds16<128>(zr[0], zr_); ds16<16>(zm[1], zr_); ds16<144>(zr[1], zr_);
        wait_lg<0>(zm[0], zm[1], zr[0], zr[1]);
#pragma unroll
        for (int jj = 0; jj < 4; ++jj) {
          u32x4 o;
#pragma unroll
          for (int q = 0; q < 4; ++q) {
            const int i = q >> 1, e = (q & 1) * 2;
            o[q] = pack2(fma1(zv[i][jj][e], zr[i][e], zm[i][e]), fma1(zv[i][jj][e + 1], zr[i][e + 1], zm[i][e + 1]));
          }
          o[3] = nohole(o[3]);
          asm volatile("global_store_dwordx4 %0, %1, %2 offset:%3 sc1\n\ts_nop 1" :: "v"(lin), "v"(o), "s"(zb), "i"(jj * 1024) : "memory");
          SB();
        }
      }
      SB();
      AB(10);
      ldtag(ef, (unsigned)((lane & 31) * 8), ws + WS_EF + 2048 + cb * 256);
      signal1(flg + 28);
    }
    const char* Bs = ws + WS_Z2 + (size_t)(wave * 16 * 48 + cb * 6) * 1024 + 2048;
    bf16x8 Bf[3][6];
    auto ldB1 = [&](int s, int j) {
      asm volatile("global_load_dwordx4 %0, %1, %2 offset:%3 " HSC : "=&v"(Bf[s % 3][j]) : "v"(lin + (unsigned)(((s + 2 * rot2) & 15) * 49152)), "s"(Bs), "i"(j * 1024 - 2048) : "memory");
    };
    f32x4 acc[4][6];
    unsigned ara[2][2];
#pragma unroll
    for (int ks = 0; ks < 2; ++ks) {
      ara[0][ks] = ringl + l15 * 128 + (((ks * 4 + g4) ^ (l15 & 7)) << 4);
      ara[1][ks] = ara[0][ks] + (M_CV - M_RING) + wave * (CV_BUF - 24576);
    }
    u32x4 af[2][4];
    auto rdA = [&](int s, int i) {
      const int q = (0x30123012 >> (4 * (s >> 1))) & 3;
      asm volatile("ds_read_b128 %0, %1 offset:%2" : "=&v"(af[s & 1][i]) : "v"(ara[q == 3][s & 1]), "i"((q % 3) * 8192 + i * 2048) : "memory");
    };
    flag_wait_asm(flg + 28, 4u);
    AB(11);
    wait_vm<0>(ef, bsv);
    waittag(ef, (unsigned)((lane & 31) * 8), ws + WS_EF + 2048 + cb * 256);
    AB(12); SS(1 - 3 * XS);
#pragma unroll
    for (int j = 0; j < 6; ++j) ldB1(0, j);
#pragma unroll
    for (int j = 0; j < 6; ++j) ldB1(1, j);
    SS(2 - 3 * XS);
    {
      flag_wait_asm(flg + 20 + wl * 4, 2u);
      const float* cp2 = (const float*)(smem + L_CS2 + (wl * 64 + l15) * 4);
      pin(ac2[0][0], ac2[0][1], ac2[1][0], ac2[1][1]);
#pragma unroll
      for (int jj = 0; jj < 2; ++jj) {
        const float nct = -add1(add1(cp2[jj * 16], cp2[32 + jj * 16]), add1(cp2[128 + jj * 16], cp2[160 + jj * 16]));
        u32x4 o;
#pragma unroll
        for (int q = 0; q < 4; ++q) {
          const int i = q >> 1, e = (q & 1) * 2;
          o[q] = pack2(mul1(fma1(xm[i][e], nct, ac2[i][jj][e]), xr[i][e]), mul1(fma1(xm[i][e + 1], nct, ac2[i][jj][e + 1]), xr[i][e + 1]));
        }
        z1put(odd * 32 + wl * 32 + jj * 16, o);
      }
      signal1(flg + 132);
      SB();
    }
    __builtin_amdgcn_s_setprio(G2P);
    f32x4 bs4 = {lo16(bsv), lo16(bsv), lo16(bsv), lo16(bsv)};
    pin(bs4);
    AB(25);
    flag_wait_asm(flg + 32, 1u);
    AB(26);
#pragma unroll
    for (int i = 0; i < 4; ++i) rdA(0, i);
    SB();
    const unsigned xa = ringl + lane * 16;
    auto putp = [&](int i, int j) { asm volatile("ds_write_b128 %0, %1 offset:%2" :: "v"(xa), "v"(acc[i][j]), "i"(i * 6144 + j * 1024) : "memory"); };
    unsigned mx[2], fv;
    u64 hm;
    auto chk = [&](auto sc, int nw, int k) {
      constexpr int s = decltype(sc)::value, sl = s % 3;
      auto d3 = [&](int j) { return __builtin_bit_cast(u32x4, Bf[sl][j])[3]; };
      if (k == 0) {
        asm volatile("s_waitcnt vmcnt(%0)" :: "i"(nw) : "memory");
        pin(Bf[sl][0], Bf[sl][1], Bf[sl][2], Bf[sl][3], Bf[sl][4], Bf[sl][5]);
        mx[0] = opq(umx(umx(d3(0), d3(1)), d3(2)));
      }
      if (k == 1) mx[1] = opq(umx(umx(d3(3), d3(4)), mx[0]));
      if (k == 2) { hm = __builtin_amdgcn_ballot_w64(umx(mx[1], d3(5)) == HOLE); asm volatile("" : "+s"(hm)); }
      if (k == 3 && __builtin_expect(hm != 0, 0)) {
        do {
          --tmo;
          asm volatile("s_waitcnt vmcnt(0)\n\ts_sleep 2" ::: "memory");
#pragma unroll
          for (int q = 0; q < (s ? 3 : 2); ++q)
            if (s + q < 16) reld6(Bf[(s + q) % 3], opq(lin) + (unsigned)(((s + q + 2 * rot2) & 15) * 49152), Bs);
          wait_vm<0>(Bf[sl][0], Bf[sl][1], Bf[sl][2], Bf[sl][3], Bf[sl][4], Bf[sl][5]);
        } while (__builtin_amdgcn_ballot_w64(umx(umx(umx(d3(0), d3(1)), d3(2)), umx(umx(d3(3), d3(4)), d3(5))) == HOLE) != 0 && tmo > 0);
      }
      SB();
    };
    chk(IC(0), 6, 0); chk(IC(0), 0, 1); chk(IC(0), 0, 2); chk(IC(0), 0, 3);
    auto mm = [&](int s, int i, int j) {
      if (s == 0 && i == 3) asm volatile("v_mfma_f32_16x16x32_bf16 %0, %1, %2, %3" : "=&v"(acc[i][j]) : "v"(Bf[0][j]), "v"(af[0][i]), "v"(bs4));
      else if (s == 0) MFMA0(acc[i][j], Bf[0][j], af[0][i]);
      else MFMA(acc[i][j], Bf[s % 3][j], af[s & 1][i]);
      SB();
    };
    auto step = [&](auto sc) {
      constexpr int s = decltype(sc)::value;
      constexpr int NA[16] = ANUM, a0 = s == 0 ? 0 : cum16(NA, s);
      static_assert(cum16(NA, 5) == 0 && cum16(NA, 7) <= 8 && cum16(NA, 9) >= 8 && cum16(NA, 11) == 16 && NA[s] <= 5, "");
      constexpr int NV = (s == 0 ? 5 : (s == 1 ? 1 : 6 + NA[s ? s - 1 : 0])) + (s < 13 ? 5 : 0) + NA[s];
      auto fad = [&](int q) { return flg + (q == 1 ? 32 : (q == 9 ? 144 : 112) + wave * 4); };
      wait_lg<0>(af[s & 1][0], af[s & 1][1], af[s & 1][2], af[s & 1][3]);
      if constexpr (s == 1 || s == 5 || s == 7 || s == 9) {
        pin(fv);
        constexpr unsigned tg = s == 1 || s == 7 ? 2u : 1u;
        if (__builtin_expect((unsigned)__builtin_amdgcn_readfirstlane((int)fv) < tg, 0)) flag_wait_asm(fad(s), tg);
      }
      SS(3 + 2 * (s - XS));
      SB();
#pragma unroll
      for (int j = 0; j < 6; ++j) {
        mm(s, 0, j);
        if (s == 0) ldB1(2, j);
        if (j == 5) chk(IC(s + 1), NV + (s == 0), 0);
        SB();
        mm(s, 1, j);
        if (j < NA[s]) ldA1(6 + ((a0 + j) >> 3), (a0 + j) & 7);
        if (j == 5) chk(IC(s + 1), 0, 1);
        SB();
        mm(s, 2, j);
        if (j < 4) rdA(s + 1, j);
        if (j == 4 && (s == 0 || s == 2 || s == YS)) signal1(flg + 96 + wave * 4);
        if (j == 4 && (s == 0 || s == 4 || s == 6 || s == 8)) asm volatile("ds_read_b32 %0, %1" : "=&v"(fv) : "v"(fad(s + 1)) : "memory");
        if (j == 4 && s == SGS) signal1(flg + 44);
        if (j == 5) chk(IC(s + 1), 0, 2);
        SB();
        mm(s, 3, j);
        if (s + 3 < 16) ldB1(s + 3, j);
        SB();
      }
      SS(4 + 2 * (s - XS));
      chk(IC(s + 1), 0, 3);
    };
    step(IC(0)); AB(13); step(IC(1)); step(IC(2)); step(IC(3)); AB(14); step(IC(4)); step(IC(5)); step(IC(6)); step(IC(7));
    step(IC(8)); step(IC(9)); step(IC(10)); step(IC(11)); step(IC(12)); step(IC(13));
    wait_lg<0>(af[0][0], af[0][1], af[0][2], af[0][3]);
    SS(3 + 2 * (14 - XS));
#pragma unroll
    for (int i = 0; i < 4; ++i) {
#pragma unroll
      for (int n = 0; n < 12; ++n) {
        if (i == 0 && n == 6) { chk(IC(15), 0, 3); wait_lg<0>(af[1][0], af[1][1], af[1][2], af[1][3]); }
        mm(14 + n / 6, i, n % 6);
        if (i == 0 && n < 2) { rdA(15, 2 * n); rdA(15, 2 * n + 1); }
        if (i == 0 && n >= 2 && n < 5) chk(IC(15), 0, n - 2);
        if (i > 0 && (n & 1)) putp(i - 1, n >> 1);
        SB();
      }
      if (i > 0) signal1(flg + ((wave + i) & 3) * 4);
      SB();
    }
    SS(4 + 2 * (14 - XS));
    AB(15);
    flag_wait_asm(flg + 56, 5u);
    s16x4 z1r[6];
    {
      const int fr = 4 * g4 + (l15 >> 2), p = lane & 3;
      const unsigned za = lb + L_Z1 + fr * 128 + (((wave * 2 + (p >> 1)) ^ (fr & 7)) << 4) + (p & 1) * 8;
      z1r[0] = tr_asm<0>(za); z1r[1] = tr_asm<2048>(za); z1r[2] = tr_asm<4096>(za);
      z1r[3] = tr_asm<6144>(za); z1r[4] = tr_asm<8192>(za); z1r[5] = tr_asm<10240>(za);
    }
    flag_wait_asm(flg + wave * 4, 3u);
    asm volatile("" : "+v"(z1r[0]), "+v"(z1r[1]), "+v"(z1r[2]), "+v"(z1r[3]), "+v"(z1r[4]), "+v"(z1r[5]));
    AB(16);
    f32x4 own[6];
    {
      f32x4 pr_[3][6];
#pragma unroll
      for (int q = 0; q < 3; ++q) {
        const char* src = smem + M_RING + ((wave + 3 - q) & 3) * 24576;
#pragma unroll
        for (int j = 0; j < 6; ++j) pr_[q][j] = *(const f32x4*)(src + q * 6144 + j * 1024 + lane * 16);
      }
      asm volatile("s_nop 7" : "+v"(acc[3][0]), "+v"(acc[3][1]), "+v"(acc[3][2]), "+v"(acc[3][3]), "+v"(acc[3][4]), "+v"(acc[3][5]));
#pragma unroll
      for (int j = 0; j < 6; ++j) own[j] = (acc[3][j] + pr_[0][j]) + (pr_[1][j] + pr_[2][j]);
    }
    SB();
    {
      unsigned pk[6][2];
#pragma unroll
      for (int j = 0; j < 6; ++j) {
        const u32x2 z = __builtin_bit_cast(u32x2, z1r[j]);
#pragma unroll
        for (int h = 0; h < 2; ++h) pk[j][h] = pk2(unpk(z[h]) * (f32x2){own[j][2 * h], own[j][2 * h + 1]});
      }
      const unsigned eo = (unsigned)((l15 + 16 * (2 * (g4 & 1) + (g4 >> 1))) * 16);
#pragma unroll
      for (int q = 0; q < 3; ++q) {
        const auto s0 = __builtin_amdgcn_permlane16_swap(pk[2 * q][0], pk[2 * q + 1][0], false, false);
        const auto s1 = __builtin_amdgcn_permlane16_swap(pk[2 * q][1], pk[2 * q + 1][1], false, false);
        st16(eo, hf + (size_t)((rb * 4 + wave) * 24 + cb * 3 + q) * 1024, (u32x4){s0[0], s1[0], s0[1], nohole(s1[1])});
      }
    }
    signal1(flg + 96 + wave * 4);
    AB(17);
    ldW();
    wait_vm<15>(ef);
    gemm3(IC(0));
    if (b == 0 && tid == 0) *(u64*)(ws + WS_EP) = tag;
  }
  AB(24);
  AB_OUT();
}

extern "C" __global__ void k_end() {}

extern "C" void kernel_launch(void* const* d_in, const int* in_sizes, int n_in,
                              void* d_out, int out_size, void* d_ws, size_t ws_size,
                              hipStream_t stream) {
  (void)hipFuncSetAttribute((const void*)k_f, hipFuncAttributeMaxDynamicSharedMemorySize, M_LDS);
  k_f<<<256, 512, M_LDS, stream>>>((const u16*)d_in[0], (const u16*)d_in[1], (const u16*)d_in[2], (const u16*)d_in[3], (const u16*)d_in[4],
                                   (const u16*)d_in[5], (const u16*)d_in[7], (const u16*)d_in[8], (const u16*)d_in[9], (const u16*)d_in[10],
                                   (u16*)d_out, (char*)d_ws);
}
```

```cpp
#include <hip/hip_runtime.h>
#include <hip/hip_bf16.h>
#include <stdint.h>
#include <type_traits>

typedef unsigned short u16;
typedef unsigned long long u64;
typedef __attribute__((ext_vector_type(8))) short bf16x8;
typedef __attribute__((ext_vector_type(4))) short s16x4;
typedef __attribute__((ext_vector_type(4))) float f32x4;
typedef __attribute__((ext_vector_type(2))) float f32x2;
typedef __attribute__((ext_vector_type(4))) unsigned int u32x4;
typedef __attribute__((ext_vector_type(2))) unsigned int u32x2;
typedef __attribute__((ext_vector_type(2))) __bf16 bf16x2n;

#define LDS_AS __attribute__((address_space(3)))
#define IC(n) std::integral_constant<int, (n)>{}
#define SB() __builtin_amdgcn_sched_barrier(0)

#define AB_DECL do {} while (0)
#define AB(i) do {} while (0)
#define AB_OUT() do {} while (0)
#define CB(i) do {} while (0)
#define SS(i) do {} while (0)
constexpr int S_ = 2048, D_ = 640, FH_ = 768, F2_ = 1536;
constexpr unsigned HOLE = 0xFFFFFFFFu;

constexpr size_t WS_EP = 0;
constexpr size_t WS_EF = 4096;
constexpr size_t WS_LF = 22528;
constexpr size_t WS_YF = 14336;
constexpr size_t WS_XF = 20480;
constexpr size_t WS_ST = 32768;
constexpr size_t WS_Z2 = WS_ST + 32 * 12 * 1024;
constexpr size_t WS_H = WS_Z2 + 64 * 48 * 1024;
constexpr size_t WS_ZX = WS_H + 128 * 24 * 1024;
constexpr size_t WS_KV = WS_ZX + 32 * 4 * 4096;
constexpr size_t WS_Y = WS_KV + D_ * S_ * 2;
constexpr size_t WS_WO = WS_Y + D_ * S_ * 2;

__device__ __forceinline__ float bf2f(u16 v) { return __uint_as_float(((unsigned)v) << 16); }
__device__ __forceinline__ unsigned pack2(float lo, float hi) {
  f32x2 v = {lo, hi};
  bf16x2n r = __builtin_convertvector(v, bf16x2n);
  return __builtin_bit_cast(unsigned, r);
}
__device__ __forceinline__ float nv(float v) { asm("" : "+v"(v)); return v; }
__device__ __forceinline__ float fma1(float a, float b, float c) { float r; asm("v_fma_f32 %0, %1, %2, %3" : "=v"(r) : "v"(a), "v"(b), "v"(c)); return r; }
__device__ __forceinline__ float add1(float a, float b) { float r; asm("v_add_f32 %0, %1, %2" : "=v"(r) : "v"(a), "v"(b)); return r; }
__device__ __forceinline__ float mul1(float a, float b) { float r; asm("v_mul_f32 %0, %1, %2" : "=v"(r) : "v"(a), "v"(b)); return r; }
__device__ __forceinline__ unsigned pack2s(float lo, float hi) { return pack2(nv(lo), nv(hi)); }
__device__ __forceinline__ float lo16(unsigned w) { return __uint_as_float(w << 16); }
__device__ __forceinline__ float hi16(unsigned w) { return __uint_as_float(w & 0xFFFF0000u); }
__device__ __forceinline__ float rbf(float f) { return lo16(pack2(f, f)); }
__device__ __forceinline__ f32x2 unpk(unsigned w) { f32x2 r = {lo16(w), hi16(w)}; return r; }
__device__ __forceinline__ unsigned pk2(f32x2 v) { return pack2(v[0], v[1]); }
__device__ __forceinline__ unsigned umx(unsigned a, unsigned b) { return a > b ? a : b; }
__device__ __forceinline__ unsigned nohole(unsigned a) { return a < HOLE - 1 ? a : HOLE - 1; }
__device__ __forceinline__ f32x2 gelu2(f32x2 x) {
  const f32x2 c1 = {-2.3022082f, -2.3022082f}, c2 = {-0.10294324f, -0.10294324f};
  const f32x2 a = x * __builtin_elementwise_fma(x * x, c2, c1);
  f32x2 e = {__builtin_amdgcn_exp2f(a[0]), __builtin_amdgcn_exp2f(a[1])};
  e = e + (f32x2){1.0f, 1.0f};
  const f32x2 r = {__builtin_amdgcn_rcpf(e[0]), __builtin_amdgcn_rcpf(e[1])};
  return x * r;
}
__device__ __forceinline__ float gelu1(float x) {
  const float a = x * __builtin_fmaf(x * x, -0.10294324f, -2.3022082f);
  return x * __builtin_amdgcn_rcpf(__builtin_amdgcn_exp2f(a) + 1.0f);
}
template <int OFF>
__device__ __forceinline__ s16x4 tr_asm(unsigned a) {
  s16x4 r;
  asm volatile("ds_read_b64_tr_b16 %0, %1 offset:%2" : "=&v"(r) : "v"(a), "i"(OFF) : "memory");
  return r;
}
__device__ __forceinline__ bf16x8 join8(s16x4 lo, s16x4 hi) {
  bf16x8 r;
  r[0] = lo[0]; r[1] = lo[1]; r[2] = lo[2]; r[3] = lo[3];
  r[4] = hi[0]; r[5] = hi[1]; r[6] = hi[2]; r[7] = hi[3];
  return r;
}
__device__ __forceinline__ unsigned lds_addr(const void* p) { return (unsigned)(uintptr_t)(LDS_AS const char*)p; }
#define WAIT_LGKM0() asm volatile("s_waitcnt lgkmcnt(0)" ::: "memory")
__device__ __forceinline__ void flag_wait_asm(unsigned faddr, unsigned target) {
  unsigned v, sv;
  asm volatile(".Lfw_%=:\n\tds_read_b32 %0, %2\n\ts_waitcnt lgkmcnt(0)\n\tv_readfirstlane_b32 %1, %0\n\ts_nop 0\n\t"
               "s_cmp_ge_u32 %1, %3\n\ts_cbranch_scc1 .Lfd_%=\n\ts_sleep 1\n\ts_branch .Lfw_%=\n\t.Lfd_%=:"
               : "=&v"(v), "=&s"(sv) : "v"(faddr), "s"(target) : "memory", "scc");
}
__device__ __forceinline__ void signal1(unsigned a, unsigned one = 1u) {
  asm volatile("s_mov_b64 exec, 1\n\tds_add_u32 %0, %1\n\ts_mov_b64 exec, -1" :: "v"(a), "v"(one) : "memory");
}
__device__ __forceinline__ void dma16(unsigned vo, const void* sb, unsigned lds) {
  asm volatile("s_mov_b32 m0, %2\n\ts_nop 0\n\tglobal_load_lds_dwordx4 %0, %1" :: "v"(vo), "s"(sb), "s"(lds) : "memory");
}
template <int LO, int GO> __device__ __forceinline__ void dma32s(unsigned vo0, unsigned vo1, const void* sb, unsigned lds) {
  asm volatile("s_add_u32 m0, %5, %6\n\ts_add_u32 vcc_lo, %2, %4\n\ts_addc_u32 vcc_hi, %3, 0\n\t"
               "global_load_lds_dwordx4 %0, vcc\n\tglobal_load_lds_dwordx4 %1, vcc offset:1024"
               :: "v"(vo0), "v"(vo1), "s"((unsigned)(uintptr_t)sb), "s"((unsigned)((uintptr_t)sb >> 32)), "i"(GO), "s"(lds), "i"(LO) : "memory", "scc", "vcc");
}
template <int LO, int GO, int WRAP, int LIM> __device__ __forceinline__ void dma32r(unsigned vo0, unsigned vo1, const void* sb, unsigned lds, int rot) {
  const char* s2 = (const char*)sb - WRAP;
  asm volatile("s_add_u32 m0, %5, %6\n\ts_cmp_ge_u32 %7, %8\n\ts_cselect_b64 vcc, %3, %2\n\ts_add_u32 vcc_lo, vcc_lo, %4\n\ts_addc_u32 vcc_hi, vcc_hi, 0\n\t"
               "global_load_lds_dwordx4 %0, vcc\n\tglobal_load_lds_dwordx4 %1, vcc offset:1024"
               :: "v"(vo0), "v"(vo1), "s"(sb), "s"(s2), "i"(GO), "s"(lds), "i"(LO), "s"(rot), "i"(LIM) : "memory", "scc", "vcc");
}
__device__ __forceinline__ void dma16o(unsigned vo, const void* sb, int go, unsigned lds, int lo) {
  asm volatile("s_add_u32 m0, %3, %4\n\ts_nop 0\n\tglobal_load_lds_dwordx4 %0, %1 offset:%2" :: "v"(vo), "s"(sb), "i"(go), "s"(lds), "i"(lo - go) : "memory", "scc");
}
__device__ __forceinline__ void st16(unsigned vo, const void* sb, u32x4 v) {
  asm volatile("global_store_dwordx4 %0, %1, %2 sc1\n\ts_nop 1" :: "v"(vo), "v"(v), "s"(sb) : "memory");
}
__device__ __forceinline__ void touch(unsigned vo, const void* sb, unsigned lds) {
  asm volatile("s_mov_b32 m0, %2\n\ts_nop 0\n\tglobal_load_lds_dword %0, %1" :: "v"(vo), "s"(sb), "s"(lds) : "memory");
}
__device__ __forceinline__ void st8(unsigned vo, const void* sb, u32x2 v) { asm volatile("global_store_dwordx2 %0, %1, %2 sc1" :: "v"(vo), "v"(v), "s"(sb) : "memory"); }
template <class A, class B> __device__ __forceinline__ void MFMA(f32x4& acc, const A& a, const B& b) {
  asm volatile("v_mfma_f32_16x16x32_bf16 %0, %1, %2, %0" : "+v"(acc) : "v"(a), "v"(b));
}
template <class A, class B> __device__ __forceinline__ void MFMA0(f32x4& acc, const A& a, const B& b) {
  asm volatile("v_mfma_f32_16x16x32_bf16 %0, %1, %2, 0" : "=&v"(acc) : "v"(a), "v"(b));
}
template <bool Z, class A, class B> __device__ __forceinline__ void MFMAW(int n, f32x4& acc, const A& a, const B& b) {
  if (n < 0) { if (Z) MFMA0(acc, a, b); else MFMA(acc, a, b); }
  else if (Z) asm volatile("s_waitcnt lgkmcnt(%3)\n\tv_mfma_f32_16x16x32_bf16 %0, %1, %2, 0" : "=&v"(acc) : "v"(a), "v"(b), "i"(n));
  else asm volatile("s_waitcnt lgkmcnt(%3)\n\tv_mfma_f32_16x16x32_bf16 %0, %1, %2, %0" : "+v"(acc) : "v"(a), "v"(b), "i"(n));
}
__device__ __forceinline__ float sel4(float a, float b, float c, float d, u64 m1, u64 m2) {
  float r, t;
  asm volatile("v_cndmask_b32_e64 %0, %2, %3, %6\n\tv_cndmask_b32_e64 %1, %4, %5, %6\n\tv_cndmask_b32_e64 %0, %0, %1, %7"
               : "=&v"(r), "=&v"(t) : "v"(a), "v"(b), "v"(c), "v"(d), "s"(m1), "s"(m2));
  return r;
}
template <class T> __device__ __forceinline__ void pin1(T& r) { asm volatile("" : "+v"(r)); }
template <class T> __device__ __forceinline__ T opq(T r) { asm volatile("" : "+v"(r)); return r; }
template <class... T> __device__ __forceinline__ void pin(T&... r) { (pin1(r), ...); }
template <int N, class... T> __device__ __forceinline__ void wait_vm(T&... r) { asm volatile("s_waitcnt vmcnt(%0)" :: "n"(N) : "memory"); (pin1(r), ...); }
template <int N, class... T> __device__ __forceinline__ void wait_lg(T&... r) { asm volatile("s_waitcnt lgkmcnt(%0)" :: "n"(N) : "memory"); (pin1(r), ...); }
template <int OFF, class T> __device__ __forceinline__ void ds16(T& d, unsigned a) {
  asm volatile("ds_read_b128 %0, %1 offset:%2" : "=&v"(d) : "v"(a), "i"(OFF) : "memory");
}
template <int OFF> __device__ __forceinline__ void dsw16(unsigned a, const f32x4& v) {
  asm volatile("ds_write_b128 %0, %1 offset:%2" :: "v"(a), "v"(v), "i"(OFF) : "memory");
}
__device__ __forceinline__ void dsw4(unsigned a, float v) { asm volatile("ds_write_b32 %0, %1" :: "v"(a), "v"(v) : "memory"); }
#define OSC ""
#define HSC ""
template <int OFF, class T> __device__ __forceinline__ void ld16h(T& d, unsigned vo, const void* sb) {
  asm volatile("global_load_dwordx4 %0, %1, %2 offset:%3 " HSC : "=&v"(d) : "v"(vo), "s"(sb), "i"(OFF) : "memory");
}
template <class T> __device__ __forceinline__ void reld6(T (&B)[6], unsigned vo, const void* sb) {
  asm volatile("global_load_dwordx4 %0, %6, %7 offset:-2048 sc1\n\tglobal_load_dwordx4 %1, %6, %7 offset:-1024 sc1\n\t"
               "global_load_dwordx4 %2, %6, %7 sc1\n\tglobal_load_dwordx4 %3, %6, %7 offset:1024 sc1\n\t"
               "global_load_dwordx4 %4, %6, %7 offset:2048 sc1\n\tglobal_load_dwordx4 %5, %6, %7 offset:3072 sc1"
               : "+v"(B[0]), "+v"(B[1]), "+v"(B[2]), "+v"(B[3]), "+v"(B[4]), "+v"(B[5]) : "v"(vo), "s"(sb) : "memory");
}

constexpr int L_FLAG = 0;
constexpr int L_DUM = 256;
constexpr int L_XM = 512;
constexpr int L_XR = 768;
constexpr int L_CS = 1024;
constexpr int L_ZS = 2048;
constexpr int L_CS2 = 3072;
constexpr int L_Z1 = 4096;
constexpr int L_RAW = L_Z1;
constexpr int L_SLOT = 16384;
constexpr int L_KV = 512, KV_P = 3088, L_WO = L_KV + 8 * KV_P;
static_assert(L_WO % 16 == 0 && L_WO + 4 * 1024 <= L_SLOT + 2 * 8192, "staging");
constexpr int L_P2 = L_SLOT + 12 * 8192;
constexpr int CV_C = 4608, CV_CS = 4160, CV_BUF = CV_C + 2 * CV_CS;
constexpr int M_CV = L_SLOT;
constexpr int M_RING = L_SLOT + 6 * 8192;
constexpr int P3_G = L_SLOT;
constexpr int P3_C = P3_G + 80 * 1024;
constexpr int yat(int j) { return M_CV + (j >> 1) * CV_BUF + 8192 + (j & 1) * 2048; }
constexpr int M_LDS = 163840;
static_assert(3 * CV_BUF + 8192 <= 6 * 8192 && P3_C + 60 * 1024 <= M_LDS && CV_BUF >= 8192 + 4096, "LDS map");

enum { UX, UA, UB, UV };
struct Unit { int kind, idx, need, free, slot; };
#define SCHED gugugugwBuluouk_uuxuu_uuxuxu_uuxuxuu_Euuuuh_vuumumuh_vuumumuhF_vuvumum_DRvuuUHmmmmSptt_VhhhttttttJItttttttt
#define STR_(x) #x
#define STR(x) STR_(x)
constexpr char SCH[] = STR(SCHED);
constexpr int NSCH = sizeof(SCH) - 1;
constexpr int XNEED[10] = {1, 2, 2, 3, 3, 4, 4, 5, 5, 6};
constexpr int VNEED[5] = {5, 6, 7, 7, 8};
constexpr int XSLOT[10] = {17, 16, 15, 13, 10, 14, 11, 17, 9, 16}, WSLOT[10] = {6, 2, 4, 0, 6, 12, 10, 4, 0, 8}, VSLOT[10] = {8, 15, 3, 2, 14, 17, 16, 15, 7, 6}, XW = 6;
constexpr int cnt(char c, int e = NSCH) { int n = 0; for (int i = 0; i < e; ++i) n += SCH[i] == c || (c == 'u' && SCH[i] == 'g'); return n; }
constexpr int pos(char c, int k = 0) { for (int i = 0; i < NSCH; ++i) if (SCH[i] == c && k-- == 0) return i; return NSCH; }
constexpr Unit unit(int n) {
  for (int b = 1; b < 11; ++b) {
    for (int c = 0; c < 10; ++c) if (XNEED[c] == b && n-- == 0) return {UX, c, b, b + 1, XSLOT[c]};
    for (int c = 0; c < 5; ++c) if (VNEED[c] == b && n-- == 0) return {UV, c, b, b + 1, VSLOT[c]};
    if (n < 2) return {n ? UB : UA, b - 1, b < 8 ? b : 8, b < 7 ? b + 2 : 9, WSLOT[b - 1] + n};
    n -= 2;
  }
  return {UV, n + 5, 10, 99, VSLOT[n + 5]};
}
constexpr int FLAGB = cnt('_', pos('D')), GZ = cnt('_', pos('E'));
constexpr int nvm(char c) { return c == 'u' ? 2 : (c == 'H' ? 5 : (c == 'U' ? 4 : (c == 'F' || c == 'h' || c == 't' || c == 'w'))); }
constexpr int behind(int g, int e, char w) {
  const int b = cnt('_', e) + 1;
  int n = 0;
  for (int i = 0; i < e; ++i) {
    const char c = SCH[i];
    if (g == 0) { if (c == 'g') n = w == '_' && unit(cnt('u', i)).need <= b ? 0 : n + 2; if (c == 'B') n += 2; continue; }
    n = (w == '_' ? (c == 'u' && unit(cnt('u', i)).need <= b) || (c == 'h' && b == FLAGB) || (c == 'k' && b == GZ) : c == w) ? 0 : n + nvm(c);
  }
  return g == 0 && b >= GZ ? 0 : n;
}
constexpr bool plan_ok() {
  if (cnt('u') != 40 || cnt('_') != 9 || cnt('x') != 5 || cnt('h') != 6 || cnt('t') != 16 || cnt('I') * cnt('J') * cnt('p') != 1 || cnt('v') != 5 || cnt('m') != 10) return false;
  if (cnt('B') * cnt('D') * cnt('E') * cnt('F') * cnt('l') * cnt('o') * cnt('k') * cnt('H') * cnt('U') * cnt('V') * cnt('R') * cnt('S') != 1) return false;
  if (pos('l') < pos('B') || pos('o') < pos('B') || pos('k') < pos('l') || pos('k') > pos('_') || pos('o') > pos('_') || cnt('g', pos('B')) != cnt('g')) return false;
  if (cnt('_', pos('h', 2)) >= FLAGB || GZ < 3 || GZ > 6 || FLAGB < GZ || FLAGB > 9 || pos('o') > pos('k') || pos('H') > pos('I') || pos('F') > pos('U')) return false;
  if (cnt('_', pos('R')) != 8 || cnt('_', pos('S')) != 8 || pos('S') < pos('R') || cnt('_', pos('I')) != 9 || pos('J') < pos('U') || cnt('_', pos('J')) != 9) return false;
  if (cnt('_', pos('V')) != 9 || cnt('u', pos('V')) < 40) return false;
  for (int c = 0; c < 5; ++c) if (cnt('_', pos('x', c)) != XNEED[c] || cnt('_', pos('v', c)) != VNEED[c] || pos('m', 2 * c) < pos('v', c)) return false;
  if (cnt('_', pos('p')) != 8 || pos('p') < pos('m', 9)) return false;
  int fr[18] = {1, 1};
  for (int i = 0; i < NSCH; ++i) if (SCH[i] == 'u' || SCH[i] == 'g') {
    const Unit u = unit(cnt('u', i));
    const int p = cnt('_', i);
    if (p < fr[u.slot] || p >= u.need) return false;
    fr[u.slot] = u.free;
  }
  return true;
}
static_assert(plan_ok(), "phase A plan");
template <int I, int E, class F> __device__ __forceinline__ void sfor(F&& f) { if constexpr (I < E) { f(IC(I)); sfor<I + 1, E>(f); } }

#define CVP -1
__device__ __forceinline__ void conv_core(unsigned buf, int lane, unsigned Dw, f32x4 (&ac)[8], unsigned pf) {
  const int l15 = lane & 15, g4 = lane >> 4;
  const int c = (l15 + 1) & 1;
  unsigned aa[4];
#pragma unroll
  for (int k = 0; k < 4; ++k) aa[k] = buf + CV_C + c * CV_CS + (2031 - l15 + 8 * g4 - c) * 2 - 512 - 1024 * k;
  unsigned bb = buf + 512 + 32 * l15 - 32 + 16 * g4;
  u32x2 A[2][8][2];
  u32x4 B[2][8];
  constexpr int TT[36] = {7, 6, 5, 4, 7, 6, 5, 4, 7, 6, 5, 3, 7, 6, 5, 4, 7, 6, 3, 2, 7, 6, 5, 4, 7, 3, 2, 1, 7, 6, 5, 4, 3, 2, 1, 0};
  constexpr int AA[36] = {0, 0, 0, 0, 1, 1, 1, 1, 2, 2, 2, 0, 3, 3, 3, 2, 4, 4, 1, 0, 5, 5, 4, 3, 6, 2, 1, 0, 7, 6, 5, 4, 3, 2, 1, 0};
  auto ld1 = [&](int p, int n) {
    if (n < 16) {
      const int a = n >> 1, o = ((a & 1) ? 0 : 128) + 2 * (n & 1);
      asm volatile("ds_read2_b32 %0, %1 offset0:%2 offset1:%3" : "=&v"(A[p][(n >> 1) & 7][n & 1]) : "v"(aa[(a >> 1) & 3]), "i"(o), "i"(o + 1) : "memory");
    } else
      asm volatile("ds_read_b128 %0, %1 offset:%2" : "=&v"(B[p][n & 7]) : "v"(bb), "i"(512 * (n & 7)) : "memory");
  };
  auto next = [&](int dl) {
#pragma unroll
    for (int k = 0; k < 4; ++k) aa[k] += dl;
    bb += dl;
    SB();
  };
  auto land = [&](int p) {
    asm volatile("s_waitcnt lgkmcnt(0)" : "+v"(A[p][0][0]), "+v"(A[p][0][1]), "+v"(A[p][1][0]), "+v"(A[p][1][1]), "+v"(A[p][2][0]), "+v"(A[p][2][1]),
                 "+v"(A[p][3][0]), "+v"(A[p][3][1]), "+v"(A[p][4][0]), "+v"(A[p][4][1]), "+v"(A[p][5][0]), "+v"(A[p][5][1]), "+v"(A[p][6][0]), "+v"(A[p][6][1]),
                 "+v"(A[p][7][0]), "+v"(A[p][7][1]), "+v"(B[p][0]), "+v"(B[p][1]), "+v"(B[p][2]), "+v"(B[p][3]), "+v"(B[p][4]), "+v"(B[p][5]),
                 "+v"(B[p][6]), "+v"(B[p][7]) :: "memory");
    SB();
  };
  auto mm = [&](int p) {
#pragma unroll
    for (int n = 0; n < 36; ++n) {
      const int t = TT[n], a = AA[n];
      const u32x4 av = {A[p][a][0][0], A[p][a][0][1], A[p][a][1][0], A[p][a][1][1]};
      MFMA(ac[t], av, B[p][t - a]);
      if (n < 24) ld1(p ^ 1, n);
      SB();
    }
    land(p ^ 1);
  };
#pragma unroll
  for (int n = 0; n < 24; ++n) ld1(0, n);
  u32x4 ad;
#pragma unroll
  for (int e = 0; e < 4; ++e) ad[e] = lane == 32 + 2 * e + (lane & 1) + (lane & 8) * 3 ? Dw << (16 * (lane & 1)) : 0u;
  pin(ad);
  land(0);
#pragma unroll
  for (int t = 0; t < 8; ++t) MFMA0(ac[t], ad, B[0][t]);
#pragma unroll 1
  for (int r = 0; r < 8; r += 2) {
    if (CVP >= 0 && !(CVP & 1)) flag_wait_asm(pf, r == CVP ? 4u : 0u);
    next(-64);
    mm(0);
    if (CVP >= 0 && (CVP & 1)) flag_wait_asm(pf, r + 1 == CVP ? 4u : 0u);
    next(r < 6 ? -64 : 0);
    mm(1);
  }
  asm volatile("s_nop 15" : "+v"(ac[0]), "+v"(ac[1]), "+v"(ac[2]), "+v"(ac[3]), "+v"(ac[4]), "+v"(ac[5]), "+v"(ac[6]), "+v"(ac[7]));
}

#define ROTA 3
#define RA1 1
#define RA2 0
#define RA3 0
#define RB1 0
#define RB2 4
#define RB3 0
#define G2P 0
#define RB4 2
#define THR 24
#define RB5 2
#define YS 8
#define WO3 0
#define C3P 0
#define FE3 9
#define WSH 1
#define CPR 1
#define STW 1
#define PN 10
#define P2P 2
#define SGS 11
#define ANUM {0, 0, 0, 0, 0, 3, 3, 2, 3, 3, 2, 0, 0, 0, 0, 0}
constexpr int cum16(const int (&a)[16], int n) { int c = 0; for (int i = 0; i < n; ++i) c += a[i]; return c; }

__global__ __launch_bounds__(512) void k_f(
    const u16* __restrict__ x, const u16* __restrict__ kin, const u16* __restrict__ vin, const u16* __restrict__ shift_k,
    const u16* __restrict__ ssm_k, const u16* __restrict__ ssm_D, const u16* __restrict__ w_in, const u16* __restrict__ w_sp,
    const u16* __restrict__ b_sp, const u16* __restrict__ w_out, u16* __restrict__ out, char* ws) {
  extern __shared__ __attribute__((aligned(16))) char smem[];
  const int tid = threadIdx.x, lane = tid & 63, wave = __builtin_amdgcn_readfirstlane(tid >> 6);
  const int b = blockIdx.x;
  const int xcd = b & 7, idx = b >> 3;
  const int rb = 8 * (xcd >> 1) + (idx >> 2), cb = 4 * (xcd & 1) + (idx & 3);
  const int t0 = rb * 64, d0 = cb * 80;
  const int g4 = lane >> 4, l15 = lane & 15;
  const unsigned lin = (unsigned)(lane * 16);
  const unsigned lb = lds_addr(smem), flg = lb + L_FLAG;
  AB_DECL;
  AB(0);
  asm volatile("" :: "s"(x), "s"(kin), "s"(vin), "s"(shift_k), "s"(ssm_k), "s"(ssm_D), "s"(w_in), "s"(w_sp), "s"(b_sp), "s"(w_out), "s"(out), "s"(ws));
  u64 tag = *(const __attribute__((address_space(4))) u64*)(uintptr_t)ws;
  int tmo = 1 << 16;
#define CWARM 1
  auto cwarm = [&]() {
    u64 pc;
    unsigned clen;
    asm volatile("s_getpc_b64 %0\n.Lpc_here_%=:\n\ts_mov_b32 %1, k_end-.Lpc_here_%=" : "=s"(pc), "=s"(clen));
    touch(min((unsigned)(idx * 1536 + min(lane, 11) * 128), clen - 4u) & ~3u, (const void*)pc, lb + L_DUM);
  };

  const int cw = wave & 3, wm = wave & 1, wl = (wave >> 1) & 1, ksb = wm ^ wl, odd = cb & 1, pr = cb >> 1;
  const int r8 = lane >> 3, c8 = lane & 7;
  unsigned voX[2], voW[2];
#pragma unroll
  for (int h = 0; h < 2; ++h) {
    voX[h] = (unsigned)((h * 8 + r8) * (D_ * 2) + ((c8 ^ (h * 4 + (r8 & 3))) << 4) - h * 1024);
    voW[h] = (unsigned)((h * 8 + r8) * (F2_ * 2) + ((c8 ^ (h * 4 + (r8 & 2))) << 4) - h * 1024);
  }
  const int rot = (RA1 * ((rb & 7) * 5 >> 2) + ROTA * (cb & 3) + RA2 * ((rb >> 3) * 5 >> 1) + RA3 * (cb >> 2)) % 10, rot2 = (RB1 * (cb & 3) + RB2 * (cb >> 2) + RB3 * (rb & 7) + RB4 * (rb >> 3) + RB5 * (cb & 1)) & 7;
  const char* gX = (const char*)x + (size_t)(t0 + cw * 16) * (D_ * 2) + rot * 128;
  const char* gW = (const char*)w_in + (size_t)(cw * 16 + rot * 64) * (F2_ * 2) + pr * 384;
  const char *gV = gW + odd * 256, *gB = gW + (odd ? 128 : FH_ * 2 + 128);
  const char* gS = (const char*)w_sp + (size_t)t0 * (S_ * 2);
  const unsigned aoff[2] = {(unsigned)(r8 * 4096 + ((c8 ^ r8) << 4)), (unsigned)(r8 * 4096 + ((c8 ^ r8) << 4) + 32768)};
  const unsigned ldw = lb + cw * 2048;
  auto issue = [&](auto nc) {
    constexpr Unit u = unit(decltype(nc)::value);
    constexpr int lo = L_SLOT + u.slot * 8192;
    if (u.kind == UX) dma32r<lo, u.idx * 128, 1280, 10 - u.idx>(voX[0], voX[1], gX, ldw, rot);
    else dma32r<lo, u.idx * (64 * F2_ * 2) + (u.kind == UA ? FH_ * 2 : 0), 640 * F2_ * 2, 10 - u.idx>(voW[0], voW[1], u.kind == UB ? gB : gV, ldw, rot);
    SB();
  };
  const int kd = d0 + min(wave, 4) * 16 + r8 * 2;
  u32x4 sw;
  {
    const int ks0 = t0 + wave * 8;
#pragma unroll
    for (int i = 0; i < 3; ++i) {
      const int id = min(i * 64 + lane, 189), r = id / 10, c = id % 10;
      const char* kp = (r < 11 ? (const char*)kin + (size_t)max(ks0 - 3 + r, 0) * (D_ * 2) : (const char*)vin + (size_t)(ks0 + r - 11) * (D_ * 2)) + cb * 160 + c * 16;
      asm volatile("s_mov_b32 m0, %1\n\ts_nop 0\n\tglobal_load_lds_dwordx4 %0, off" :: "v"(kp), "s"(lb + L_KV + wave * KV_P + i * 1024) : "memory");
    }
    if (CWARM && wave == 5) cwarm();
    SB();
  }
  auto ldsw = [&]() { asm volatile("global_load_dwordx4 %0, %1, %2" : "=&v"(sw) : "v"(kd * 8), "s"(shift_k) : "memory"); SB(); };
  unsigned kk[11], vv[8];
  u32x4 oa, ob;
  auto kvld = [&]() {
    const char* kl = smem + L_KV + c8 * KV_P + min(wave, 4) * 32 + r8 * 4;
#pragma unroll
    for (int i = 0; i < 11; ++i) kk[i] = *(const unsigned*)(kl + i * 160);
#pragma unroll
    for (int i = 0; i < 8; ++i) vv[i] = *(const unsigned*)(kl + (11 + i) * 160);
  };
  auto kvm = [&](int e0, int e1) {
    const f32x2 w0 = {lo16(sw[0]), lo16(sw[2])}, w1 = {hi16(sw[0]), hi16(sw[2])};
    const f32x2 w2 = {lo16(sw[1]), lo16(sw[3])}, w3 = {hi16(sw[1]), hi16(sw[3])};
    f32x2 kf[11], rr[8];
#pragma unroll
    for (int i = 2 * e0; i < 2 * e1 + 3; ++i) kf[i] = unpk((i < 3 && t0 + c8 == 0) ? 0u : kk[i]);
#pragma unroll
    for (int i = 2 * e0; i < 2 * e1; ++i) {
      f32x2 a0 = kf[i] * w3;
      a0 = __builtin_elementwise_fma(kf[i + 1], w2, a0);
      a0 = __builtin_elementwise_fma(kf[i + 2], w1, a0);
      a0 = __builtin_elementwise_fma(kf[i + 3], w0, a0);
      rr[i] = a0 * unpk(vv[i]);
    }
#pragma unroll
    for (int e = e0; e < e1; ++e) { oa[e] = pack2(rr[2 * e][0], rr[2 * e + 1][0]); ob[e] = pack2(rr[2 * e][1], rr[2 * e + 1][1]); }
  };
  auto kvst = [&]() {
    const unsigned ko = (unsigned)((kd * S_ + t0 + c8 * 8) * 2);
    st16(ko, ws + WS_KV, oa);
    st16(ko, ws + WS_KV + S_ * 2, ob);
    SB();
  };
  auto barrier0 = [&](int nv) {
    SB();
    asm volatile("s_waitcnt vmcnt(%0) lgkmcnt(0)\n\ts_barrier" :: "n"(nv) : "memory");
    SB();
    asm volatile("" : "+v"(sw), "+s"(tag));
    tag += 1;
    asm volatile("" : "+s"(tag));
  };
  unsigned pX[2], pW[2];
#pragma unroll
  for (int k = 0; k < 2; ++k) {
    const int q = l15 >> 2, p = lane & 3, sz = (q >> 1) | ((g4 & 1) << 1);
    pX[k] = lb + (wm * 32 + q * 8 + p) * 128 + ((((k ^ ksb) * 4 + g4) ^ (l15 & 7)) << 4);
    pW[k] = lb + ((k ^ ksb) * 32 + g4 * 8 + q) * 128 + (((sz << 1) | (p >> 1)) << 4) + (p & 1) * 8;
  }
  bf16x8 ones;
#pragma unroll
  for (int e = 0; e < 8; ++e) ones[e] = (short)0x3F80;
  f32x4 xm[2], xr[2];
  auto ldxs = [&]() {
#pragma unroll
    for (int i = 0; i < 2; ++i) {
      xm[i] = *(const f32x4*)(smem + L_XM + (wm * 32 + g4 * 8 + i * 4) * 4);
      xr[i] = *(const f32x4*)(smem + L_XR + (wm * 32 + g4 * 8 + i * 4) * 4);
    }
  };
  auto act = [&](const f32x4& a, float c, int i) {
    const f32x2 nc = {-c, -c};
    const f32x2 lo = gelu2(__builtin_elementwise_fma((f32x2){xm[i][0], xm[i][1]}, nc, (f32x2){a[0], a[1]}) * (f32x2){xr[i][0], xr[i][1]});
    const f32x2 hi = gelu2(__builtin_elementwise_fma((f32x2){xm[i][2], xm[i][3]}, nc, (f32x2){a[2], a[3]}) * (f32x2){xr[i][2], xr[i][3]});
    return (f32x4){lo[0], lo[1], hi[0], hi[1]};
  };
  auto pack8 = [&](const f32x4& a, const f32x4& c) { return (u32x4){pack2(a[0], a[1]), pack2(a[2], a[3]), pack2(c[0], c[1]), pack2(c[2], c[3])}; };
  auto z1at = [&](int f0) { return (u32x4*)(smem + L_Z1 + (f0 + l15) * 128 + (((wm * 4 + g4) ^ (l15 & 7)) << 4)); };
  auto z1put = [&](int f0, const u32x4& v) { *z1at(f0) = v; };
  f32x4 ac2[2][2], cs2[2];
  s16x4 VF[10][2][2];
  unsigned aV[2][2];
  auto rdV = [&](auto hc_, auto nc, int r) {
    constexpr int n = decltype(nc)::value, c = 5 * decltype(hc_)::value + (n >> 1), so = (VSLOT[c] - 2) * 8192, po = n < 2 ? -1 : ((VSLOT[n < 2 ? c : c - 1] - 2) * 8192) >> 16;
    const int jj = r >> 1;
    if (!(r & 1)) {
      if ((so >> 16) != po) aV[n & 1][jj] = opq((pW[n & 1] ^ (unsigned)(wl * 64 + jj * 32)) + L_SLOT + 2 * 8192 + (so & ~0xFFFF));
      VF[n][jj][0] = tr_asm<(so & 0xFFFF)>(aV[n & 1][jj]);
    } else VF[n][jj][1] = tr_asm<(so & 0xFFFF) + 512>(aV[n & 1][jj]);
    SB();
  };
  auto mm2 = [&](auto hc_, auto nc, bf16x8 (*X)[2][2], int nw, auto&& nxt) {
    constexpr int n = decltype(nc)::value, j = n >> 1, k = n & 1;
    constexpr bool z = n == 0 && !decltype(hc_)::value;
    const bf16x8 w0 = join8(VF[n][0][0], VF[n][0][1]), w1 = join8(VF[n][1][0], VF[n][1][1]);
    if (k == 0) { MFMAW<n == 0>(nw, cs2[0], ones, w0); MFMAW<n == 0>(-1, cs2[1], ones, w1); }
    MFMAW<z>(k ? nw : -1, ac2[0][0], X[j][k][0], w0); nxt(0);
    MFMAW<z>(-1, ac2[1][0], X[j][k][1], w0); nxt(1);
    MFMAW<z>(-1, ac2[0][1], X[j][k][0], w1); nxt(2);
    MFMAW<z>(-1, ac2[1][1], X[j][k][1], w1); nxt(3);
    if constexpr (n == 9) {
      pin(cs2[0], cs2[1]);
      if (g4 < 2) *(float*)(smem + L_CS2 + (wave * 32 + lane) * 4) = g4 ? cs2[1][0] : cs2[0][0];
    }
    SB();
  };
  const unsigned p2a = lb + L_P2 + cw * 4096 + lin;
  auto sttag = [&](const char* p) {
    asm volatile("s_mov_b64 exec, 1\n\tglobal_store_dwordx2 %0, %1, %2 sc1\n\ts_mov_b64 exec, -1" :: "v"(lin), "v"((u32x2){opq((unsigned)tag), opq((unsigned)(tag >> 32))}), "s"(p) : "memory");
  };
  auto ldtag = [&](u32x2& f, unsigned vo, const char* p) { asm volatile("global_load_dwordx2 %0, %1, %2 sc1" : "=&v"(f) : "v"(vo), "s"(p) : "memory"); };
  auto notag = [&](const u32x2& f) { return __builtin_amdgcn_ballot_w64(f[0] != (unsigned)tag || f[1] != (unsigned)(tag >> 32)) != 0; };
  auto waittag = [&](u32x2& f, unsigned vo, const char* p) {
    while (__builtin_expect(notag(f) && tmo > 0, 0)) {
      --tmo;
      __builtin_amdgcn_s_sleep(4);
      ldtag(f, vo, p);
      wait_vm<0>(f);
    }
  };
  u32x2 ef;

  char* const hf = ws + WS_H;
  bf16x8 Wq[15];
  const char* wg = ws + WS_WO + (size_t)(wave * 120 + cb * 5) * 1024;
  auto ldW = [&]() {
    ldtag(ef, (unsigned)((lane & 3) * 8), ws + WS_LF + (rb * 8 + wave) * 32);
#pragma unroll
    for (int n = 0; n < 15; ++n)
      asm volatile("global_load_dwordx4 %0, %1, %2 offset:%3 sc1" : "=&v"(Wq[n]) : "v"(lin), "s"(wg + (n / 5) * 40960 + 2048), "i"((n % 5) * 1024 - 2048) : "memory");
    SB();
  };
  f32x4 ac3[5];
  auto gemm3 = [&](auto khc) {
    constexpr int kh = decltype(khc)::value;
    unsigned hoff[4];
#pragma unroll
    for (int it = 0; it < 4; ++it) hoff[it] = (unsigned)(((rb * 4 + ((cw + 1 + it) & 3)) * 24 + 3 * wave) * 1024) + lin;
    u32x4 Hq[4][3];
    auto ldh1 = [&](int it, int k) {
      asm volatile("global_load_dwordx4 %0, %1, %2 offset:%3 " HSC : "=&v"(Hq[it][k]) : "v"(hoff[it]), "s"(hf), "i"(k * 1024) : "memory");
    };
    waittag(ef, (unsigned)((lane & 3) * 8), ws + WS_LF + (rb * 8 + wave) * 32);
    AB(18);
    signal1(flg + 48);
    flag_wait_asm(flg + 48, 8u);
    AB(19);
    __builtin_amdgcn_s_setprio(3);
    ldh1(0, 0); ldh1(0, 1); ldh1(0, 2);
    __builtin_amdgcn_s_setprio(2);
    ldh1(1, 0); ldh1(1, 1); ldh1(1, 2);
    __builtin_amdgcn_s_setprio(kh * C3P);
    SB();
    const unsigned px = lb + (kh ? P3_C + cw * 15360 : P3_G + cw * 20480) + lin;
    f32x4 pa[5][5], ae[5];
    auto put = [&](int it, int j) { asm volatile("ds_write_b128 %0, %1 offset:%2" :: "v"(px), "v"((it & 1) ? ac3[j] : ae[j]), "i"((it * 5 + j) * 1024) : "memory"); };
    auto fetch = [&](int n, int g, int q) {
      const int ms = (cw - 1 - q) & 3;
      const unsigned pn = lb + (g ? P3_C + ms * 15360 : P3_G + ms * 20480) + lin;
#pragma unroll
      for (int j = 0; j < 5; ++j) asm volatile("ds_read_b128 %0, %1 offset:%2" : "=&v"(pa[n][j]) : "v"(pn), "i"((q * 5 + j) * 1024) : "memory");
      SB();
    };
    sfor<0, 4>([&](auto itc) {
      constexpr int it = decltype(itc)::value;
      wait_vm<(it < 3 ? 3 : 0)>(Hq[it][0], Hq[it][1], Hq[it][2]);
      if constexpr (it == 0) pin(Wq[0], Wq[1], Wq[2], Wq[3], Wq[4], Wq[5], Wq[6], Wq[7], Wq[8], Wq[9], Wq[10], Wq[11], Wq[12], Wq[13], Wq[14]);
      auto bad = [&]() { return __builtin_amdgcn_ballot_w64(umx(umx(Hq[it][0][3], Hq[it][1][3]), Hq[it][2][3]) == HOLE) != 0; };
      if (__builtin_expect(bad(), 0)) {
        do {
          --tmo;
          asm volatile("s_sleep 2\n\tglobal_load_dwordx4 %0, %3, %4 sc1\n\tglobal_load_dwordx4 %1, %3, %4 offset:1024 sc1\n\t"
                       "global_load_dwordx4 %2, %3, %4 offset:2048 sc1\n\ts_waitcnt vmcnt(0)"
                       : "+v"(Hq[it][0]), "+v"(Hq[it][1]), "+v"(Hq[it][2]) : "v"(hoff[it]), "s"(hf) : "memory");
        } while (bad() && tmo > 0);
      }
      SB();
#pragma unroll
      for (int k = 0; k < 3; ++k)
#pragma unroll
        for (int j = 0; j < 5; ++j) {
          f32x4& ac = (it & 1) ? ac3[j] : ae[j];
          if (k == 0) MFMA0(ac, Wq[j], Hq[it][0]); else MFMA(ac, Wq[k * 5 + j], Hq[it][k]);
          if constexpr (it + 2 < 4) { if (k == 0 && j < 3) ldh1(it + 2 < 4 ? it + 2 : 0, j); }
          const int m = k * 5 + j - WO3;
          if (it > 0 && m >= 0 && m < 10 && !(m & 1)) put(it - 1, m >> 1);
          if (it > 0 && m == 8) signal1(flg + 160 + (it < 3 ? kh : 2) * 16 + ((cw + it) & 3) * 4);
          if (it == 3 && m == FE3) { flag_wait_asm(flg + 160 + wave * 4, 2u); fetch(0, kh, 0); fetch(1, kh, 1); }
          SB();
        }
      if constexpr (it == 3) {
        if (kh) {
          flag_wait_asm(flg + 192 + cw * 4, 3u);
          fetch(2, 1, 2); fetch(3, 0, 2); fetch(4, 0, 3);
        } else asm volatile("s_waitcnt lgkmcnt(0)\n\ts_nop 4" ::: "memory");
        pin(pa[0][0], pa[0][1], pa[0][2], pa[0][3], pa[0][4], pa[1][0], pa[1][1], pa[1][2], pa[1][3], pa[1][4]);
#pragma unroll
        for (int j = 0; j < 5; ++j) {
          pa[0][j] += pa[1][j];
          pin(pa[0][j], ac3[j]);
          ac3[j] += pa[0][j];
          if (!kh) put(3, j);
        }
        if (kh) {
          wait_lg<5>(pa[2][0], pa[2][1], pa[2][2], pa[2][3], pa[2][4], pa[3][0], pa[3][1], pa[3][2], pa[3][3], pa[3][4]);
#pragma unroll
          for (int j = 0; j < 5; ++j) ac3[j] += pa[2][j] + pa[3][j];
          wait_lg<0>(pa[4][0], pa[4][1], pa[4][2], pa[4][3], pa[4][4]);
#pragma unroll
          for (int j = 0; j < 5; ++j) ac3[j] += pa[4][j];
        } else signal1(flg + 192 + cw * 4);
      }
      SB();
      if constexpr (it == 0) AB(20);
      if constexpr (it == 2) AB(21);
    });
    AB(22);
  };
  if (wave >= 4) {
    const int wid = rb * 4 + cw;
    __builtin_amdgcn_s_setprio(3);
    ldsw();
    if (wid < 120) dma16((unsigned)((lane >> 1) * (D_ * 2) + (lane & 1) * 16), w_out + (size_t)(wid / 5) * 32 * D_ + d0 + (wid % 5) * 16, lb + L_WO + cw * 1024);
    SB();
    u32x4 hole = {HOLE, HOLE, HOLE, HOLE};
    bf16x8 XF[5][2][2];
    auto rdXF = [&](auto cc) {
      constexpr int c = decltype(cc)::value, so = L_SLOT + XSLOT[c] * 8192;
#pragma unroll
      for (int k = 0; k < 2; ++k) {
        const unsigned a = pX[k] + (so & ~0x7FFF);
        ds16<(so & 0x7FFF)>(XF[c][k][0], a);
        ds16<(so & 0x7FFF) + 512>(XF[c][k][1], a);
      }
      SB();
    };
    u32x4 TA[16];
    auto ldT = [&](u32x4& r, int d, int w, int i) {
      asm volatile("global_load_dwordx4 %0, %1, %2" : "=&v"(r) : "v"(aoff[i & 1] + (unsigned)(((d + rot2) & 7) * 128)), "s"(gS + ((w + 1 + (i >> 1)) & 3) * 65536 + w * 1024) : "memory");
    };
    auto ldx = [&](int c) {
      u32x4 xv;
      asm volatile("global_load_dwordx4 %0, %1, %2\n\ts_waitcnt vmcnt(0)" : "=&v"(xv) : "v"((unsigned)(lane * (D_ * 2) + c * 16)), "s"(x + (size_t)t0 * D_) : "memory");
      return xv;
    };
    const bool docv = cw < ((rb & 1) ? 2 : 3);
    const int d = d0 + (cw < 2 ? 2 * rb + cw : 64 + (rb >> 1));
    u32x4 hc[4], uc[4];
    unsigned Dw;
    auto ldhc = [&]() {
#pragma unroll
      for (int k = 0; k < 4; ++k) asm volatile("global_load_dwordx4 %0, %1, %2 offset:%3" : "=&v"(hc[k]) : "v"(lin), "s"(ssm_k + (size_t)d * S_), "i"(k * 1024) : "memory");
      asm volatile("global_load_ushort %0, %1, %2" : "=&v"(Dw) : "v"(0u), "s"(ssm_D + d) : "memory");
    };
    unsigned rwo;
    f32x2 ra, rc;
    auto rdraw = [&]() { asm volatile("ds_read_b64 %0, %2\n\tds_read_b64 %1, %2 offset:512" : "=&v"(ra), "=&v"(rc) : "v"(rwo) : "memory"); SB(); };
    const char* efp = ws + WS_EF + cb * 256;
    char* cb_ = smem + M_CV + cw * CV_BUF;
    auto run = [&](auto ic) {
      constexpr int i = decltype(ic)::value, p = cnt('_', i) + 1;
      constexpr char c = SCH[i];
      if constexpr (c == '_') {
        CB(3 * (p - XBAR));
        asm volatile("s_waitcnt vmcnt(%0) lgkmcnt(0)" :: "n"(behind(1, i, '_')) : "memory");
        CB(3 * (p - XBAR) + 1);
        asm volatile("s_barrier" ::: "memory");
        CB(3 * (p - XBAR) + 2);
        SB();
#pragma unroll
        for (int q = 0; q < 5; ++q) if (XNEED[q] + 1 == p) pin(XF[q][0][0], XF[q][0][1], XF[q][1][0], XF[q][1][1]);
        if constexpr (p == 7) {
          rwo = lb + L_RAW + ((lane >> 3) * 4 + (lane & 3)) * 16 + (lane & 4) * 2;
          pin(rwo);
        }
        if constexpr (p == 9) __builtin_amdgcn_s_setprio(CPR);
        if constexpr (p == 2) AB(4);
        if constexpr (p == 6) AB(5);
        if constexpr (p == 9) AB(6);
      }
      if constexpr (c == 'u') issue(IC(cnt('u', i)));
      if constexpr (c == 'x') rdXF(IC(cnt('x', i)));
      if constexpr (c == 'B') {
        AB(1);
        barrier0(behind(1, i, 'B'));
        pin(ones, hole);
      }
      if constexpr (c == 'v') sfor<2 * cnt('v', i), 2 * cnt('v', i) + 2>([&](auto nc) { rdV(IC(0), nc, 0); rdV(IC(0), nc, 1); rdV(IC(0), nc, 2); rdV(IC(0), nc, 3); });
      if constexpr (c == 'm') {
        constexpr int n = cnt('m', i);
        mm2(IC(0), IC(n), XF, VNEED[n >> 1] < p - 1 || (n & 1) ? -1 : 0, [](int) {});
      }
      if constexpr (c == 'p') {
        asm volatile("s_nop 7" : "+v"(ac2[0][0]), "+v"(ac2[0][1]), "+v"(ac2[1][0]), "+v"(ac2[1][1]));
        dsw16<0>(p2a, ac2[0][0]); dsw16<1024>(p2a, ac2[1][0]); dsw16<2048>(p2a, ac2[0][1]); dsw16<3072>(p2a, ac2[1][1]);
      }
      if constexpr (c == 'w')
        touch((unsigned)(min(lane, 19) * (F2_ * 2)), (const char*)w_in + (size_t)(((rb + WSH) & 7) * 80 + cw * 20) * (F2_ * 2) + pr * 384 + odd * 256, lb + L_DUM);
      if constexpr (c == 'l') { if (wave == 7) kvld(); }
      if constexpr (c == 'k') { if (wave == 7) { kvm(0, 4); kvst(); } }
      if constexpr (c == 'E' || c == 'D') { if (wave == 5) sttag(ws + WS_EF + (c == 'D' ? 2048 : 0) + (cb * 32 + rb) * 8); }
      if constexpr (c == 'U') {
        wait_vm<behind(1, i, 'F')>(ef);
        waittag(ef, (unsigned)((lane & 31) * 8), efp);
#pragma unroll
        for (int k = 0; k < 4; ++k) asm volatile("global_load_dwordx4 %0, %1, %2 offset:%3 sc1" : "=&v"(uc[k]) : "v"(lin), "s"(ws + WS_KV + (size_t)d * (S_ * 2)), "i"(k * 1024) : "memory");
      }
      if constexpr (c == 'F') ldtag(ef, (unsigned)((lane & 31) * 8), efp);
      if constexpr (c == 'o') {
        if (wid < 120) {
          const unsigned ra = lb + L_WO + cw * 1024 + (8 * g4 + (l15 >> 2)) * 32 + 8 * (lane & 3);
          s16x4 lo = tr_asm<0>(ra), hi = tr_asm<128>(ra);
          wait_lg<0>(lo, hi);
          st16(lin, ws + WS_WO + (size_t)((wid / 5) * 40 + cb * 5 + wid % 5) * 1024, __builtin_bit_cast(u32x4, join8(lo, hi)));
        }
      }
      if constexpr (c == 'h') {
        constexpr int n = cnt('h', i);
        st16(lin, ws + (n < 3 ? WS_Z2 + (size_t)((rb * 2 + wl) * 48 + cb * 6 + wm * 3 + n) * 1024 : WS_H + (size_t)((rb * 4 + cw) * 24 + cb * 3 + n - 3) * 1024), hole);
      }
      if constexpr (c == 't') { constexpr int n = cnt('t', i); ldT(TA[n], 3 + (n >> 3), cw, n & 7); }
      if constexpr (c == 'H') ldhc();
      if constexpr (c == 'R') rdraw();
      if constexpr (c == 'V') {
        wait_vm<behind(1, i, '_')>();
        signal1(flg + 60);
        AB(2);
      }
      if constexpr (c == 'I') {
        wait_vm<behind(1, i, 'H')>(hc[0], hc[1], hc[2], hc[3], Dw);
        if (docv) {
          if (lane < 36) *(u32x4*)(cb_ + (lane < 32 ? lane * 16 : CV_C + ((lane >> 1) & 1) * CV_CS + 4096 + (lane & 1) * 16)) = (u32x4){0u, 0u, 0u, 0u};
#pragma unroll
          for (int k = 0; k < 4; ++k) {
            const int a = k * 64 + lane;
            const int h0 = k ? __builtin_amdgcn_readlane((int)hc[k ? k - 1 : 0][3], 63) : 0;
            const unsigned hp = (unsigned)__builtin_amdgcn_update_dpp(h0, (int)hc[k][3], 0x138, 0xF, 0xF, false);
            const unsigned w[5] = {hp, hc[k][0], hc[k][1], hc[k][2], hc[k][3]};
            u32x4 sw, mx;
#pragma unroll
            for (int q = 0; q < 4; ++q) {
              sw[q] = (w[4 - q] >> 16) | (w[4 - q] << 16);
              mx[q] = (w[4 - q] & 0xFFFFu) | (w[3 - q] & 0xFFFF0000u);
            }
            char* cp = cb_ + CV_C + (2040 - 8 * a) * 2;
            *(u32x4*)(cp) = sw;
            *(u32x4*)(cp + CV_CS) = mx;
          }
        }
      }
      if constexpr (c == 'J') {
        wait_vm<behind(1, i, 'U')>(uc[0], uc[1], uc[2], uc[3]);
        if (docv) {
#pragma unroll
          for (int k = 0; k < 4; ++k) *(u32x4*)(cb_ + 512 + (k * 64 + lane) * 16) = uc[k];
        }
      }
      if constexpr (c == 'S') {
        wait_lg<0>(ra, rc);
        if (wave == 6) {
          const float s1 = nv(ra[0]) + rc[0], fm = s1 * (1.0f / 640.0f), sm = s1 * fm;
          float fq = (ra[1] - sm) + rc[1];
          if (__builtin_expect(__builtin_amdgcn_ballot_w64(!(fq >= 1e-3f * sm)) != 0, 0)) {
            const f32x2 mm = {fm, fm};
            f32x2 q2 = {0.f, 0.f};
#pragma unroll 1
            for (int c = 0; c < 80; ++c) {
              const u32x4 xv = ldx(c);
#pragma unroll
              for (int e = 0; e < 4; ++e) { const f32x2 d = unpk(xv[e]) - mm; q2 = __builtin_elementwise_fma(d, d, q2); }
            }
            fq = q2[0] + q2[1];
          }
          const float vbf = rbf(fmaxf(fq, 0.f) * (1.0f / 640.0f));
          *(float*)(smem + L_XM + lane * 4) = rbf(fm);
          *(float*)(smem + L_XR + lane * 4) = __builtin_amdgcn_rcpf(rbf(__builtin_amdgcn_sqrtf(rbf(vbf + 1e-5f))));
        }
      }
      SB();
    };
    sfor<0, NSCH>(run);
    __builtin_amdgcn_s_setprio(0);
    AB(9);
    auto ldS = [&](auto dc, auto wc) {
      constexpr int dd = decltype(dc)::value, w = decltype(wc)::value;
      const unsigned to = (unsigned)(((dd + rot2) & 7) * 128);
      if (THR < 60) wait_vm<THR>();
      dma32s<M_RING + w * 24576 + (2 - dd) * 8192, ((w + 1) & 3) * 65536 + w * 1024>(aoff[0] + to, aoff[1] - 1024 + to, gS, lb);
      if (THR < 60) wait_vm<THR>();
      dma32s<M_RING + w * 24576 + (2 - dd) * 8192 + 2048, ((w + 2) & 3) * 65536 + w * 1024>(aoff[0] + to, aoff[1] - 1024 + to, gS, lb);
      if (THR < 60) wait_vm<THR>();
      dma32s<M_RING + w * 24576 + (2 - dd) * 8192 + 4096, ((w + 3) & 3) * 65536 + w * 1024>(aoff[0] + to, aoff[1] - 1024 + to, gS, lb);
      if (THR < 60) wait_vm<THR>();
      dma32s<M_RING + w * 24576 + (2 - dd) * 8192 + 6144, w * 65536 + w * 1024>(aoff[0] + to, aoff[1] - 1024 + to, gS, lb);
      SB();
    };
    auto epi2 = [&]() {
      flag_wait_asm(flg + 132, 4u);
#pragma unroll
      for (int jj = 0; jj < 2; ++jj) {
        u32x4* zp = z1at(odd * 32 + wl * 32 + jj * 16);
        u32x4 o = *zp;
#pragma unroll
        for (int q = 0; q < 4; ++q) o[q] = pack2s(gelu1(nv(lo16(o[q]))), gelu1(hi16(o[q])));
        *zp = o;
      }
      signal1(flg + 56);
      SB();
    };
    auto putT = [&](const u32x4* T, unsigned a, unsigned f) {
#pragma unroll
      for (int i = 0; i < 8; ++i) asm volatile("ds_write_b128 %0, %1 offset:%2" :: "v"(a), "v"(T[i]), "i"(i * 1024) : "memory");
      signal1(f);
    };
    const unsigned tx = lb + M_CV + cw * CV_BUF + lin;
    auto landT = [&]() {
      sttag(ws + WS_LF + (rb * 8 + cb) * 32 + cw * 8);
#pragma unroll
      for (int n = 0; n < 16; ++n) pin(TA[n]);
      putT(TA, tx, flg + 112 + cw * 4);
    };
    auto putT4 = [&]() {
      flag_wait_asm(flg + 96 + cw * 4, 1u);
      putT(TA + 8, lb + M_RING + cw * 24576 + 16384 + lin, flg + 112 + cw * 4);
    };
    if (docv) {
      flag_wait_asm(flg + 36, 4u);
      AB(12);
      f32x4 ac[8];
      conv_core(lds_addr(cb_), lane, Dw, ac, flg + 28);
      AB(13);
      signal1(flg + 40);
      wait_vm<0>();
      landT();
#pragma unroll
      for (int k = 0; k < 4; ++k) {
        u32x2 ov[2];
#pragma unroll
        for (int t = 0; t < 2; ++t)
#pragma unroll
          for (int h = 0; h < 2; ++h) ov[t][h] = pack2(ac[2 * k + t][2 * h], ac[2 * k + t][2 * h + 1]);
        const auto s0 = __builtin_amdgcn_permlane16_swap(ov[0][0], ov[1][0], false, false);
        const auto s1 = __builtin_amdgcn_permlane16_swap(ov[0][1], ov[1][1], false, false);
        st16((unsigned)((256 * (g4 & 1) + 16 * l15 + 8 * (g4 >> 1)) * 2), ws + WS_Y + (size_t)d * (S_ * 2) + k * 1024, (u32x4){s0[0], s1[0], s0[1], s1[1]});
      }
      wait_vm<0>();
      sttag(ws + WS_YF + d * 8);
      putT4();
      epi2();
    } else if (cw != 3) {
      wait_vm<0>();
      landT();
      putT4();
      epi2();
    } else {
      ldS(IC(0), IC(0)); ldS(IC(0), IC(1)); ldS(IC(0), IC(2)); ldS(IC(1), IC(1)); ldS(IC(2), IC(1));
      wait_vm<40>();
      landT();
      AB(12);
      flag_wait_asm(flg + 16, 4u);
      AB(8);
      wait_vm<24>();
      ldS(IC(0), IC(3)); ldS(IC(1), IC(0)); ldS(IC(1), IC(2)); ldS(IC(1), IC(3));
      wait_vm<24>();
      signal1(flg + 32);
      ldS(IC(2), IC(0)); ldS(IC(2), IC(2)); ldS(IC(2), IC(3));
      AB(10);
      u32x4 TB[32];
#pragma unroll
      for (int n = 0; n < 32; ++n) {
        if (n == 8) wait_vm<32>();
        if (THR < 60 && !(n & 1)) wait_vm<THR>();
        if (THR < 32 && n == THR) signal1(flg + 32);
        ldT(TB[n], 5, n >> 3, n & 7);
      }
      wait_vm<32>();
      if (THR >= 32) signal1(flg + 32);
      AB(11);
      wait_vm<0>();
#pragma unroll
      for (int n = 0; n < 32; ++n) pin(TB[n]);
      putT4();
#pragma unroll
      for (int w = 0; w < 4; ++w) {
        flag_wait_asm(flg + 96 + w * 4, 2u);
        putT(TB + 8 * w, lb + M_RING + w * 24576 + 8192 + lin, flg + 144 + w * 4);
      }
      epi2();
      if (!odd) {
        u32x4 zx[4];
        ldtag(ef, (unsigned)((lane & 1) * 8), ws + WS_XF + (rb * 4 + pr) * 16);
        wait_vm<0>(ef);
        waittag(ef, (unsigned)((lane & 1) * 8), ws + WS_XF + (rb * 4 + pr) * 16);
#pragma unroll
        for (int c = 0; c < 4; ++c)
          asm volatile("global_load_dwordx4 %0, %1, %2 offset:%3 sc1" : "=&v"(zx[c]) : "v"(lin), "s"(ws + WS_ZX + (size_t)(rb * 4 + pr) * 4096), "i"(c * 1024) : "memory");
        wait_vm<0>(zx[0], zx[1], zx[2], zx[3]);
#pragma unroll
        for (int c = 0; c < 4; ++c) *(u32x4*)(smem + L_Z1 + (64 + (c & 1) * 16 + l15) * 128 + ((((c >> 1) * 4 + g4) ^ (l15 & 7)) << 4)) = zx[c];
      }
      signal1(flg + 56);
      AB(13);
    }
    SB();
    wait_vm<0>();
    AB(14);
    u32x2 xv[5];
    f32x2 sv[5][2];
    flag_wait_asm(flg + 40, (rb & 1) ? 2u : 3u);
    const int yc = min(cw * 24, 56);
    const unsigned yfo = (unsigned)(min(lane, 23) * 8);
    const char* yfp = ws + WS_YF + (d0 + yc) * 8;
    auto ldy = [&]() {
#pragma unroll
      for (int n = 0; n < 3; ++n) {
        const int q = (yc >> 3) + n;
        asm volatile("s_mov_b32 m0, %2\n\ts_nop 0\n\tglobal_load_lds_dwordx4 %0, %1 sc1" :: "v"((unsigned)(r8 * (S_ * 2) + ((c8 ^ (r8 & 6)) << 4))),
                     "s"(ws + WS_Y + ((size_t)(d0 + q * 8) * S_ + t0) * 2), "s"(lb + yat(0) + (q >> 2) * CV_BUF + (q & 3) * 1024) : "memory");
      }
    };
    flag_wait_asm(flg + 96 + cw * 4, 3u);
    ldtag(ef, yfo, yfp);
    wait_vm<0>(ef);
    flag_wait_asm(flg + 44, 4u);
    waittag(ef, yfo, yfp);
    AB(15);
    ldy();
#pragma unroll
    for (int j = 0; j < 5; ++j)
      asm volatile("global_load_dwordx2 %0, %1, %2 offset:%3" : "=&v"(xv[j]) : "v"((unsigned)(l15 * D_ * 2 + 8 * g4)), "s"(x + (size_t)(t0 + cw * 16) * D_ + d0), "i"(j * 32) : "memory");
    ldW();
    wait_vm<16>(xv[0], xv[1], xv[2], xv[3], xv[4]);
    AB(17);
    signal1(flg + 52);
    flag_wait_asm(flg + 52, 4u);
    {
      const int yr_ = 4 * g4 + (l15 >> 2);
      const unsigned ya = lb + yr_ * 128 + (((cw * 2 + ((lane >> 1) & 1)) ^ (yr_ & 6)) << 4) + (lane & 1) * 8;
      const float nm = -*(const float*)(smem + L_XM + (cw * 16 + l15) * 4), sr = *(const float*)(smem + L_XR + (cw * 16 + l15) * 4);
      s16x4 yr[5] = {tr_asm<yat(0)>(ya), tr_asm<yat(1)>(ya), tr_asm<yat(2)>(ya), tr_asm<yat(3)>(ya), tr_asm<yat(4)>(ya)};
      wait_lg<0>(yr[0], yr[1], yr[2], yr[3], yr[4]);
      AB(16);
      flag_wait_asm(flg + 96 + cw * 4, 4u);
#pragma unroll
      for (int j = 0; j < 5; ++j) {
        const u32x2 yw = __builtin_bit_cast(u32x2, yr[j]);
#pragma unroll
        for (int h = 0; h < 2; ++h) {
          sv[j][h] = ((unpk(xv[j][h]) + (f32x2){nm, nm}) * (f32x2){sr, sr}) * unpk(yw[h]);
          pin(sv[j][h]);
        }
      }
      SB();
    }
    wait_vm<15>(ef);
    gemm3(IC(1));
    unsigned pk[5][2];
#pragma unroll
    for (int j = 0; j < 5; ++j)
#pragma unroll
      for (int h = 0; h < 2; ++h) {
        const f32x2 o2 = {ac3[j][2 * h], ac3[j][2 * h + 1]};
        const f32x2 a = o2 * (f32x2){-1.4426950f, -1.4426950f};
        const f32x2 e = (f32x2){__builtin_amdgcn_exp2f(a[0]), __builtin_amdgcn_exp2f(a[1])} + (f32x2){1.0f, 1.0f};
        const f32x2 r = {__builtin_amdgcn_rcpf(e[0]), __builtin_amdgcn_rcpf(e[1])};
        pk[j][h] = pk2(__builtin_elementwise_fma(o2 * r, sv[j][h], unpk(xv[j][h])));
      }
    const u16* op = out + (size_t)(t0 + cw * 16) * D_ + d0;
#pragma unroll
    for (int q = 0; q < 2; ++q) {
      const auto s0 = __builtin_amdgcn_permlane16_swap(pk[2 * q][0], pk[2 * q + 1][0], false, false);
      const auto s1 = __builtin_amdgcn_permlane16_swap(pk[2 * q][1], pk[2 * q + 1][1], false, false);
      asm volatile("global_store_dwordx4 %0, %1, %2 offset:%3 " OSC "\n\ts_nop 1" :: "v"((unsigned)(l15 * D_ * 2 + 32 * (g4 & 1) + 16 * (g4 >> 1))), "v"((u32x4){s0[0], s1[0], s0[1], s1[1]}),
                   "s"(op), "i"(64 * q) : "memory");
    }
    asm volatile("global_store_dwordx2 %0, %1, %2 offset:128 " OSC :: "v"((unsigned)(l15 * D_ * 2 + 8 * g4)), "v"((u32x2){pk[4][0], pk[4][1]}), "s"(op) : "memory");
  } else {
    ldsw();
    sfor<0, pos('B')>([&](auto ic) { if constexpr (SCH[ic] == 'g') issue(IC(cnt('u', ic))); });
    if (tid < 64) *(unsigned*)(smem + L_FLAG + tid * 4) = 0u;
    pin(ones);
    AB(1);
    barrier0(behind(0, pos('B'), 'B'));
    kvld();
    kvm(0, 4);
    kvst();
    const bool isz2 = !(odd && wl);
    const char* stp = ws + WS_ST + (size_t)((rb * 12 + pr * 3 + odd * 2 + wl) * 1024 + wm * 512);
    if (STW && isz2) st8(lin >> 1, stp, (u32x2){0u, ~(unsigned)tag});
    const char* Ab[4];
#pragma unroll
    for (int i = 0; i < 4; ++i) Ab[i] = (const char*)w_sp + (size_t)(t0 + ((wave + 1 + i) & 3) * 16) * (S_ * 2) + wave * 1024;
    const unsigned ringl = lb + M_RING + wave * 24576;
    auto ldA1 = [&](int d, int i) {
      dma16o(aoff[i & 1] + (unsigned)(((d + rot2) & 7) * 128), Ab[i >> 1], 0, d == 7 ? lb + M_CV + wave * CV_BUF : ringl, i * 1024);
    };
    unsigned bsv;
    {
      f32x4 zv[2][4], cs[4];
      s16x4 WB[20][4][2];
      bf16x8 XG[10][2][2];
      unsigned aX[2][2], aW[2][2][4];
#pragma unroll
      for (int k = 0; k < 2; ++k)
#pragma unroll
        for (int w = 0; w < 2; ++w) {
          aX[w][k] = pX[k] + L_SLOT + XW * 8192 + w * 65536;
          pin(aX[w][k]);
#pragma unroll
          for (int jj = 0; jj < 4; ++jj) { aW[w][k][jj] = (pW[k] ^ (unsigned)(jj * 32)) + L_SLOT + wl * 8192 + w * 65536; pin(aW[w][k][jj]); }
        }
      auto rd1 = [&](auto nc, int r) {
        constexpr int n = decltype(nc)::value, j = n >> 1, k = n & 1, c = n > 0 && n < 11 ? n - 1 : 0, sx = (XSLOT[c] - XW) * 8192, sa = WSLOT[j] * 8192;
        if (r < 0) { if (r & 1) ds16<(sx & 0xFFFF) + 512>(XG[c][(r + 4) >> 1][1], aX[sx >> 16][(r + 4) >> 1]); else ds16<(sx & 0xFFFF)>(XG[c][(r + 4) >> 1][0], aX[sx >> 16][(r + 4) >> 1]); }
        else if (r & 1) WB[n][r >> 1][1] = tr_asm<(sa & 0xFFFF) + 512>(aW[sa >> 16][k][r >> 1]);
        else WB[n][r >> 1][0] = tr_asm<(sa & 0xFFFF)>(aW[sa >> 16][k][r >> 1]);
        SB();
      };
      auto bar = [&](auto bc) {
        SB();
        constexpr int p = decltype(bc)::value;
        CB(3 * (p - XBAR));
        if constexpr (p == 1) asm volatile("s_waitcnt vmcnt(%0) lgkmcnt(0)\n\ts_barrier" :: "n"(behind(0, pos('_', p - 1), '_')) : "memory");
        else if constexpr (p <= GZ) asm volatile("s_waitcnt vmcnt(%0) lgkmcnt(8)\n\ts_barrier" :: "n"(behind(0, pos('_', p - 1), '_')) : "memory");
        else if constexpr (p >= 8) asm volatile("s_waitcnt lgkmcnt(0)\n\ts_barrier" ::: "memory");
        else if constexpr (p < 7) asm volatile("s_waitcnt lgkmcnt(8)\n\ts_barrier" ::: "memory");
        else asm volatile("s_barrier" ::: "memory");
        CB(3 * (p - XBAR) + 2);
        SB();
      };
      float cp[4];
      f32x4 st[2][2], rv;
      const unsigned rwa = lb + L_RAW + (ksb * 2 + wm) * 256 + l15 * 16 + ((l15 >> 2) == g4 ? 0 : 1024);
      const unsigned csa = lb + L_CS + (wave * 64 + lane) * 4;
      auto kstep = [&](auto nc) {
        constexpr int n = decltype(nc)::value, j = n >> 1, k = n & 1, nn = n + 1, r0 = nn >= 2 && nn <= 10 ? -4 : 0;
        constexpr int sc = n == 1 ? 0 : n - 2, ns = !k ? 0 : (n == 1 || n == 11 ? 1 : 2);
        if constexpr (k) bar(IC(nn / 2 + 1));
        int r = r0, c = 0;
        auto nxt = [&]() { if (r < 8) { rd1(IC(nn), r++); ++c; } };
        auto sel = [&](int q) {
          const f32x4& sq = st[q & 1][q >> 1];
          rv[q] = sel4(sq[0], sq[1], sq[2], sq[3], 0xAAAAAAAAAAAAAAAAull, 0xCCCCCCCCCCCCCCCCull);
          SB();
        };
#pragma unroll
        for (int jj = 0; jj < 4; ++jj) {
          const bf16x8 wf = join8(WB[n][jj][0], WB[n][jj][1]);
          MFMAW<n == 0>(jj == 0 ? 4 : (jj == 2 ? c : -1), zv[0][jj], XG[j][k][0], wf);
          SB();
          nxt();
          if (n == 12) nxt();
          if (n == 12 && jj == 2) sel(2);
          MFMAW<n == 0>(-1, zv[1][jj], XG[j][k][1], wf);
          SB();
          nxt();
          if (n == 12) nxt();
          if (n == 12 && (jj == 1 || jj == 2)) sel(jj == 1 ? 0 : 3);
          if (n == 12 && jj == 2) { dsw16<0>(rwa, rv); SB(); }
          if (k == 0) {
            MFMAW<n == 0>(-1, cs[jj], ones, wf);
            SB();
            nxt();
            if (n == 12) nxt();
            if (n == 12 && jj == 1) sel(1);
          }
#pragma unroll
          for (int q = 0; q < ns; ++q) {
            const bf16x8& xs = XG[sc + q][1][jj >> 1];
            MFMAW<n == 1>(-1, st[jj & 1][jj >> 1], xs, (jj & 1) ? xs : ones);
            SB();
            nxt();
          }
        }
      };
      auto lstep = [&](auto nc, auto ac, auto bc, int w) {
        constexpr int n = decltype(nc)::value, j = n >> 1, k = n & 1, na = decltype(ac)::value, nb = decltype(bc)::value;
        int r = 0;
        auto nxt = [&]() { if (na > 0 && r < 16) { if (r < 8) rd1(ac, r); else rd1(bc, r - 8); ++r; } };
#pragma unroll
        for (int jj = 0; jj < 4; ++jj) {
          const bf16x8 wf = join8(WB[n][jj][0], WB[n][jj][1]);
          if (k == 0) { MFMAW<false>(jj == 0 ? w : -1, cs[jj], ones, wf); SB(); nxt(); nxt(); }
          MFMAW<false>(jj == 0 && k ? w : -1, zv[0][jj], XG[j][k][0], wf);
          SB();
          nxt();
          if (k) nxt();
          MFMAW<false>(-1, zv[1][jj], XG[j][k][1], wf);
          SB();
          nxt();
          if (k) nxt();
          if (n == 15 && jj == 2) {
            dsw4(csa, sel4(cs[0][0], cs[1][0], cs[2][0], cs[3][0], 0xFFFF0000FFFF0000ull, 0xFFFFFFFF00000000ull));
            SB();
          }
        }
      };
      bar(IC(1));
      AB(2);
#pragma unroll
      for (int r = -4; r < 8; ++r) rd1(IC(0), r);
      kstep(IC(0)); AB(4); kstep(IC(1)); kstep(IC(2)); kstep(IC(3)); kstep(IC(4)); kstep(IC(5)); kstep(IC(6)); kstep(IC(7)); kstep(IC(8)); AB(5); kstep(IC(9));
      kstep(IC(10)); kstep(IC(11)); kstep(IC(12));
      bar(IC(8));
      lstep(IC(13), IC(14), IC(16), -1);
      lstep(IC(14), IC(18), IC(15), 8);
      lstep(IC(16), IC(17), IC(19), 15);
      lstep(IC(18), IC(0), IC(0), 15);
      lstep(IC(15), IC(0), IC(0), -1);
      lstep(IC(17), IC(0), IC(0), 9);
      bar(IC(9));
      __builtin_amdgcn_s_setprio(2);
#pragma unroll
      for (int q = 0; q < 4; ++q) cp[q] = *(const float*)(smem + L_CS + ((wave ^ 1) * 64 + q * 16 + l15) * 4);
      ldxs();
      SB();
      lstep(IC(19), IC(0), IC(0), -1);
      AB(6);
      float ct[4];
#pragma unroll
      for (int jj = 0; jj < 4; ++jj) ct[jj] = cs[jj][0] + cp[jj];
#pragma unroll
      for (int jj = 0; jj < 4; ++jj) {
        if (jj == 0) asm volatile("s_nop 3");
        if (jj == 2) SB();
        if (!(jj & 1)) pin(zv[0][jj], zv[1][jj], zv[0][jj + 1], zv[1][jj + 1]);
#pragma unroll
        for (int i = 0; i < 2; ++i) zv[i][jj] = act(zv[i][jj], ct[jj], i);
      }
      if (isz2) {
        f32x4 pv[2], s1[2], s2[2];
#pragma unroll
        for (int i = 0; i < 2; ++i) {
#pragma unroll
          for (int e = 0; e < 4; ++e) {
            const float z0 = zv[i][0][e];
            pv[i][e] = __int_as_float(__builtin_amdgcn_update_dpp(0, __float_as_int(z0), 0x150, 0xF, 0xF, true));
          }
          const f32x4 a0 = zv[i][0] - pv[i], a1 = zv[i][1] - pv[i], a2 = zv[i][2] - pv[i], a3 = zv[i][3] - pv[i];
          s1[i] = (a0 + a1) + (a2 + a3);
          s2[i] = __builtin_elementwise_fma(a3, a3, __builtin_elementwise_fma(a2, a2, __builtin_elementwise_fma(a1, a1, a0 * a0)));
        }
        float r[8], q[4];
#define DA(d, s, ctl, bm) "v_add_f32_dpp " d ", " s ", " s " " ctl " row_mask:0xf bank_mask:" bm "\n\t"
        asm volatile("s_nop 1\n\t"
                     DA("%0", "%12", "row_ror:8", "0x3") DA("%1", "%13", "row_ror:8", "0x3") DA("%2", "%14", "row_ror:8", "0x3") DA("%3", "%15", "row_ror:8", "0x3")
                     DA("%4", "%16", "row_ror:8", "0x3") DA("%5", "%17", "row_ror:8", "0x3") DA("%6", "%18", "row_ror:8", "0x3") DA("%7", "%19", "row_ror:8", "0x3")
                     DA("%0", "%20", "row_ror:8", "0xc") DA("%1", "%21", "row_ror:8", "0xc") DA("%2", "%22", "row_ror:8", "0xc") DA("%3", "%23", "row_ror:8", "0xc")
                     DA("%4", "%24", "row_ror:8", "0xc") DA("%5", "%25", "row_ror:8", "0xc") DA("%6", "%26", "row_ror:8", "0xc") DA("%7", "%27", "row_ror:8", "0xc")
                     DA("%8", "%0", "row_half_mirror", "0x5") DA("%9", "%1", "row_half_mirror", "0x5") DA("%10", "%2", "row_half_mirror", "0x5") DA("%11", "%3", "row_half_mirror", "0x5")
                     DA("%8", "%4", "row_half_mirror", "0xa") DA("%9", "%5", "row_half_mirror", "0xa") DA("%10", "%6", "row_half_mirror", "0xa") DA("%11", "%7", "row_half_mirror", "0xa")
                     DA("%8", "%8", "quad_perm:[1,0,3,2]", "0xf") DA("%9", "%9", "quad_perm:[1,0,3,2]", "0xf") DA("%10", "%10", "quad_perm:[1,0,3,2]", "0xf") DA("%11", "%11", "quad_perm:[1,0,3,2]", "0xf")
                     DA("%8", "%8", "quad_perm:[2,3,0,1]", "0xf") DA("%9", "%9", "quad_perm:[2,3,0,1]", "0xf") DA("%10", "%10", "quad_perm:[2,3,0,1]", "0xf") DA("%11", "%11", "quad_perm:[2,3,0,1]", "0xf")
                     : "=&v"(r[0]), "=&v"(r[1]), "=&v"(r[2]), "=&v"(r[3]), "=&v"(r[4]), "=&v"(r[5]), "=&v"(r[6]), "=&v"(r[7]),
                       "=&v"(q[0]), "=&v"(q[1]), "=&v"(q[2]), "=&v"(q[3])
                     : "v"(s1[0][0]), "v"(s2[0][0]), "v"(s1[0][1]), "v"(s2[0][1]), "v"(s1[0][2]), "v"(s2[0][2]), "v"(s1[0][3]), "v"(s2[0][3]),
                       "v"(s1[1][0]), "v"(s2[1][0]), "v"(s1[1][1]), "v"(s2[1][1]), "v"(s1[1][2]), "v"(s2[1][2]), "v"(s1[1][3]), "v"(s2[1][3]));
#undef DA
        const f32x4 p4 = (lane & 8) ? pv[1] : pv[0];
        const float pa = (lane & 4) ? p4[2] : p4[0], pb = (lane & 4) ? p4[3] : p4[1];
        const float pp = (lane & 2) ? pb : pa, S1 = (lane & 2) ? q[2] : q[0], S2 = (lane & 2) ? q[3] : q[1];
        const float dl = S1 * (1.0f / 64.0f);
        st8(lin >> 1, stp, (u32x2){__float_as_uint((lane & 1) ? fmaxf(S2 - S1 * dl, 0.f) : pp + dl), (unsigned)tag});
      } else {
#pragma unroll
        for (int jj = 0; jj < 4; ++jj) {
          u32x4 o = pack8(zv[0][jj], zv[1][jj]);
          if (jj >= 2) z1put(jj * 16 - 32, o);
          else st16(lin, ws + WS_ZX + (size_t)((rb * 4 + pr) * 4 + wm * 2 + jj) * 1024, o);
        }
      }
      signal1(flg + 36);
      SB();
      AB(7);
      __builtin_amdgcn_s_setprio(P2P);
      unsigned po = (unsigned)((lane >> 5) * 6144 + (lane & 31) * 16);
      const char* pb = ws + WS_ST + (size_t)rb * 12288 + wm * 512 + 2048;
      u32x4 sg[6];
      auto ldsg = [&]() {
#pragma unroll
        for (int c = 0; c < 6; ++c) asm volatile("global_load_dwordx4 %0, %1, %2 offset:%3 sc1" : "=&v"(sg[c]) : "v"(po), "s"(pb), "i"(c * 1024 - 2048) : "memory");
      };
      auto look = [&]() {
        wait_vm<0>();
        ldsg();
        asm volatile("global_load_ushort %0, %1, %2" : "=&v"(bsv) : "v"((unsigned)(l15 * 2)), "s"(b_sp + t0 + wave * 16) : "memory");
        if (!isz2) sttag(ws + WS_XF + (rb * 4 + pr) * 16 + wm * 8);
        SB();
      };
      flag_wait_asm(flg + 60, 4u);
      ds16<0>(ac2[0][0], p2a); ds16<1024>(ac2[1][0], p2a); ds16<2048>(ac2[0][1], p2a); ds16<3072>(ac2[1][1], p2a);
      sfor<0, 4>([&](auto nc) { rdV(IC(1), nc, 0); rdV(IC(1), nc, 1); rdV(IC(1), nc, 2); rdV(IC(1), nc, 3); });
      sfor<0, 10>([&](auto nc) {
        constexpr int n = decltype(nc)::value;
        mm2(IC(1), nc, XG + 5, n == 0 ? 12 : (n < 6 ? 15 : 4 * (9 - n)), [&](int r) {
          SB();
          if constexpr (n < 3) { if (r < 2) { rdV(IC(1), IC(4 + 2 * n), 2 * r); rdV(IC(1), IC(4 + 2 * n), 2 * r + 1); } else { rdV(IC(1), IC(5 + 2 * n), 2 * r - 4); rdV(IC(1), IC(5 + 2 * n), 2 * r - 3); } }
        });
        if constexpr (n == 2) signal1(flg + 16);
        if constexpr (n == PN) look();
      });
      signal1(flg + 20 + wl * 4);
      AB(3);
      if (PN > 9) look();
      AB(8);
      wait_vm<1>(sg[0], sg[1], sg[2], sg[3], sg[4], sg[5]);
      if (isz2) {
        const unsigned zw = lb + L_ZS + wave * 256 + lane * 4, zr_ = lb + L_ZS + wave * 256 + g4 * 32;
        const char* zb = ws + WS_Z2 + (size_t)((rb * 2 + wm) * 48 + pr * 12 + odd * 8 + wl * 4) * 1024;
        auto bad = [&]() {
          unsigned an = sg[0][1] & sg[0][3], on = sg[0][1] | sg[0][3];
#pragma unroll
          for (int c = 1; c < 6; ++c) { an &= sg[c][1] & sg[c][3]; on |= sg[c][1] | sg[c][3]; }
          return __builtin_amdgcn_ballot_w64(((an ^ (unsigned)tag) | (on ^ (unsigned)tag)) != 0) != 0;
        };
        if (__builtin_expect(bad(), 0)) {
          do {
            --tmo;
            reld6(sg, po, pb);
            wait_vm<0>(sg[0], sg[1], sg[2], sg[3], sg[4], sg[5]);
          } while (bad() && tmo > 0);
        }
        AB(9);
        auto f = [&](int c, int e) { return __uint_as_float(sg[c][e]); };
        auto sum6 = [&](int e) { return add1(add1(add1(f(0, e), f(1, e)), add1(f(2, e), f(3, e))), add1(f(4, e), f(5, e))); };
        const float nmh = sum6(0) * (-1.0f / 6.0f), mh = -nmh;
        float qa = sum6(2) * (1.0f / 64.0f), qb;
#pragma unroll
        for (int c = 0; c < 6; c += 2) { const float u = add1(f(c, 0), nmh), v = add1(f(c + 1, 0), nmh); qa = fma1(u, u, qa); qb = c ? fma1(v, v, qb) : v * v; }
        const float Mh = add1(qa, qb);
        const auto sm = __builtin_amdgcn_permlane32_swap(__float_as_uint(mh), __float_as_uint(mh), false, false);
        const auto sM = __builtin_amdgcn_permlane32_swap(__float_as_uint(Mh), __float_as_uint(Mh), false, false);
        const float dm = __uint_as_float(sm[0]) - __uint_as_float(sm[1]), mean = rbf(add1(__uint_as_float(sm[0]), __uint_as_float(sm[1])) * 0.5f);
        const float vbf = rbf(__builtin_fmaf(dm * dm, 3.0f, add1(__uint_as_float(sM[0]), __uint_as_float(sM[1]))) * (1.0f / 12.0f));
        const float rz = __builtin_amdgcn_rcpf(rbf(__builtin_amdgcn_sqrtf(rbf(vbf + 1e-5f))));
        dsw4(zw, lane < 32 ? -mean * rz : rz);
        f32x4 zm[2], zr[2];
        ds16<0>(zm[0], zr_); ds16<128>(zr[0], zr_); ds16<16>(zm[1], zr_); ds16<144>(zr[1], zr_);
        wait_lg<0>(zm[0], zm[1], zr[0], zr[1]);
#pragma unroll
        for (int jj = 0; jj < 4; ++jj) {
          u32x4 o;
#pragma unroll
          for (int q = 0; q < 4; ++q) {
            const int i = q >> 1, e = (q & 1) * 2;
            o[q] = pack2(fma1(zv[i][jj][e], zr[i][e], zm[i][e]), fma1(zv[i][jj][e + 1], zr[i][e + 1], zm[i][e + 1]));
          }
          o[3] = nohole(o[3]);
          asm volatile("global_store_dwordx4 %0, %1, %2 offset:%3 sc1\n\ts_nop 1" :: "v"(lin), "v"(o), "s"(zb), "i"(jj * 1024) : "memory");
          SB();
        }
      }
      SB();
      AB(10);
      ldtag(ef, (unsigned)((lane & 31) * 8), ws + WS_EF + 2048 + cb * 256);
      signal1(flg + 28);
    }
    const char* Bs = ws + WS_Z2 + (size_t)(wave * 16 * 48 + cb * 6) * 1024 + 2048;
    bf16x8 Bf[3][6];
    auto ldB1 = [&](int s, int j) {
      asm volatile("global_load_dwordx4 %0, %1, %2 offset:%3 " HSC : "=&v"(Bf[s % 3][j]) : "v"(lin + (unsigned)(((s + 2 * rot2) & 15) * 49152)), "s"(Bs), "i"(j * 1024 - 2048) : "memory");
    };
    f32x4 acc[4][6];
    unsigned ara[2][2];
#pragma unroll
    for (int ks = 0; ks < 2; ++ks) {
      ara[0][ks] = ringl + l15 * 128 + (((ks * 4 + g4) ^ (l15 & 7)) << 4);
      ara[1][ks] = ara[0][ks] + (M_CV - M_RING) + wave * (CV_BUF - 24576);
    }
    u32x4 af[2][4];
    auto rdA = [&](int s, int i) {
      const int q = (0x30123012 >> (4 * (s >> 1))) & 3;
      asm volatile("ds_read_b128 %0, %1 offset:%2" : "=&v"(af[s & 1][i]) : "v"(ara[q == 3][s & 1]), "i"((q % 3) * 8192 + i * 2048) : "memory");
    };
    flag_wait_asm(flg + 28, 4u);
    AB(11);
    wait_vm<0>(ef, bsv);
    waittag(ef, (unsigned)((lane & 31) * 8), ws + WS_EF + 2048 + cb * 256);
    AB(12); SS(1 - 3 * XS);
#pragma unroll
    for (int j = 0; j < 6; ++j) ldB1(0, j);
#pragma unroll
    for (int j = 0; j < 6; ++j) ldB1(1, j);
    SS(2 - 3 * XS);
    {
      flag_wait_asm(flg + 20 + wl * 4, 2u);
      const float* cp2 = (const float*)(smem + L_CS2 + (wl * 64 + l15) * 4);
      pin(ac2[0][0], ac2[0][1], ac2[1][0], ac2[1][1]);
#pragma unroll
      for (int jj = 0; jj < 2; ++jj) {
        const float nct = -add1(add1(cp2[jj * 16], cp2[32 + jj * 16]), add1(cp2[128 + jj * 16], cp2[160 + jj * 16]));
        u32x4 o;
#pragma unroll
        for (int q = 0; q < 4; ++q) {
          const int i = q >> 1, e = (q & 1) * 2;
          o[q] = pack2(mul1(fma1(xm[i][e], nct, ac2[i][jj][e]), xr[i][e]), mul1(fma1(xm[i][e + 1], nct, ac2[i][jj][e + 1]), xr[i][e + 1]));
        }
        z1put(odd * 32 + wl * 32 + jj * 16, o);
      }
      signal1(flg + 132);
      SB();
    }
    __builtin_amdgcn_s_setprio(G2P);
    f32x4 bs4 = {lo16(bsv), lo16(bsv), lo16(bsv), lo16(bsv)};
    pin(bs4);
    AB(25);
    flag_wait_asm(flg + 32, 1u);
    AB(26);
#pragma unroll
    for (int i = 0; i < 4; ++i) rdA(0, i);
    SB();
    const unsigned xa = ringl + lane * 16;
    auto putp = [&](int i, int j) { asm volatile("ds_write_b128 %0, %1 offset:%2" :: "v"(xa), "v"(acc[i][j]), "i"(i * 6144 + j * 1024) : "memory"); };
    unsigned mx[2], fv;
    u64 hm;
    auto chk = [&](auto sc, int nw, int k) {
      constexpr int s = decltype(sc)::value, sl = s % 3;
      auto d3 = [&](int j) { return __builtin_bit_cast(u32x4, Bf[sl][j])[3]; };
      if (k == 0) {
        asm volatile("s_waitcnt vmcnt(%0)" :: "i"(nw) : "memory");
        pin(Bf[sl][0], Bf[sl][1], Bf[sl][2], Bf[sl][3], Bf[sl][4], Bf[sl][5]);
        mx[0] = opq(umx(umx(d3(0), d3(1)), d3(2)));
      }
      if (k == 1) mx[1] = opq(umx(umx(d3(3), d3(4)), mx[0]));
      if (k == 2) { hm = __builtin_amdgcn_ballot_w64(umx(mx[1], d3(5)) == HOLE); asm volatile("" : "+s"(hm)); }
      if (k == 3 && __builtin_expect(hm != 0, 0)) {
        do {
          --tmo;
          asm volatile("s_waitcnt vmcnt(0)\n\ts_sleep 2" ::: "memory");
#pragma unroll
          for (int q = 0; q < (s ? 3 : 2); ++q)
            if (s + q < 16) reld6(Bf[(s + q) % 3], opq(lin) + (unsigned)(((s + q + 2 * rot2) & 15) * 49152), Bs);
          wait_vm<0>(Bf[sl][0], Bf[sl][1], Bf[sl][2], Bf[sl][3], Bf[sl][4], Bf[sl][5]);
        } while (__builtin_amdgcn_ballot_w64(umx(umx(umx(d3(0), d3(1)), d3(2)), umx(umx(d3(3), d3(4)), d3(5))) == HOLE) != 0 && tmo > 0);
      }
      SB();
    };
    chk(IC(0), 6, 0); chk(IC(0), 0, 1); chk(IC(0), 0, 2); chk(IC(0), 0, 3);
    auto mm = [&](int s, int i, int j) {
      if (s == 0 && i == 3) asm volatile("v_mfma_f32_16x16x32_bf16 %0, %1, %2, %3" : "=&v"(acc[i][j]) : "v"(Bf[0][j]), "v"(af[0][i]), "v"(bs4));
      else if (s == 0) MFMA0(acc[i][j], Bf[0][j], af[0][i]);
      else MFMA(acc[i][j], Bf[s % 3][j], af[s & 1][i]);
      SB();
    };
    auto step = [&](auto sc) {
      constexpr int s = decltype(sc)::value;
      constexpr int NA[16] = ANUM, a0 = s == 0 ? 0 : cum16(NA, s);
      static_assert(cum16(NA, 5) == 0 && cum16(NA, 7) <= 8 && cum16(NA, 9) >= 8 && cum16(NA, 11) == 16 && NA[s] <= 5, "");
      constexpr int NV = (s == 0 ? 5 : (s == 1 ? 1 : 6 + NA[s ? s - 1 : 0])) + (s < 13 ? 5 : 0) + NA[s];
      auto fad = [&](int q) { return flg + (q == 1 ? 32 : (q == 9 ? 144 : 112) + wave * 4); };
      wait_lg<0>(af[s & 1][0], af[s & 1][1], af[s & 1][2], af[s & 1][3]);
      if constexpr (s == 1 || s == 5 || s == 7 || s == 9) {
        pin(fv);
        constexpr unsigned tg = s == 1 || s == 7 ? 2u : 1u;
        if (__builtin_expect((unsigned)__builtin_amdgcn_readfirstlane((int)fv) < tg, 0)) flag_wait_asm(fad(s), tg);
      }
      SS(3 + 2 * (s - XS));
      SB();
#pragma unroll
      for (int j = 0; j < 6; ++j) {
        mm(s, 0, j);
        if (s == 0) ldB1(2, j);
        if (j == 5) chk(IC(s + 1), NV + (s == 0), 0);
        SB();
        mm(s, 1, j);
        if (j < NA[s]) ldA1(6 + ((a0 + j) >> 3), (a0 + j) & 7);
        if (j == 5) chk(IC(s + 1), 0, 1);
        SB();
        mm(s, 2, j);
        if (j < 4) rdA(s + 1, j);
        if (j == 4 && (s == 0 || s == 2 || s == YS)) signal1(flg + 96 + wave * 4);
        if (j == 4 && (s == 0 || s == 4 || s == 6 || s == 8)) asm volatile("ds_read_b32 %0, %1" : "=&v"(fv) : "v"(fad(s + 1)) : "memory");
        if (j == 4 && s == SGS) signal1(flg + 44);
        if (j == 5) chk(IC(s + 1), 0, 2);
        SB();
        mm(s, 3, j);
        if (s + 3 < 16) ldB1(s + 3, j);
        SB();
      }
      SS(4 + 2 * (s - XS));
      chk(IC(s + 1), 0, 3);
    };
    step(IC(0)); AB(13); step(IC(1)); step(IC(2)); step(IC(3)); AB(14); step(IC(4)); step(IC(5)); step(IC(6)); step(IC(7));
    step(IC(8)); step(IC(9)); step(IC(10)); step(IC(11)); step(IC(12)); step(IC(13));
    wait_lg<0>(af[0][0], af[0][1], af[0][2], af[0][3]);
    SS(3 + 2 * (14 - XS));
#pragma unroll
    for (int i = 0; i < 4; ++i) {
#pragma unroll
      for (int n = 0; n < 12; ++n) {
        if (i == 0 && n == 6) { chk(IC(15), 0, 3); wait_lg<0>(af[1][0], af[1][1], af[1][2], af[1][3]); }
        mm(14 + n / 6, i, n % 6);
        if (i == 0 && n < 2) { rdA(15, 2 * n); rdA(15, 2 * n + 1); }
        if (i == 0 && n >= 2 && n < 5) chk(IC(15), 0, n - 2);
        if (i > 0 && (n & 1)) putp(i - 1, n >> 1);
        SB();
      }
      if (i > 0) signal1(flg + ((wave + i) & 3) * 4);
      SB();
    }
    SS(4 + 2 * (14 - XS));
    AB(15);
    flag_wait_asm(flg + 56, 5u);
    s16x4 z1r[6];
    {
      const int fr = 4 * g4 + (l15 >> 2), p = lane & 3;
      const unsigned za = lb + L_Z1 + fr * 128 + (((wave * 2 + (p >> 1)) ^ (fr & 7)) << 4) + (p & 1) * 8;
      z1r[0] = tr_asm<0>(za); z1r[1] = tr_asm<2048>(za); z1r[2] = tr_asm<4096>(za);
      z1r[3] = tr_asm<6144>(za); z1r[4] = tr_asm<8192>(za); z1r[5] = tr_asm<10240>(za);
    }
    flag_wait_asm(flg + wave * 4, 3u);
    asm volatile("" : "+v"(z1r[0]), "+v"(z1r[1]), "+v"(z1r[2]), "+v"(z1r[3]), "+v"(z1r[4]), "+v"(z1r[5]));
    AB(16);
    f32x4 own[6];
    {
      f32x4 pr_[3][6];
#pragma unroll
      for (int q = 0; q < 3; ++q) {
        const char* src = smem + M_RING + ((wave + 3 - q) & 3) * 24576;
#pragma unroll
        for (int j = 0; j < 6; ++j) pr_[q][j] = *(const f32x4*)(src + q * 6144 + j * 1024 + lane * 16);
      }
      asm volatile("s_nop 7" : "+v"(acc[3][0]), "+v"(acc[3][1]), "+v"(acc[3][2]), "+v"(acc[3][3]), "+v"(acc[3][4]), "+v"(acc[3][5]));
#pragma unroll
      for (int j = 0; j < 6; ++j) own[j] = (acc[3][j] + pr_[0][j]) + (pr_[1][j] + pr_[2][j]);
    }
    SB();
    {
      unsigned pk[6][2];
#pragma unroll
      for (int j = 0; j < 6; ++j) {
        const u32x2 z = __builtin_bit_cast(u32x2, z1r[j]);
#pragma unroll
        for (int h = 0; h < 2; ++h) pk[j][h] = pk2(unpk(z[h]) * (f32x2){own[j][2 * h], own[j][2 * h + 1]});
      }
      const unsigned eo = (unsigned)((l15 + 16 * (2 * (g4 & 1) + (g4 >> 1))) * 16);
#pragma unroll
      for (int q = 0; q < 3; ++q) {
        const auto s0 = __builtin_amdgcn_permlane16_swap(pk[2 * q][0], pk[2 * q + 1][0], false, false);
        const auto s1 = __builtin_amdgcn_permlane16_swap(pk[2 * q][1], pk[2 * q + 1][1], false, false);
        st16(eo, hf + (size_t)((rb * 4 + wave) * 24 + cb * 3 + q) * 1024, (u32x4){s0[0], s1[0], s0[1], nohole(s1[1])});
      }
    }
    signal1(flg + 96 + wave * 4);
    AB(17);
    ldW();
    wait_vm<15>(ef);
    gemm3(IC(0));
    if (b == 0 && tid == 0) *(u64*)(ws + WS_EP) = tag;
  }
  AB(24);
  AB_OUT();
}

extern "C" __global__ void k_end() {}

extern "C" void kernel_launch(void* const* d_in, const int* in_sizes, int n_in,
                              void* d_out, int out_size, void* d_ws, size_t ws_size,
                              hipStream_t stream) {
  (void)hipFuncSetAttribute((const void*)k_f, hipFuncAttributeMaxDynamicSharedMemorySize, M_LDS);
  k_f<<<256, 512, M_LDS, stream>>>((const u16*)d_in[0], (const u16*)d_in[1], (const u16*)d_in[2], (const u16*)d_in[3], (const u16*)d_in[4],
                                   (const u16*)d_in[5], (const u16*)d_in[7], (const u16*)d_in[8], (const u16*)d_in[9], (const u16*)d_in[10],
                                   (u16*)d_out, (char*)d_ws);
}
```

```cpp
#include <hip/hip_runtime.h>
#include <hip/hip_bf16.h>
#include <stdint.h>
#include <type_traits>

typedef unsigned short u16;
typedef unsigned long long u64;
typedef __attribute__((ext_vector_type(8))) short bf16x8;
typedef __attribute__((ext_vector_type(4))) short s16x4;
typedef __attribute__((ext_vector_type(4))) float f32x4;
typedef __attribute__((ext_vector_type(2))) float f32x2;
typedef __attribute__((ext_vector_type(4))) unsigned int u32x4;
typedef __attribute__((ext_vector_type(2))) unsigned int u32x2;
typedef __attribute__((ext_vector_type(2))) __bf16 bf16x2n;

#define LDS_AS __attribute__((address_space(3)))
#define IC(n) std::integral_constant<int, (n)>{}
#define SB() __builtin_amdgcn_sched_barrier(0)

#define AB_DECL do {} while (0)
#define AB(i) do {} while (0)
#define AB_OUT() do {} while (0)
#define CB(i) do {} while (0)
#define SS(i) do {} while (0)
constexpr int S_ = 2048, D_ = 640, FH_ = 768, F2_ = 1536;
constexpr unsigned HOLE = 0xFFFFFFFFu;

constexpr size_t WS_EP = 0;
constexpr size_t WS_EF = 4096;
constexpr size_t WS_LF = 22528;
constexpr size_t WS_YF = 14336;
constexpr size_t WS_XF = 20480;
constexpr size_t WS_ST = 32768;
constexpr size_t WS_Z2 = WS_ST + 32 * 12 * 1024;
constexpr size_t WS_H = WS_Z2 + 64 * 48 * 1024;
constexpr size_t WS_ZX = WS_H + 128 * 24 * 1024;
constexpr size_t WS_KV = WS_ZX + 32 * 4 * 4096;
constexpr size_t WS_Y = WS_KV + D_ * S_ * 2;
constexpr size_t WS_WO = WS_Y + D_ * S_ * 2;

__device__ __forceinline__ float bf2f(u16 v) { return __uint_as_float(((unsigned)v) << 16); }
__device__ __forceinline__ unsigned pack2(float lo, float hi) {
  f32x2 v = {lo, hi};
  bf16x2n r = __builtin_convertvector(v, bf16x2n);
  return __builtin_bit_cast(unsigned, r);
}
__device__ __forceinline__ float nv(float v) { asm("" : "+v"(v)); return v; }
__device__ __forceinline__ float fma1(float a, float b, float c) { float r; asm("v_fma_f32 %0, %1, %2, %3" : "=v"(r) : "v"(a), "v"(b), "v"(c)); return r; }
__device__ __forceinline__ float add1(float a, float b) { float r; asm("v_add_f32 %0, %1, %2" : "=v"(r) : "v"(a), "v"(b)); return r; }
__device__ __forceinline__ float mul1(float a, float b) { float r; asm("v_mul_f32 %0, %1, %2" : "=v"(r) : "v"(a), "v"(b)); return r; }
__device__ __forceinline__ unsigned pack2s(float lo, float hi) { return pack2(nv(lo), nv(hi)); }
__device__ __forceinline__ float lo16(unsigned w) { return __uint_as_float(w << 16); }
__device__ __forceinline__ float hi16(unsigned w) { return __uint_as_float(w & 0xFFFF0000u); }
__device__ __forceinline__ float rbf(float f) { return lo16(pack2(f, f)); }
__device__ __forceinline__ f32x2 unpk(unsigned w) { f32x2 r = {lo16(w), hi16(w)}; return r; }
__device__ __forceinline__ unsigned pk2(f32x2 v) { return pack2(v[0], v[1]); }
__device__ __forceinline__ unsigned umx(unsigned a, unsigned b) { return a > b ? a : b; }
__device__ __forceinline__ unsigned nohole(unsigned a) { return a < HOLE - 1 ? a : HOLE - 1; }
__device__ __forceinline__ f32x2 gelu2(f32x2 x) {
  const f32x2 c1 = {-2.3022082f, -2.3022082f}, c2 = {-0.10294324f, -0.10294324f};
  const f32x2 a = x * __builtin_elementwise_fma(x * x, c2, c1);
  f32x2 e = {__builtin_amdgcn_exp2f(a[0]), __builtin_amdgcn_exp2f(a[1])};
  e = e + (f32x2){1.0f, 1.0f};
  const f32x2 r = {__builtin_amdgcn_rcpf(e[0]), __builtin_amdgcn_rcpf(e[1])};
  return x * r;
}
__device__ __forceinline__ float gelu1(float x) {
  const float a = x * __builtin_fmaf(x * x, -0.10294324f, -2.3022082f);
  return x * __builtin_amdgcn_rcpf(__builtin_amdgcn_exp2f(a) + 1.0f);
}
template <int OFF>
__device__ __forceinline__ s16x4 tr_asm(unsigned a) {
  s16x4 r;
  asm volatile("ds_read_b64_tr_b16 %0, %1 offset:%2" : "=&v"(r) : "v"(a), "i"(OFF) : "memory");
  return r;
}
__device__ __forceinline__ bf16x8 join8(s16x4 lo, s16x4 hi) {
  bf16x8 r;
  r[0] = lo[0]; r[1] = lo[1]; r[2] = lo[2]; r[3] = lo[3];
  r[4] = hi[0]; r[5] = hi[1]; r[6] = hi[2]; r[7] = hi[3];
  return r;
}
__device__ __forceinline__ unsigned lds_addr(const void* p) { return (unsigned)(uintptr_t)(LDS_AS const char*)p; }
#define HIREG(sym, r) ".set " sym ", 0\n.set hir_st, 0\n.irpc c, \"" r "\"\n.ifc \\c,:\n.set hir_st, 1\n.else\n.ifc \\c,]\n.set hir_st, 0\n.else\n.if hir_st\n.set " sym ", " sym " * 10 + \\c\n.endif\n.endif\n.endif\n.endr\n"
#define MF "v_mfma_f32_16x16x32_bf16 "
#define WAIT_LGKM0() asm volatile("s_waitcnt lgkmcnt(0)" ::: "memory")
__device__ __forceinline__ void flag_wait_asm(unsigned faddr, unsigned target) {
  unsigned v, sv;
  asm volatile(".Lfw_%=:\n\tds_read_b32 %0, %2\n\ts_waitcnt lgkmcnt(0)\n\tv_readfirstlane_b32 %1, %0\n\ts_nop 0\n\t"
               "s_cmp_ge_u32 %1, %3\n\ts_cbranch_scc1 .Lfd_%=\n\ts_sleep 1\n\ts_branch .Lfw_%=\n\t.Lfd_%=:"
               : "=&v"(v), "=&s"(sv) : "v"(faddr), "s"(target) : "memory", "scc");
}
__device__ __forceinline__ void signal1(unsigned a, unsigned one = 1u) {
  asm volatile("s_mov_b64 exec, 1\n\tds_add_u32 %0, %1\n\ts_mov_b64 exec, -1" :: "v"(a), "v"(one) : "memory");
}
__device__ __forceinline__ void dma16(unsigned vo, const void* sb, unsigned lds) {
  asm volatile("s_mov_b32 m0, %2\n\ts_nop 0\n\tglobal_load_lds_dwordx4 %0, %1" :: "v"(vo), "s"(sb), "s"(lds) : "memory");
}
template <int LO, int GO> __device__ __forceinline__ void dma32s(unsigned vo0, unsigned vo1, const void* sb, unsigned lds) {
  asm volatile("s_add_u32 m0, %5, %6\n\ts_add_u32 vcc_lo, %2, %4\n\ts_addc_u32 vcc_hi, %3, 0\n\t"
               "global_load_lds_dwordx4 %0, vcc\n\tglobal_load_lds_dwordx4 %1, vcc offset:1024"
               :: "v"(vo0), "v"(vo1), "s"((unsigned)(uintptr_t)sb), "s"((unsigned)((uintptr_t)sb >> 32)), "i"(GO), "s"(lds), "i"(LO) : "memory", "scc", "vcc");
}
template <int LO, int GO, int WRAP, int LIM> __device__ __forceinline__ void dma32r(unsigned vo0, unsigned vo1, const void* sb, unsigned lds, int rot) {
  const char* s2 = (const char*)sb - WRAP;
  asm volatile("s_add_u32 m0, %5, %6\n\ts_cmp_ge_u32 %7, %8\n\ts_cselect_b64 vcc, %3, %2\n\ts_add_u32 vcc_lo, vcc_lo, %4\n\ts_addc_u32 vcc_hi, vcc_hi, 0\n\t"
               "global_load_lds_dwordx4 %0, vcc\n\tglobal_load_lds_dwordx4 %1, vcc offset:1024"
               :: "v"(vo0), "v"(vo1), "s"(sb), "s"(s2), "i"(GO), "s"(lds), "i"(LO), "s"(rot), "i"(LIM) : "memory", "scc", "vcc");
}
__device__ __forceinline__ void dma16o(unsigned vo, const void* sb, int go, unsigned lds, int lo) {
  asm volatile("s_add_u32 m0, %3, %4\n\ts_nop 0\n\tglobal_load_lds_dwordx4 %0, %1 offset:%2" :: "v"(vo), "s"(sb), "i"(go), "s"(lds), "i"(lo - go) : "memory", "scc");
}
__device__ __forceinline__ void st16(unsigned vo, const void* sb, u32x4 v) {
  asm volatile("global_store_dwordx4 %0, %1, %2 sc1\n\ts_nop 1" :: "v"(vo), "v"(v), "s"(sb) : "memory");
}
__device__ __forceinline__ void touch(unsigned vo, const void* sb, unsigned lds) {
  asm volatile("s_mov_b32 m0, %2\n\ts_nop 0\n\tglobal_load_lds_dword %0, %1" :: "v"(vo), "s"(sb), "s"(lds) : "memory");
}
__device__ __forceinline__ void st8(unsigned vo, const void* sb, u32x2 v) { asm volatile("global_store_dwordx2 %0, %1, %2 sc1" :: "v"(vo), "v"(v), "s"(sb) : "memory"); }
template <class A, class B> __device__ __forceinline__ void MFMA(f32x4& acc, const A& a, const B& b) {
  asm volatile("v_mfma_f32_16x16x32_bf16 %0, %1, %2, %0" : "+v"(acc) : "v"(a), "v"(b));
}
template <class A, class B> __device__ __forceinline__ void MFMA0(f32x4& acc, const A& a, const B& b) {
  asm volatile("v_mfma_f32_16x16x32_bf16 %0, %1, %2, 0" : "=&v"(acc) : "v"(a), "v"(b));
}
template <bool Z, class A, class B> __device__ __forceinline__ void MFMAW(int n, f32x4& acc, const A& a, const B& b) {
  if (n < 0) { if (Z) MFMA0(acc, a, b); else MFMA(acc, a, b); }
  else if (Z) asm volatile("s_waitcnt lgkmcnt(%3)\n\tv_mfma_f32_16x16x32_bf16 %0, %1, %2, 0" : "=&v"(acc) : "v"(a), "v"(b), "i"(n));
  else asm volatile("s_waitcnt lgkmcnt(%3)\n\tv_mfma_f32_16x16x32_bf16 %0, %1, %2, %0" : "+v"(acc) : "v"(a), "v"(b), "i"(n));
}
__device__ __forceinline__ float sel4(float a, float b, float c, float d, u64 m1, u64 m2) {
  float r, t;
  asm volatile("v_cndmask_b32_e64 %0, %2, %3, %6\n\tv_cndmask_b32_e64 %1, %4, %5, %6\n\tv_cndmask_b32_e64 %0, %0, %1, %7"
               : "=&v"(r), "=&v"(t) : "v"(a), "v"(b), "v"(c), "v"(d), "s"(m1), "s"(m2));
  return r;
}
template <class T> __device__ __forceinline__ void pin1(T& r) { asm volatile("" : "+v"(r)); }
template <class T> __device__ __forceinline__ T opq(T r) { asm volatile("" : "+v"(r)); return r; }
template <class... T> __device__ __forceinline__ void pin(T&... r) { (pin1(r), ...); }
template <int N, class... T> __device__ __forceinline__ void wait_vm(T&... r) { asm volatile("s_waitcnt vmcnt(%0)" :: "n"(N) : "memory"); (pin1(r), ...); }
template <int N, class... T> __device__ __forceinline__ void wait_lg(T&... r) { asm volatile("s_waitcnt lgkmcnt(%0)" :: "n"(N) : "memory"); (pin1(r), ...); }
template <int OFF, class T> __device__ __forceinline__ void ds16(T& d, unsigned a) {
  asm volatile("ds_read_b128 %0, %1 offset:%2" : "=&v"(d) : "v"(a), "i"(OFF) : "memory");
}
template <int OFF> __device__ __forceinline__ void dsw16(unsigned a, const f32x4& v) {
  asm volatile("ds_write_b128 %0, %1 offset:%2" :: "v"(a), "v"(v), "i"(OFF) : "memory");
}
__device__ __forceinline__ void dsw4(unsigned a, float v) { asm volatile("ds_write_b32 %0, %1" :: "v"(a), "v"(v) : "memory"); }
#define OSC ""
#define HSC ""
template <int OFF, class T> __device__ __forceinline__ void ld16h(T& d, unsigned vo, const void* sb) {
  asm volatile("global_load_dwordx4 %0, %1, %2 offset:%3 " HSC : "=&v"(d) : "v"(vo), "s"(sb), "i"(OFF) : "memory");
}
template <class T> __device__ __forceinline__ void reld6(T (&B)[6], unsigned vo, const void* sb) {
  asm volatile("global_load_dwordx4 %0, %6, %7 offset:-2048 sc1\n\tglobal_load_dwordx4 %1, %6, %7 offset:-1024 sc1\n\t"
               "global_load_dwordx4 %2, %6, %7 sc1\n\tglobal_load_dwordx4 %3, %6, %7 offset:1024 sc1\n\t"
               "global_load_dwordx4 %4, %6, %7 offset:2048 sc1\n\tglobal_load_dwordx4 %5, %6, %7 offset:3072 sc1"
               : "+v"(B[0]), "+v"(B[1]), "+v"(B[2]), "+v"(B[3]), "+v"(B[4]), "+v"(B[5]) : "v"(vo), "s"(sb) : "memory");
}

constexpr int L_FLAG = 0;
constexpr int L_DUM = 256;
constexpr int L_XM = 512;
constexpr int L_XR = 768;
constexpr int L_CS = 1024;
constexpr int L_ZS = 2048;
constexpr int L_CS2 = 3072;
constexpr int L_Z1 = 4096;
constexpr int L_RAW = L_Z1;
constexpr int L_SLOT = 16384;
constexpr int L_KV = 512, KV_P = 3088, L_WO = L_KV + 8 * KV_P;
static_assert(L_WO % 16 == 0 && L_WO + 4 * 1024 <= L_SLOT + 2 * 8192, "staging");
constexpr int L_P2 = L_SLOT + 12 * 8192;
constexpr int CV_C = 4608, CV_CS = 4160, CV_BUF = CV_C + 2 * CV_CS;
constexpr int M_CV = L_SLOT;
constexpr int M_RING = L_SLOT + 6 * 8192;
constexpr int P3_G = L_SLOT;
constexpr int P3_C = P3_G + 80 * 1024;
constexpr int yat(int j) { return M_CV + (j >> 1) * CV_BUF + 8192 + (j & 1) * 2048; }
constexpr int M_LDS = 163840;
static_assert(3 * CV_BUF + 8192 <= 6 * 8192 && P3_C + 60 * 1024 <= M_LDS && CV_BUF >= 8192 + 4096, "LDS map");

enum { UX, UA, UB, UV };
struct Unit { int kind, idx, need, free, slot; };
#define SCHED gugugugwBuluouk_uuxuu_uuxuxu_uuxuxuu_Euuuuh_vuumumuh_vuumumuhF_vuvumum_DRvuuUHmmmmSptt_VhhhttttttJItttttttt
#define STR_(x) #x
#define STR(x) STR_(x)
constexpr char SCH[] = STR(SCHED);
constexpr int NSCH = sizeof(SCH) - 1;
constexpr int XNEED[10] = {1, 2, 2, 3, 3, 4, 4, 5, 5, 6};
constexpr int VNEED[5] = {5, 6, 7, 7, 8};
constexpr int XSLOT[10] = {17, 16, 15, 13, 10, 14, 11, 17, 9, 16}, WSLOT[10] = {6, 2, 4, 0, 6, 12, 10, 4, 0, 8}, VSLOT[10] = {8, 15, 3, 2, 14, 17, 16, 15, 7, 6}, XW = 6;
constexpr int cnt(char c, int e = NSCH) { int n = 0; for (int i = 0; i < e; ++i) n += SCH[i] == c || (c == 'u' && SCH[i] == 'g'); return n; }
constexpr int pos(char c, int k = 0) { for (int i = 0; i < NSCH; ++i) if (SCH[i] == c && k-- == 0) return i; return NSCH; }
constexpr Unit unit(int n) {
  for (int b = 1; b < 11; ++b) {
    for (int c = 0; c < 10; ++c) if (XNEED[c] == b && n-- == 0) return {UX, c, b, b + 1, XSLOT[c]};
    for (int c = 0; c < 5; ++c) if (VNEED[c] == b && n-- == 0) return {UV, c, b, b + 1, VSLOT[c]};
    if (n < 2) return {n ? UB : UA, b - 1, b < 8 ? b : 8, b < 7 ? b + 2 : 9, WSLOT[b - 1] + n};
    n -= 2;
  }
  return {UV, n + 5, 10, 99, VSLOT[n + 5]};
}
constexpr int FLAGB = cnt('_', pos('D')), GZ = cnt('_', pos('E'));
constexpr int nvm(char c) { return c == 'u' ? 2 : (c == 'H' ? 5 : (c == 'U' ? 4 : (c == 'F' || c == 'h' || c == 't' || c == 'w'))); }
constexpr int behind(int g, int e, char w) {
  const int b = cnt('_', e) + 1;
  int n = 0;
  for (int i = 0; i < e; ++i) {
    const char c = SCH[i];
    if (g == 0) { if (c == 'g') n = w == '_' && unit(cnt('u', i)).need <= b ? 0 : n + 2; if (c == 'B') n += 2; continue; }
    n = (w == '_' ? (c == 'u' && unit(cnt('u', i)).need <= b) || (c == 'h' && b == FLAGB) || (c == 'k' && b == GZ) : c == w) ? 0 : n + nvm(c);
  }
  return g == 0 && b >= GZ ? 0 : n;
}
constexpr bool plan_ok() {
  if (cnt('u') != 40 || cnt('_') != 9 || cnt('x') != 5 || cnt('h') != 6 || cnt('t') != 16 || cnt('I') * cnt('J') * cnt('p') != 1 || cnt('v') != 5 || cnt('m') != 10) return false;
  if (cnt('B') * cnt('D') * cnt('E') * cnt('F') * cnt('l') * cnt('o') * cnt('k') * cnt('H') * cnt('U') * cnt('V') * cnt('R') * cnt('S') != 1) return false;
  if (pos('l') < pos('B') || pos('o') < pos('B') || pos('k') < pos('l') || pos('k') > pos('_') || pos('o') > pos('_') || cnt('g', pos('B')) != cnt('g')) return false;
  if (cnt('_', pos('h', 2)) >= FLAGB || GZ < 3 || GZ > 6 || FLAGB < GZ || FLAGB > 9 || pos('o') > pos('k') || pos('H') > pos('I') || pos('F') > pos('U')) return false;
  if (cnt('_', pos('R')) != 8 || cnt('_', pos('S')) != 8 || pos('S') < pos('R') || cnt('_', pos('I')) != 9 || pos('J') < pos('U') || cnt('_', pos('J')) != 9) return false;
  if (cnt('_', pos('V')) != 9 || cnt('u', pos('V')) < 40) return false;
  for (int c = 0; c < 5; ++c) if (cnt('_', pos('x', c)) != XNEED[c] || cnt('_', pos('v', c)) != VNEED[c] || pos('m', 2 * c) < pos('v', c)) return false;
  if (cnt('_', pos('p')) != 8 || pos('p') < pos('m', 9)) return false;
  int fr[18] = {1, 1};
  for (int i = 0; i < NSCH; ++i) if (SCH[i] == 'u' || SCH[i] == 'g') {
    const Unit u = unit(cnt('u', i));
    const int p = cnt('_', i);
    if (p < fr[u.slot] || p >= u.need) return false;
    fr[u.slot] = u.free;
  }
  return true;
}
static_assert(plan_ok(), "phase A plan");
template <int I, int E, class F> __device__ __forceinline__ void sfor(F&& f) { if constexpr (I < E) { f(IC(I)); sfor<I + 1, E>(f); } }

#define CVP -1
__device__ __forceinline__ void conv_core(unsigned buf, int lane, unsigned Dw, f32x4 (&ac)[8], unsigned pf) {
  const int l15 = lane & 15, g4 = lane >> 4;
  const int c = (l15 + 1) & 1;
  unsigned aa[4];
#pragma unroll
  for (int k = 0; k < 4; ++k) aa[k] = buf + CV_C + c * CV_CS + (2031 - l15 + 8 * g4 - c) * 2 - 512 - 1024 * k;
  unsigned bb = buf + 512 + 32 * l15 - 32 + 16 * g4;
  u32x2 A[2][8][2];
  u32x4 B[2][8];
  constexpr int TT[36] = {7, 6, 5, 4, 7, 6, 5, 4, 7, 6, 5, 3, 7, 6, 5, 4, 7, 6, 3, 2, 7, 6, 5, 4, 7, 3, 2, 1, 7, 6, 5, 4, 3, 2, 1, 0};
  constexpr int AA[36] = {0, 0, 0, 0, 1, 1, 1, 1, 2, 2, 2, 0, 3, 3, 3, 2, 4, 4, 1, 0, 5, 5, 4, 3, 6, 2, 1, 0, 7, 6, 5, 4, 3, 2, 1, 0};
  auto ld1 = [&](int p, int n) {
    if (n < 16) {
      const int a = n >> 1, o = ((a & 1) ? 0 : 128) + 2 * (n & 1);
      asm volatile("ds_read2_b32 %0, %1 offset0:%2 offset1:%3" : "=&v"(A[p][(n >> 1) & 7][n & 1]) : "v"(aa[(a >> 1) & 3]), "i"(o), "i"(o + 1) : "memory");
    } else
      asm volatile("ds_read_b128 %0, %1 offset:%2" : "=&v"(B[p][n & 7]) : "v"(bb), "i"(512 * (n & 7)) : "memory");
  };
  auto next = [&](int dl) {
#pragma unroll
    for (int k = 0; k < 4; ++k) aa[k] += dl;
    bb += dl;
    SB();
  };
  auto land = [&](int p) {
    asm volatile("s_waitcnt lgkmcnt(0)" : "+v"(A[p][0][0]), "+v"(A[p][0][1]), "+v"(A[p][1][0]), "+v"(A[p][1][1]), "+v"(A[p][2][0]), "+v"(A[p][2][1]),
                 "+v"(A[p][3][0]), "+v"(A[p][3][1]), "+v"(A[p][4][0]), "+v"(A[p][4][1]), "+v"(A[p][5][0]), "+v"(A[p][5][1]), "+v"(A[p][6][0]), "+v"(A[p][6][1]),
                 "+v"(A[p][7][0]), "+v"(A[p][7][1]), "+v"(B[p][0]), "+v"(B[p][1]), "+v"(B[p][2]), "+v"(B[p][3]), "+v"(B[p][4]), "+v"(B[p][5]),
                 "+v"(B[p][6]), "+v"(B[p][7]) :: "memory");
    SB();
  };
  auto mm = [&](int p) {
#pragma unroll
    for (int n = 0; n < 36; ++n) {
      const int t = TT[n], a = AA[n];
      const u32x4 av = {A[p][a][0][0], A[p][a][0][1], A[p][a][1][0], A[p][a][1][1]};
      MFMA(ac[t], av, B[p][t - a]);
      if (n < 24) ld1(p ^ 1, n);
      SB();
    }
    land(p ^ 1);
  };
#pragma unroll
  for (int n = 0; n < 24; ++n) ld1(0, n);
  u32x4 ad;
#pragma unroll
  for (int e = 0; e < 4; ++e) ad[e] = lane == 32 + 2 * e + (lane & 1) + (lane & 8) * 3 ? Dw << (16 * (lane & 1)) : 0u;
  pin(ad);
  land(0);
#pragma unroll
  for (int t = 0; t < 8; ++t) MFMA0(ac[t], ad, B[0][t]);
#pragma unroll 1
  for (int r = 0; r < 8; r += 2) {
    if (CVP >= 0 && !(CVP & 1)) flag_wait_asm(pf, r == CVP ? 4u : 0u);
    next(-64);
    mm(0);
    if (CVP >= 0 && (CVP & 1)) flag_wait_asm(pf, r + 1 == CVP ? 4u : 0u);
    next(r < 6 ? -64 : 0);
    mm(1);
  }
  asm volatile("s_nop 15" : "+v"(ac[0]), "+v"(ac[1]), "+v"(ac[2]), "+v"(ac[3]), "+v"(ac[4]), "+v"(ac[5]), "+v"(ac[6]), "+v"(ac[7]));
}

#define ROTA 3
#define RA1 1
#define RA2 0
#define RA3 0
#define RB1 0
#define RB2 4
#define RB3 0
#define G2P 0
#define RB4 2
#define THR 24
#define RB5 2
#define YS 8
#define WO3 0
#define C3P 0
#define FE3 9
#define WSH 1
#define T3L 4
#define B2E 0
#define CPR 1
#define STW 1
#define PN 10
#define P2P 2
#define SGS 11
#define ANUM {0, 0, 0, 0, 0, 0, 0, 2, 2, 2, 2, 0, 0, 0, 0, 0}
constexpr int cum16(const int (&a)[16], int n) { int c = 0; for (int i = 0; i < n; ++i) c += a[i]; return c; }

__global__ __launch_bounds__(512) void k_f(
    const u16* __restrict__ x, const u16* __restrict__ kin, const u16* __restrict__ vin, const u16* __restrict__ shift_k,
    const u16* __restrict__ ssm_k, const u16* __restrict__ ssm_D, const u16* __restrict__ w_in, const u16* __restrict__ w_sp,
    const u16* __restrict__ b_sp, const u16* __restrict__ w_out, u16* __restrict__ out, char* ws) {
  extern __shared__ __attribute__((aligned(16))) char smem[];
  const int tid = threadIdx.x, lane = tid & 63, wave = __builtin_amdgcn_readfirstlane(tid >> 6);
  const int b = blockIdx.x;
  const int xcd = b & 7, idx = b >> 3;
  const int rb = 8 * (xcd >> 1) + (idx >> 2), cb = 4 * (xcd & 1) + (idx & 3);
  const int t0 = rb * 64, d0 = cb * 80;
  const int g4 = lane >> 4, l15 = lane & 15;
  const unsigned lin = (unsigned)(lane * 16);
  const unsigned lb = lds_addr(smem), flg = lb + L_FLAG;
  AB_DECL;
  AB(0);
  asm volatile("" :: "s"(x), "s"(kin), "s"(vin), "s"(shift_k), "s"(ssm_k), "s"(ssm_D), "s"(w_in), "s"(w_sp), "s"(b_sp), "s"(w_out), "s"(out), "s"(ws));
  u64 tag = *(const __attribute__((address_space(4))) u64*)(uintptr_t)ws;
  int tmo = 1 << 16;
#define CWARM 1
  auto cwarm = [&]() {
    u64 pc;
    unsigned clen;
    asm volatile("s_getpc_b64 %0\n.Lpc_here_%=:\n\ts_mov_b32 %1, .Lcode_end-.Lpc_here_%=\n\t.subsection 2\n.Lcode_end:\n\t.fill 256, 4, 0xBF800000\n\t.subsection 0" : "=s"(pc), "=s"(clen));
    touch(min((unsigned)(idx * 1536 + min(lane, 11) * 128), clen - 4u) & ~3u, (const void*)pc, lb + L_DUM);
  };

  const int cw = wave & 3, wm = wave & 1, wl = (wave >> 1) & 1, ksb = wm ^ wl, odd = cb & 1, pr = cb >> 1;
  const int r8 = lane >> 3, c8 = lane & 7;
  unsigned voX[2], voW[2];
#pragma unroll
  for (int h = 0; h < 2; ++h) {
    voX[h] = (unsigned)((h * 8 + r8) * (D_ * 2) + ((c8 ^ (h * 4 + (r8 & 3))) << 4) - h * 1024);
    voW[h] = (unsigned)((h * 8 + r8) * (F2_ * 2) + ((c8 ^ (h * 4 + (r8 & 2))) << 4) - h * 1024);
  }
  const int rot = (RA1 * ((rb & 7) * 5 >> 2) + ROTA * (cb & 3) + RA2 * ((rb >> 3) * 5 >> 1) + RA3 * (cb >> 2)) % 10, rot2 = (RB1 * (cb & 3) + RB2 * (cb >> 2) + RB3 * (rb & 7) + RB4 * (rb >> 3) + RB5 * (cb & 1)) & 7;
  const char* gX = (const char*)x + (size_t)(t0 + cw * 16) * (D_ * 2) + rot * 128;
  const char* gW = (const char*)w_in + (size_t)(cw * 16 + rot * 64) * (F2_ * 2) + pr * 384;
  const char *gV = gW + odd * 256, *gB = gW + (odd ? 128 : FH_ * 2 + 128);
  const char* gS = (const char*)w_sp + (size_t)t0 * (S_ * 2);
  const unsigned aoff[2] = {(unsigned)(r8 * 4096 + ((c8 ^ r8) << 4)), (unsigned)(r8 * 4096 + ((c8 ^ r8) << 4) + 32768)};
  const unsigned ldw = lb + cw * 2048;
  auto issue = [&](auto nc) {
    constexpr Unit u = unit(decltype(nc)::value);
    constexpr int lo = L_SLOT + u.slot * 8192;
    if (u.kind == UX) dma32r<lo, u.idx * 128, 1280, 10 - u.idx>(voX[0], voX[1], gX, ldw, rot);
    else dma32r<lo, u.idx * (64 * F2_ * 2) + (u.kind == UA ? FH_ * 2 : 0), 640 * F2_ * 2, 10 - u.idx>(voW[0], voW[1], u.kind == UB ? gB : gV, ldw, rot);
    SB();
  };
  const int kd = d0 + min(wave, 4) * 16 + r8 * 2;
  u32x4 sw;
  {
    const int ks0 = t0 + wave * 8;
#pragma unroll
    for (int i = 0; i < 3; ++i) {
      const int id = min(i * 64 + lane, 189), r = id / 10, c = id % 10;
      const char* kp = (r < 11 ? (const char*)kin + (size_t)max(ks0 - 3 + r, 0) * (D_ * 2) : (const char*)vin + (size_t)(ks0 + r - 11) * (D_ * 2)) + cb * 160 + c * 16;
      asm volatile("s_mov_b32 m0, %1\n\ts_nop 0\n\tglobal_load_lds_dwordx4 %0, off" :: "v"(kp), "s"(lb + L_KV + wave * KV_P + i * 1024) : "memory");
    }
    if (CWARM && wave == 5) cwarm();
    SB();
  }
  auto ldsw = [&]() { asm volatile("global_load_dwordx4 %0, %1, %2" : "=&v"(sw) : "v"(kd * 8), "s"(shift_k) : "memory"); SB(); };
  unsigned kk[11], vv[8];
  u32x4 oa, ob;
  auto kvld = [&]() {
    const char* kl = smem + L_KV + c8 * KV_P + min(wave, 4) * 32 + r8 * 4;
#pragma unroll
    for (int i = 0; i < 11; ++i) kk[i] = *(const unsigned*)(kl + i * 160);
#pragma unroll
    for (int i = 0; i < 8; ++i) vv[i] = *(const unsigned*)(kl + (11 + i) * 160);
  };
  auto kvm = [&](int e0, int e1) {
    const f32x2 w0 = {lo16(sw[0]), lo16(sw[2])}, w1 = {hi16(sw[0]), hi16(sw[2])};
    const f32x2 w2 = {lo16(sw[1]), lo16(sw[3])}, w3 = {hi16(sw[1]), hi16(sw[3])};
    f32x2 kf[11], rr[8];
#pragma unroll
    for (int i = 2 * e0; i < 2 * e1 + 3; ++i) kf[i] = unpk((i < 3 && t0 + c8 == 0) ? 0u : kk[i]);
#pragma unroll
    for (int i = 2 * e0; i < 2 * e1; ++i) {
      f32x2 a0 = kf[i] * w3;
      a0 = __builtin_elementwise_fma(kf[i + 1], w2, a0);
      a0 = __builtin_elementwise_fma(kf[i + 2], w1, a0);
      a0 = __builtin_elementwise_fma(kf[i + 3], w0, a0);
      rr[i] = a0 * unpk(vv[i]);
    }
#pragma unroll
    for (int e = e0; e < e1; ++e) { oa[e] = pack2(rr[2 * e][0], rr[2 * e + 1][0]); ob[e] = pack2(rr[2 * e][1], rr[2 * e + 1][1]); }
  };
  auto kvst = [&]() {
    const unsigned ko = (unsigned)((kd * S_ + t0 + c8 * 8) * 2);
    st16(ko, ws + WS_KV, oa);
    st16(ko, ws + WS_KV + S_ * 2, ob);
    SB();
  };
  auto barrier0 = [&](int nv) {
    SB();
    asm volatile("s_waitcnt vmcnt(%0) lgkmcnt(0)\n\ts_barrier" :: "n"(nv) : "memory");
    SB();
    asm volatile("" : "+v"(sw), "+s"(tag));
    tag += 1;
    asm volatile("" : "+s"(tag));
  };
  unsigned pX[2], pW[2];
#pragma unroll
  for (int k = 0; k < 2; ++k) {
    const int q = l15 >> 2, p = lane & 3, sz = (q >> 1) | ((g4 & 1) << 1);
    pX[k] = lb + (wm * 32 + q * 8 + p) * 128 + ((((k ^ ksb) * 4 + g4) ^ (l15 & 7)) << 4);
    pW[k] = lb + ((k ^ ksb) * 32 + g4 * 8 + q) * 128 + (((sz << 1) | (p >> 1)) << 4) + (p & 1) * 8;
  }
  bf16x8 ones;
#pragma unroll
  for (int e = 0; e < 8; ++e) ones[e] = (short)0x3F80;
  f32x4 xm[2], xr[2];
  auto ldxs = [&]() {
#pragma unroll
    for (int i = 0; i < 2; ++i) {
      xm[i] = *(const f32x4*)(smem + L_XM + (wm * 32 + g4 * 8 + i * 4) * 4);
      xr[i] = *(const f32x4*)(smem + L_XR + (wm * 32 + g4 * 8 + i * 4) * 4);
    }
  };
  auto act = [&](const f32x4& a, float c, int i) {
    const f32x2 nc = {-c, -c};
    const f32x2 lo = gelu2(__builtin_elementwise_fma((f32x2){xm[i][0], xm[i][1]}, nc, (f32x2){a[0], a[1]}) * (f32x2){xr[i][0], xr[i][1]});
    const f32x2 hi = gelu2(__builtin_elementwise_fma((f32x2){xm[i][2], xm[i][3]}, nc, (f32x2){a[2], a[3]}) * (f32x2){xr[i][2], xr[i][3]});
    return (f32x4){lo[0], lo[1], hi[0], hi[1]};
  };
  auto pack8 = [&](const f32x4& a, const f32x4& c) { return (u32x4){pack2(a[0], a[1]), pack2(a[2], a[3]), pack2(c[0], c[1]), pack2(c[2], c[3])}; };
  auto z1at = [&](int f0) { return (u32x4*)(smem + L_Z1 + (f0 + l15) * 128 + (((wm * 4 + g4) ^ (l15 & 7)) << 4)); };
  auto z1put = [&](int f0, const u32x4& v) { *z1at(f0) = v; };
  f32x4 ac2[2][2], cs2[2];
  s16x4 VF[10][2][2];
  unsigned aV[2][2];
  auto rdV = [&](auto hc_, auto nc, int r) {
    constexpr int n = decltype(nc)::value, c = 5 * decltype(hc_)::value + (n >> 1), so = (VSLOT[c] - 2) * 8192, po = n < 2 ? -1 : ((VSLOT[n < 2 ? c : c - 1] - 2) * 8192) >> 16;
    const int jj = r >> 1;
    if (!(r & 1)) {
      if ((so >> 16) != po) aV[n & 1][jj] = opq((pW[n & 1] ^ (unsigned)(wl * 64 + jj * 32)) + L_SLOT + 2 * 8192 + (so & ~0xFFFF));
      VF[n][jj][0] = tr_asm<(so & 0xFFFF)>(aV[n & 1][jj]);
    } else VF[n][jj][1] = tr_asm<(so & 0xFFFF) + 512>(aV[n & 1][jj]);
    SB();
  };
  auto mm2 = [&](auto hc_, auto nc, bf16x8 (*X)[2][2], int nw, auto&& nxt) {
    constexpr int n = decltype(nc)::value, j = n >> 1, k = n & 1;
    constexpr bool z = n == 0 && !decltype(hc_)::value;
    const bf16x8 w0 = join8(VF[n][0][0], VF[n][0][1]), w1 = join8(VF[n][1][0], VF[n][1][1]);
    if (k == 0) { MFMAW<n == 0>(nw, cs2[0], ones, w0); MFMAW<n == 0>(-1, cs2[1], ones, w1); }
    MFMAW<z>(k ? nw : -1, ac2[0][0], X[j][k][0], w0); nxt(0);
    MFMAW<z>(-1, ac2[1][0], X[j][k][1], w0); nxt(1);
    MFMAW<z>(-1, ac2[0][1], X[j][k][0], w1); nxt(2);
    MFMAW<z>(-1, ac2[1][1], X[j][k][1], w1); nxt(3);
    if constexpr (n == 9) {
      pin(cs2[0], cs2[1]);
      if (g4 < 2) *(float*)(smem + L_CS2 + (wave * 32 + lane) * 4) = g4 ? cs2[1][0] : cs2[0][0];
    }
    SB();
  };
  const unsigned p2a = lb + L_P2 + cw * 4096 + lin;
  auto sttag = [&](const char* p) {
    asm volatile("s_mov_b64 exec, 1\n\tglobal_store_dwordx2 %0, %1, %2 sc1\n\ts_mov_b64 exec, -1" :: "v"(lin), "v"((u32x2){opq((unsigned)tag), opq((unsigned)(tag >> 32))}), "s"(p) : "memory");
  };
  auto ldtag = [&](u32x2& f, unsigned vo, const char* p) { asm volatile("global_load_dwordx2 %0, %1, %2 sc1" : "=&v"(f) : "v"(vo), "s"(p) : "memory"); };
  auto notag = [&](const u32x2& f) { return __builtin_amdgcn_ballot_w64(f[0] != (unsigned)tag || f[1] != (unsigned)(tag >> 32)) != 0; };
  auto waittag = [&](u32x2& f, unsigned vo, const char* p) {
    while (__builtin_expect(notag(f) && tmo > 0, 0)) {
      --tmo;
      __builtin_amdgcn_s_sleep(4);
      ldtag(f, vo, p);
      wait_vm<0>(f);
    }
  };
  u32x2 ef;

  char* const hf = ws + WS_H;
  bf16x8 Wq[15];
  const char* wg = ws + WS_WO + (size_t)(wave * 120 + cb * 5) * 1024;
  auto ldW = [&]() {
    ldtag(ef, (unsigned)((lane & 3) * 8), ws + WS_LF + (rb * 8 + wave) * 32);
#pragma unroll
    for (int n = 0; n < 15; ++n)
      asm volatile("global_load_dwordx4 %0, %1, %2 offset:%3 sc1" : "=&v"(Wq[n]) : "v"(lin), "s"(wg + (n / 5) * 40960 + 2048), "i"((n % 5) * 1024 - 2048) : "memory");
    SB();
  };
  f32x4 ac3[5];
  auto gemm3 = [&](auto khc) {
    constexpr int kh = decltype(khc)::value;
    unsigned hoff[4];
#pragma unroll
    for (int it = 0; it < 4; ++it) hoff[it] = (unsigned)(((rb * 4 + ((cw + 1 + it) & 3)) * 24 + 3 * wave) * 1024) + lin;
    u32x4 Hq[4][3];
    auto ldh1 = [&](int it, int k) {
      asm volatile("global_load_dwordx4 %0, %1, %2 offset:%3 " HSC : "=&v"(Hq[it][k]) : "v"(hoff[it]), "s"(hf), "i"(k * 1024) : "memory");
    };
    waittag(ef, (unsigned)((lane & 3) * 8), ws + WS_LF + (rb * 8 + wave) * 32);
    AB(18);
    signal1(flg + 48);
    flag_wait_asm(flg + 48, 8u);
    AB(19);
    __builtin_amdgcn_s_setprio(3);
    ldh1(0, 0); ldh1(0, 1); ldh1(0, 2);
    __builtin_amdgcn_s_setprio(2);
    ldh1(1, 0); ldh1(1, 1); ldh1(1, 2);
    __builtin_amdgcn_s_setprio(kh * C3P);
    SB();
    const unsigned px = lb + (kh ? P3_C + cw * 15360 : P3_G + cw * 20480) + lin;
    f32x4 pa[5][5], ae[5];
    auto put = [&](int it, int j) { asm volatile("ds_write_b128 %0, %1 offset:%2" :: "v"(px), "v"((it & 1) ? ac3[j] : ae[j]), "i"((it * 5 + j) * 1024) : "memory"); };
    auto fetch = [&](int n, int g, int q) {
      const int ms = (cw - 1 - q) & 3;
      const unsigned pn = lb + (g ? P3_C + ms * 15360 : P3_G + ms * 20480) + lin;
#pragma unroll
      for (int j = 0; j < 5; ++j) asm volatile("ds_read_b128 %0, %1 offset:%2" : "=&v"(pa[n][j]) : "v"(pn), "i"((q * 5 + j) * 1024) : "memory");
      SB();
    };
    sfor<0, 4>([&](auto itc) {
      constexpr int it = decltype(itc)::value;
      wait_vm<(it < 3 ? 3 : 0)>(Hq[it][0], Hq[it][1], Hq[it][2]);
      if constexpr (it == 0) pin(Wq[0], Wq[1], Wq[2], Wq[3], Wq[4], Wq[5], Wq[6], Wq[7], Wq[8], Wq[9], Wq[10], Wq[11], Wq[12], Wq[13], Wq[14]);
      auto bad = [&]() { return __builtin_amdgcn_ballot_w64(umx(umx(Hq[it][0][3], Hq[it][1][3]), Hq[it][2][3]) == HOLE) != 0; };
      if (__builtin_expect(bad(), 0)) {
        do {
          --tmo;
          asm volatile("s_sleep 2\n\tglobal_load_dwordx4 %0, %3, %4 sc1\n\tglobal_load_dwordx4 %1, %3, %4 offset:1024 sc1\n\t"
                       "global_load_dwordx4 %2, %3, %4 offset:2048 sc1\n\ts_waitcnt vmcnt(0)"
                       : "+v"(Hq[it][0]), "+v"(Hq[it][1]), "+v"(Hq[it][2]) : "v"(hoff[it]), "s"(hf) : "memory");
        } while (bad() && tmo > 0);
      }
      SB();
#pragma unroll
      for (int k = 0; k < 3; ++k)
#pragma unroll
        for (int j = 0; j < 5; ++j) {
          f32x4& ac = (it & 1) ? ac3[j] : ae[j];
          if (k == 0) MFMA0(ac, Wq[j], Hq[it][0]); else MFMA(ac, Wq[k * 5 + j], Hq[it][k]);
          if constexpr (it + 2 < 4) { if (k == 0 && j < 3) ldh1(it + 2 < 4 ? it + 2 : 0, j); }
          const int m = k * 5 + j - WO3;
          if (it > 0 && m >= 0 && m < 10 && !(m & 1)) put(it - 1, m >> 1);
          if (it > 0 && m == 8) signal1(flg + 160 + (it < 3 ? kh : 2) * 16 + ((cw + it) & 3) * 4);
          if (it == 3 && m == FE3) { flag_wait_asm(flg + 160 + wave * 4, 2u); fetch(0, kh, 0); fetch(1, kh, 1); }
          SB();
        }
      if constexpr (it == 3) {
        if (kh) {
          flag_wait_asm(flg + 192 + cw * 4, 3u);
          fetch(2, 1, 2); fetch(3, 0, 2); fetch(4, 0, 3);
        } else asm volatile("s_waitcnt lgkmcnt(0)\n\ts_nop 4" ::: "memory");
        pin(pa[0][0], pa[0][1], pa[0][2], pa[0][3], pa[0][4], pa[1][0], pa[1][1], pa[1][2], pa[1][3], pa[1][4]);
#pragma unroll
        for (int j = 0; j < 5; ++j) {
          pa[0][j] += pa[1][j];
          pin(pa[0][j], ac3[j]);
          ac3[j] += pa[0][j];
          if (!kh) put(3, j);
        }
        if (kh) {
          wait_lg<5>(pa[2][0], pa[2][1], pa[2][2], pa[2][3], pa[2][4], pa[3][0], pa[3][1], pa[3][2], pa[3][3], pa[3][4]);
#pragma unroll
          for (int j = 0; j < 5; ++j) ac3[j] += pa[2][j] + pa[3][j];
          wait_lg<0>(pa[4][0], pa[4][1], pa[4][2], pa[4][3], pa[4][4]);
#pragma unroll
          for (int j = 0; j < 5; ++j) ac3[j] += pa[4][j];
        } else signal1(flg + 192 + cw * 4);
      }
      SB();
      if constexpr (it == 0) AB(20);
      if constexpr (it == 2) AB(21);
    });
    AB(22);
  };
  if (wave >= 4) {
    const int wid = rb * 4 + cw;
    __builtin_amdgcn_s_setprio(3);
    ldsw();
    if (wid < 120) dma16((unsigned)((lane >> 1) * (D_ * 2) + (lane & 1) * 16), w_out + (size_t)(wid / 5) * 32 * D_ + d0 + (wid % 5) * 16, lb + L_WO + cw * 1024);
    SB();
    u32x4 hole = {HOLE, HOLE, HOLE, HOLE};
    bf16x8 XF[5][2][2];
    auto rdXF = [&](auto cc) {
      constexpr int c = decltype(cc)::value, so = L_SLOT + XSLOT[c] * 8192;
#pragma unroll
      for (int k = 0; k < 2; ++k) {
        const unsigned a = pX[k] + (so & ~0x7FFF);
        ds16<(so & 0x7FFF)>(XF[c][k][0], a);
        ds16<(so & 0x7FFF) + 512>(XF[c][k][1], a);
      }
      SB();
    };
    u32x4 TA[16];
    auto ldT = [&](u32x4& r, int d, int w, int i) {
      asm volatile("global_load_dwordx4 %0, %1, %2" : "=&v"(r) : "v"(aoff[i & 1] + (unsigned)(((d + rot2) & 7) * 128)), "s"(gS + ((w + 1 + (i >> 1)) & 3) * 65536 + w * 1024) : "memory");
    };
    auto ldx = [&](int c) {
      u32x4 xv;
      asm volatile("global_load_dwordx4 %0, %1, %2\n\ts_waitcnt vmcnt(0)" : "=&v"(xv) : "v"((unsigned)(lane * (D_ * 2) + c * 16)), "s"(x + (size_t)t0 * D_) : "memory");
      return xv;
    };
    const bool docv = cw < ((rb & 1) ? 2 : 3);
    const int d = d0 + (cw < 2 ? 2 * rb + cw : 64 + (rb >> 1));
    u32x4 hc[4], uc[4];
    unsigned Dw;
    auto ldhc = [&]() {
#pragma unroll
      for (int k = 0; k < 4; ++k) asm volatile("global_load_dwordx4 %0, %1, %2 offset:%3" : "=&v"(hc[k]) : "v"(lin), "s"(ssm_k + (size_t)d * S_), "i"(k * 1024) : "memory");
      asm volatile("global_load_ushort %0, %1, %2" : "=&v"(Dw) : "v"(0u), "s"(ssm_D + d) : "memory");
    };
    unsigned rwo;
    f32x2 ra, rc;
    auto rdraw = [&]() { asm volatile("ds_read_b64 %0, %2\n\tds_read_b64 %1, %2 offset:512" : "=&v"(ra), "=&v"(rc) : "v"(rwo) : "memory"); SB(); };
    const char* efp = ws + WS_EF + cb * 256;
    char* cb_ = smem + M_CV + cw * CV_BUF;
    auto run = [&](auto ic) {
      constexpr int i = decltype(ic)::value, p = cnt('_', i) + 1;
      constexpr char c = SCH[i];
      if constexpr (c == '_') {
        CB(3 * (p - XBAR));
        asm volatile("s_waitcnt vmcnt(%0) lgkmcnt(0)" :: "n"(behind(1, i, '_')) : "memory");
        CB(3 * (p - XBAR) + 1);
        asm volatile("s_barrier" ::: "memory");
        CB(3 * (p - XBAR) + 2);
        SB();
#pragma unroll
        for (int q = 0; q < 5; ++q) if (XNEED[q] + 1 == p) pin(XF[q][0][0], XF[q][0][1], XF[q][1][0], XF[q][1][1]);
        if constexpr (p == 7) {
          rwo = lb + L_RAW + ((lane >> 3) * 4 + (lane & 3)) * 16 + (lane & 4) * 2;
          pin(rwo);
        }
        if constexpr (p == 9) __builtin_amdgcn_s_setprio(CPR);
        if constexpr (p == 2) AB(4);
        if constexpr (p == 6) AB(5);
        if constexpr (p == 9) AB(6);
      }
      if constexpr (c == 'u') issue(IC(cnt('u', i)));
      if constexpr (c == 'x') rdXF(IC(cnt('x', i)));
      if constexpr (c == 'B') {
        AB(1);
        barrier0(behind(1, i, 'B'));
        pin(ones, hole);
      }
      if constexpr (c == 'v') sfor<2 * cnt('v', i), 2 * cnt('v', i) + 2>([&](auto nc) { rdV(IC(0), nc, 0); rdV(IC(0), nc, 1); rdV(IC(0), nc, 2); rdV(IC(0), nc, 3); });
      if constexpr (c == 'm') {
        constexpr int n = cnt('m', i);
        mm2(IC(0), IC(n), XF, VNEED[n >> 1] < p - 1 || (n & 1) ? -1 : 0, [](int) {});
      }
      if constexpr (c == 'p') {
        asm volatile("s_nop 7" : "+v"(ac2[0][0]), "+v"(ac2[0][1]), "+v"(ac2[1][0]), "+v"(ac2[1][1]));
        dsw16<0>(p2a, ac2[0][0]); dsw16<1024>(p2a, ac2[1][0]); dsw16<2048>(p2a, ac2[0][1]); dsw16<3072>(p2a, ac2[1][1]);
      }
      if constexpr (c == 'w')
        touch((unsigned)(min(lane, 19) * (F2_ * 2)), (const char*)w_in + (size_t)(((rb + WSH) & 7) * 80 + cw * 20) * (F2_ * 2) + pr * 384 + odd * 256, lb + L_DUM);
      if constexpr (c == 'l') { if (wave == 7) kvld(); }
      if constexpr (c == 'k') { if (wave == 7) { kvm(0, 4); kvst(); } }
      if constexpr (c == 'E' || c == 'D') { if (wave == 5) sttag(ws + WS_EF + (c == 'D' ? 2048 : 0) + (cb * 32 + rb) * 8); }
      if constexpr (c == 'U') {
        wait_vm<behind(1, i, 'F')>(ef);
        waittag(ef, (unsigned)((lane & 31) * 8), efp);
#pragma unroll
        for (int k = 0; k < 4; ++k) asm volatile("global_load_dwordx4 %0, %1, %2 offset:%3 sc1" : "=&v"(uc[k]) : "v"(lin), "s"(ws + WS_KV + (size_t)d * (S_ * 2)), "i"(k * 1024) : "memory");
      }
      if constexpr (c == 'F') ldtag(ef, (unsigned)((lane & 31) * 8), efp);
      if constexpr (c == 'o') {
        if (wid < 120) {
          const unsigned ra = lb + L_WO + cw * 1024 + (8 * g4 + (l15 >> 2)) * 32 + 8 * (lane & 3);
          s16x4 lo = tr_asm<0>(ra), hi = tr_asm<128>(ra);
          wait_lg<0>(lo, hi);
          st16(lin, ws + WS_WO + (size_t)((wid / 5) * 40 + cb * 5 + wid % 5) * 1024, __builtin_bit_cast(u32x4, join8(lo, hi)));
        }
      }
      if constexpr (c == 'h') {
        constexpr int n = cnt('h', i);
        st16(lin, ws + (n < 3 ? WS_Z2 + (size_t)((rb * 2 + wl) * 48 + cb * 6 + wm * 3 + n) * 1024 : WS_H + (size_t)((rb * 4 + cw) * 24 + cb * 3 + n - 3) * 1024), hole);
      }
      if constexpr (c == 't') { constexpr int n = cnt('t', i); ldT(TA[n], 4 + (n >> 3), cw, n & 7); }
      if constexpr (c == 'H') ldhc();
      if constexpr (c == 'R') rdraw();
      if constexpr (c == 'V') {
        wait_vm<behind(1, i, '_')>();
        signal1(flg + 60);
        AB(2);
      }
      if constexpr (c == 'I') {
        wait_vm<behind(1, i, 'H')>(hc[0], hc[1], hc[2], hc[3], Dw);
        if (docv) {
          if (lane < 36) *(u32x4*)(cb_ + (lane < 32 ? lane * 16 : CV_C + ((lane >> 1) & 1) * CV_CS + 4096 + (lane & 1) * 16)) = (u32x4){0u, 0u, 0u, 0u};
#pragma unroll
          for (int k = 0; k < 4; ++k) {
            const int a = k * 64 + lane;
            const int h0 = k ? __builtin_amdgcn_readlane((int)hc[k ? k - 1 : 0][3], 63) : 0;
            const unsigned hp = (unsigned)__builtin_amdgcn_update_dpp(h0, (int)hc[k][3], 0x138, 0xF, 0xF, false);
            const unsigned w[5] = {hp, hc[k][0], hc[k][1], hc[k][2], hc[k][3]};
            u32x4 sw, mx;
#pragma unroll
            for (int q = 0; q < 4; ++q) {
              sw[q] = (w[4 - q] >> 16) | (w[4 - q] << 16);
              mx[q] = (w[4 - q] & 0xFFFFu) | (w[3 - q] & 0xFFFF0000u);
            }
            char* cp = cb_ + CV_C + (2040 - 8 * a) * 2;
            *(u32x4*)(cp) = sw;
            *(u32x4*)(cp + CV_CS) = mx;
          }
        }
      }
      if constexpr (c == 'J') {
        wait_vm<behind(1, i, 'U')>(uc[0], uc[1], uc[2], uc[3]);
        if (docv) {
#pragma unroll
          for (int k = 0; k < 4; ++k) *(u32x4*)(cb_ + 512 + (k * 64 + lane) * 16) = uc[k];
        }
      }
      if constexpr (c == 'S') {
        wait_lg<0>(ra, rc);
        if (wave == 6) {
          const float s1 = nv(ra[0]) + rc[0], fm = s1 * (1.0f / 640.0f), sm = s1 * fm;
          float fq = (ra[1] - sm) + rc[1];
          if (__builtin_expect(__builtin_amdgcn_ballot_w64(!(fq >= 1e-3f * sm)) != 0, 0)) {
            const f32x2 mm = {fm, fm};
            f32x2 q2 = {0.f, 0.f};
#pragma unroll 1
            for (int c = 0; c < 80; ++c) {
              const u32x4 xv = ldx(c);
#pragma unroll
              for (int e = 0; e < 4; ++e) { const f32x2 d = unpk(xv[e]) - mm; q2 = __builtin_elementwise_fma(d, d, q2); }
            }
            fq = q2[0] + q2[1];
          }
          const float vbf = rbf(fmaxf(fq, 0.f) * (1.0f / 640.0f));
          *(float*)(smem + L_XM + lane * 4) = rbf(fm);
          *(float*)(smem + L_XR + lane * 4) = __builtin_amdgcn_rcpf(rbf(__builtin_amdgcn_sqrtf(rbf(vbf + 1e-5f))));
        }
      }
      SB();
    };
    sfor<0, NSCH>(run);
    __builtin_amdgcn_s_setprio(0);
    AB(9);
    auto ldS = [&](auto dc, auto wc) {
      constexpr int dd = decltype(dc)::value, w = decltype(wc)::value;
      const unsigned to = (unsigned)(((dd + rot2) & 7) * 128);
      if (THR < 60) wait_vm<THR>();
      dma32s<M_RING + w * 24576 + (2 - dd) * 8192, ((w + 1) & 3) * 65536 + w * 1024>(aoff[0] + to, aoff[1] - 1024 + to, gS, lb);
      if (THR < 60) wait_vm<THR>();
      dma32s<M_RING + w * 24576 + (2 - dd) * 8192 + 2048, ((w + 2) & 3) * 65536 + w * 1024>(aoff[0] + to, aoff[1] - 1024 + to, gS, lb);
      if (THR < 60) wait_vm<THR>();
      dma32s<M_RING + w * 24576 + (2 - dd) * 8192 + 4096, ((w + 3) & 3) * 65536 + w * 1024>(aoff[0] + to, aoff[1] - 1024 + to, gS, lb);
      if (THR < 60) wait_vm<THR>();
      dma32s<M_RING + w * 24576 + (2 - dd) * 8192 + 6144, w * 65536 + w * 1024>(aoff[0] + to, aoff[1] - 1024 + to, gS, lb);
      SB();
    };
    auto epi2 = [&]() {
      flag_wait_asm(flg + 132, 4u);
#pragma unroll
      for (int jj = 0; jj < 2; ++jj) {
        u32x4* zp = z1at(odd * 32 + wl * 32 + jj * 16);
        u32x4 o = *zp;
#pragma unroll
        for (int q = 0; q < 4; ++q) o[q] = pack2s(gelu1(nv(lo16(o[q]))), gelu1(hi16(o[q])));
        *zp = o;
      }
      signal1(flg + 56);
      SB();
    };
    auto putT = [&](const u32x4* T, unsigned a, unsigned f) {
#pragma unroll
      for (int i = 0; i < 8; ++i) asm volatile("ds_write_b128 %0, %1 offset:%2" :: "v"(a), "v"(T[i]), "i"(i * 1024) : "memory");
      signal1(f);
    };
    const unsigned tx = lb + M_CV + cw * CV_BUF + lin;
    auto landT = [&]() {
      sttag(ws + WS_LF + (rb * 8 + cb) * 32 + cw * 8);
      putT(TA, tx, flg + 112 + cw * 4);
    };
    auto putT4 = [&]() {
      flag_wait_asm(flg + 96 + cw * 4, 1u);
      putT(TA + 8, lb + M_RING + cw * 24576 + 8192 + lin, flg + 112 + cw * 4);
    };
    if (docv) {
      flag_wait_asm(flg + 36, 4u);
      AB(12);
      f32x4 ac[8];
      conv_core(lds_addr(cb_), lane, Dw, ac, flg + 28);
      AB(13);
      signal1(flg + 40);
      wait_vm<0>();
      landT();
#pragma unroll
      for (int k = 0; k < 4; ++k) {
        u32x2 ov[2];
#pragma unroll
        for (int t = 0; t < 2; ++t)
#pragma unroll
          for (int h = 0; h < 2; ++h) ov[t][h] = pack2(ac[2 * k + t][2 * h], ac[2 * k + t][2 * h + 1]);
        const auto s0 = __builtin_amdgcn_permlane16_swap(ov[0][0], ov[1][0], false, false);
        const auto s1 = __builtin_amdgcn_permlane16_swap(ov[0][1], ov[1][1], false, false);
        st16((unsigned)((256 * (g4 & 1) + 16 * l15 + 8 * (g4 >> 1)) * 2), ws + WS_Y + (size_t)d * (S_ * 2) + k * 1024, (u32x4){s0[0], s1[0], s0[1], s1[1]});
      }
      wait_vm<0>();
      sttag(ws + WS_YF + d * 8);
      epi2();
      putT4();
    } else if (cw != 3) {
      wait_vm<0>();
      landT();
      epi2();
      putT4();
    } else {
      ldS(IC(0), IC(0)); ldS(IC(0), IC(1)); ldS(IC(0), IC(2)); ldS(IC(1), IC(1)); ldS(IC(2), IC(1));
      wait_vm<40>();
      landT();
      AB(12);
      flag_wait_asm(flg + 16, 4u);
      AB(8);
      wait_vm<24>();
      ldS(IC(0), IC(3)); ldS(IC(1), IC(0)); ldS(IC(1), IC(2)); ldS(IC(1), IC(3));
      wait_vm<24>();
      signal1(flg + 32);
      ldS(IC(2), IC(0)); ldS(IC(2), IC(2)); ldS(IC(2), IC(3));
      ldtag(ef, (unsigned)((lane & 1) * 8), ws + WS_XF + (rb * 4 + pr) * 16);
      AB(10);
      u32x4 TB[32];
      auto ring2 = [&]() { dsw4(flg + 224 + (lane & 3) * 4, __uint_as_float(1u)); };
#pragma unroll
      for (int n = 0; n < 32; ++n) {
        if (n == 8) wait_vm<32>();
        if (THR < 60 && !(n & 1)) wait_vm<THR>();
        if (THR < 32 && n == THR) ring2();
        ldT(TB[n], 6, n >> 3, n & 7);
      }
      wait_vm<32>(ef);
      if (THR >= 32) ring2();
      AB(11);
      if (!odd) {
        u32x4 zx[4];
        waittag(ef, (unsigned)((lane & 1) * 8), ws + WS_XF + (rb * 4 + pr) * 16);
#pragma unroll
        for (int c = 0; c < 4; ++c)
          asm volatile("global_load_dwordx4 %0, %1, %2 offset:%3 sc1" : "=&v"(zx[c]) : "v"(lin), "s"(ws + WS_ZX + (size_t)(rb * 4 + pr) * 4096), "i"(c * 1024) : "memory");
        wait_vm<0>(zx[0], zx[1], zx[2], zx[3]);
#pragma unroll
        for (int c = 0; c < 4; ++c) *(u32x4*)(smem + L_Z1 + (64 + (c & 1) * 16 + l15) * 128 + ((((c >> 1) * 4 + g4) ^ (l15 & 7)) << 4)) = zx[c];
      }
      wait_vm<0>();
      putT4();
#pragma unroll
      for (int w = 0; w < 4; ++w) {
        flag_wait_asm(flg + 96 + w * 4, 2u);
        putT(TB + 8 * w, lb + M_RING + w * 24576 + lin, flg + 144 + w * 4);
      }
      epi2();
      signal1(flg + 56);
      AB(13);
    }
    SB();
    wait_vm<0>();
    AB(14);
    u32x2 xv[5];
    f32x2 sv[5][2];
    flag_wait_asm(flg + 40, (rb & 1) ? 2u : 3u);
    const int yc = min(cw * 24, 56);
    const unsigned yfo = (unsigned)(min(lane, 23) * 8);
    const char* yfp = ws + WS_YF + (d0 + yc) * 8;
    auto ldy = [&]() {
#pragma unroll
      for (int n = 0; n < 3; ++n) {
        const int q = (yc >> 3) + n;
        asm volatile("s_mov_b32 m0, %2\n\ts_nop 0\n\tglobal_load_lds_dwordx4 %0, %1 sc1" :: "v"((unsigned)(r8 * (S_ * 2) + ((c8 ^ (r8 & 6)) << 4))),
                     "s"(ws + WS_Y + ((size_t)(d0 + q * 8) * S_ + t0) * 2), "s"(lb + yat(0) + (q >> 2) * CV_BUF + (q & 3) * 1024) : "memory");
      }
    };
    flag_wait_asm(flg + 96 + cw * 4, 3u);
    ldtag(ef, yfo, yfp);
    wait_vm<0>(ef);
    flag_wait_asm(flg + 44, 4u);
    waittag(ef, yfo, yfp);
    AB(15);
    ldy();
#pragma unroll
    for (int j = 0; j < 5; ++j)
      asm volatile("global_load_dwordx2 %0, %1, %2 offset:%3" : "=&v"(xv[j]) : "v"((unsigned)(l15 * D_ * 2 + 8 * g4)), "s"(x + (size_t)(t0 + cw * 16) * D_ + d0), "i"(j * 32) : "memory");
    ldW();
    wait_vm<16>(xv[0], xv[1], xv[2], xv[3], xv[4]);
    AB(17);
    signal1(flg + 52);
    flag_wait_asm(flg + 52, 4u);
    {
      const int yr_ = 4 * g4 + (l15 >> 2);
      const unsigned ya = lb + yr_ * 128 + (((cw * 2 + ((lane >> 1) & 1)) ^ (yr_ & 6)) << 4) + (lane & 1) * 8;
      const float nm = -*(const float*)(smem + L_XM + (cw * 16 + l15) * 4), sr = *(const float*)(smem + L_XR + (cw * 16 + l15) * 4);
      s16x4 yr[5] = {tr_asm<yat(0)>(ya), tr_asm<yat(1)>(ya), tr_asm<yat(2)>(ya), tr_asm<yat(3)>(ya), tr_asm<yat(4)>(ya)};
      wait_lg<0>(yr[0], yr[1], yr[2], yr[3], yr[4]);
      AB(16);
      flag_wait_asm(flg + 96 + cw * 4, 4u);
#pragma unroll
      for (int j = 0; j < 5; ++j) {
        const u32x2 yw = __builtin_bit_cast(u32x2, yr[j]);
#pragma unroll
        for (int h = 0; h < 2; ++h) {
          sv[j][h] = ((unpk(xv[j][h]) + (f32x2){nm, nm}) * (f32x2){sr, sr}) * unpk(yw[h]);
          pin(sv[j][h]);
        }
      }
      SB();
    }
    wait_vm<15>(ef);
    gemm3(IC(1));
    unsigned pk[5][2];
#pragma unroll
    for (int j = 0; j < 5; ++j)
#pragma unroll
      for (int h = 0; h < 2; ++h) {
        const f32x2 o2 = {ac3[j][2 * h], ac3[j][2 * h + 1]};
        const f32x2 a = o2 * (f32x2){-1.4426950f, -1.4426950f};
        const f32x2 e = (f32x2){__builtin_amdgcn_exp2f(a[0]), __builtin_amdgcn_exp2f(a[1])} + (f32x2){1.0f, 1.0f};
        const f32x2 r = {__builtin_amdgcn_rcpf(e[0]), __builtin_amdgcn_rcpf(e[1])};
        pk[j][h] = pk2(__builtin_elementwise_fma(o2 * r, sv[j][h], unpk(xv[j][h])));
      }
    const u16* op = out + (size_t)(t0 + cw * 16) * D_ + d0;
#pragma unroll
    for (int q = 0; q < 2; ++q) {
      const auto s0 = __builtin_amdgcn_permlane16_swap(pk[2 * q][0], pk[2 * q + 1][0], false, false);
      const auto s1 = __builtin_amdgcn_permlane16_swap(pk[2 * q][1], pk[2 * q + 1][1], false, false);
      asm volatile("global_store_dwordx4 %0, %1, %2 offset:%3 " OSC "\n\ts_nop 1" :: "v"((unsigned)(l15 * D_ * 2 + 32 * (g4 & 1) + 16 * (g4 >> 1))), "v"((u32x4){s0[0], s1[0], s0[1], s1[1]}),
                   "s"(op), "i"(64 * q) : "memory");
    }
    asm volatile("global_store_dwordx2 %0, %1, %2 offset:128 " OSC :: "v"((unsigned)(l15 * D_ * 2 + 8 * g4)), "v"((u32x2){pk[4][0], pk[4][1]}), "s"(op) : "memory");
  } else {
    ldsw();
    sfor<0, pos('B')>([&](auto ic) { if constexpr (SCH[ic] == 'g') issue(IC(cnt('u', ic))); });
    if (tid < 64) *(unsigned*)(smem + L_FLAG + tid * 4) = 0u;
    pin(ones);
    AB(1);
    barrier0(behind(0, pos('B'), 'B'));
    kvld();
    kvm(0, 4);
    kvst();
    const bool isz2 = !(odd && wl);
    const char* stp = ws + WS_ST + (size_t)((rb * 12 + pr * 3 + odd * 2 + wl) * 1024 + wm * 512);
    if (STW && isz2) st8(lin >> 1, stp, (u32x2){0u, ~(unsigned)tag});
    const char* Ab[4];
#pragma unroll
    for (int i = 0; i < 4; ++i) Ab[i] = (const char*)w_sp + (size_t)(t0 + ((wave + 1 + i) & 3) * 16) * (S_ * 2) + wave * 1024 + 4096 - i * 2048;
    const unsigned ringl = lb + M_RING + wave * 24576;
    unsigned av[2], xl = ringl + 16384 + 4096;
    unsigned bsv;
    {
      f32x4 zv[2][4], cs[4];
      s16x4 WB[20][4][2];
      bf16x8 XG[10][2][2];
      unsigned aX[2][2], aW[2][2][4];
#pragma unroll
      for (int k = 0; k < 2; ++k)
#pragma unroll
        for (int w = 0; w < 2; ++w) {
          aX[w][k] = pX[k] + L_SLOT + XW * 8192 + w * 65536;
          pin(aX[w][k]);
#pragma unroll
          for (int jj = 0; jj < 4; ++jj) { aW[w][k][jj] = (pW[k] ^ (unsigned)(jj * 32)) + L_SLOT + wl * 8192 + w * 65536; pin(aW[w][k][jj]); }
        }
      auto rd1 = [&](auto nc, int r) {
        constexpr int n = decltype(nc)::value, j = n >> 1, k = n & 1, c = n > 0 && n < 11 ? n - 1 : 0, sx = (XSLOT[c] - XW) * 8192, sa = WSLOT[j] * 8192;
        if (r < 0) { if (r & 1) ds16<(sx & 0xFFFF) + 512>(XG[c][(r + 4) >> 1][1], aX[sx >> 16][(r + 4) >> 1]); else ds16<(sx & 0xFFFF)>(XG[c][(r + 4) >> 1][0], aX[sx >> 16][(r + 4) >> 1]); }
        else if (r & 1) WB[n][r >> 1][1] = tr_asm<(sa & 0xFFFF) + 512>(aW[sa >> 16][k][r >> 1]);
        else WB[n][r >> 1][0] = tr_asm<(sa & 0xFFFF)>(aW[sa >> 16][k][r >> 1]);
        SB();
      };
      auto bar = [&](auto bc) {
        SB();
        constexpr int p = decltype(bc)::value;
        CB(3 * (p - XBAR));
        if constexpr (p == 1) asm volatile("s_waitcnt vmcnt(%0) lgkmcnt(0)\n\ts_barrier" :: "n"(behind(0, pos('_', p - 1), '_')) : "memory");
        else if constexpr (p <= GZ) asm volatile("s_waitcnt vmcnt(%0) lgkmcnt(8)\n\ts_barrier" :: "n"(behind(0, pos('_', p - 1), '_')) : "memory");
        else if constexpr (p >= 8) asm volatile("s_waitcnt lgkmcnt(0)\n\ts_barrier" ::: "memory");
        else if constexpr (p < 7) asm volatile("s_waitcnt lgkmcnt(8)\n\ts_barrier" ::: "memory");
        else asm volatile("s_barrier" ::: "memory");
        CB(3 * (p - XBAR) + 2);
        SB();
      };
      float cp[4];
      f32x4 st[2][2], rv;
      const unsigned rwa = lb + L_RAW + (ksb * 2 + wm) * 256 + l15 * 16 + ((l15 >> 2) == g4 ? 0 : 1024);
      const unsigned csa = lb + L_CS + (wave * 64 + lane) * 4;
      auto kstep = [&](auto nc) {
        constexpr int n = decltype(nc)::value, j = n >> 1, k = n & 1, nn = n + 1, r0 = nn >= 2 && nn <= 10 ? -4 : 0;
        constexpr int sc = n == 1 ? 0 : n - 2, ns = !k ? 0 : (n == 1 || n == 11 ? 1 : 2);
        if constexpr (k) bar(IC(nn / 2 + 1));
        int r = r0, c = 0;
        auto nxt = [&]() { if (r < 8) { rd1(IC(nn), r++); ++c; } };
        auto sel = [&](int q) {
          const f32x4& sq = st[q & 1][q >> 1];
          rv[q] = sel4(sq[0], sq[1], sq[2], sq[3], 0xAAAAAAAAAAAAAAAAull, 0xCCCCCCCCCCCCCCCCull);
          SB();
        };
#pragma unroll
        for (int jj = 0; jj < 4; ++jj) {
          const bf16x8 wf = join8(WB[n][jj][0], WB[n][jj][1]);
          MFMAW<n == 0>(jj == 0 ? 4 : (jj == 2 ? c : -1), zv[0][jj], XG[j][k][0], wf);
          SB();
          nxt();
          if (n == 12) nxt();
          if (n == 12 && jj == 2) sel(2);
          MFMAW<n == 0>(-1, zv[1][jj], XG[j][k][1], wf);
          SB();
          nxt();
          if (n == 12) nxt();
          if (n == 12 && (jj == 1 || jj == 2)) sel(jj == 1 ? 0 : 3);
          if (n == 12 && jj == 2) { dsw16<0>(rwa, rv); SB(); }
          if (k == 0) {
            MFMAW<n == 0>(-1, cs[jj], ones, wf);
            SB();
            nxt();
            if (n == 12) nxt();
            if (n == 12 && jj == 1) sel(1);
          }
#pragma unroll
          for (int q = 0; q < ns; ++q) {
            const bf16x8& xs = XG[sc + q][1][jj >> 1];
            MFMAW<n == 1>(-1, st[jj & 1][jj >> 1], xs, (jj & 1) ? xs : ones);
            SB();
            nxt();
          }
        }
      };
      auto lstep = [&](auto nc, auto ac, auto bc, int w) {
        constexpr int n = decltype(nc)::value, j = n >> 1, k = n & 1, na = decltype(ac)::value, nb = decltype(bc)::value;
        int r = 0;
        auto nxt = [&]() { if (na > 0 && r < 16) { if (r < 8) rd1(ac, r); else rd1(bc, r - 8); ++r; } };
#pragma unroll
        for (int jj = 0; jj < 4; ++jj) {
          const bf16x8 wf = join8(WB[n][jj][0], WB[n][jj][1]);
          if (k == 0) { MFMAW<false>(jj == 0 ? w : -1, cs[jj], ones, wf); SB(); nxt(); nxt(); }
          MFMAW<false>(jj == 0 && k ? w : -1, zv[0][jj], XG[j][k][0], wf);
          SB();
          nxt();
          if (k) nxt();
          MFMAW<false>(-1, zv[1][jj], XG[j][k][1], wf);
          SB();
          nxt();
          if (k) nxt();
          if (n == 15 && jj == 2) {
            dsw4(csa, sel4(cs[0][0], cs[1][0], cs[2][0], cs[3][0], 0xFFFF0000FFFF0000ull, 0xFFFFFFFF00000000ull));
            SB();
          }
        }
      };
      bar(IC(1));
      AB(2);
#pragma unroll
      for (int r = -4; r < 8; ++r) rd1(IC(0), r);
      kstep(IC(0)); AB(4); kstep(IC(1)); kstep(IC(2)); kstep(IC(3)); kstep(IC(4)); kstep(IC(5)); kstep(IC(6)); kstep(IC(7)); kstep(IC(8)); AB(5); kstep(IC(9));
      kstep(IC(10)); kstep(IC(11)); kstep(IC(12));
      bar(IC(8));
      lstep(IC(13), IC(14), IC(16), -1);
      lstep(IC(14), IC(18), IC(15), 8);
      lstep(IC(16), IC(17), IC(19), 15);
      lstep(IC(18), IC(0), IC(0), 15);
      lstep(IC(15), IC(0), IC(0), -1);
      lstep(IC(17), IC(0), IC(0), 9);
      bar(IC(9));
      __builtin_amdgcn_s_setprio(2);
#pragma unroll
      for (int q = 0; q < 4; ++q) cp[q] = *(const float*)(smem + L_CS + ((wave ^ 1) * 64 + q * 16 + l15) * 4);
      ldxs();
      SB();
      lstep(IC(19), IC(0), IC(0), -1);
      AB(6);
      float ct[4];
#pragma unroll
      for (int jj = 0; jj < 4; ++jj) ct[jj] = cs[jj][0] + cp[jj];
#pragma unroll
      for (int jj = 0; jj < 4; ++jj) {
        if (jj == 0) asm volatile("s_nop 3");
        if (jj == 2) SB();
        if (!(jj & 1)) pin(zv[0][jj], zv[1][jj], zv[0][jj + 1], zv[1][jj + 1]);
#pragma unroll
        for (int i = 0; i < 2; ++i) zv[i][jj] = act(zv[i][jj], ct[jj], i);
      }
      if (isz2) {
        f32x4 pv[2], s1[2], s2[2];
#pragma unroll
        for (int i = 0; i < 2; ++i) {
#pragma unroll
          for (int e = 0; e < 4; ++e) {
            const float z0 = zv[i][0][e];
            pv[i][e] = __int_as_float(__builtin_amdgcn_update_dpp(0, __float_as_int(z0), 0x150, 0xF, 0xF, true));
          }
          const f32x4 a0 = zv[i][0] - pv[i], a1 = zv[i][1] - pv[i], a2 = zv[i][2] - pv[i], a3 = zv[i][3] - pv[i];
          s1[i] = (a0 + a1) + (a2 + a3);
          s2[i] = __builtin_elementwise_fma(a3, a3, __builtin_elementwise_fma(a2, a2, __builtin_elementwise_fma(a1, a1, a0 * a0)));
        }
        float r[8], q[4];
#define DA(d, s, ctl, bm) "v_add_f32_dpp " d ", " s ", " s " " ctl " row_mask:0xf bank_mask:" bm "\n\t"
        asm volatile("s_nop 1\n\t"
                     DA("%0", "%12", "row_ror:8", "0x3") DA("%1", "%13", "row_ror:8", "0x3") DA("%2", "%14", "row_ror:8", "0x3") DA("%3", "%15", "row_ror:8", "0x3")
                     DA("%4", "%16", "row_ror:8", "0x3") DA("%5", "%17", "row_ror:8", "0x3") DA("%6", "%18", "row_ror:8", "0x3") DA("%7", "%19", "row_ror:8", "0x3")
                     DA("%0", "%20", "row_ror:8", "0xc") DA("%1", "%21", "row_ror:8", "0xc") DA("%2", "%22", "row_ror:8", "0xc") DA("%3", "%23", "row_ror:8", "0xc")
                     DA("%4", "%24", "row_ror:8", "0xc") DA("%5", "%25", "row_ror:8", "0xc") DA("%6", "%26", "row_ror:8", "0xc") DA("%7", "%27", "row_ror:8", "0xc")
                     DA("%8", "%0", "row_half_mirror", "0x5") DA("%9", "%1", "row_half_mirror", "0x5") DA("%10", "%2", "row_half_mirror", "0x5") DA("%11", "%3", "row_half_mirror", "0x5")
                     DA("%8", "%4", "row_half_mirror", "0xa") DA("%9", "%5", "row_half_mirror", "0xa") DA("%10", "%6", "row_half_mirror", "0xa") DA("%11", "%7", "row_half_mirror", "0xa")
                     DA("%8", "%8", "quad_perm:[1,0,3,2]", "0xf") DA("%9", "%9", "quad_perm:[1,0,3,2]", "0xf") DA("%10", "%10", "quad_perm:[1,0,3,2]", "0xf") DA("%11", "%11", "quad_perm:[1,0,3,2]", "0xf")
                     DA("%8", "%8", "quad_perm:[2,3,0,1]", "0xf") DA("%9", "%9", "quad_perm:[2,3,0,1]", "0xf") DA("%10", "%10", "quad_perm:[2,3,0,1]", "0xf") DA("%11", "%11", "quad_perm:[2,3,0,1]", "0xf")
                     : "=&v"(r[0]), "=&v"(r[1]), "=&v"(r[2]), "=&v"(r[3]), "=&v"(r[4]), "=&v"(r[5]), "=&v"(r[6]), "=&v"(r[7]),
                       "=&v"(q[0]), "=&v"(q[1]), "=&v"(q[2]), "=&v"(q[3])
                     : "v"(s1[0][0]), "v"(s2[0][0]), "v"(s1[0][1]), "v"(s2[0][1]), "v"(s1[0][2]), "v"(s2[0][2]), "v"(s1[0][3]), "v"(s2[0][3]),
                       "v"(s1[1][0]), "v"(s2[1][0]), "v"(s1[1][1]), "v"(s2[1][1]), "v"(s1[1][2]), "v"(s2[1][2]), "v"(s1[1][3]), "v"(s2[1][3]));
#undef DA
        const f32x4 p4 = (lane & 8) ? pv[1] : pv[0];
        const float pa = (lane & 4) ? p4[2] : p4[0], pb = (lane & 4) ? p4[3] : p4[1];
        const float pp = (lane & 2) ? pb : pa, S1 = (lane & 2) ? q[2] : q[0], S2 = (lane & 2) ? q[3] : q[1];
        const float dl = S1 * (1.0f / 64.0f);
        st8(lin >> 1, stp, (u32x2){__float_as_uint((lane & 1) ? fmaxf(S2 - S1 * dl, 0.f) : pp + dl), (unsigned)tag});
      } else {
#pragma unroll
        for (int jj = 0; jj < 4; ++jj) {
          u32x4 o = pack8(zv[0][jj], zv[1][jj]);
          if (jj >= 2) z1put(jj * 16 - 32, o);
          else st16(lin, ws + WS_ZX + (size_t)((rb * 4 + pr) * 4 + wm * 2 + jj) * 1024, o);
        }
      }
      signal1(flg + 36);
      SB();
      AB(7);
      __builtin_amdgcn_s_setprio(P2P);
      unsigned po = (unsigned)((lane >> 5) * 6144 + (lane & 31) * 16);
      const char* pb = ws + WS_ST + (size_t)rb * 12288 + wm * 512 + 2048;
      u32x4 sg[6];
      auto ldsg = [&]() {
#pragma unroll
        for (int c = 0; c < 6; ++c) asm volatile("global_load_dwordx4 %0, %1, %2 offset:%3 sc1" : "=&v"(sg[c]) : "v"(po), "s"(pb), "i"(c * 1024 - 2048) : "memory");
      };
      auto look = [&]() {
        wait_vm<0>();
        ldsg();
        asm volatile("global_load_ushort %0, %1, %2" : "=&v"(bsv) : "v"((unsigned)(l15 * 2)), "s"(b_sp + t0 + wave * 16) : "memory");
        if (!isz2) sttag(ws + WS_XF + (rb * 4 + pr) * 16 + wm * 8);
        SB();
      };
      flag_wait_asm(flg + 60, 4u);
      ds16<0>(ac2[0][0], p2a); ds16<1024>(ac2[1][0], p2a); ds16<2048>(ac2[0][1], p2a); ds16<3072>(ac2[1][1], p2a);
      sfor<0, 4>([&](auto nc) { rdV(IC(1), nc, 0); rdV(IC(1), nc, 1); rdV(IC(1), nc, 2); rdV(IC(1), nc, 3); });
      sfor<0, 10>([&](auto nc) {
        constexpr int n = decltype(nc)::value;
        mm2(IC(1), nc, XG + 5, n == 0 ? 12 : (n < 6 ? 15 : 4 * (9 - n)), [&](int r) {
          SB();
          if constexpr (n < 3) { if (r < 2) { rdV(IC(1), IC(4 + 2 * n), 2 * r); rdV(IC(1), IC(4 + 2 * n), 2 * r + 1); } else { rdV(IC(1), IC(5 + 2 * n), 2 * r - 4); rdV(IC(1), IC(5 + 2 * n), 2 * r - 3); } }
        });
        if constexpr (n == 2) signal1(flg + 16);
        if constexpr (n == PN) look();
      });
      signal1(flg + 20 + wl * 4);
      AB(3);
      if (PN > 9) look();
      AB(8);
      wait_vm<1>(sg[0], sg[1], sg[2], sg[3], sg[4], sg[5]);
      if (isz2) {
        const unsigned zw = lb + L_ZS + wave * 256 + lane * 4, zr_ = lb + L_ZS + wave * 256 + g4 * 32;
        const char* zb = ws + WS_Z2 + (size_t)((rb * 2 + wm) * 48 + pr * 12 + odd * 8 + wl * 4) * 1024;
        auto bad = [&]() {
          unsigned an = sg[0][1] & sg[0][3], on = sg[0][1] | sg[0][3];
#pragma unroll
          for (int c = 1; c < 6; ++c) { an &= sg[c][1] & sg[c][3]; on |= sg[c][1] | sg[c][3]; }
          return __builtin_amdgcn_ballot_w64(((an ^ (unsigned)tag) | (on ^ (unsigned)tag)) != 0) != 0;
        };
        if (__builtin_expect(bad(), 0)) {
          do {
            --tmo;
            reld6(sg, po, pb);
            wait_vm<0>(sg[0], sg[1], sg[2], sg[3], sg[4], sg[5]);
          } while (bad() && tmo > 0);
        }
        AB(9);
        auto f = [&](int c, int e) { return __uint_as_float(sg[c][e]); };
        auto sum6 = [&](int e) { return add1(add1(add1(f(0, e), f(1, e)), add1(f(2, e), f(3, e))), add1(f(4, e), f(5, e))); };
        const float nmh = sum6(0) * (-1.0f / 6.0f), mh = -nmh;
        float qa = sum6(2) * (1.0f / 64.0f), qb;
#pragma unroll
        for (int c = 0; c < 6; c += 2) { const float u = add1(f(c, 0), nmh), v = add1(f(c + 1, 0), nmh); qa = fma1(u, u, qa); qb = c ? fma1(v, v, qb) : v * v; }
        const float Mh = add1(qa, qb);
        const auto sm = __builtin_amdgcn_permlane32_swap(__float_as_uint(mh), __float_as_uint(mh), false, false);
        const auto sM = __builtin_amdgcn_permlane32_swap(__float_as_uint(Mh), __float_as_uint(Mh), false, false);
        const float dm = __uint_as_float(sm[0]) - __uint_as_float(sm[1]), mean = rbf(add1(__uint_as_float(sm[0]), __uint_as_float(sm[1])) * 0.5f);
        const float vbf = rbf(__builtin_fmaf(dm * dm, 3.0f, add1(__uint_as_float(sM[0]), __uint_as_float(sM[1]))) * (1.0f / 12.0f));
        const float rz = __builtin_amdgcn_rcpf(rbf(__builtin_amdgcn_sqrtf(rbf(vbf + 1e-5f))));
        dsw4(zw, lane < 32 ? -mean * rz : rz);
        f32x4 zm[2], zr[2];
        ds16<0>(zm[0], zr_); ds16<128>(zr[0], zr_); ds16<16>(zm[1], zr_); ds16<144>(zr[1], zr_);
        wait_lg<0>(zm[0], zm[1], zr[0], zr[1]);
#pragma unroll
        for (int jj = 0; jj < 4; ++jj) {
          u32x4 o;
#pragma unroll
          for (int q = 0; q < 4; ++q) {
            const int i = q >> 1, e = (q & 1) * 2;
            o[q] = pack2(fma1(zv[i][jj][e], zr[i][e], zm[i][e]), fma1(zv[i][jj][e + 1], zr[i][e + 1], zm[i][e + 1]));
          }
          o[3] = nohole(o[3]);
          asm volatile("global_store_dwordx4 %0, %1, %2 offset:%3 sc1\n\ts_nop 1" :: "v"(lin), "v"(o), "s"(zb), "i"(jj * 1024) : "memory");
          SB();
        }
      }
      SB();
      AB(10);
      ldtag(ef, (unsigned)((lane & 31) * 8), ws + WS_EF + 2048 + cb * 256);
      signal1(flg + 28);
    }
    const char* Bs = ws + WS_Z2 + (size_t)(wave * 16 * 48 + cb * 6) * 1024 + 2048;
    bf16x8 Bf[3][6];
    unsigned bv[2], bo;
    auto ldB1 = [&](int s, int j) {
      asm volatile("global_load_dwordx4 %0, %1, %2 offset:%3 " HSC : "=&v"(Bf[s % 3][j]) : "v"(bv[s & 1]), "s"(Bs), "i"(j * 1024 - 2048) : "memory");
    };
    auto nxb = [&](int s, int k) {
      if (k == 0) asm volatile("s_add_u32 %0, %0, 0xc000\n\ts_cmp_eq_u32 %0, 0xc0000\n\ts_cselect_b32 %0, 0, %0" : "+s"(bo) :: "scc");
      else asm volatile("v_or_b32 %0, %1, %2" : "=v"(bv[s & 1]) : "s"(bo), "v"(lin));
    };
    f32x4 acc[4][6];
    unsigned ara[2][2];
#pragma unroll
    for (int ks = 0; ks < 2; ++ks) {
      ara[0][ks] = ringl + l15 * 128 + (((ks * 4 + g4) ^ (l15 & 7)) << 4);
      ara[1][ks] = ara[0][ks] + (M_CV - M_RING) + wave * (CV_BUF - 24576);
    }
    u32x4 af[2][4];
    auto rdA = [&](int s, int i) {
      const int q = (0x20132012 >> (4 * (s >> 1))) & 3;
      asm volatile("ds_read_b128 %0, %1 offset:%2" : "=&v"(af[s & 1][i]) : "v"(ara[q == 3][s & 1]), "i"((q % 3) * 8192 + i * 2048) : "memory");
    };
    bo = (unsigned)(rot2 * 2 * 49152);
    asm volatile("" : "+s"(xl), "+s"(Ab[0]), "+s"(Ab[1]), "+s"(Ab[2]), "+s"(Ab[3]));
    auto adA = [&](int d) {
#pragma unroll
      for (int q = 0; q < 2; ++q) av[q] = opq(aoff[q] - q * 1024 + (unsigned)(((d + rot2) & 7) * 128));
    };
    auto ldA1 = [&](int a) { asm volatile("global_load_lds_dwordx4 %0, %1 offset:%2" :: "v"(av[a & 1]), "s"(Ab[a >> 1]), "i"(a * 1024 - 4096) : "memory"); };
    adA(3);
    flag_wait_asm(flg + 32, 1u);
#pragma unroll
    for (int i = 0; i < 8; ++i) rdA(i >> 2, i & 3);
    asm volatile("s_mov_b32 m0, %0" :: "s"(xl));
    wait_lg<0>(af[0][0], af[0][1], af[0][2], af[0][3], af[1][0], af[1][1], af[1][2], af[1][3]);
#pragma unroll
    for (int a = 0; a < 8 - T3L; ++a) ldA1(a);
    nxb(0, 1); nxb(1, 0); nxb(1, 1);
    flag_wait_asm(flg + 28, 4u);
    AB(11);
    wait_vm<8 - T3L>(ef, bsv);
    waittag(ef, (unsigned)((lane & 31) * 8), ws + WS_EF + 2048 + cb * 256);
    AB(12); SS(1 - 3 * XS);
#pragma unroll
    for (int j = 0; j < 6; ++j) ldB1(0, j);
#pragma unroll
    for (int j = 0; j < 6; ++j) ldB1(1, j);
#pragma unroll
    for (int a = 8 - T3L; a < 8; ++a) ldA1(a);
    adA(7);
    nxb(2, 0); nxb(2, 1); nxb(3, 0); nxb(3, 1);
#pragma unroll
    for (int j = 0; j < 6 * B2E; ++j) ldB1(2, j);
    SS(2 - 3 * XS);
    {
      flag_wait_asm(flg + 20 + wl * 4, 2u);
      const float* cp2 = (const float*)(smem + L_CS2 + (wl * 64 + l15) * 4);
      pin(ac2[0][0], ac2[0][1], ac2[1][0], ac2[1][1]);
#pragma unroll
      for (int jj = 0; jj < 2; ++jj) {
        const float nct = -add1(add1(cp2[jj * 16], cp2[32 + jj * 16]), add1(cp2[128 + jj * 16], cp2[160 + jj * 16]));
        u32x4 o;
#pragma unroll
        for (int q = 0; q < 4; ++q) {
          const int i = q >> 1, e = (q & 1) * 2;
          o[q] = pack2(mul1(fma1(xm[i][e], nct, ac2[i][jj][e]), xr[i][e]), mul1(fma1(xm[i][e + 1], nct, ac2[i][jj][e + 1]), xr[i][e + 1]));
        }
        z1put(odd * 32 + wl * 32 + jj * 16, o);
      }
      signal1(flg + 132);
      SB();
    }
    __builtin_amdgcn_s_setprio(G2P);
    f32x4 bs4 = {lo16(bsv), lo16(bsv), lo16(bsv), lo16(bsv)};
    pin(bs4);
    const unsigned xa = ringl + lane * 16;
    auto putp = [&](int i, int j) { asm volatile("ds_write_b128 %0, %1 offset:%2" :: "v"(xa), "v"(acc[i][j]), "i"(i * 6144 + j * 1024) : "memory"); };
    unsigned fv, fw = flg + wave * 4, cv = 1u;
    pin(fw);
#define CK0 HIREG("h0", "%0") HIREG("h1", "%1") HIREG("h2", "%2") HIREG("h3", "%3") HIREG("h4", "%4") HIREG("h5", "%5") "s_waitcnt vmcnt(%[nw])\n\tv_max3_u32 %6, v[h0], v[h1], v[h2]\n\t"
#define CK1 "v_max3_u32 %6, %6, v[h3], v[h4]\n\tv_max_u32 %6, %6, v[h5]\n\t"
#define CK2 "v_cmp_eq_u32 vcc, -1, %6\n\ts_waitcnt lgkmcnt(0)\n\t"
#define CKA(d) "s_add_u32 vcc_lo, %[r2], %[st] + " d "\n\ts_and_b32 vcc_lo, vcc_lo, 15\n\ts_mul_i32 vcc_lo, vcc_lo, 0xc000\n\tv_or_b32 %6, vcc_lo, %[li]\n\t"
#define CKL(a, b, c, d, e, f) "global_load_dwordx4 " a ", %6, %[bs] offset:-2048 sc1\n\tglobal_load_dwordx4 " b ", %6, %[bs] offset:-1024 sc1\n\tglobal_load_dwordx4 " c ", %6, %[bs] sc1\n\t" \
            "global_load_dwordx4 " d ", %6, %[bs] offset:1024 sc1\n\tglobal_load_dwordx4 " e ", %6, %[bs] offset:2048 sc1\n\tglobal_load_dwordx4 " f ", %6, %[bs] offset:3072 sc1\n\t"
#define CK3X(x) "s_cbranch_vccnz .Lck_%=\n.Lcb_%=:\n\t.subsection 1\n.Lck_%=:\n\ts_waitcnt vmcnt(0)\n\ts_sleep 2\n\t" CKA("0") CKL("%0", "%1", "%2", "%3", "%4", "%5") x \
            "s_waitcnt vmcnt(0)\n\tv_max3_u32 %6, v[h0], v[h1], v[h2]\n\t" CK1 "v_cmp_eq_u32 vcc, -1, %6\n\t" \
            "s_sub_i32 %7, %7, 1\n\ts_cmp_lt_i32 %7, 1\n\ts_cbranch_scc1 .Lcb_%=\n\ts_cbranch_vccnz .Lck_%=\n\ts_branch .Lcb_%=\n\t.subsection 0"
#define CK3 CK3X("")
#define CK4 CK3X(CKA("1") CKL("%[n0]", "%[n1]", "%[n2]", "%[n3]", "%[n4]", "%[n5]"))
#define CKN(sl) [n0] "+v"(Bf[sl][0]), [n1] "+v"(Bf[sl][1]), [n2] "+v"(Bf[sl][2]), [n3] "+v"(Bf[sl][3]), [n4] "+v"(Bf[sl][4]), [n5] "+v"(Bf[sl][5])
#define CKO(sl) "+v"(Bf[sl][0]), "+v"(Bf[sl][1]), "+v"(Bf[sl][2]), "+v"(Bf[sl][3]), "+v"(Bf[sl][4]), "+v"(Bf[sl][5]), "=&v"(mx), "+s"(tmo)
#define CKI(s_, nw_) [r2] "s"(2 * rot2), [st] "i"(s_), [li] "v"(lin), [bs] "s"(Bs), [nw] "i"(nw_)
    unsigned mx;
    asm volatile(CK0 CK1 CK2 CK4 : CKO(0), CKN(1) : CKI(0, 6 + T3L + 6 * B2E) : "memory", "vcc", "scc");
    auto mm = [&](int s, int i, int j) {
      if (s == 0 && i == 3) asm volatile("v_mfma_f32_16x16x32_bf16 %0, %1, %2, %3" : "=&v"(acc[i][j]) : "v"(Bf[0][j]), "v"(af[0][i]), "v"(bs4));
      else if (s == 0) MFMA0(acc[i][j], Bf[0][j], af[0][i]);
      else MFMA(acc[i][j], Bf[s % 3][j], af[s & 1][i]);
      SB();
    };
    auto step = [&](auto sc) {
      constexpr int s = decltype(sc)::value;
      constexpr int NA[16] = ANUM, a0 = s == 0 ? 0 : cum16(NA, s);
      static_assert(cum16(NA, 7) == 0 && cum16(NA, 11) == 8 && NA[s] <= 5, "");
      constexpr int NV = (s == 0 ? 5 : (s == 1 && !B2E ? 1 : 6 + NA[s ? s - 1 : 0])) + (s < 13 ? 5 : 0) + NA[s];
      static_assert(NA[s] <= 3, "");
      SS(3 + 2 * (s - XS));
      SB();
#pragma unroll
      for (int j = 0; j < 5; ++j) {
        auto& B = Bf[s % 3][j];
        if (false) {}
        else if (j == 4 && s > 0 && s < 12)
          asm volatile(MF "%0, %2, %3, %0\n\ts_add_u32 %1, %1, 0xc000\n\ts_cmp_eq_u32 %1, 0xc0000\n\ts_cselect_b32 %1, 0, %1" : "+v"(acc[0][j]), "+s"(bo) : "v"(B), "v"(af[s & 1][0]) : "scc");
        else if (j == 0 && (s == 1 || s == 7 || s == 9 || s == 11)) {
          unsigned sv;
          asm volatile(MF "%0, %3, %4, %0\n\tv_readfirstlane_b32 %2, %1\n\ts_cmp_lt_u32 %2, %6\n\ts_cbranch_scc1 .Lfk_%=\n.Lfb_%=:\n\t.subsection 1\n.Lfk_%=:\n\t"
                       "ds_read_b32 %1, %5 offset:%7\n\ts_waitcnt lgkmcnt(0)\n\tv_readfirstlane_b32 %2, %1\n\ts_cmp_lt_u32 %2, %6\n\ts_cbranch_scc1 .Lfk_%=\n\ts_branch .Lfb_%=\n\t.subsection 0"
                       : "+v"(acc[0][j]), "+v"(fv), "=&s"(sv) : "v"(B), "v"(af[s & 1][0]), "v"(fw), "i"(s == 9 ? 2 : 1), "i"(s == 1 ? 224 : (s == 11 ? 144 : 112)) : "memory", "scc");
        } else mm(s, 0, j);
        if (s == 0 && !B2E) ldB1(2, j);
        SB();
        if (j == 4 && s > 0 && s < 12) asm volatile(MF "%0, %2, %3, %0\n\tv_or_b32 %1, %4, %5" : "+v"(acc[1][j]), "=&v"(bv[s & 1]) : "v"(B), "v"(af[s & 1][1]), "s"(bo), "v"(lin));
        else mm(s, 1, j);
        if (j < NA[s]) ldA1(a0 + j);
        SB();
        mm(s, 2, j);
        if (j < 4 && s > 0) rdA(s + 1, j);
        if (j == 4 && (s == 2 || s == 4 || s == YS)) asm volatile("ds_write_b32 %1, %0 offset:96\n\tv_add_u32 %0, 1, %0" : "+v"(cv) : "v"(fw) : "memory");
        if (j == 4 && (s == 0 || s == 6 || s == 8 || s == 10)) asm volatile("ds_read_b32 %0, %1 offset:%2" : "=&v"(fv) : "v"(fw), "i"(s == 0 ? 224 : (s == 10 ? 144 : 112)) : "memory");
        if (j == 4 && s == SGS) signal1(flg + 44);
        SB();
        mm(s, 3, j);
        if (s + 3 < 16) ldB1(s + 3, j);
        SB();
      }
      constexpr int sn = (s + 1) % 3;
      if constexpr (s == 0) {
        mm(0, 0, 5); if (!B2E) ldB1(2, 5); mm(0, 1, 5); nxb(4, 0); mm(0, 2, 5); nxb(4, 1); mm(0, 3, 5); ldB1(3, 5);
        asm volatile(CK0 CK1 CK2 CK4 : CKO(1), CKN(2) : CKI(1, 12 + T3L) : "memory", "vcc", "scc");
      } else if constexpr (s < 13)
        asm volatile(MF "%8, %12, %19, %8\n\t" CK0 MF "%9, %12, %20, %9\n\t" CK1 MF "%10, %12, %21, %10\n\t" CK2 MF "%11, %12, %22, %11\n\t"
                     "global_load_dwordx4 %12, %23, %[bs] offset:3072 " HSC "\n\t" CK4
                     : CKO(sn), "+v"(acc[0][5]), "+v"(acc[1][5]), "+v"(acc[2][5]), "+v"(acc[3][5]), "+v"(Bf[s % 3][5]), CKN((s + 2) % 3)
                     : "v"(af[s & 1][0]), "v"(af[s & 1][1]), "v"(af[s & 1][2]), "v"(af[s & 1][3]), "v"(bv[~s & 1]), CKI(s + 1, NV) : "memory", "vcc", "scc");
      else
        asm volatile(MF "%8, %12, %13, %8\n\t" CK0 MF "%9, %12, %14, %9\n\t" CK1 MF "%10, %12, %15, %10\n\t" CK2 MF "%11, %12, %16, %11\n\t" CK3
                     : CKO(sn), "+v"(acc[0][5]), "+v"(acc[1][5]), "+v"(acc[2][5]), "+v"(acc[3][5])
                     : "v"(Bf[s % 3][5]), "v"(af[s & 1][0]), "v"(af[s & 1][1]), "v"(af[s & 1][2]), "v"(af[s & 1][3]), CKI(s + 1, NV) : "memory", "vcc", "scc");
      pin(af[~s & 1][0], af[~s & 1][1], af[~s & 1][2], af[~s & 1][3]);
      SB();
      SS(4 + 2 * (s - XS));
    };
    step(IC(0)); AB(13); step(IC(1)); step(IC(2)); step(IC(3)); AB(14); step(IC(4)); step(IC(5)); step(IC(6)); step(IC(7));
    step(IC(8)); step(IC(9)); step(IC(10)); step(IC(11)); step(IC(12)); step(IC(13));
    SS(3 + 2 * (14 - XS));
#pragma unroll
    for (int i = 0; i < 4; ++i) {
#pragma unroll
      for (int n = 0; n < 12; ++n) {
        if (i == 0 && n == 2) {
          asm volatile(MF "%8, %12, %16, %8\n\t" CK0 MF "%9, %13, %16, %9\n\t" CK1 MF "%10, %14, %16, %10\n\t" CK2 MF "%11, %15, %16, %11\n\t" CK3
                       : CKO(0), "+v"(acc[0][2]), "+v"(acc[0][3]), "+v"(acc[0][4]), "+v"(acc[0][5])
                       : "v"(Bf[2][2]), "v"(Bf[2][3]), "v"(Bf[2][4]), "v"(Bf[2][5]), "v"(af[0][0]), CKI(15, 0) : "memory", "vcc", "scc");
          pin(af[1][0], af[1][1], af[1][2], af[1][3]);
        }
        if (i > 0 || n < 2 || n > 5) mm(14 + n / 6, i, n % 6);
        if (i == 0 && n < 2) { rdA(15, 2 * n); rdA(15, 2 * n + 1); }
        if (i > 0 && (n & 1)) putp(i - 1, n >> 1);
        SB();
      }
      if (i > 0) signal1(flg + ((wave + i) & 3) * 4);
      SB();
    }
    SS(4 + 2 * (14 - XS));
    AB(15);
    flag_wait_asm(flg + 56, 5u);
    s16x4 z1r[6];
    {
      const int fr = 4 * g4 + (l15 >> 2), p = lane & 3;
      const unsigned za = lb + L_Z1 + fr * 128 + (((wave * 2 + (p >> 1)) ^ (fr & 7)) << 4) + (p & 1) * 8;
      z1r[0] = tr_asm<0>(za); z1r[1] = tr_asm<2048>(za); z1r[2] = tr_asm<4096>(za);
      z1r[3] = tr_asm<6144>(za); z1r[4] = tr_asm<8192>(za); z1r[5] = tr_asm<10240>(za);
    }
    flag_wait_asm(flg + wave * 4, 3u);
    asm volatile("" : "+v"(z1r[0]), "+v"(z1r[1]), "+v"(z1r[2]), "+v"(z1r[3]), "+v"(z1r[4]), "+v"(z1r[5]));
    AB(16);
    f32x4 own[6];
    {
      f32x4 pr_[3][6];
#pragma unroll
      for (int q = 0; q < 3; ++q) {
        const char* src = smem + M_RING + ((wave + 3 - q) & 3) * 24576;
#pragma unroll
        for (int j = 0; j < 6; ++j) pr_[q][j] = *(const f32x4*)(src + q * 6144 + j * 1024 + lane * 16);
      }
      asm volatile("s_nop 7" : "+v"(acc[3][0]), "+v"(acc[3][1]), "+v"(acc[3][2]), "+v"(acc[3][3]), "+v"(acc[3][4]), "+v"(acc[3][5]));
#pragma unroll
      for (int j = 0; j < 6; ++j) own[j] = (acc[3][j] + pr_[0][j]) + (pr_[1][j] + pr_[2][j]);
    }
    SB();
    {
      unsigned pk[6][2];
#pragma unroll
      for (int j = 0; j < 6; ++j) {
        const u32x2 z = __builtin_bit_cast(u32x2, z1r[j]);
#pragma unroll
        for (int h = 0; h < 2; ++h) pk[j][h] = pk2(unpk(z[h]) * (f32x2){own[j][2 * h], own[j][2 * h + 1]});
      }
      const unsigned eo = (unsigned)((l15 + 16 * (2 * (g4 & 1) + (g4 >> 1))) * 16);
#pragma unroll
      for (int q = 0; q < 3; ++q) {
        const auto s0 = __builtin_amdgcn_permlane16_swap(pk[2 * q][0], pk[2 * q + 1][0], false, false);
        const auto s1 = __builtin_amdgcn_permlane16_swap(pk[2 * q][1], pk[2 * q + 1][1], false, false);
        st16(eo, hf + (size_t)((rb * 4 + wave) * 24 + cb * 3 + q) * 1024, (u32x4){s0[0], s1[0], s0[1], nohole(s1[1])});
      }
    }
    signal1(flg + 96 + wave * 4);
    AB(17);
    ldW();
    wait_vm<15>(ef);
    gemm3(IC(0));
    if (b == 0 && tid == 0) *(u64*)(ws + WS_EP) = tag;
  }
  AB(24);
  AB_OUT();
}

extern "C" void kernel_launch(void* const* d_in, const int* in_sizes, int n_in,
                              void* d_out, int out_size, void* d_ws, size_t ws_size,
                              hipStream_t stream) {
  (void)hipFuncSetAttribute((const void*)k_f, hipFuncAttributeMaxDynamicSharedMemorySize, M_LDS);
  k_f<<<256, 512, M_LDS, stream>>>((const u16*)d_in[0], (const u16*)d_in[1], (const u16*)d_in[2], (const u16*)d_in[3], (const u16*)d_in[4],
                                   (const u16*)d_in[5], (const u16*)d_in[7], (const u16*)d_in[8], (const u16*)d_in[9], (const u16*)d_in[10],
                                   (u16*)d_out, (char*)d_ws);
}
```

```cpp
#include <hip/hip_runtime.h>
#include <hip/hip_bf16.h>
#include <stdint.h>
#include <type_traits>

typedef unsigned short u16;
typedef unsigned long long u64;
typedef __attribute__((ext_vector_type(8))) short bf16x8;
typedef __attribute__((ext_vector_type(4))) short s16x4;
typedef __attribute__((ext_vector_type(4))) float f32x4;
typedef __attribute__((ext_vector_type(2))) float f32x2;
typedef __attribute__((ext_vector_type(4))) unsigned int u32x4;
typedef __attribute__((ext_vector_type(2))) unsigned int u32x2;
typedef __attribute__((ext_vector_type(2))) __bf16 bf16x2n;

#define LDS_AS __attribute__((address_space(3)))
#define IC(n) std::integral_constant<int, (n)>{}
#define SB() __builtin_amdgcn_sched_barrier(0)

#define AB_DECL do {} while (0)
#define AB(i) do {} while (0)
#define AB_OUT() do {} while (0)
#define CB(i) do {} while (0)
#define SS(i) do {} while (0)
constexpr int S_ = 2048, D_ = 640, FH_ = 768, F2_ = 1536;
constexpr unsigned HOLE = 0xFFFFFFFFu;

constexpr size_t WS_EP = 0;
constexpr size_t WS_EF = 4096;
constexpr size_t WS_LF = 22528;
constexpr size_t WS_YF = 14336;
constexpr size_t WS_XF = 20480;
constexpr size_t WS_ST = 32768;
constexpr size_t WS_Z2 = WS_ST + 32 * 12 * 1024;
constexpr size_t WS_H = WS_Z2 + 64 * 48 * 1024;
constexpr size_t WS_ZX = WS_H + 128 * 24 * 1024;
constexpr size_t WS_KV = WS_ZX + 32 * 4 * 4096;
constexpr size_t WS_Y = WS_KV + D_ * S_ * 2;
constexpr size_t WS_WO = WS_Y + D_ * S_ * 2;

__device__ __forceinline__ float bf2f(u16 v) { return __uint_as_float(((unsigned)v) << 16); }
__device__ __forceinline__ unsigned pack2(float lo, float hi) {
  f32x2 v = {lo, hi};
  bf16x2n r = __builtin_convertvector(v, bf16x2n);
  return __builtin_bit_cast(unsigned, r);
}
__device__ __forceinline__ float nv(float v) { asm("" : "+v"(v)); return v; }
__device__ __forceinline__ float fma1(float a, float b, float c) { float r; asm("v_fma_f32 %0, %1, %2, %3" : "=v"(r) : "v"(a), "v"(b), "v"(c)); return r; }
__device__ __forceinline__ float add1(float a, float b) { float r; asm("v_add_f32 %0, %1, %2" : "=v"(r) : "v"(a), "v"(b)); return r; }
__device__ __forceinline__ float mul1(float a, float b) { float r; asm("v_mul_f32 %0, %1, %2" : "=v"(r) : "v"(a), "v"(b)); return r; }
__device__ __forceinline__ unsigned pack2s(float lo, float hi) { return pack2(nv(lo), nv(hi)); }
__device__ __forceinline__ float lo16(unsigned w) { return __uint_as_float(w << 16); }
__device__ __forceinline__ float hi16(unsigned w) { return __uint_as_float(w & 0xFFFF0000u); }
__device__ __forceinline__ float rbf(float f) { return lo16(pack2(f, f)); }
__device__ __forceinline__ f32x2 unpk(unsigned w) { f32x2 r = {lo16(w), hi16(w)}; return r; }
__device__ __forceinline__ unsigned pk2(f32x2 v) { return pack2(v[0], v[1]); }
__device__ __forceinline__ unsigned umx(unsigned a, unsigned b) { return a > b ? a : b; }
__device__ __forceinline__ unsigned nohole(unsigned a) { return a < HOLE - 1 ? a : HOLE - 1; }
__device__ __forceinline__ f32x2 gelu2(f32x2 x) {
  const f32x2 c1 = {-2.3022082f, -2.3022082f}, c2 = {-0.10294324f, -0.10294324f};
  const f32x2 a = x * __builtin_elementwise_fma(x * x, c2, c1);
  f32x2 e = {__builtin_amdgcn_exp2f(a[0]), __builtin_amdgcn_exp2f(a[1])};
  e = e + (f32x2){1.0f, 1.0f};
  const f32x2 r = {__builtin_amdgcn_rcpf(e[0]), __builtin_amdgcn_rcpf(e[1])};
  return x * r;
}
__device__ __forceinline__ float gelu1(float x) {
  const float a = x * __builtin_fmaf(x * x, -0.10294324f, -2.3022082f);
  return x * __builtin_amdgcn_rcpf(__builtin_amdgcn_exp2f(a) + 1.0f);
}
template <int OFF>
__device__ __forceinline__ s16x4 tr_asm(unsigned a) {
  s16x4 r;
  asm volatile("ds_read_b64_tr_b16 %0, %1 offset:%2" : "=&v"(r) : "v"(a), "i"(OFF) : "memory");
  return r;
}
__device__ __forceinline__ bf16x8 join8(s16x4 lo, s16x4 hi) {
  bf16x8 r;
  r[0] = lo[0]; r[1] = lo[1]; r[2] = lo[2]; r[3] = lo[3];
  r[4] = hi[0]; r[5] = hi[1]; r[6] = hi[2]; r[7] = hi[3];
  return r;
}
__device__ __forceinline__ unsigned lds_addr(const void* p) { return (unsigned)(uintptr_t)(LDS_AS const char*)p; }
#define HIREG(sym, r) ".set " sym ", 0\n.set hir_st, 0\n.irpc c, \"" r "\"\n.ifc \\c,:\n.set hir_st, 1\n.else\n.ifc \\c,]\n.set hir_st, 0\n.else\n.if hir_st\n.set " sym ", " sym " * 10 + \\c\n.endif\n.endif\n.endif\n.endr\n"
#define MF "v_mfma_f32_16x16x32_bf16 "
#define WAIT_LGKM0() asm volatile("s_waitcnt lgkmcnt(0)" ::: "memory")
#define SLP 2
template <int SL = 1> __device__ __forceinline__ void flag_wait_asm(unsigned faddr, unsigned target) {
  unsigned v, sv;
  asm volatile(".Lfw_%=:\n\tds_read_b32 %0, %2\n\ts_waitcnt lgkmcnt(0)\n\tv_readfirstlane_b32 %1, %0\n\ts_nop 0\n\t"
               "s_cmp_ge_u32 %1, %3\n\ts_cbranch_scc1 .Lfd_%=\n\ts_sleep %4\n\ts_branch .Lfw_%=\n\t.Lfd_%=:"
               : "=&v"(v), "=&s"(sv) : "v"(faddr), "s"(target), "i"(SL) : "memory", "scc");
}
__device__ __forceinline__ void flag_wait2(unsigned faddr, unsigned target) {
  unsigned v, w, sv;
  asm volatile(".Lgw_%=:\n\tds_read_b32 %0, %3\n\tds_read_b32 %1, %3 offset:4\n\ts_waitcnt lgkmcnt(0)\n\tv_min_u32 %0, %0, %1\n\ts_nop 0\n\tv_readfirstlane_b32 %2, %0\n\ts_nop 0\n\t"
               "s_cmp_ge_u32 %2, %4\n\ts_cbranch_scc1 .Lgd_%=\n\ts_sleep %5\n\ts_branch .Lgw_%=\n\t.Lgd_%=:"
               : "=&v"(v), "=&v"(w), "=&s"(sv) : "v"(faddr), "s"(target), "i"(SLP) : "memory", "scc");
}
__device__ __forceinline__ void signal1(unsigned a, unsigned one = 1u) {
  asm volatile("s_mov_b64 exec, 1\n\tds_add_u32 %0, %1\n\ts_mov_b64 exec, -1" :: "v"(a), "v"(one) : "memory");
}
__device__ __forceinline__ void dma16(unsigned vo, const void* sb, unsigned lds) {
  asm volatile("s_mov_b32 m0, %2\n\ts_nop 0\n\tglobal_load_lds_dwordx4 %0, %1" :: "v"(vo), "s"(sb), "s"(lds) : "memory");
}
template <int LO, int GO> __device__ __forceinline__ void dma32s(unsigned vo0, unsigned vo1, const void* sb, unsigned lds) {
  asm volatile("s_add_u32 m0, %5, %6\n\ts_add_u32 vcc_lo, %2, %4\n\ts_addc_u32 vcc_hi, %3, 0\n\t"
               "global_load_lds_dwordx4 %0, vcc\n\tglobal_load_lds_dwordx4 %1, vcc offset:1024"
               :: "v"(vo0), "v"(vo1), "s"((unsigned)(uintptr_t)sb), "s"((unsigned)((uintptr_t)sb >> 32)), "i"(GO), "s"(lds), "i"(LO) : "memory", "scc", "vcc");
}
template <int LO, int GO, int WRAP, int LIM> __device__ __forceinline__ void dma32r(unsigned vo0, unsigned vo1, const void* sb, unsigned lds, int rot) {
  const char* s2 = (const char*)sb - WRAP;
  asm volatile("s_add_u32 m0, %5, %6\n\ts_cmp_ge_u32 %7, %8\n\ts_cselect_b64 vcc, %3, %2\n\ts_add_u32 vcc_lo, vcc_lo, %4\n\ts_addc_u32 vcc_hi, vcc_hi, 0\n\t"
               "global_load_lds_dwordx4 %0, vcc\n\tglobal_load_lds_dwordx4 %1, vcc offset:1024"
               :: "v"(vo0), "v"(vo1), "s"(sb), "s"(s2), "i"(GO), "s"(lds), "i"(LO), "s"(rot), "i"(LIM) : "memory", "scc", "vcc");
}
__device__ __forceinline__ void dma16o(unsigned vo, const void* sb, int go, unsigned lds, int lo) {
  asm volatile("s_add_u32 m0, %3, %4\n\ts_nop 0\n\tglobal_load_lds_dwordx4 %0, %1 offset:%2" :: "v"(vo), "s"(sb), "i"(go), "s"(lds), "i"(lo - go) : "memory", "scc");
}
__device__ __forceinline__ void st16(unsigned vo, const void* sb, u32x4 v) {
  asm volatile("global_store_dwordx4 %0, %1, %2 sc1\n\ts_nop 1" :: "v"(vo), "v"(v), "s"(sb) : "memory");
}
__device__ __forceinline__ void touch(unsigned vo, const void* sb, unsigned lds) {
  asm volatile("s_mov_b32 m0, %2\n\ts_nop 0\n\tglobal_load_lds_dword %0, %1" :: "v"(vo), "s"(sb), "s"(lds) : "memory");
}
__device__ __forceinline__ void st8(unsigned vo, const void* sb, u32x2 v) { asm volatile("global_store_dwordx2 %0, %1, %2 sc1" :: "v"(vo), "v"(v), "s"(sb) : "memory"); }
template <class A, class B> __device__ __forceinline__ void MFMA(f32x4& acc, const A& a, const B& b) {
  asm volatile("v_mfma_f32_16x16x32_bf16 %0, %1, %2, %0" : "+v"(acc) : "v"(a), "v"(b));
}
template <class A, class B> __device__ __forceinline__ void MFMA0(f32x4& acc, const A& a, const B& b) {
  asm volatile("v_mfma_f32_16x16x32_bf16 %0, %1, %2, 0" : "=&v"(acc) : "v"(a), "v"(b));
}
template <bool Z, class A, class B> __device__ __forceinline__ void MFMAW(int n, f32x4& acc, const A& a, const B& b) {
  if (n < 0) { if (Z) MFMA0(acc, a, b); else MFMA(acc, a, b); }
  else if (Z) asm volatile("s_waitcnt lgkmcnt(%3)\n\tv_mfma_f32_16x16x32_bf16 %0, %1, %2, 0" : "=&v"(acc) : "v"(a), "v"(b), "i"(n));
  else asm volatile("s_waitcnt lgkmcnt(%3)\n\tv_mfma_f32_16x16x32_bf16 %0, %1, %2, %0" : "+v"(acc) : "v"(a), "v"(b), "i"(n));
}
__device__ __forceinline__ float sel4(float a, float b, float c, float d, u64 m1, u64 m2) {
  float r, t;
  asm volatile("v_cndmask_b32_e64 %0, %2, %3, %6\n\tv_cndmask_b32_e64 %1, %4, %5, %6\n\tv_cndmask_b32_e64 %0, %0, %1, %7"
               : "=&v"(r), "=&v"(t) : "v"(a), "v"(b), "v"(c), "v"(d), "s"(m1), "s"(m2));
  return r;
}
template <class T> __device__ __forceinline__ void pin1(T& r) { asm volatile("" : "+v"(r)); }
template <class T> __device__ __forceinline__ T opq(T r) { asm volatile("" : "+v"(r)); return r; }
template <class... T> __device__ __forceinline__ void pin(T&... r) { (pin1(r), ...); }
template <int N, class... T> __device__ __forceinline__ void wait_vm(T&... r) { asm volatile("s_waitcnt vmcnt(%0)" :: "n"(N) : "memory"); (pin1(r), ...); }
template <int N, class... T> __device__ __forceinline__ void wait_lg(T&... r) { asm volatile("s_waitcnt lgkmcnt(%0)" :: "n"(N) : "memory"); (pin1(r), ...); }
template <int OFF, class T> __device__ __forceinline__ void ds16(T& d, unsigned a) {
  asm volatile("ds_read_b128 %0, %1 offset:%2" : "=&v"(d) : "v"(a), "i"(OFF) : "memory");
}
template <int OFF> __device__ __forceinline__ void dsw16(unsigned a, const f32x4& v) {
  asm volatile("ds_write_b128 %0, %1 offset:%2" :: "v"(a), "v"(v), "i"(OFF) : "memory");
}
__device__ __forceinline__ void dsw4(unsigned a, float v) { asm volatile("ds_write_b32 %0, %1" :: "v"(a), "v"(v) : "memory"); }
#define OSC ""
#define HSC ""
template <int OFF, class T> __device__ __forceinline__ void ld16h(T& d, unsigned vo, const void* sb) {
  asm volatile("global_load_dwordx4 %0, %1, %2 offset:%3 " HSC : "=&v"(d) : "v"(vo), "s"(sb), "i"(OFF) : "memory");
}
template <class T> __device__ __forceinline__ void reld6(T (&B)[6], unsigned vo, const void* sb) {
  asm volatile("global_load_dwordx4 %0, %6, %7 offset:-2048 sc1\n\tglobal_load_dwordx4 %1, %6, %7 offset:-1024 sc1\n\t"
               "global_load_dwordx4 %2, %6, %7 sc1\n\tglobal_load_dwordx4 %3, %6, %7 offset:1024 sc1\n\t"
               "global_load_dwordx4 %4, %6, %7 offset:2048 sc1\n\tglobal_load_dwordx4 %5, %6, %7 offset:3072 sc1"
               : "+v"(B[0]), "+v"(B[1]), "+v"(B[2]), "+v"(B[3]), "+v"(B[4]), "+v"(B[5]) : "v"(vo), "s"(sb) : "memory");
}

constexpr int L_FLAG = 0;
constexpr int L_DUM = 256;
constexpr int L_XM = 512;
constexpr int L_XR = 768;
constexpr int L_CS = 1024;
constexpr int L_ZS = 2048;
constexpr int L_CS2 = 3072;
constexpr int L_Z1 = 4096;
constexpr int L_RAW = L_Z1;
constexpr int L_SLOT = 16384;
constexpr int L_KV = 512, KV_P = 3088, L_WO = L_KV + 8 * KV_P;
static_assert(L_WO % 16 == 0 && L_WO + 4 * 1024 <= L_SLOT + 2 * 8192, "staging");
constexpr int L_P2 = L_SLOT + 12 * 8192;
constexpr int CV_C = 4608, CV_CS = 4160, CV_BUF = CV_C + 2 * CV_CS;
constexpr int M_CV = L_SLOT;
constexpr int M_RING = L_SLOT + 6 * 8192;
constexpr int P3_G = L_SLOT;
constexpr int P3_C = P3_G + 80 * 1024;
constexpr int yat(int j) { return M_CV + (j >> 1) * CV_BUF + 8192 + (j & 1) * 2048; }
constexpr int M_LDS = 163840;
static_assert(3 * CV_BUF + 8192 <= 6 * 8192 && P3_C + 60 * 1024 <= M_LDS && CV_BUF >= 8192 + 4096, "LDS map");

enum { UX, UA, UB, UV };
struct Unit { int kind, idx, need, free, slot; };
#define SCHED gugugugwBuluouk_uuxuu_uuxuxu_uuxuxuu_Euuuuh_vuumumuh_vuumumuhF_vuvumum_DRvuuUHmmmmSptt_VhhhttttttJItttttttt
#define STR_(x) #x
#define STR(x) STR_(x)
constexpr char SCH[] = STR(SCHED);
constexpr int NSCH = sizeof(SCH) - 1;
constexpr int XNEED[10] = {1, 2, 2, 3, 3, 4, 4, 5, 5, 6};
constexpr int VNEED[5] = {5, 6, 7, 7, 8};
constexpr int XSLOT[10] = {17, 16, 15, 13, 10, 14, 11, 17, 9, 16}, WSLOT[10] = {6, 2, 4, 0, 6, 12, 10, 4, 0, 8}, VSLOT[10] = {8, 15, 3, 2, 14, 17, 16, 15, 7, 6}, XW = 6;
constexpr int cnt(char c, int e = NSCH) { int n = 0; for (int i = 0; i < e; ++i) n += SCH[i] == c || (c == 'u' && SCH[i] == 'g'); return n; }
constexpr int pos(char c, int k = 0) { for (int i = 0; i < NSCH; ++i) if (SCH[i] == c && k-- == 0) return i; return NSCH; }
constexpr Unit unit(int n) {
  for (int b = 1; b < 11; ++b) {
    for (int c = 0; c < 10; ++c) if (XNEED[c] == b && n-- == 0) return {UX, c, b, b + 1, XSLOT[c]};
    for (int c = 0; c < 5; ++c) if (VNEED[c] == b && n-- == 0) return {UV, c, b, b + 1, VSLOT[c]};
    if (n < 2) return {n ? UB : UA, b - 1, b < 8 ? b : 8, b < 7 ? b + 2 : 9, WSLOT[b - 1] + n};
    n -= 2;
  }
  return {UV, n + 5, 10, 99, VSLOT[n + 5]};
}
constexpr int FLAGB = cnt('_', pos('D')), GZ = cnt('_', pos('E'));
constexpr int nvm(char c) { return c == 'u' ? 2 : (c == 'H' ? 5 : (c == 'U' ? 4 : (c == 'F' || c == 'h' || c == 't' || c == 'w'))); }
constexpr int behind(int g, int e, char w) {
  const int b = cnt('_', e) + 1;
  int n = 0;
  for (int i = 0; i < e; ++i) {
    const char c = SCH[i];
    if (g == 0) { if (c == 'g') n = w == '_' && unit(cnt('u', i)).need <= b ? 0 : n + 2; if (c == 'B') n += 2; continue; }
    n = (w == '_' ? (c == 'u' && unit(cnt('u', i)).need <= b) || (c == 'h' && b == FLAGB) || (c == 'k' && b == GZ) : c == w) ? 0 : n + nvm(c);
  }
  return g == 0 && b >= GZ ? 0 : n;
}
constexpr bool plan_ok() {
  if (cnt('u') != 40 || cnt('_') != 9 || cnt('x') != 5 || cnt('h') != 6 || cnt('t') != 16 || cnt('I') * cnt('J') * cnt('p') != 1 || cnt('v') != 5 || cnt('m') != 10) return false;
  if (cnt('B') * cnt('D') * cnt('E') * cnt('F') * cnt('l') * cnt('o') * cnt('k') * cnt('H') * cnt('U') * cnt('V') * cnt('R') * cnt('S') != 1) return false;
  if (pos('l') < pos('B') || pos('o') < pos('B') || pos('k') < pos('l') || pos('k') > pos('_') || pos('o') > pos('_') || cnt('g', pos('B')) != cnt('g')) return false;
  if (cnt('_', pos('h', 2)) >= FLAGB || GZ < 3 || GZ > 6 || FLAGB < GZ || FLAGB > 9 || pos('o') > pos('k') || pos('H') > pos('I') || pos('F') > pos('U')) return false;
  if (cnt('_', pos('R')) != 8 || cnt('_', pos('S')) != 8 || pos('S') < pos('R') || cnt('_', pos('I')) != 9 || pos('J') < pos('U') || cnt('_', pos('J')) != 9) return false;
  if (cnt('_', pos('V')) != 9 || cnt('u', pos('V')) < 40) return false;
  for (int c = 0; c < 5; ++c) if (cnt('_', pos('x', c)) != XNEED[c] || cnt('_', pos('v', c)) != VNEED[c] || pos('m', 2 * c) < pos('v', c)) return false;
  if (cnt('_', pos('p')) != 8 || pos('p') < pos('m', 9)) return false;
  int fr[18] = {1, 1};
  for (int i = 0; i < NSCH; ++i) if (SCH[i] == 'u' || SCH[i] == 'g') {
    const Unit u = unit(cnt('u', i));
    const int p = cnt('_', i);
    if (p < fr[u.slot] || p >= u.need) return false;
    fr[u.slot] = u.free;
  }
  return true;
}
static_assert(plan_ok(), "phase A plan");
template <int I, int E, class F> __device__ __forceinline__ void sfor(F&& f) { if constexpr (I < E) { f(IC(I)); sfor<I + 1, E>(f); } }

#define CVP -1
__device__ __forceinline__ void conv_core(unsigned buf, int lane, unsigned Dw, f32x4 (&ac)[8], unsigned pf) {
  const int l15 = lane & 15, g4 = lane >> 4;
  const int c = (l15 + 1) & 1;
  unsigned aa[4];
#pragma unroll
  for (int k = 0; k < 4; ++k) aa[k] = buf + CV_C + c * CV_CS + (2031 - l15 + 8 * g4 - c) * 2 - 512 - 1024 * k;
  unsigned bb = buf + 512 + 32 * l15 - 32 + 16 * g4;
  u32x2 A[2][8][2];
  u32x4 B[2][8];
  constexpr int TT[36] = {7, 6, 5, 4, 7, 6, 5, 4, 7, 6, 5, 3, 7, 6, 5, 4, 7, 6, 3, 2, 7, 6, 5, 4, 7, 3, 2, 1, 7, 6, 5, 4, 3, 2, 1, 0};
  constexpr int AA[36] = {0, 0, 0, 0, 1, 1, 1, 1, 2, 2, 2, 0, 3, 3, 3, 2, 4, 4, 1, 0, 5, 5, 4, 3, 6, 2, 1, 0, 7, 6, 5, 4, 3, 2, 1, 0};
  auto ld1 = [&](int p, int n) {
    if (n < 16) {
      const int a = n >> 1, o = ((a & 1) ? 0 : 128) + 2 * (n & 1);
      asm volatile("ds_read2_b32 %0, %1 offset0:%2 offset1:%3" : "=&v"(A[p][(n >> 1) & 7][n & 1]) : "v"(aa[(a >> 1) & 3]), "i"(o), "i"(o + 1) : "memory");
    } else
      asm volatile("ds_read_b128 %0, %1 offset:%2" : "=&v"(B[p][n & 7]) : "v"(bb), "i"(512 * (n & 7)) : "memory");
  };
  auto next = [&](int dl) {
#pragma unroll
    for (int k = 0; k < 4; ++k) aa[k] += dl;
    bb += dl;
    SB();
  };
  auto land = [&](int p) {
    asm volatile("s_waitcnt lgkmcnt(0)" : "+v"(A[p][0][0]), "+v"(A[p][0][1]), "+v"(A[p][1][0]), "+v"(A[p][1][1]), "+v"(A[p][2][0]), "+v"(A[p][2][1]),
                 "+v"(A[p][3][0]), "+v"(A[p][3][1]), "+v"(A[p][4][0]), "+v"(A[p][4][1]), "+v"(A[p][5][0]), "+v"(A[p][5][1]), "+v"(A[p][6][0]), "+v"(A[p][6][1]),
                 "+v"(A[p][7][0]), "+v"(A[p][7][1]), "+v"(B[p][0]), "+v"(B[p][1]), "+v"(B[p][2]), "+v"(B[p][3]), "+v"(B[p][4]), "+v"(B[p][5]),
                 "+v"(B[p][6]), "+v"(B[p][7]) :: "memory");
    SB();
  };
  auto mm = [&](int p) {
#pragma unroll
    for (int n = 0; n < 36; ++n) {
      const int t = TT[n], a = AA[n];
      const u32x4 av = {A[p][a][0][0], A[p][a][0][1], A[p][a][1][0], A[p][a][1][1]};
      MFMA(ac[t], av, B[p][t - a]);
      if (n < 24) ld1(p ^ 1, n);
      SB();
    }
    land(p ^ 1);
  };
#pragma unroll
  for (int n = 0; n < 24; ++n) ld1(0, n);
  u32x4 ad;
#pragma unroll
  for (int e = 0; e < 4; ++e) ad[e] = lane == 32 + 2 * e + (lane & 1) + (lane & 8) * 3 ? Dw << (16 * (lane & 1)) : 0u;
  pin(ad);
  land(0);
#pragma unroll
  for (int t = 0; t < 8; ++t) MFMA0(ac[t], ad, B[0][t]);
#pragma unroll 1
  for (int r = 0; r < 8; r += 2) {
    if (CVP >= 0 && !(CVP & 1)) flag_wait_asm(pf, r == CVP ? 4u : 0u);
    next(-64);
    mm(0);
    if (CVP >= 0 && (CVP & 1)) flag_wait_asm(pf, r + 1 == CVP ? 4u : 0u);
    next(r < 6 ? -64 : 0);
    mm(1);
  }
  asm volatile("s_nop 15" : "+v"(ac[0]), "+v"(ac[1]), "+v"(ac[2]), "+v"(ac[3]), "+v"(ac[4]), "+v"(ac[5]), "+v"(ac[6]), "+v"(ac[7]));
}

#define ROTA 3
#define RA1 1
#define RA2 0
#define RA3 0
#define RB1 0
#define RB2 4
#define RB3 0
#define G2P 0
#define RB4 2
#define THR 24
#define RB5 2
#define YS 8
#define WO3 0
#define C3P 0
#define FE3 9
#define WSH 1
#define T3L 4
#define B2E 0
#define BDL 0
#define PAE 0
#define BDM 2
#define T7D 1
#define PRE 1
#define RDM 0x6666u
#define KC 40
#define NWY 4
#define CPR 1
#define STW 1
#define PN 10
#define P2P 2
#define SGS 11

__global__ __launch_bounds__(512) void k_f(
    const u16* __restrict__ x, const u16* __restrict__ kin, const u16* __restrict__ vin, const u16* __restrict__ shift_k,
    const u16* __restrict__ ssm_k, const u16* __restrict__ ssm_D, const u16* __restrict__ w_in, const u16* __restrict__ w_sp,
    const u16* __restrict__ b_sp, const u16* __restrict__ w_out, u16* __restrict__ out, char* ws) {
  extern __shared__ __attribute__((aligned(16))) char smem[];
  const int tid = threadIdx.x, lane = tid & 63, wave = __builtin_amdgcn_readfirstlane(tid >> 6);
  const int b = blockIdx.x;
  const int xcd = b & 7, idx = b >> 3;
  const int rb = 8 * (xcd >> 1) + (idx >> 2), cb = 4 * (xcd & 1) + (idx & 3);
  const int t0 = rb * 64, d0 = cb * 80;
  const int g4 = lane >> 4, l15 = lane & 15;
  const unsigned lin = (unsigned)(lane * 16);
  const unsigned lb = lds_addr(smem), flg = lb + L_FLAG;
  AB_DECL;
  AB(0);
  asm volatile("" :: "s"(x), "s"(kin), "s"(vin), "s"(shift_k), "s"(ssm_k), "s"(ssm_D), "s"(w_in), "s"(w_sp), "s"(b_sp), "s"(w_out), "s"(out), "s"(ws));
  u64 tag = *(const __attribute__((address_space(4))) u64*)(uintptr_t)ws;
  int tmo = 1 << 16;
#define CWARM 1
  auto cwarm = [&]() {
    u64 pc;
    unsigned clen;
    asm volatile("s_getpc_b64 %0\n.Lpc_here_%=:\n\ts_mov_b32 %1, .Lcode_end-.Lpc_here_%=\n\t.subsection 2\n.Lcode_end:\n\t.fill 256, 4, 0xBF800000\n\t.subsection 0" : "=s"(pc), "=s"(clen));
    touch(min((unsigned)(idx * 1536 + min(lane, 11) * 128), clen - 4u) & ~3u, (const void*)pc, lb + L_DUM);
  };

  const int cw = wave & 3, wm = wave & 1, wl = (wave >> 1) & 1, ksb = wm ^ wl, odd = cb & 1, pr = cb >> 1;
  const int r8 = lane >> 3, c8 = lane & 7;
  unsigned voX[2], voW[2];
#pragma unroll
  for (int h = 0; h < 2; ++h) {
    voX[h] = (unsigned)((h * 8 + r8) * (D_ * 2) + ((c8 ^ (h * 4 + (r8 & 3))) << 4) - h * 1024);
    voW[h] = (unsigned)((h * 8 + r8) * (F2_ * 2) + ((c8 ^ (h * 4 + (r8 & 2))) << 4) - h * 1024);
  }
  const int rot = (RA1 * ((rb & 7) * 5 >> 2) + ROTA * (cb & 3) + RA2 * ((rb >> 3) * 5 >> 1) + RA3 * (cb >> 2)) % 10, rot2 = (RB1 * (cb & 3) + RB2 * (cb >> 2) + RB3 * (rb & 7) + RB4 * (rb >> 3) + RB5 * (cb & 1)) & 7;
  const char* gX = (const char*)x + (size_t)(t0 + cw * 16) * (D_ * 2) + rot * 128;
  const char* gW = (const char*)w_in + (size_t)(cw * 16 + rot * 64) * (F2_ * 2) + pr * 384;
  const char *gV = gW + odd * 256, *gB = gW + (odd ? 128 : FH_ * 2 + 128);
  const char* gS = (const char*)w_sp + (size_t)t0 * (S_ * 2);
  const unsigned aoff[2] = {(unsigned)(r8 * 4096 + ((c8 ^ r8) << 4)), (unsigned)(r8 * 4096 + ((c8 ^ r8) << 4) + 32768)};
  const unsigned ldw = lb + cw * 2048;
  auto issue = [&](auto nc) {
    constexpr Unit u = unit(decltype(nc)::value);
    constexpr int lo = L_SLOT + u.slot * 8192;
    if (u.kind == UX) dma32r<lo, u.idx * 128, 1280, 10 - u.idx>(voX[0], voX[1], gX, ldw, rot);
    else dma32r<lo, u.idx * (64 * F2_ * 2) + (u.kind == UA ? FH_ * 2 : 0), 640 * F2_ * 2, 10 - u.idx>(voW[0], voW[1], u.kind == UB ? gB : gV, ldw, rot);
    SB();
  };
  const int kd = d0 + min(wave, 4) * 16 + r8 * 2;
  u32x4 sw;
  {
    const int ks0 = t0 + wave * 8;
#pragma unroll
    for (int i = 0; i < 3; ++i) {
      const int id = min(i * 64 + lane, 189), r = id / 10, c = id % 10;
      const char* kp = (r < 11 ? (const char*)kin + (size_t)max(ks0 - 3 + r, 0) * (D_ * 2) : (const char*)vin + (size_t)(ks0 + r - 11) * (D_ * 2)) + cb * 160 + c * 16;
      asm volatile("s_mov_b32 m0, %1\n\ts_nop 0\n\tglobal_load_lds_dwordx4 %0, off" :: "v"(kp), "s"(lb + L_KV + wave * KV_P + i * 1024) : "memory");
    }
    if (CWARM && wave == 5) cwarm();
    SB();
  }
  auto ldsw = [&]() { asm volatile("global_load_dwordx4 %0, %1, %2" : "=&v"(sw) : "v"(kd * 8), "s"(shift_k) : "memory"); SB(); };
  unsigned kk[11], vv[8];
  u32x4 oa, ob;
  auto kvld = [&]() {
    const char* kl = smem + L_KV + c8 * KV_P + min(wave, 4) * 32 + r8 * 4;
#pragma unroll
    for (int i = 0; i < 11; ++i) kk[i] = *(const unsigned*)(kl + i * 160);
#pragma unroll
    for (int i = 0; i < 8; ++i) vv[i] = *(const unsigned*)(kl + (11 + i) * 160);
  };
  auto kvm = [&](int e0, int e1) {
    const f32x2 w0 = {lo16(sw[0]), lo16(sw[2])}, w1 = {hi16(sw[0]), hi16(sw[2])};
    const f32x2 w2 = {lo16(sw[1]), lo16(sw[3])}, w3 = {hi16(sw[1]), hi16(sw[3])};
    f32x2 kf[11], rr[8];
#pragma unroll
    for (int i = 2 * e0; i < 2 * e1 + 3; ++i) kf[i] = unpk((i < 3 && t0 + c8 == 0) ? 0u : kk[i]);
#pragma unroll
    for (int i = 2 * e0; i < 2 * e1; ++i) {
      f32x2 a0 = kf[i] * w3;
      a0 = __builtin_elementwise_fma(kf[i + 1], w2, a0);
      a0 = __builtin_elementwise_fma(kf[i + 2], w1, a0);
      a0 = __builtin_elementwise_fma(kf[i + 3], w0, a0);
      rr[i] = a0 * unpk(vv[i]);
    }
#pragma unroll
    for (int e = e0; e < e1; ++e) { oa[e] = pack2(rr[2 * e][0], rr[2 * e + 1][0]); ob[e] = pack2(rr[2 * e][1], rr[2 * e + 1][1]); }
  };
  auto kvst = [&]() {
    const unsigned ko = (unsigned)((kd * S_ + t0 + c8 * 8) * 2);
    st16(ko, ws + WS_KV, oa);
    st16(ko, ws + WS_KV + S_ * 2, ob);
    SB();
  };
  auto barrier0 = [&](int nv) {
    SB();
    asm volatile("s_waitcnt vmcnt(%0) lgkmcnt(0)\n\ts_barrier" :: "n"(nv) : "memory");
    SB();
    asm volatile("" : "+v"(sw), "+s"(tag));
    tag += 1;
    asm volatile("" : "+s"(tag));
  };
  unsigned pX[2], pW[2];
#pragma unroll
  for (int k = 0; k < 2; ++k) {
    const int q = l15 >> 2, p = lane & 3, sz = (q >> 1) | ((g4 & 1) << 1);
    pX[k] = lb + (wm * 32 + q * 8 + p) * 128 + ((((k ^ ksb) * 4 + g4) ^ (l15 & 7)) << 4);
    pW[k] = lb + ((k ^ ksb) * 32 + g4 * 8 + q) * 128 + (((sz << 1) | (p >> 1)) << 4) + (p & 1) * 8;
  }
  bf16x8 ones;
#pragma unroll
  for (int e = 0; e < 8; ++e) ones[e] = (short)0x3F80;
  f32x4 xm[2], xr[2];
  auto ldxs = [&]() {
#pragma unroll
    for (int i = 0; i < 2; ++i) {
      xm[i] = *(const f32x4*)(smem + L_XM + (wm * 32 + g4 * 8 + i * 4) * 4);
      xr[i] = *(const f32x4*)(smem + L_XR + (wm * 32 + g4 * 8 + i * 4) * 4);
    }
  };
  auto act = [&](const f32x4& a, float c, int i) {
    const f32x2 nc = {-c, -c};
    const f32x2 lo = gelu2(__builtin_elementwise_fma((f32x2){xm[i][0], xm[i][1]}, nc, (f32x2){a[0], a[1]}) * (f32x2){xr[i][0], xr[i][1]});
    const f32x2 hi = gelu2(__builtin_elementwise_fma((f32x2){xm[i][2], xm[i][3]}, nc, (f32x2){a[2], a[3]}) * (f32x2){xr[i][2], xr[i][3]});
    return (f32x4){lo[0], lo[1], hi[0], hi[1]};
  };
  auto pack8 = [&](const f32x4& a, const f32x4& c) { return (u32x4){pack2(a[0], a[1]), pack2(a[2], a[3]), pack2(c[0], c[1]), pack2(c[2], c[3])}; };
  auto z1at = [&](int f0) { return (u32x4*)(smem + L_Z1 + (f0 + l15) * 128 + (((wm * 4 + g4) ^ (l15 & 7)) << 4)); };
  auto z1put = [&](int f0, const u32x4& v) { *z1at(f0) = v; };
  f32x4 ac2[2][2], cs2[2];
  s16x4 VF[10][2][2];
  unsigned aV[2][2];
  auto rdV = [&](auto hc_, auto nc, int r) {
    constexpr int n = decltype(nc)::value, c = 5 * decltype(hc_)::value + (n >> 1), so = (VSLOT[c] - 2) * 8192, po = n < 2 ? -1 : ((VSLOT[n < 2 ? c : c - 1] - 2) * 8192) >> 16;
    const int jj = r >> 1;
    if (!(r & 1)) {
      if ((so >> 16) != po) aV[n & 1][jj] = opq((pW[n & 1] ^ (unsigned)(wl * 64 + jj * 32)) + L_SLOT + 2 * 8192 + (so & ~0xFFFF));
      VF[n][jj][0] = tr_asm<(so & 0xFFFF)>(aV[n & 1][jj]);
    } else VF[n][jj][1] = tr_asm<(so & 0xFFFF) + 512>(aV[n & 1][jj]);
    SB();
  };
  auto mm2 = [&](auto hc_, auto nc, bf16x8 (*X)[2][2], int nw, auto&& nxt) {
    constexpr int n = decltype(nc)::value, j = n >> 1, k = n & 1;
    constexpr bool z = n == 0 && !decltype(hc_)::value;
    const bf16x8 w0 = join8(VF[n][0][0], VF[n][0][1]), w1 = join8(VF[n][1][0], VF[n][1][1]);
    if (k == 0) { MFMAW<n == 0>(nw, cs2[0], ones, w0); MFMAW<n == 0>(-1, cs2[1], ones, w1); }
    MFMAW<z>(k ? nw : -1, ac2[0][0], X[j][k][0], w0); nxt(0);
    MFMAW<z>(-1, ac2[1][0], X[j][k][1], w0); nxt(1);
    MFMAW<z>(-1, ac2[0][1], X[j][k][0], w1); nxt(2);
    MFMAW<z>(-1, ac2[1][1], X[j][k][1], w1); nxt(3);
    if constexpr (n == 9) {
      pin(cs2[0], cs2[1]);
      if (g4 < 2) *(float*)(smem + L_CS2 + (wave * 32 + lane) * 4) = g4 ? cs2[1][0] : cs2[0][0];
    }
    SB();
  };
  const unsigned p2a = lb + L_P2 + cw * 4096 + lin;
  auto sttag = [&](const char* p) {
    asm volatile("s_mov_b64 exec, 1\n\tglobal_store_dwordx2 %0, %1, %2 sc1\n\ts_mov_b64 exec, -1" :: "v"(lin), "v"((u32x2){opq((unsigned)tag), opq((unsigned)(tag >> 32))}), "s"(p) : "memory");
  };
  auto ldtag = [&](u32x2& f, unsigned vo, const char* p) { asm volatile("global_load_dwordx2 %0, %1, %2 sc1" : "=&v"(f) : "v"(vo), "s"(p) : "memory"); };
  auto notag = [&](const u32x2& f) { return __builtin_amdgcn_ballot_w64(f[0] != (unsigned)tag || f[1] != (unsigned)(tag >> 32)) != 0; };
  auto waittag = [&](u32x2& f, unsigned vo, const char* p) {
    while (__builtin_expect(notag(f) && tmo > 0, 0)) {
      --tmo;
      __builtin_amdgcn_s_sleep(4);
      ldtag(f, vo, p);
      wait_vm<0>(f);
    }
  };
  u32x2 ef;

  char* const hf = ws + WS_H;
  bf16x8 Wq[15];
  const char* wg = ws + WS_WO + (size_t)(wave * 120 + cb * 5) * 1024;
  auto ldW = [&](int n0, int n1) {
    if (n0 == 0) ldtag(ef, (unsigned)((lane & 3) * 8), ws + WS_LF + (rb * 8 + wave) * 32);
#pragma unroll
    for (int n = n0; n < n1; ++n)
      asm volatile("global_load_dwordx4 %0, %1, %2 offset:%3 sc1" : "=&v"(Wq[n]) : "v"(lin), "s"(wg + (n / 5) * 40960 + 2048), "i"((n % 5) * 1024 - 2048) : "memory");
    SB();
  };
  f32x4 ac3[5];
  auto gemm3 = [&](auto khc, auto&& pre) {
    constexpr int kh = decltype(khc)::value;
    unsigned hoff[4];
#pragma unroll
    for (int it = 0; it < 4; ++it) hoff[it] = (unsigned)(((rb * 4 + ((cw + 1 + it) & 3)) * 24 + 3 * wave) * 1024) + lin;
    u32x4 Hq[4][3];
    auto ldh1 = [&](int it, int k) {
      asm volatile("global_load_dwordx4 %0, %1, %2 offset:%3 " HSC : "=&v"(Hq[it][k]) : "v"(hoff[it]), "s"(hf), "i"(k * 1024) : "memory");
    };
    waittag(ef, (unsigned)((lane & 3) * 8), ws + WS_LF + (rb * 8 + wave) * 32);
    AB(18);
    if (!kh) signal1(flg + 48);
    if (PRE) pre();
    flag_wait_asm(flg + 48, 4u);
    AB(19);
    __builtin_amdgcn_s_setprio(3);
    ldh1(0, 0); ldh1(0, 1); ldh1(0, 2);
    __builtin_amdgcn_s_setprio(2);
    ldh1(1, 0); ldh1(1, 1); ldh1(1, 2);
    __builtin_amdgcn_s_setprio(kh * C3P);
    SB();
    if (!PRE) pre();
    flag_wait_asm(flg + 64, 4u);
    const unsigned px = lb + (kh ? P3_C + cw * 15360 : P3_G + cw * 20480) + lin;
    f32x4 pa[5][5], ae[5];
    auto put = [&](int it, int j) { asm volatile("ds_write_b128 %0, %1 offset:%2" :: "v"(px), "v"((it & 1) ? ac3[j] : ae[j]), "i"((it * 5 + j) * 1024) : "memory"); };
    auto fetch = [&](int n, int g, int q) {
      const int ms = (cw - 1 - q) & 3;
      const unsigned pn = lb + (g ? P3_C + ms * 15360 : P3_G + ms * 20480) + lin;
#pragma unroll
      for (int j = 0; j < 5; ++j) asm volatile("ds_read_b128 %0, %1 offset:%2" : "=&v"(pa[n][j]) : "v"(pn), "i"((q * 5 + j) * 1024) : "memory");
      SB();
    };
    sfor<0, 4>([&](auto itc) {
      constexpr int it = decltype(itc)::value;
      wait_vm<(it < 3 ? 3 : 0)>(Hq[it][0], Hq[it][1], Hq[it][2]);
      if constexpr (it == 0) pin(Wq[0], Wq[1], Wq[2], Wq[3], Wq[4], Wq[5], Wq[6], Wq[7], Wq[8], Wq[9], Wq[10], Wq[11], Wq[12], Wq[13], Wq[14]);
      auto bad = [&]() { return __builtin_amdgcn_ballot_w64(umx(umx(Hq[it][0][3], Hq[it][1][3]), Hq[it][2][3]) == HOLE) != 0; };
      if (__builtin_expect(bad(), 0)) {
        do {
          --tmo;
          asm volatile("s_sleep 2\n\tglobal_load_dwordx4 %0, %3, %4 sc1\n\tglobal_load_dwordx4 %1, %3, %4 offset:1024 sc1\n\t"
                       "global_load_dwordx4 %2, %3, %4 offset:2048 sc1\n\ts_waitcnt vmcnt(0)"
                       : "+v"(Hq[it][0]), "+v"(Hq[it][1]), "+v"(Hq[it][2]) : "v"(hoff[it]), "s"(hf) : "memory");
        } while (bad() && tmo > 0);
      }
      SB();
#pragma unroll
      for (int k = 0; k < 3; ++k)
#pragma unroll
        for (int j = 0; j < 5; ++j) {
          f32x4& ac = (it & 1) ? ac3[j] : ae[j];
          if (k == 0) MFMA0(ac, Wq[j], Hq[it][0]); else MFMA(ac, Wq[k * 5 + j], Hq[it][k]);
          if constexpr (it + 2 < 4) { if (k == 0 && j < 3) ldh1(it + 2 < 4 ? it + 2 : 0, j); }
          const int m = k * 5 + j - WO3;
          if (it > 0 && m >= 0 && m < 10 && !(m & 1)) put(it - 1, m >> 1);
          if (it > 0 && m == 8) signal1(flg + 160 + (it < 3 ? kh : 2) * 16 + ((cw + it) & 3) * 4);
          if (it == 3 && m == FE3) { flag_wait_asm(flg + 160 + wave * 4, 2u); fetch(0, kh, 0); fetch(1, kh, 1); }
          SB();
        }
      if constexpr (it == 3) {
        if (kh) {
          flag_wait_asm(flg + 192 + cw * 4, 3u);
          fetch(2, 1, 2); fetch(3, 0, 2); fetch(4, 0, 3);
        } else asm volatile("s_waitcnt lgkmcnt(0)\n\ts_nop 4" ::: "memory");
        pin(pa[0][0], pa[0][1], pa[0][2], pa[0][3], pa[0][4], pa[1][0], pa[1][1], pa[1][2], pa[1][3], pa[1][4]);
#pragma unroll
        for (int j = 0; j < 5; ++j) {
          pa[0][j] += pa[1][j];
          pin(pa[0][j], ac3[j]);
          ac3[j] += pa[0][j];
          if (!kh) put(3, j);
        }
        if (kh) {
          wait_lg<5>(pa[2][0], pa[2][1], pa[2][2], pa[2][3], pa[2][4], pa[3][0], pa[3][1], pa[3][2], pa[3][3], pa[3][4]);
#pragma unroll
          for (int j = 0; j < 5; ++j) ac3[j] += pa[2][j] + pa[3][j];
          wait_lg<0>(pa[4][0], pa[4][1], pa[4][2], pa[4][3], pa[4][4]);
#pragma unroll
          for (int j = 0; j < 5; ++j) ac3[j] += pa[4][j];
        } else signal1(flg + 192 + cw * 4);
      }
      SB();
      if constexpr (it == 0) AB(20);
      if constexpr (it == 2) AB(21);
    });
    AB(22);
  };
  if (wave >= 4) {
    const int wid = rb * 4 + cw;
    __builtin_amdgcn_s_setprio(3);
    ldsw();
    if (wid < 120) dma16((unsigned)((lane >> 1) * (D_ * 2) + (lane & 1) * 16), w_out + (size_t)(wid / 5) * 32 * D_ + d0 + (wid % 5) * 16, lb + L_WO + cw * 1024);
    SB();
    u32x4 hole = {HOLE, HOLE, HOLE, HOLE};
    bf16x8 XF[5][2][2];
    auto rdXF = [&](auto cc) {
      constexpr int c = decltype(cc)::value, so = L_SLOT + XSLOT[c] * 8192;
#pragma unroll
      for (int k = 0; k < 2; ++k) {
        const unsigned a = pX[k] + (so & ~0x7FFF);
        ds16<(so & 0x7FFF)>(XF[c][k][0], a);
        ds16<(so & 0x7FFF) + 512>(XF[c][k][1], a);
      }
      SB();
    };
    u32x4 TA[16];
    auto ldT = [&](u32x4& r, int d, int w, int i) {
      asm volatile("global_load_dwordx4 %0, %1, %2" : "=&v"(r) : "v"(aoff[i & 1] + (unsigned)(((d + rot2) & 7) * 128)), "s"(gS + (((w & 2) + 2 + (i >> 1)) & 3) * 65536 + w * 1024) : "memory");
    };
    auto ldx = [&](int c) {
      u32x4 xv;
      asm volatile("global_load_dwordx4 %0, %1, %2\n\ts_waitcnt vmcnt(0)" : "=&v"(xv) : "v"((unsigned)(lane * (D_ * 2) + c * 16)), "s"(x + (size_t)t0 * D_) : "memory");
      return xv;
    };
    const bool docv = cw < ((rb & 1) ? 2 : 3);
    const int d = d0 + (cw < 2 ? 2 * rb + cw : 64 + (rb >> 1));
    u32x4 hc[4], uc[4];
    const unsigned clin = docv ? lin : 0u;
    unsigned Dw;
    auto ldhc = [&]() {
#pragma unroll
      for (int k = 0; k < 4; ++k) asm volatile("global_load_dwordx4 %0, %1, %2 offset:%3" : "=&v"(hc[k]) : "v"(clin), "s"(ssm_k + (size_t)d * S_), "i"(k * 1024) : "memory");
      asm volatile("global_load_ushort %0, %1, %2" : "=&v"(Dw) : "v"(0u), "s"(ssm_D + d) : "memory");
    };
    unsigned rwo;
    f32x2 ra, rc;
    auto rdraw = [&]() { asm volatile("ds_read_b64 %0, %2\n\tds_read_b64 %1, %2 offset:512" : "=&v"(ra), "=&v"(rc) : "v"(rwo) : "memory"); SB(); };
    const char* efp = ws + WS_EF + cb * 256;
    char* cb_ = smem + M_CV + cw * CV_BUF;
    auto run = [&](auto ic) {
      constexpr int i = decltype(ic)::value, p = cnt('_', i) + 1;
      constexpr char c = SCH[i];
      if constexpr (c == '_') {
        CB(3 * (p - XBAR));
        asm volatile("s_waitcnt vmcnt(%0) lgkmcnt(0)" :: "n"(behind(1, i, '_')) : "memory");
        CB(3 * (p - XBAR) + 1);
        asm volatile("s_barrier" ::: "memory");
        CB(3 * (p - XBAR) + 2);
        SB();
#pragma unroll
        for (int q = 0; q < 5; ++q) if (XNEED[q] + 1 == p) pin(XF[q][0][0], XF[q][0][1], XF[q][1][0], XF[q][1][1]);
        if constexpr (p == 7) {
          rwo = lb + L_RAW + ((lane >> 3) * 4 + (lane & 3)) * 16 + (lane & 4) * 2;
          pin(rwo);
        }
        if constexpr (p == 9) __builtin_amdgcn_s_setprio(CPR);
        if constexpr (p == 2) AB(4);
        if constexpr (p == 6) AB(5);
        if constexpr (p == 9) AB(6);
      }
      if constexpr (c == 'u') issue(IC(cnt('u', i)));
      if constexpr (c == 'x') rdXF(IC(cnt('x', i)));
      if constexpr (c == 'B') {
        AB(1);
        barrier0(behind(1, i, 'B'));
        pin(ones, hole);
      }
      if constexpr (c == 'v') sfor<2 * cnt('v', i), 2 * cnt('v', i) + 2>([&](auto nc) { rdV(IC(0), nc, 0); rdV(IC(0), nc, 1); rdV(IC(0), nc, 2); rdV(IC(0), nc, 3); });
      if constexpr (c == 'm') {
        constexpr int n = cnt('m', i);
        mm2(IC(0), IC(n), XF, VNEED[n >> 1] < p - 1 || (n & 1) ? -1 : 0, [](int) {});
      }
      if constexpr (c == 'p') {
        asm volatile("s_nop 7" : "+v"(ac2[0][0]), "+v"(ac2[0][1]), "+v"(ac2[1][0]), "+v"(ac2[1][1]));
        dsw16<0>(p2a, ac2[0][0]); dsw16<1024>(p2a, ac2[1][0]); dsw16<2048>(p2a, ac2[0][1]); dsw16<3072>(p2a, ac2[1][1]);
      }
      if constexpr (c == 'w')
        touch((unsigned)(min(lane, 19) * (F2_ * 2)), (const char*)w_in + (size_t)(((rb + WSH) & 7) * 80 + cw * 20) * (F2_ * 2) + pr * 384 + odd * 256, lb + L_DUM);
      if constexpr (c == 'l') { if (wave == 7) kvld(); }
      if constexpr (c == 'k') { if (wave == 7) { kvm(0, 4); kvst(); } }
      if constexpr (c == 'E' || c == 'D') { if (wave == 5) sttag(ws + WS_EF + (c == 'D' ? 2048 : 0) + (cb * 32 + rb) * 8); }
      if constexpr (c == 'U') {
        wait_vm<behind(1, i, 'F')>(ef);
        waittag(ef, (unsigned)((lane & 31) * 8), efp);
#pragma unroll
        for (int k = 0; k < 4; ++k) asm volatile("global_load_dwordx4 %0, %1, %2 offset:%3 sc1" : "=&v"(uc[k]) : "v"(clin), "s"(ws + WS_KV + (size_t)d * (S_ * 2)), "i"(k * 1024) : "memory");
      }
      if constexpr (c == 'F') ldtag(ef, (unsigned)((lane & 31) * 8), efp);
      if constexpr (c == 'o') {
        if (wid < 120) {
          const unsigned ra = lb + L_WO + cw * 1024 + (8 * g4 + (l15 >> 2)) * 32 + 8 * (lane & 3);
          s16x4 lo = tr_asm<0>(ra), hi = tr_asm<128>(ra);
          wait_lg<0>(lo, hi);
          st16(lin, ws + WS_WO + (size_t)((wid / 5) * 40 + cb * 5 + wid % 5) * 1024, __builtin_bit_cast(u32x4, join8(lo, hi)));
        }
      }
      if constexpr (c == 'h') {
        constexpr int n = cnt('h', i);
        st16(lin, ws + (n < 3 ? WS_Z2 + (size_t)((rb * 2 + wl) * 48 + cb * 6 + wm * 3 + n) * 1024 : WS_H + (size_t)((rb * 4 + cw) * 24 + cb * 3 + n - 3) * 1024), hole);
      }
      if constexpr (c == 't') { constexpr int n = cnt('t', i); ldT(TA[n], 4 + (n >> 3), cw, n & 7); }
      if constexpr (c == 'H') ldhc();
      if constexpr (c == 'R') rdraw();
      if constexpr (c == 'V') {
        wait_vm<behind(1, i, '_')>();
        signal1(flg + 60);
        AB(2);
      }
      if constexpr (c == 'I') {
        wait_vm<behind(1, i, 'H')>(hc[0], hc[1], hc[2], hc[3], Dw);
        if (docv) {
          if (lane < 36) *(u32x4*)(cb_ + (lane < 32 ? lane * 16 : CV_C + ((lane >> 1) & 1) * CV_CS + 4096 + (lane & 1) * 16)) = (u32x4){0u, 0u, 0u, 0u};
#pragma unroll
          for (int k = 0; k < 4; ++k) {
            const int a = k * 64 + lane;
            const int h0 = k ? __builtin_amdgcn_readlane((int)hc[k ? k - 1 : 0][3], 63) : 0;
            const unsigned hp = (unsigned)__builtin_amdgcn_update_dpp(h0, (int)hc[k][3], 0x138, 0xF, 0xF, false);
            const unsigned w[5] = {hp, hc[k][0], hc[k][1], hc[k][2], hc[k][3]};
            u32x4 sw, mx;
#pragma unroll
            for (int q = 0; q < 4; ++q) {
              sw[q] = (w[4 - q] >> 16) | (w[4 - q] << 16);
              mx[q] = (w[4 - q] & 0xFFFFu) | (w[3 - q] & 0xFFFF0000u);
            }
            char* cp = cb_ + CV_C + (2040 - 8 * a) * 2;
            *(u32x4*)(cp) = sw;
            *(u32x4*)(cp + CV_CS) = mx;
          }
        }
      }
      if constexpr (c == 'J') {
        wait_vm<behind(1, i, 'U')>(uc[0], uc[1], uc[2], uc[3]);
        if (docv) {
#pragma unroll
          for (int k = 0; k < 4; ++k) *(u32x4*)(cb_ + 512 + (k * 64 + lane) * 16) = uc[k];
        }
      }
      if constexpr (c == 'S') {
        wait_lg<0>(ra, rc);
        if (wave == 6) {
          const float s1 = nv(ra[0]) + rc[0], fm = s1 * (1.0f / 640.0f), sm = s1 * fm;
          float fq = (ra[1] - sm) + rc[1];
          if (__builtin_expect(__builtin_amdgcn_ballot_w64(!(fq >= 1e-3f * sm)) != 0, 0)) {
            const f32x2 mm = {fm, fm};
            f32x2 q2 = {0.f, 0.f};
#pragma unroll 1
            for (int c = 0; c < 80; ++c) {
              const u32x4 xv = ldx(c);
#pragma unroll
              for (int e = 0; e < 4; ++e) { const f32x2 d = unpk(xv[e]) - mm; q2 = __builtin_elementwise_fma(d, d, q2); }
            }
            fq = q2[0] + q2[1];
          }
          const float vbf = rbf(fmaxf(fq, 0.f) * (1.0f / 640.0f));
          *(float*)(smem + L_XM + lane * 4) = rbf(fm);
          *(float*)(smem + L_XR + lane * 4) = __builtin_amdgcn_rcpf(rbf(__builtin_amdgcn_sqrtf(rbf(vbf + 1e-5f))));
        }
      }
      SB();
    };
    sfor<0, NSCH>(run);
    __builtin_amdgcn_s_setprio(0);
    AB(9);
    auto ldS = [&](auto dc, auto wc) {
      constexpr int dd = decltype(dc)::value, w = decltype(wc)::value;
      const unsigned to = (unsigned)(((dd + rot2) & 7) * 128);
      if (THR < 60) wait_vm<THR>();
      dma32s<M_RING + w * 24576 + (2 - dd) * 8192, (((w & 2) + 2) & 3) * 65536 + w * 1024>(aoff[0] + to, aoff[1] - 1024 + to, gS, lb);
      if (THR < 60) wait_vm<THR>();
      dma32s<M_RING + w * 24576 + (2 - dd) * 8192 + 2048, (((w & 2) + 3) & 3) * 65536 + w * 1024>(aoff[0] + to, aoff[1] - 1024 + to, gS, lb);
      if (THR < 60) wait_vm<THR>();
      dma32s<M_RING + w * 24576 + (2 - dd) * 8192 + 4096, (w & 2) * 65536 + w * 1024>(aoff[0] + to, aoff[1] - 1024 + to, gS, lb);
      if (THR < 60) wait_vm<THR>();
      dma32s<M_RING + w * 24576 + (2 - dd) * 8192 + 6144, ((w & 2) + 1) * 65536 + w * 1024>(aoff[0] + to, aoff[1] - 1024 + to, gS, lb);
      SB();
    };
    auto epi2 = [&]() {
      flag_wait_asm<SLP>(flg + 132, 4u);
#pragma unroll
      for (int jj = 0; jj < 2; ++jj) {
        u32x4* zp = z1at(odd * 32 + wl * 32 + jj * 16);
        u32x4 o = *zp;
#pragma unroll
        for (int q = 0; q < 4; ++q) o[q] = pack2s(gelu1(nv(lo16(o[q]))), gelu1(hi16(o[q])));
        *zp = o;
      }
      signal1(flg + 56);
      SB();
    };
    auto putT = [&](const u32x4* T, unsigned a, unsigned f) {
#pragma unroll
      for (int i = 0; i < 8; ++i) asm volatile("ds_write_b128 %0, %1 offset:%2" :: "v"(a), "v"(T[i]), "i"(i * 1024) : "memory");
      signal1(f);
    };
    const unsigned tx = lb + M_CV + cw * CV_BUF + lin;
    auto landT = [&]() {
      sttag(ws + WS_LF + (rb * 8 + cb) * 32 + cw * 8);
      putT(TA, tx, flg + 112 + wl * 4);
    };
    auto putT4 = [&]() {
      flag_wait2(flg + 96 + wl * 8, 1u);
      putT(TA + 8, lb + M_RING + cw * 24576 + 8192 + lin, flg + 120 + wl * 4);
    };
    if (docv) {
      flag_wait_asm(flg + 36, 4u);
      AB(12);
      f32x4 ac[8];
      conv_core(lds_addr(cb_), lane, Dw, ac, flg + 28);
      AB(13);
      signal1(flg + 40);
      wait_vm<0>();
      landT();
#pragma unroll
      for (int k = 0; k < 4; ++k) {
        u32x2 ov[2];
#pragma unroll
        for (int t = 0; t < 2; ++t)
#pragma unroll
          for (int h = 0; h < 2; ++h) ov[t][h] = pack2(ac[2 * k + t][2 * h], ac[2 * k + t][2 * h + 1]);
        const auto s0 = __builtin_amdgcn_permlane16_swap(ov[0][0], ov[1][0], false, false);
        const auto s1 = __builtin_amdgcn_permlane16_swap(ov[0][1], ov[1][1], false, false);
        st16((unsigned)((256 * (g4 & 1) + 16 * l15 + 8 * (g4 >> 1)) * 2), ws + WS_Y + (size_t)d * (S_ * 2) + k * 1024, (u32x4){s0[0], s1[0], s0[1], s1[1]});
      }
      wait_vm<0>();
      sttag(ws + WS_YF + d * 8);
      epi2();
      putT4();
    } else if (cw != 3) {
      wait_vm<0>();
      landT();
      epi2();
      putT4();
    } else {
      ldS(IC(0), IC(0)); ldS(IC(0), IC(1)); ldS(IC(0), IC(2)); ldS(IC(1), IC(1)); ldS(IC(2), IC(1));
      wait_vm<40>();
      landT();
      AB(12);
      flag_wait_asm(flg + 16, 4u);
      AB(8);
      wait_vm<24>();
      ldS(IC(0), IC(3)); ldS(IC(1), IC(0)); ldS(IC(1), IC(2)); ldS(IC(1), IC(3));
      wait_vm<24>();
      signal1(flg + 32);
      ldS(IC(2), IC(0)); ldS(IC(2), IC(2)); ldS(IC(2), IC(3));
      ldtag(ef, (unsigned)((lane & 1) * 8), ws + WS_XF + (rb * 4 + pr) * 16);
      AB(10);
      u32x4 TB[32];
      auto ring2 = [&]() { dsw4(flg + 224 + (lane & 1) * 4, __uint_as_float(2u)); };
#pragma unroll
      for (int n = 0; n < 32; ++n) {
        if (n == 8) wait_vm<32>();
        if (THR < 60 && !(n & 1)) wait_vm<THR>();
        if (THR < 32 && n == THR) ring2();
        ldT(TB[n], 6, n >> 3, n & 7);
      }
      wait_vm<32>(ef);
      if (THR >= 32) ring2();
      AB(11);
      if (!odd) {
        u32x4 zx[4];
        waittag(ef, (unsigned)((lane & 1) * 8), ws + WS_XF + (rb * 4 + pr) * 16);
#pragma unroll
        for (int c = 0; c < 4; ++c)
          asm volatile("global_load_dwordx4 %0, %1, %2 offset:%3 sc1" : "=&v"(zx[c]) : "v"(lin), "s"(ws + WS_ZX + (size_t)(rb * 4 + pr) * 4096), "i"(c * 1024) : "memory");
        wait_vm<0>(zx[0], zx[1], zx[2], zx[3]);
#pragma unroll
        for (int c = 0; c < 4; ++c) *(u32x4*)(smem + L_Z1 + (64 + (c & 1) * 16 + l15) * 128 + ((((c >> 1) * 4 + g4) ^ (l15 & 7)) << 4)) = zx[c];
      }
      wait_vm<0>();
      epi2();
      signal1(flg + 56);
      putT4();
#pragma unroll
      for (int w = 0; w < 4; ++w) {
        if (!(w & 1)) flag_wait2(flg + 96 + w * 4, 2u);
        putT(TB + 8 * w, lb + M_RING + w * 24576 + lin, flg + 144 + (w >> 1) * 4);
      }
      AB(13);
    }
    SB();
    wait_vm<0>();
    AB(14);
    u32x2 xv[5];
    f32x2 sv[5][2];
    flag_wait_asm<SLP>(flg + 40, (rb & 1) ? 2u : 3u);
    const int yc = min(cw * 24, 56);
    const unsigned yfo = (unsigned)(min(lane, 23) * 8);
    const char* yfp = ws + WS_YF + (d0 + yc) * 8;
    auto ldy = [&]() {
#pragma unroll
      for (int n = 0; n < 3; ++n) {
        const int q = (yc >> 3) + n;
        asm volatile("s_mov_b32 m0, %2\n\ts_nop 0\n\tglobal_load_lds_dwordx4 %0, %1 sc1" :: "v"((unsigned)(r8 * (S_ * 2) + ((c8 ^ (r8 & 6)) << 4))),
                     "s"(ws + WS_Y + ((size_t)(d0 + q * 8) * S_ + t0) * 2), "s"(lb + yat(0) + (q >> 2) * CV_BUF + (q & 3) * 1024) : "memory");
      }
    };
    unsigned xo[3];
#pragma unroll
    for (int n = 0; n < 3; ++n) { const int id = min(n * 64 + lane, 159); xo[n] = opq((unsigned)((id / 10) * (D_ * 2) + (id % 10) * 16)); }
    flag_wait2(flg + 96 + wl * 8, 3u);
    {
      const unsigned to = (unsigned)(((7 + rot2) & 7) * 128);
      for (int w = cw; w < (cw < 2 ? cw + 1 : 4 - (cw & 1)); ++w)
        sfor<0, 4>([&](auto ic) {
          constexpr int i = decltype(ic)::value;
          dma32s<M_RING + 16384 + i * 2048, 0>(aoff[0] + to, aoff[1] - 1024 + to, gS + ((2 * wl + 2 + i) & 3) * 65536 + w * 1024, lb + w * 24576);
          if (T7D) __builtin_amdgcn_s_sleep(T7D);
        });
    }
    flag_wait2(flg + 96 + wl * 8, 4u);
    ldtag(ef, yfo, yfp);
    wait_vm<1>();
    signal1(flg + 152 + wl * 4, cw < 2 ? 1u : 2u - 2u * (cw & 1));
    wait_vm<0>(ef);
    flag_wait_asm<SLP / 2 + 1>(flg + 44, 4u);
    waittag(ef, yfo, yfp);
    AB(15);
    ldy();
    static_assert(SGS >= 10, "");
#pragma unroll
    for (int n = 0; n < 3; ++n) dma16(xo[n], x + (size_t)(t0 + cw * 16) * D_ + d0, lb + M_RING + cw * 24576 + 8192 + n * 1024);
    ldW(0, NWY);
    wait_vm<NWY>(ef);
    AB(17);
    signal1(flg + 52);
    ldW(NWY, 15);
    gemm3(IC(1), [&]() {
      flag_wait_asm(flg + 52, 4u);
      const int yr_ = 4 * g4 + (l15 >> 2);
      const unsigned ya = lb + yr_ * 128 + (((cw * 2 + ((lane >> 1) & 1)) ^ (yr_ & 6)) << 4) + (lane & 1) * 8;
      const float nm = -*(const float*)(smem + L_XM + (cw * 16 + l15) * 4), sr = *(const float*)(smem + L_XR + (cw * 16 + l15) * 4);
      s16x4 yr[5] = {tr_asm<yat(0)>(ya), tr_asm<yat(1)>(ya), tr_asm<yat(2)>(ya), tr_asm<yat(3)>(ya), tr_asm<yat(4)>(ya)};
#pragma unroll
      for (int j = 0; j < 5; ++j) asm volatile("ds_read_b64 %0, %1 offset:%2" : "=&v"(xv[j]) : "v"(lb + M_RING + cw * 24576 + 8192 + l15 * 160 + g4 * 8), "i"(j * 32) : "memory");
      signal1(flg + 64);
      wait_lg<0>(xv[0], xv[1], xv[2], xv[3], xv[4], yr[0], yr[1], yr[2], yr[3], yr[4]);
#pragma unroll
      for (int j = 0; j < 5; ++j) {
        const u32x2 yw = __builtin_bit_cast(u32x2, yr[j]);
#pragma unroll
        for (int h = 0; h < 2; ++h) {
          sv[j][h] = ((unpk(xv[j][h]) + (f32x2){nm, nm}) * (f32x2){sr, sr}) * unpk(yw[h]);
          pin(sv[j][h]);
        }
      }
      SB();
      AB(16);
    });
    unsigned pk[5][2];
#pragma unroll
    for (int j = 0; j < 5; ++j)
#pragma unroll
      for (int h = 0; h < 2; ++h) {
        const f32x2 o2 = {ac3[j][2 * h], ac3[j][2 * h + 1]};
        const f32x2 a = o2 * (f32x2){-1.4426950f, -1.4426950f};
        const f32x2 e = (f32x2){__builtin_amdgcn_exp2f(a[0]), __builtin_amdgcn_exp2f(a[1])} + (f32x2){1.0f, 1.0f};
        const f32x2 r = {__builtin_amdgcn_rcpf(e[0]), __builtin_amdgcn_rcpf(e[1])};
        pk[j][h] = pk2(__builtin_elementwise_fma(o2 * r, sv[j][h], unpk(xv[j][h])));
      }
    const u16* op = out + (size_t)(t0 + cw * 16) * D_ + d0;
#pragma unroll
    for (int q = 0; q < 2; ++q) {
      const auto s0 = __builtin_amdgcn_permlane16_swap(pk[2 * q][0], pk[2 * q + 1][0], false, false);
      const auto s1 = __builtin_amdgcn_permlane16_swap(pk[2 * q][1], pk[2 * q + 1][1], false, false);
      asm volatile("global_store_dwordx4 %0, %1, %2 offset:%3 " OSC "\n\ts_nop 1" :: "v"((unsigned)(l15 * D_ * 2 + 32 * (g4 & 1) + 16 * (g4 >> 1))), "v"((u32x4){s0[0], s1[0], s0[1], s1[1]}),
                   "s"(op), "i"(64 * q) : "memory");
    }
    asm volatile("global_store_dwordx2 %0, %1, %2 offset:128 " OSC :: "v"((unsigned)(l15 * D_ * 2 + 8 * g4)), "v"((u32x2){pk[4][0], pk[4][1]}), "s"(op) : "memory");
  } else {
    ldsw();
    sfor<0, pos('B')>([&](auto ic) { if constexpr (SCH[ic] == 'g') issue(IC(cnt('u', ic))); });
    if (tid < 64) *(unsigned*)(smem + L_FLAG + tid * 4) = 0u;
    pin(ones);
    AB(1);
    barrier0(behind(0, pos('B'), 'B'));
    kvld();
    kvm(0, 4);
    kvst();
    const bool isz2 = !(odd && wl);
    const char* stp = ws + WS_ST + (size_t)((rb * 12 + pr * 3 + odd * 2 + wl) * 1024 + wm * 512);
    if (STW && isz2) st8(lin >> 1, stp, (u32x2){0u, ~(unsigned)tag});
    const char* Ab[4];
#pragma unroll
    for (int i = 0; i < 4; ++i) Ab[i] = (const char*)w_sp + (size_t)(t0 + ((2 * wl + 2 + i) & 3) * 16) * (S_ * 2) + wave * 1024 + 4096 - i * 2048;
    const unsigned ringl = lb + M_RING + wave * 24576;
    unsigned av[2], xl = ringl + 16384 + 4096;
    unsigned bsv[2];
    {
      f32x4 zv[2][4], cs[4];
      s16x4 WB[20][4][2];
      bf16x8 XG[10][2][2];
      unsigned aX[2][2], aW[2][2][4];
#pragma unroll
      for (int k = 0; k < 2; ++k)
#pragma unroll
        for (int w = 0; w < 2; ++w) {
          aX[w][k] = pX[k] + L_SLOT + XW * 8192 + w * 65536;
          pin(aX[w][k]);
#pragma unroll
          for (int jj = 0; jj < 4; ++jj) { aW[w][k][jj] = (pW[k] ^ (unsigned)(jj * 32)) + L_SLOT + wl * 8192 + w * 65536; pin(aW[w][k][jj]); }
        }
      auto rd1 = [&](auto nc, int r) {
        constexpr int n = decltype(nc)::value, j = n >> 1, k = n & 1, c = n > 0 && n < 11 ? n - 1 : 0, sx = (XSLOT[c] - XW) * 8192, sa = WSLOT[j] * 8192;
        if (r < 0) { if (r & 1) ds16<(sx & 0xFFFF) + 512>(XG[c][(r + 4) >> 1][1], aX[sx >> 16][(r + 4) >> 1]); else ds16<(sx & 0xFFFF)>(XG[c][(r + 4) >> 1][0], aX[sx >> 16][(r + 4) >> 1]); }
        else if (r & 1) WB[n][r >> 1][1] = tr_asm<(sa & 0xFFFF) + 512>(aW[sa >> 16][k][r >> 1]);
        else WB[n][r >> 1][0] = tr_asm<(sa & 0xFFFF)>(aW[sa >> 16][k][r >> 1]);
        SB();
      };
      auto bar = [&](auto bc) {
        SB();
        constexpr int p = decltype(bc)::value;
        CB(3 * (p - XBAR));
        if constexpr (p == 1) asm volatile("s_waitcnt vmcnt(%0) lgkmcnt(0)\n\ts_barrier" :: "n"(behind(0, pos('_', p - 1), '_')) : "memory");
        else if constexpr (p <= GZ) asm volatile("s_waitcnt vmcnt(%0) lgkmcnt(8)\n\ts_barrier" :: "n"(behind(0, pos('_', p - 1), '_')) : "memory");
        else if constexpr (p >= 8) asm volatile("s_waitcnt lgkmcnt(0)\n\ts_barrier" ::: "memory");
        else if constexpr (p < 7) asm volatile("s_waitcnt lgkmcnt(8)\n\ts_barrier" ::: "memory");
        else asm volatile("s_barrier" ::: "memory");
        CB(3 * (p - XBAR) + 2);
        SB();
      };
      float cp[4];
      f32x4 st[2][2], rv;
      const unsigned rwa = lb + L_RAW + (ksb * 2 + wm) * 256 + l15 * 16 + ((l15 >> 2) == g4 ? 0 : 1024);
      const unsigned csa = lb + L_CS + (wave * 64 + lane) * 4;
      auto kstep = [&](auto nc) {
        constexpr int n = decltype(nc)::value, j = n >> 1, k = n & 1, nn = n + 1, r0 = nn >= 2 && nn <= 10 ? -4 : 0;
        constexpr int sc = n == 1 ? 0 : n - 2, ns = !k ? 0 : (n == 1 || n == 11 ? 1 : 2);
        if constexpr (k) bar(IC(nn / 2 + 1));
        int r = r0, c = 0;
        auto nxt = [&]() { if (r < 8) { rd1(IC(nn), r++); ++c; } };
        auto sel = [&](int q) {
          const f32x4& sq = st[q & 1][q >> 1];
          rv[q] = sel4(sq[0], sq[1], sq[2], sq[3], 0xAAAAAAAAAAAAAAAAull, 0xCCCCCCCCCCCCCCCCull);
          SB();
        };
#pragma unroll
        for (int jj = 0; jj < 4; ++jj) {
          const bf16x8 wf = join8(WB[n][jj][0], WB[n][jj][1]);
          MFMAW<n == 0>(jj == 0 ? 4 : (jj == 2 ? c : -1), zv[0][jj], XG[j][k][0], wf);
          SB();
          nxt();
          if (n == 12) nxt();
          if (n == 12 && jj == 2) sel(2);
          MFMAW<n == 0>(-1, zv[1][jj], XG[j][k][1], wf);
          SB();
          nxt();
          if (n == 12) nxt();
          if (n == 12 && (jj == 1 || jj == 2)) sel(jj == 1 ? 0 : 3);
          if (n == 12 && jj == 2) { dsw16<0>(rwa, rv); SB(); }
          if (k == 0) {
            MFMAW<n == 0>(-1, cs[jj], ones, wf);
            SB();
            nxt();
            if (n == 12) nxt();
            if (n == 12 && jj == 1) sel(1);
          }
#pragma unroll
          for (int q = 0; q < ns; ++q) {
            const bf16x8& xs = XG[sc + q][1][jj >> 1];
            MFMAW<n == 1>(-1, st[jj & 1][jj >> 1], xs, (jj & 1) ? xs : ones);
            SB();
            nxt();
          }
        }
      };
      auto lstep = [&](auto nc, auto ac, auto bc, int w) {
        constexpr int n = decltype(nc)::value, j = n >> 1, k = n & 1, na = decltype(ac)::value, nb = decltype(bc)::value;
        int r = 0;
        auto nxt = [&]() { if (na > 0 && r < 16) { if (r < 8) rd1(ac, r); else rd1(bc, r - 8); ++r; } };
#pragma unroll
        for (int jj = 0; jj < 4; ++jj) {
          const bf16x8 wf = join8(WB[n][jj][0], WB[n][jj][1]);
          if (k == 0) { MFMAW<false>(jj == 0 ? w : -1, cs[jj], ones, wf); SB(); nxt(); nxt(); }
          MFMAW<false>(jj == 0 && k ? w : -1, zv[0][jj], XG[j][k][0], wf);
          SB();
          nxt();
          if (k) nxt();
          MFMAW<false>(-1, zv[1][jj], XG[j][k][1], wf);
          SB();
          nxt();
          if (k) nxt();
          if (n == 15 && jj == 2) {
            dsw4(csa, sel4(cs[0][0], cs[1][0], cs[2][0], cs[3][0], 0xFFFF0000FFFF0000ull, 0xFFFFFFFF00000000ull));
            SB();
          }
        }
      };
      bar(IC(1));
      AB(2);
#pragma unroll
      for (int r = -4; r < 8; ++r) rd1(IC(0), r);
      kstep(IC(0)); AB(4); kstep(IC(1)); kstep(IC(2)); kstep(IC(3)); kstep(IC(4)); kstep(IC(5)); kstep(IC(6)); kstep(IC(7)); kstep(IC(8)); AB(5); kstep(IC(9));
      kstep(IC(10)); kstep(IC(11)); kstep(IC(12));
      bar(IC(8));
      lstep(IC(13), IC(14), IC(16), -1);
      lstep(IC(14), IC(18), IC(15), 8);
      lstep(IC(16), IC(17), IC(19), 15);
      lstep(IC(18), IC(0), IC(0), 15);
      lstep(IC(15), IC(0), IC(0), -1);
      lstep(IC(17), IC(0), IC(0), 9);
      bar(IC(9));
      __builtin_amdgcn_s_setprio(2);
#pragma unroll
      for (int q = 0; q < 4; ++q) cp[q] = *(const float*)(smem + L_CS + ((wave ^ 1) * 64 + q * 16 + l15) * 4);
      ldxs();
      SB();
      lstep(IC(19), IC(0), IC(0), -1);
      AB(6);
      float ct[4];
#pragma unroll
      for (int jj = 0; jj < 4; ++jj) ct[jj] = cs[jj][0] + cp[jj];
#pragma unroll
      for (int jj = 0; jj < 4; ++jj) {
        if (jj == 0) asm volatile("s_nop 3");
        if (jj == 2) SB();
        if (!(jj & 1)) pin(zv[0][jj], zv[1][jj], zv[0][jj + 1], zv[1][jj + 1]);
#pragma unroll
        for (int i = 0; i < 2; ++i) zv[i][jj] = act(zv[i][jj], ct[jj], i);
      }
      if (isz2) {
        f32x4 pv[2], s1[2], s2[2];
#pragma unroll
        for (int i = 0; i < 2; ++i) {
#pragma unroll
          for (int e = 0; e < 4; ++e) {
            const float z0 = zv[i][0][e];
            pv[i][e] = __int_as_float(__builtin_amdgcn_update_dpp(0, __float_as_int(z0), 0x150, 0xF, 0xF, true));
          }
          const f32x4 a0 = zv[i][0] - pv[i], a1 = zv[i][1] - pv[i], a2 = zv[i][2] - pv[i], a3 = zv[i][3] - pv[i];
          s1[i] = (a0 + a1) + (a2 + a3);
          s2[i] = __builtin_elementwise_fma(a3, a3, __builtin_elementwise_fma(a2, a2, __builtin_elementwise_fma(a1, a1, a0 * a0)));
        }
        float r[8], q[4];
#define DA(d, s, ctl, bm) "v_add_f32_dpp " d ", " s ", " s " " ctl " row_mask:0xf bank_mask:" bm "\n\t"
        asm volatile("s_nop 1\n\t"
                     DA("%0", "%12", "row_ror:8", "0x3") DA("%1", "%13", "row_ror:8", "0x3") DA("%2", "%14", "row_ror:8", "0x3") DA("%3", "%15", "row_ror:8", "0x3")
                     DA("%4", "%16", "row_ror:8", "0x3") DA("%5", "%17", "row_ror:8", "0x3") DA("%6", "%18", "row_ror:8", "0x3") DA("%7", "%19", "row_ror:8", "0x3")
                     DA("%0", "%20", "row_ror:8", "0xc") DA("%1", "%21", "row_ror:8", "0xc") DA("%2", "%22", "row_ror:8", "0xc") DA("%3", "%23", "row_ror:8", "0xc")
                     DA("%4", "%24", "row_ror:8", "0xc") DA("%5", "%25", "row_ror:8", "0xc") DA("%6", "%26", "row_ror:8", "0xc") DA("%7", "%27", "row_ror:8", "0xc")
                     DA("%8", "%0", "row_half_mirror", "0x5") DA("%9", "%1", "row_half_mirror", "0x5") DA("%10", "%2", "row_half_mirror", "0x5") DA("%11", "%3", "row_half_mirror", "0x5")
                     DA("%8", "%4", "row_half_mirror", "0xa") DA("%9", "%5", "row_half_mirror", "0xa") DA("%10", "%6", "row_half_mirror", "0xa") DA("%11", "%7", "row_half_mirror", "0xa")
                     DA("%8", "%8", "quad_perm:[1,0,3,2]", "0xf") DA("%9", "%9", "quad_perm:[1,0,3,2]", "0xf") DA("%10", "%10", "quad_perm:[1,0,3,2]", "0xf") DA("%11", "%11", "quad_perm:[1,0,3,2]", "0xf")
                     DA("%8", "%8", "quad_perm:[2,3,0,1]", "0xf") DA("%9", "%9", "quad_perm:[2,3,0,1]", "0xf") DA("%10", "%10", "quad_perm:[2,3,0,1]", "0xf") DA("%11", "%11", "quad_perm:[2,3,0,1]", "0xf")
                     : "=&v"(r[0]), "=&v"(r[1]), "=&v"(r[2]), "=&v"(r[3]), "=&v"(r[4]), "=&v"(r[5]), "=&v"(r[6]), "=&v"(r[7]),
                       "=&v"(q[0]), "=&v"(q[1]), "=&v"(q[2]), "=&v"(q[3])
                     : "v"(s1[0][0]), "v"(s2[0][0]), "v"(s1[0][1]), "v"(s2[0][1]), "v"(s1[0][2]), "v"(s2[0][2]), "v"(s1[0][3]), "v"(s2[0][3]),
                       "v"(s1[1][0]), "v"(s2[1][0]), "v"(s1[1][1]), "v"(s2[1][1]), "v"(s1[1][2]), "v"(s2[1][2]), "v"(s1[1][3]), "v"(s2[1][3]));
#undef DA
        const f32x4 p4 = (lane & 8) ? pv[1] : pv[0];
        const float pa = (lane & 4) ? p4[2] : p4[0], pb = (lane & 4) ? p4[3] : p4[1];
        const float pp = (lane & 2) ? pb : pa, S1 = (lane & 2) ? q[2] : q[0], S2 = (lane & 2) ? q[3] : q[1];
        const float dl = S1 * (1.0f / 64.0f);
        st8(lin >> 1, stp, (u32x2){__float_as_uint((lane & 1) ? fmaxf(S2 - S1 * dl, 0.f) : pp + dl), (unsigned)tag});
      } else {
#pragma unroll
        for (int jj = 0; jj < 4; ++jj) {
          u32x4 o = pack8(zv[0][jj], zv[1][jj]);
          if (jj >= 2) z1put(jj * 16 - 32, o);
          else st16(lin, ws + WS_ZX + (size_t)((rb * 4 + pr) * 4 + wm * 2 + jj) * 1024, o);
        }
      }
      signal1(flg + 36);
      SB();
      AB(7);
      __builtin_amdgcn_s_setprio(P2P);
      unsigned po = isz2 ? (unsigned)((lane >> 5) * 6144 + (lane & 31) * 16) : 0u;
      const char* pb = ws + WS_ST + (size_t)rb * 12288 + wm * 512 + 2048;
      u32x4 sg[6];
      auto ldsg = [&]() {
#pragma unroll
        for (int c = 0; c < 6; ++c) asm volatile("global_load_dwordx4 %0, %1, %2 offset:%3 sc1" : "=&v"(sg[c]) : "v"(po), "s"(pb), "i"(c * 1024 - 2048) : "memory");
      };
      auto look = [&]() {
        wait_vm<0>();
        ldsg();
        asm volatile("global_load_ushort %0, %2, %3\n\tglobal_load_ushort %1, %2, %3 offset:32" : "=&v"(bsv[0]), "=&v"(bsv[1]) : "v"((unsigned)(l15 * 2)), "s"(b_sp + t0 + wl * 32) : "memory");
        if (!isz2) sttag(ws + WS_XF + (rb * 4 + pr) * 16 + wm * 8);
        SB();
      };
      flag_wait_asm(flg + 60, 4u);
      ds16<0>(ac2[0][0], p2a); ds16<1024>(ac2[1][0], p2a); ds16<2048>(ac2[0][1], p2a); ds16<3072>(ac2[1][1], p2a);
      sfor<0, 4>([&](auto nc) { rdV(IC(1), nc, 0); rdV(IC(1), nc, 1); rdV(IC(1), nc, 2); rdV(IC(1), nc, 3); });
      sfor<0, 10>([&](auto nc) {
        constexpr int n = decltype(nc)::value;
        mm2(IC(1), nc, XG + 5, n == 0 ? 12 : (n < 6 ? 15 : 4 * (9 - n)), [&](int r) {
          SB();
          if constexpr (n < 3) { if (r < 2) { rdV(IC(1), IC(4 + 2 * n), 2 * r); rdV(IC(1), IC(4 + 2 * n), 2 * r + 1); } else { rdV(IC(1), IC(5 + 2 * n), 2 * r - 4); rdV(IC(1), IC(5 + 2 * n), 2 * r - 3); } }
        });
        if constexpr (n == 2) signal1(flg + 16);
        if constexpr (n == PN) look();
      });
      signal1(flg + 20 + wl * 4);
      AB(3);
      if (PN > 9) look();
      AB(8);
      wait_vm<2>(sg[0], sg[1], sg[2], sg[3], sg[4], sg[5]);
      if (isz2) {
        const unsigned zw = lb + L_ZS + wave * 256 + lane * 4, zr_ = lb + L_ZS + wave * 256 + g4 * 32;
        const char* zb = ws + WS_Z2 + (size_t)((rb * 2 + wm) * 48 + pr * 12 + odd * 8 + wl * 4) * 1024;
        auto bad = [&]() {
          unsigned an = sg[0][1] & sg[0][3], on = sg[0][1] | sg[0][3];
#pragma unroll
          for (int c = 1; c < 6; ++c) { an &= sg[c][1] & sg[c][3]; on |= sg[c][1] | sg[c][3]; }
          return __builtin_amdgcn_ballot_w64(((an ^ (unsigned)tag) | (on ^ (unsigned)tag)) != 0) != 0;
        };
        if (__builtin_expect(bad(), 0)) {
          do {
            --tmo;
            reld6(sg, po, pb);
            wait_vm<0>(sg[0], sg[1], sg[2], sg[3], sg[4], sg[5]);
          } while (bad() && tmo > 0);
        }
        AB(9);
        auto f = [&](int c, int e) { return __uint_as_float(sg[c][e]); };
        auto sum6 = [&](int e) { return add1(add1(add1(f(0, e), f(1, e)), add1(f(2, e), f(3, e))), add1(f(4, e), f(5, e))); };
        const float nmh = sum6(0) * (-1.0f / 6.0f), mh = -nmh;
        float qa = sum6(2) * (1.0f / 64.0f), qb;
#pragma unroll
        for (int c = 0; c < 6; c += 2) { const float u = add1(f(c, 0), nmh), v = add1(f(c + 1, 0), nmh); qa = fma1(u, u, qa); qb = c ? fma1(v, v, qb) : v * v; }
        const float Mh = add1(qa, qb);
        const auto sm = __builtin_amdgcn_permlane32_swap(__float_as_uint(mh), __float_as_uint(mh), false, false);
        const auto sM = __builtin_amdgcn_permlane32_swap(__float_as_uint(Mh), __float_as_uint(Mh), false, false);
        const float dm = __uint_as_float(sm[0]) - __uint_as_float(sm[1]), mean = rbf(add1(__uint_as_float(sm[0]), __uint_as_float(sm[1])) * 0.5f);
        const float vbf = rbf(__builtin_fmaf(dm * dm, 3.0f, add1(__uint_as_float(sM[0]), __uint_as_float(sM[1]))) * (1.0f / 12.0f));
        const float rz = __builtin_amdgcn_rcpf(rbf(__builtin_amdgcn_sqrtf(rbf(vbf + 1e-5f))));
        dsw4(zw, lane < 32 ? -mean * rz : rz);
        f32x4 zm[2], zr[2];
        ds16<0>(zm[0], zr_); ds16<128>(zr[0], zr_); ds16<16>(zm[1], zr_); ds16<144>(zr[1], zr_);
        wait_lg<0>(zm[0], zm[1], zr[0], zr[1]);
#pragma unroll
        for (int jj = 0; jj < 4; ++jj) {
          u32x4 o;
#pragma unroll
          for (int q = 0; q < 4; ++q) {
            const int i = q >> 1, e = (q & 1) * 2;
            o[q] = pack2(fma1(zv[i][jj][e], zr[i][e], zm[i][e]), fma1(zv[i][jj][e + 1], zr[i][e + 1], zm[i][e + 1]));
          }
          o[3] = nohole(o[3]);
          asm volatile("global_store_dwordx4 %0, %1, %2 offset:%3 sc1\n\ts_nop 1" :: "v"(lin), "v"(o), "s"(zb), "i"(jj * 1024) : "memory");
          SB();
        }
      }
      SB();
      AB(10);
      ldtag(ef, (unsigned)((lane & 31) * 8), ws + WS_EF + 2048 + cb * 256);
    }
    const char* Bs[2] = {ws + WS_Z2 + (size_t)(wl * 32 * 48 + cb * 6 + wm * 3) * 1024, ws + WS_Z2 + (size_t)((wl * 32 + 16) * 48 + cb * 6 + wm * 3) * 1024};
    bf16x8 Bf[3][6];
    unsigned bv[2], bo;
    auto ldB1 = [&](int s, int j) {
      asm volatile("global_load_dwordx4 %0, %1, %2 offset:%3 " HSC : "=&v"(Bf[s % 3][j]) : "v"(bv[s & 1]), "s"(Bs[j / 3]), "i"((j % 3) * 1024) : "memory");
    };
    auto nxb = [&](int s, int k) {
      if (k == 0) asm volatile("s_add_u32 %0, %0, 0xc000\n\ts_cmp_eq_u32 %0, 0xc0000\n\ts_cselect_b32 %0, 0, %0" : "+s"(bo) :: "scc");
      else asm volatile("v_or_b32 %0, %1, %2" : "=v"(bv[s & 1]) : "s"(bo), "v"(lin));
    };
    f32x4 acc[4][3];
    unsigned ara[2][2];
#pragma unroll
    for (int ks = 0; ks < 2; ++ks) {
      ara[0][ks] = lb + M_RING + wl * 49152 + l15 * 128 + (((ks * 4 + g4) ^ (l15 & 7)) << 4);
      ara[1][ks] = ara[0][ks] + (M_CV - M_RING) + wl * (2 * CV_BUF - 49152);
    }
    u32x4 af[3][8];
    auto rdA = [&](int s, int i) {
      const int q = (0x20132012 >> (4 * (s >> 1))) & 3;
      asm volatile("ds_read_b128 %0, %1 offset:%2" : "=&v"(af[s == 15 ? 2 : s & 1][i]) : "v"(ara[q == 3][s & 1]), "i"((q % 3) * 8192 + (i & 3) * 2048 + (i >> 2) * (q == 3 ? CV_BUF : 24576)) : "memory");
    };
    bo = (unsigned)(rot2 * 2 * 49152);
    asm volatile("" : "+s"(xl), "+s"(Ab[0]), "+s"(Ab[1]), "+s"(Ab[2]), "+s"(Ab[3]));
    auto adA = [&](int d) {
#pragma unroll
      for (int q = 0; q < 2; ++q) av[q] = opq(aoff[q] - q * 1024 + (unsigned)(((d + rot2) & 7) * 128));
    };
    auto ldA1 = [&](int a) { asm volatile("global_load_lds_dwordx4 %0, %1 offset:%2" :: "v"(av[a & 1]), "s"(Ab[a >> 1]), "i"(a * 1024 - 4096) : "memory"); };
    adA(3);
    flag_wait_asm(flg + 32, 1u);
#pragma unroll
    for (int i = 0; i < 16; ++i) rdA(i >> 3, i & 7);
    signal1(flg + 28);
    asm volatile("s_mov_b32 m0, %0" :: "s"(xl));
    nxb(0, 1); nxb(1, 0); nxb(1, 1);
    auto preact = [&]() {
      flag_wait_asm(flg + 20 + wl * 4, 2u);
      const float* cp2 = (const float*)(smem + L_CS2 + (wl * 64 + l15) * 4);
      pin(ac2[0][0], ac2[0][1], ac2[1][0], ac2[1][1]);
#pragma unroll
      for (int jj = 0; jj < 2; ++jj) {
        const float nct = -add1(add1(cp2[jj * 16], cp2[32 + jj * 16]), add1(cp2[128 + jj * 16], cp2[160 + jj * 16]));
        u32x4 o;
#pragma unroll
        for (int q = 0; q < 4; ++q) {
          const int i = q >> 1, e = (q & 1) * 2;
          o[q] = pack2(mul1(fma1(xm[i][e], nct, ac2[i][jj][e]), xr[i][e]), mul1(fma1(xm[i][e + 1], nct, ac2[i][jj][e + 1]), xr[i][e + 1]));
        }
        z1put(odd * 32 + wl * 32 + jj * 16, o);
      }
      signal1(flg + 132);
      SB();
    };
    if (PAE) preact();
    flag_wait_asm(flg + 28, 4u);
#pragma unroll
    for (int i = 0; i < 16; ++i) pin(af[i >> 3][i & 7]);
#pragma unroll
    for (int a = 0; a < 8 - T3L; ++a) ldA1(a);
    unsigned fv, fw = flg + wave * 4, fp = flg + wl * 4, cv = 1u;
    unsigned fo = flg, fx = flg + (wave ^ 2) * 4, xa = lb + M_CV + wave * CV_BUF + lin, xb = lb + M_CV + (wave ^ 2) * CV_BUF + lin, za[2];
    unsigned eo = (unsigned)((g4 & 1) * 24576 + (l15 + 16 * (g4 >> 1)) * 16);
    const char* hp = hf + (size_t)((rb * 4 + wl * 2) * 24 + cb * 3) * 1024 + wm * 1536;
#pragma unroll
    for (int q = 0; q < 2; ++q) {
      const int fr = 4 * g4 + (l15 >> 2), p = lane & 3;
      za[q] = lb + L_Z1 + wm * 6144 + fr * 128 + (((wl * 4 + q * 2 + (p >> 1)) ^ (fr & 7)) << 4) + (p & 1) * 8;
    }
    pin(fw, fp, fo, fx, xa, xb, za[0], za[1], eo);
    asm volatile("" : "+s"(hp));
    AB(11);
    wait_vm<8 - T3L>(ef, bsv[0], bsv[1]);
    waittag(ef, (unsigned)((lane & 31) * 8), ws + WS_EF + 2048 + cb * 256);
    if (BDL && (wave & BDM)) __builtin_amdgcn_s_sleep(BDL);
    AB(12); SS(1 - 3 * XS);
#pragma unroll
    for (int j = 0; j < 6; ++j) ldB1(0, j);
#pragma unroll
    for (int j = 0; j < 6; ++j) ldB1(1, j);
#pragma unroll
    for (int a = 8 - T3L; a < 8; ++a) ldA1(a);
    nxb(2, 0); nxb(2, 1); nxb(3, 0); nxb(3, 1);
#pragma unroll
    for (int j = 0; j < 6 * B2E; ++j) ldB1(2, j);
    SS(2 - 3 * XS);
    if (!PAE) preact();
    __builtin_amdgcn_s_setprio(G2P);
    f32x4 bs4[2] = {{lo16(bsv[0]), lo16(bsv[0]), lo16(bsv[0]), lo16(bsv[0])}, {lo16(bsv[1]), lo16(bsv[1]), lo16(bsv[1]), lo16(bsv[1])}};
    pin(bs4[0], bs4[1]);
#define CK0 HIREG("h0", "%0") HIREG("h1", "%1") HIREG("h2", "%2") HIREG("h3", "%3") HIREG("h4", "%4") HIREG("h5", "%5") "s_waitcnt vmcnt(%[nw])\n\tv_max3_u32 %6, v[h0], v[h1], v[h2]\n\t"
#define CK1 "v_max3_u32 %6, %6, v[h3], v[h4]\n\tv_max_u32 %6, %6, v[h5]\n\t"
#define CK2 "v_cmp_eq_u32 vcc, -1, %6\n\ts_waitcnt lgkmcnt(0)\n\t"
#define CKA(d) "s_add_u32 vcc_lo, %[r2], %[st] + " d "\n\ts_and_b32 vcc_lo, vcc_lo, 15\n\ts_mul_i32 vcc_lo, vcc_lo, 0xc000\n\tv_or_b32 %6, vcc_lo, %[li]\n\t"
#define CKL(a, b, c, d, e, f) "global_load_dwordx4 " a ", %6, %[bs] sc1\n\tglobal_load_dwordx4 " b ", %6, %[bs] offset:1024 sc1\n\tglobal_load_dwordx4 " c ", %6, %[bs] offset:2048 sc1\n\t" \
            "global_load_dwordx4 " d ", %6, %[bt] sc1\n\tglobal_load_dwordx4 " e ", %6, %[bt] offset:1024 sc1\n\tglobal_load_dwordx4 " f ", %6, %[bt] offset:2048 sc1\n\t"
#define CK3X(x) "s_cbranch_vccnz .Lck_%=\n.Lcb_%=:\n\t.subsection 1\n.Lck_%=:\n\ts_waitcnt vmcnt(0)\n\ts_sleep 2\n\t" CKA("0") CKL("%0", "%1", "%2", "%3", "%4", "%5") x \
            "s_waitcnt vmcnt(0)\n\tv_max3_u32 %6, v[h0], v[h1], v[h2]\n\t" CK1 "v_cmp_eq_u32 vcc, -1, %6\n\t" \
            "s_sub_i32 %7, %7, 1\n\ts_cmp_lt_i32 %7, 1\n\ts_cbranch_scc1 .Lcb_%=\n\ts_cbranch_vccnz .Lck_%=\n\ts_branch .Lcb_%=\n\t.subsection 0"
#define CK3 CK3X("")
#define CK4 CK3X(CKA("1") CKL("%[n0]", "%[n1]", "%[n2]", "%[n3]", "%[n4]", "%[n5]"))
#define CKN(sl) [n0] "+v"(Bf[sl][0]), [n1] "+v"(Bf[sl][1]), [n2] "+v"(Bf[sl][2]), [n3] "+v"(Bf[sl][3]), [n4] "+v"(Bf[sl][4]), [n5] "+v"(Bf[sl][5])
#define CKO(sl) "+v"(Bf[sl][0]), "+v"(Bf[sl][1]), "+v"(Bf[sl][2]), "+v"(Bf[sl][3]), "+v"(Bf[sl][4]), "+v"(Bf[sl][5]), "=&v"(mx), "+s"(tmo)
#define CKI(s_, nw_) [r2] "s"(2 * rot2), [st] "i"(s_), [li] "v"(lin), [bs] "s"(Bs[0]), [bt] "s"(Bs[1]), [nw] "i"(nw_)
    unsigned mx;
    asm volatile(CK0 CK1 CK2 CK4 : CKO(0), CKN(1) : CKI(0, 6 + T3L + 6 * B2E) : "memory", "vcc", "scc");
    auto mm = [&](int s, int i, int j) {
      if (s == 0 && j < 3 && i >= 2) asm volatile(MF "%0, %1, %2, %3" : "=&v"(acc[i][j]) : "v"(Bf[0][j]), "v"(af[0][i]), "v"(bs4[i & 1]));
      else if (s == 0 && j < 3) MFMA0(acc[i][j], Bf[0][j], af[0][i]);
      else MFMA(acc[i][j % 3], Bf[s % 3][j], af[s == 15 ? 2 : s & 1][(j / 3) * 4 + i]);
      SB();
    };
    auto mmf = [&](int s, int i, int j, unsigned a, int off, int tg) {
      unsigned sv;
      asm volatile(MF "%0, %3, %4, %0\n\tv_readfirstlane_b32 %2, %1\n\ts_cmp_lt_u32 %2, %7\n\ts_cbranch_scc1 .Lfk_%=\n.Lfb_%=:\n\t.subsection 1\n.Lfk_%=:\n\t"
                   "ds_read_b32 %1, %5 offset:%6\n\ts_waitcnt lgkmcnt(0)\n\tv_readfirstlane_b32 %2, %1\n\ts_cmp_lt_u32 %2, %7\n\ts_cbranch_scc1 .Lfk_%=\n\ts_branch .Lfb_%=\n\t.subsection 0"
                   : "+v"(acc[i][j % 3]), "+v"(fv), "=&s"(sv) : "v"(Bf[s % 3][j]), "v"(af[s == 15 ? 2 : s & 1][(j / 3) * 4 + i]), "v"(a), "i"(off), "i"(tg) : "memory", "scc");
      SB();
    };
    auto step = [&](auto sc) {
      constexpr int s = decltype(sc)::value;
      constexpr int NV = (s == 1 && !B2E ? 1 : 6) + (s < 13 ? 5 : 0);
      constexpr int FO[8] = {224, 0, 232, 112, 120, 144, 152, 0};
      auto rdg = [&](int g) { if (s > 0 && (RDM >> g & 1)) rdA(s + 1, __builtin_popcount(RDM & ((1u << g) - 1))); };
      SS(3 + s);
      SB();
#pragma unroll
      for (int j = 0; j < 5; ++j) {
        auto& B = Bf[s % 3][j];
        if (j == 4 && s > 0 && s < 12)
          asm volatile(MF "%0, %2, %3, %0\n\ts_add_u32 %1, %1, 0xc000\n\ts_cmp_eq_u32 %1, 0xc0000\n\ts_cselect_b32 %1, 0, %1" : "+v"(acc[0][1]), "+s"(bo) : "v"(B), "v"(af[s & 1][4]) : "scc");
        else if (j == 0 && (s & 1) && FO[s >> 1]) mmf(s, 0, 0, fp, FO[s >> 1], 2);
        else mm(s, 0, j);
        if (s == 0 && !B2E) ldB1(2, j);
        rdg(4 * j);
        if (s == 13 && j > 0) rdA(15, 2 * j - 1);
        SB();
        if (j == 4 && s > 0 && s < 12) asm volatile(MF "%0, %2, %3, %0\n\tv_or_b32 %1, %4, %5" : "+v"(acc[1][1]), "=&v"(bv[s & 1]) : "v"(B), "v"(af[s & 1][5]), "s"(bo), "v"(lin));
        else mm(s, 1, j);
        rdg(4 * j + 1);
        SB();
        mm(s, 2, j);
        rdg(4 * j + 2);
        if (j == 4 && (s == 2 || s == 4 || s == 6 || s == 8)) asm volatile("ds_write_b32 %1, %0 offset:96\n\tv_add_u32 %0, 1, %0" : "+v"(cv) : "v"(fw) : "memory");
        if (j == 4 && !(s & 1) && FO[s >> 1]) asm volatile("ds_read_b32 %0, %1 offset:%2" : "=&v"(fv) : "v"(fp), "i"(FO[s >> 1]) : "memory");
        if (j == 4 && s == 3) signal1(fp + 232);
        if (j == 4 && s == SGS) signal1(flg + 44);
        SB();
        mm(s, 3, j);
        if (s + 3 < 16) ldB1(s + 3, j);
        else if (j < 4) rdA(15, 2 * j);
        else asm volatile("ds_read_b32 %0, %1 offset:56" : "=&v"(fv) : "v"(fo) : "memory");
        SB();
      }
      constexpr int sn = (s + 1) % 3;
      if constexpr (s == 0) {
        mm(0, 0, 5); if (!B2E) ldB1(2, 5); mm(0, 1, 5); nxb(4, 0); mm(0, 2, 5); nxb(4, 1); mm(0, 3, 5); ldB1(3, 5);
        asm volatile(CK0 CK1 CK2 CK4 : CKO(1), CKN(2) : CKI(1, 12 + T3L) : "memory", "vcc", "scc");
      } else if constexpr (s < 13)
        asm volatile(MF "%8, %12, %19, %8\n\t" CK0 MF "%9, %12, %20, %9\n\t" CK1 MF "%10, %12, %21, %10\n\t" CK2 MF "%11, %12, %22, %11\n\t"
                     "global_load_dwordx4 %12, %23, %[bt] offset:2048 " HSC "\n\t" CK4
                     : CKO(sn), "+v"(acc[0][2]), "+v"(acc[1][2]), "+v"(acc[2][2]), "+v"(acc[3][2]), "+v"(Bf[s % 3][5]), CKN((s + 2) % 3)
                     : "v"(af[s & 1][4]), "v"(af[s & 1][5]), "v"(af[s & 1][6]), "v"(af[s & 1][7]), "v"(bv[~s & 1]), CKI(s + 1, NV) : "memory", "vcc", "scc");
      else
        asm volatile(MF "%8, %12, %13, %8\n\t" CK0 MF "%9, %12, %14, %9\n\t" CK1 MF "%10, %12, %15, %10\n\t" CK2 MF "%11, %12, %16, %11\n\t" CK3
                     : CKO(sn), "+v"(acc[0][2]), "+v"(acc[1][2]), "+v"(acc[2][2]), "+v"(acc[3][2])
                     : "v"(Bf[s % 3][5]), "v"(af[s & 1][4]), "v"(af[s & 1][5]), "v"(af[s & 1][6]), "v"(af[s & 1][7]), CKI(s + 1, NV) : "memory", "vcc", "scc");
#pragma unroll
      for (int i = 0; i < 8; ++i) pin(af[~s & 1][i]);
      SB();
    };
    step(IC(0)); AB(13); step(IC(1)); step(IC(2)); step(IC(3)); AB(14); step(IC(4)); step(IC(5)); step(IC(6)); step(IC(7));
    step(IC(8)); step(IC(9)); step(IC(10)); step(IC(11)); step(IC(12)); step(IC(13));
    SS(17);
#pragma unroll
    for (int i = 0; i < 8; ++i) pin(af[2][i]);
    s16x4 z1r[2][3];
    f32x2 zf[12];
    f32x4 pr_[2][3];
#pragma unroll
    for (int m = 0; m < 48; ++m) {
      const int i = m / 12, n = m % 12;
      if (m == 0) mmf(14, 0, 0, fo, 56, 5);
      else if (m == 2)
        asm volatile(MF "%8, %11, %15, %8\n\t" CK0 MF "%9, %12, %16, %9\n\t" CK1 MF "%10, %13, %16, %10\n\t" CK2 MF "%8, %14, %16, %8\n\t" CK3
                     : CKO(0), "+v"(acc[0][2]), "+v"(acc[0][0]), "+v"(acc[0][1])
                     : "v"(Bf[2][2]), "v"(Bf[2][3]), "v"(Bf[2][4]), "v"(Bf[2][5]), "v"(af[0][0]), "v"(af[0][4]), CKI(15, 0) : "memory", "vcc", "scc");
      else if (m == KC) { wait_lg<0>(fv); mmf(14 + n / 6, i, n % 6, fx, 0, 1); }
      else if (m < 2 || m > 5) mm(14 + n / 6, i, n % 6);
      if (m == 1) {
#pragma unroll
        for (int q = 0; q < 2; ++q) { z1r[q][0] = tr_asm<0>(za[q]); z1r[q][1] = tr_asm<2048>(za[q]); z1r[q][2] = tr_asm<4096>(za[q]); }
      }
      if (m == 5) pin(z1r[0][0], z1r[0][1], z1r[0][2], z1r[1][0], z1r[1][1], z1r[1][2]);
      const int u = m < 14 ? m - 6 : (m - 14) / 2 + 8;
      if (m >= 6 && (m < 14 || !(m & 1)) && u < 12) { zf[u] = unpk(__builtin_bit_cast(u32x2, z1r[u / 6][u / 2 % 3])[u & 1]); pin(zf[u]); }
      if ((i == 1 || i == 2) && n >= 2 && n < 8 && (n & 1)) asm volatile("ds_write_b128 %0, %1 offset:%2" :: "v"(xa), "v"(acc[i - 1][n / 2 - 1]), "i"((i * 3 + n / 2 - 4) * 1024) : "memory");
      if (m == 32) asm volatile("ds_write_b32 %1, %0" :: "v"(cv), "v"(fw) : "memory");
      if (m == KC - 4) asm volatile("ds_read_b32 %0, %1" : "=&v"(fv) : "v"(fx) : "memory");
      if (m >= KC && m < KC + 3)
#pragma unroll
        for (int q = 0; q < 2; ++q) asm volatile("ds_read_b128 %0, %1 offset:%2" : "=&v"(pr_[q][m - KC]) : "v"(xb), "i"((q * 3 + m - KC) * 1024) : "memory");
      SB();
    }
    SS(19);
    AB(15);
    wait_lg<0>(pr_[0][0], pr_[0][1], pr_[0][2], pr_[1][0], pr_[1][1], pr_[1][2]);
    AB(16);
    unsigned pk[2][3][2];
    auto hm = [&](int i, int j) {
      pin(acc[2 + i][j]);
      const f32x4 o = acc[2 + i][j] + pr_[i][j];
#pragma unroll
      for (int h = 0; h < 2; ++h) pk[i][j][h] = pk2(zf[i * 6 + j * 2 + h] * (f32x2){o[2 * h], o[2 * h + 1]});
    };
    hm(0, 0); hm(0, 1);
    SB();
#pragma unroll
    for (int j = 0; j < 3; ++j) {
      if (j == 2) hm(0, 2);
      hm(1, j);
      const auto s0 = __builtin_amdgcn_permlane16_swap(pk[0][j][0], pk[1][j][0], false, false);
      const auto s1 = __builtin_amdgcn_permlane16_swap(pk[0][j][1], pk[1][j][1], false, false);
      asm volatile("global_store_dwordx4 %0, %1, %2 offset:%3 sc1\n\ts_nop 1" :: "v"(eo), "v"((u32x4){s0[0], s1[0], s0[1], nohole(s1[1])}), "s"(hp), "i"(j * 512) : "memory");
      SB();
    }
    AB(17);
    ldW(0, 15);
    wait_vm<15>(ef);
    gemm3(IC(0), []() {});
    if (b == 0 && tid == 0) *(u64*)(ws + WS_EP) = tag;
  }
  AB(24);
  AB_OUT();
}

extern "C" void kernel_launch(void* const* d_in, const int* in_sizes, int n_in,
                              void* d_out, int out_size, void* d_ws, size_t ws_size,
                              hipStream_t stream) {
  (void)hipFuncSetAttribute((const void*)k_f, hipFuncAttributeMaxDynamicSharedMemorySize, M_LDS);
  k_f<<<256, 512, M_LDS, stream>>>((const u16*)d_in[0], (const u16*)d_in[1], (const u16*)d_in[2], (const u16*)d_in[3], (const u16*)d_in[4],
                                   (const u16*)d_in[5], (const u16*)d_in[7], (const u16*)d_in[8], (const u16*)d_in[9], (const u16*)d_in[10],
                                   (u16*)d_out, (char*)d_ws);
}
```

```cpp
#include <hip/hip_runtime.h>
#include <hip/hip_bf16.h>
#include <stdint.h>
#include <type_traits>

typedef unsigned short u16;
typedef unsigned long long u64;
typedef __attribute__((ext_vector_type(8))) short bf16x8;
typedef __attribute__((ext_vector_type(4))) short s16x4;
typedef __attribute__((ext_vector_type(4))) float f32x4;
typedef __attribute__((ext_vector_type(2))) float f32x2;
typedef __attribute__((ext_vector_type(4))) unsigned int u32x4;
typedef __attribute__((ext_vector_type(2))) unsigned int u32x2;
typedef __attribute__((ext_vector_type(2))) __bf16 bf16x2n;

#define LDS_AS __attribute__((address_space(3)))
#define IC(n) std::integral_constant<int, (n)>{}
#define SB() __builtin_amdgcn_sched_barrier(0)

#define AB_DECL do {} while (0)
#define AB(i) do {} while (0)
#define AB_OUT() do {} while (0)
#define CB(i) do {} while (0)
#define SS(i) do {} while (0)
constexpr int S_ = 2048, D_ = 640, FH_ = 768, F2_ = 1536;
constexpr unsigned HOLE = 0xFFFFFFFFu;

constexpr size_t WS_EP = 0;
constexpr size_t WS_EF = 4096;
constexpr size_t WS_LF = 22528;
constexpr size_t WS_YF = 14336;
constexpr size_t WS_XF = 20480;
constexpr size_t WS_ST = 32768;
constexpr size_t WS_Z2 = WS_ST + 32 * 12 * 1024;
constexpr size_t WS_H = WS_Z2 + 64 * 48 * 1024;
constexpr size_t WS_ZX = WS_H + 128 * 24 * 1024;
constexpr size_t WS_KV = WS_ZX + 32 * 4 * 4096;
constexpr size_t WS_Y = WS_KV + D_ * S_ * 2;
constexpr size_t WS_WO = WS_Y + D_ * S_ * 2;

__device__ __forceinline__ unsigned pack2(float lo, float hi) {
  f32x2 v = {lo, hi};
  bf16x2n r = __builtin_convertvector(v, bf16x2n);
  return __builtin_bit_cast(unsigned, r);
}
__device__ __forceinline__ float nv(float v) { asm("" : "+v"(v)); return v; }
__device__ __forceinline__ float fma1(float a, float b, float c) { float r; asm("v_fma_f32 %0, %1, %2, %3" : "=v"(r) : "v"(a), "v"(b), "v"(c)); return r; }
__device__ __forceinline__ float add1(float a, float b) { float r; asm("v_add_f32 %0, %1, %2" : "=v"(r) : "v"(a), "v"(b)); return r; }
__device__ __forceinline__ float mul1(float a, float b) { float r; asm("v_mul_f32 %0, %1, %2" : "=v"(r) : "v"(a), "v"(b)); return r; }
__device__ __forceinline__ unsigned pack2s(float lo, float hi) { return pack2(nv(lo), nv(hi)); }
__device__ __forceinline__ float lo16(unsigned w) { return __uint_as_float(w << 16); }
__device__ __forceinline__ float hi16(unsigned w) { return __uint_as_float(w & 0xFFFF0000u); }
__device__ __forceinline__ float rbf(float f) { return lo16(pack2(f, f)); }
__device__ __forceinline__ f32x2 unpk(unsigned w) { f32x2 r = {lo16(w), hi16(w)}; return r; }
__device__ __forceinline__ unsigned pk2(f32x2 v) { return pack2(v[0], v[1]); }
__device__ __forceinline__ unsigned umx(unsigned a, unsigned b) { return a > b ? a : b; }
__device__ __forceinline__ unsigned nohole(unsigned a) { return a < HOLE - 1 ? a : HOLE - 1; }
__device__ __forceinline__ f32x2 gelu2(f32x2 x) {
  const f32x2 c1 = {-2.3022082f, -2.3022082f}, c2 = {-0.10294324f, -0.10294324f};
  const f32x2 a = x * __builtin_elementwise_fma(x * x, c2, c1);
  f32x2 e = {__builtin_amdgcn_exp2f(a[0]), __builtin_amdgcn_exp2f(a[1])};
  e = e + (f32x2){1.0f, 1.0f};
  const f32x2 r = {__builtin_amdgcn_rcpf(e[0]), __builtin_amdgcn_rcpf(e[1])};
  return x * r;
}
__device__ __forceinline__ float gelu1(float x) {
  const float a = x * __builtin_fmaf(x * x, -0.10294324f, -2.3022082f);
  return x * __builtin_amdgcn_rcpf(__builtin_amdgcn_exp2f(a) + 1.0f);
}
template <int OFF>
__device__ __forceinline__ s16x4 tr_asm(unsigned a) {
  s16x4 r;
  asm volatile("ds_read_b64_tr_b16 %0, %1 offset:%2" : "=&v"(r) : "v"(a), "i"(OFF) : "memory");
  return r;
}
__device__ __forceinline__ bf16x8 join8(s16x4 lo, s16x4 hi) {
  bf16x8 r;
  r[0] = lo[0]; r[1] = lo[1]; r[2] = lo[2]; r[3] = lo[3];
  r[4] = hi[0]; r[5] = hi[1]; r[6] = hi[2]; r[7] = hi[3];
  return r;
}
__device__ __forceinline__ unsigned lds_addr(const void* p) { return (unsigned)(uintptr_t)(LDS_AS const char*)p; }
#define HIREG(sym, r) ".set " sym ", 0\n.set hir_st, 0\n.irpc c, \"" r "\"\n.ifc \\c,:\n.set hir_st, 1\n.else\n.ifc \\c,]\n.set hir_st, 0\n.else\n.if hir_st\n.set " sym ", " sym " * 10 + \\c\n.endif\n.endif\n.endif\n.endr\n"
#define MF "v_mfma_f32_16x16x32_bf16 "
#define SLP 2
template <int SL = 1> __device__ __forceinline__ void flag_wait_asm(unsigned faddr, unsigned target) {
  unsigned v, sv;
  asm volatile(".Lfw_%=:\n\tds_read_b32 %0, %2\n\ts_waitcnt lgkmcnt(0)\n\tv_readfirstlane_b32 %1, %0\n\ts_nop 0\n\t"
               "s_cmp_ge_u32 %1, %3\n\ts_cbranch_scc1 .Lfd_%=\n\ts_sleep %4\n\ts_branch .Lfw_%=\n\t.Lfd_%=:"
               : "=&v"(v), "=&s"(sv) : "v"(faddr), "s"(target), "i"(SL) : "memory", "scc");
}
__device__ __forceinline__ void flag_wait2(unsigned faddr, unsigned target) {
  unsigned v, w, sv;
  asm volatile(".Lgw_%=:\n\tds_read_b32 %0, %3\n\tds_read_b32 %1, %3 offset:4\n\ts_waitcnt lgkmcnt(0)\n\tv_min_u32 %0, %0, %1\n\ts_nop 0\n\tv_readfirstlane_b32 %2, %0\n\ts_nop 0\n\t"
               "s_cmp_ge_u32 %2, %4\n\ts_cbranch_scc1 .Lgd_%=\n\ts_sleep %5\n\ts_branch .Lgw_%=\n\t.Lgd_%=:"
               : "=&v"(v), "=&v"(w), "=&s"(sv) : "v"(faddr), "s"(target), "i"(SLP) : "memory", "scc");
}
__device__ __forceinline__ void signal1(unsigned a, unsigned one = 1u) {
  asm volatile("s_mov_b64 exec, 1\n\tds_add_u32 %0, %1\n\ts_mov_b64 exec, -1" :: "v"(a), "v"(one) : "memory");
}
__device__ __forceinline__ void dma16(unsigned vo, const void* sb, unsigned lds) {
  asm volatile("s_mov_b32 m0, %2\n\ts_nop 0\n\tglobal_load_lds_dwordx4 %0, %1" :: "v"(vo), "s"(sb), "s"(lds) : "memory");
}
template <int LO, int GO> __device__ __forceinline__ void dma32s(unsigned vo0, unsigned vo1, const void* sb, unsigned lds) {
  asm volatile("s_add_u32 m0, %5, %6\n\ts_add_u32 vcc_lo, %2, %4\n\ts_addc_u32 vcc_hi, %3, 0\n\t"
               "global_load_lds_dwordx4 %0, vcc\n\tglobal_load_lds_dwordx4 %1, vcc offset:1024"
               :: "v"(vo0), "v"(vo1), "s"((unsigned)(uintptr_t)sb), "s"((unsigned)((uintptr_t)sb >> 32)), "i"(GO), "s"(lds), "i"(LO) : "memory", "scc", "vcc");
}
template <int LO, int GO, int WRAP, int LIM> __device__ __forceinline__ void dma32r(unsigned vo0, unsigned vo1, const void* sb, unsigned lds, int rot) {
  const char* s2 = (const char*)sb - WRAP;
  asm volatile("s_add_u32 m0, %5, %6\n\ts_cmp_ge_u32 %7, %8\n\ts_cselect_b64 vcc, %3, %2\n\ts_add_u32 vcc_lo, vcc_lo, %4\n\ts_addc_u32 vcc_hi, vcc_hi, 0\n\t"
               "global_load_lds_dwordx4 %0, vcc\n\tglobal_load_lds_dwordx4 %1, vcc offset:1024"
               :: "v"(vo0), "v"(vo1), "s"(sb), "s"(s2), "i"(GO), "s"(lds), "i"(LO), "s"(rot), "i"(LIM) : "memory", "scc", "vcc");
}
__device__ __forceinline__ void st16(unsigned vo, const void* sb, u32x4 v) {
  asm volatile("global_store_dwordx4 %0, %1, %2 sc1\n\ts_nop 1" :: "v"(vo), "v"(v), "s"(sb) : "memory");
}
__device__ __forceinline__ void touch(unsigned vo, const void* sb, unsigned lds) {
  asm volatile("s_mov_b32 m0, %2\n\ts_nop 0\n\tglobal_load_lds_dword %0, %1" :: "v"(vo), "s"(sb), "s"(lds) : "memory");
}
__device__ __forceinline__ void st8(unsigned vo, const void* sb, u32x2 v) { asm volatile("global_store_dwordx2 %0, %1, %2 sc1" :: "v"(vo), "v"(v), "s"(sb) : "memory"); }
template <class A, class B> __device__ __forceinline__ void MFMA(f32x4& acc, const A& a, const B& b) {
  asm volatile("v_mfma_f32_16x16x32_bf16 %0, %1, %2, %0" : "+v"(acc) : "v"(a), "v"(b));
}
template <class A, class B> __device__ __forceinline__ void MFMA0(f32x4& acc, const A& a, const B& b) {
  asm volatile("v_mfma_f32_16x16x32_bf16 %0, %1, %2, 0" : "=&v"(acc) : "v"(a), "v"(b));
}
template <bool Z, class A, class B> __device__ __forceinline__ void MFMAW(int n, f32x4& acc, const A& a, const B& b) {
  if (n < 0) { if (Z) MFMA0(acc, a, b); else MFMA(acc, a, b); }
  else if (Z) asm volatile("s_waitcnt lgkmcnt(%3)\n\tv_mfma_f32_16x16x32_bf16 %0, %1, %2, 0" : "=&v"(acc) : "v"(a), "v"(b), "i"(n));
  else asm volatile("s_waitcnt lgkmcnt(%3)\n\tv_mfma_f32_16x16x32_bf16 %0, %1, %2, %0" : "+v"(acc) : "v"(a), "v"(b), "i"(n));
}
__device__ __forceinline__ float sel4(float a, float b, float c, float d, u64 m1, u64 m2) {
  float r, t;
  asm volatile("v_cndmask_b32_e64 %0, %2, %3, %6\n\tv_cndmask_b32_e64 %1, %4, %5, %6\n\tv_cndmask_b32_e64 %0, %0, %1, %7"
               : "=&v"(r), "=&v"(t) : "v"(a), "v"(b), "v"(c), "v"(d), "s"(m1), "s"(m2));
  return r;
}
template <class T> __device__ __forceinline__ void pin1(T& r) { asm volatile("" : "+v"(r)); }
template <class T> __device__ __forceinline__ T opq(T r) { asm volatile("" : "+v"(r)); return r; }
template <class... T> __device__ __forceinline__ void pin(T&... r) { (pin1(r), ...); }
template <int N, class... T> __device__ __forceinline__ void wait_vm(T&... r) { asm volatile("s_waitcnt vmcnt(%0)" :: "n"(N) : "memory"); (pin1(r), ...); }
template <int N, class... T> __device__ __forceinline__ void wait_lg(T&... r) { asm volatile("s_waitcnt lgkmcnt(%0)" :: "n"(N) : "memory"); (pin1(r), ...); }
template <int OFF, class T> __device__ __forceinline__ void ds16(T& d, unsigned a) {
  asm volatile("ds_read_b128 %0, %1 offset:%2" : "=&v"(d) : "v"(a), "i"(OFF) : "memory");
}
template <int OFF> __device__ __forceinline__ void dsw16(unsigned a, const f32x4& v) {
  asm volatile("ds_write_b128 %0, %1 offset:%2" :: "v"(a), "v"(v), "i"(OFF) : "memory");
}
__device__ __forceinline__ void dsw4(unsigned a, float v) { asm volatile("ds_write_b32 %0, %1" :: "v"(a), "v"(v) : "memory"); }
#define HSC ""
template <class T> __device__ __forceinline__ void reld6(T (&B)[6], unsigned vo, const void* sb) {
  asm volatile("global_load_dwordx4 %0, %6, %7 offset:-2048 sc1\n\tglobal_load_dwordx4 %1, %6, %7 offset:-1024 sc1\n\t"
               "global_load_dwordx4 %2, %6, %7 sc1\n\tglobal_load_dwordx4 %3, %6, %7 offset:1024 sc1\n\t"
               "global_load_dwordx4 %4, %6, %7 offset:2048 sc1\n\tglobal_load_dwordx4 %5, %6, %7 offset:3072 sc1"
               : "+v"(B[0]), "+v"(B[1]), "+v"(B[2]), "+v"(B[3]), "+v"(B[4]), "+v"(B[5]) : "v"(vo), "s"(sb) : "memory");
}

constexpr int L_FLAG = 0;
constexpr int L_DUM = 256;
constexpr int L_XM = 512;
constexpr int L_XR = 768;
constexpr int L_CS = 1024;
constexpr int L_ZS = 2048;
constexpr int L_CS2 = 3072;
constexpr int L_Z1 = 4096;
constexpr int L_RAW = L_Z1;
constexpr int L_SLOT = 16384;
constexpr int L_KV = 512, KV_P = 3088, L_WO = L_KV + 8 * KV_P;
static_assert(L_WO % 16 == 0 && L_WO + 4 * 1024 <= L_SLOT + 2 * 8192, "staging");
constexpr int L_P2 = L_SLOT + 12 * 8192;
constexpr int CV_C = 4608, CV_CS = 4160, CV_BUF = CV_C + 2 * CV_CS;
constexpr int M_CV = L_SLOT;
constexpr int M_RING = L_SLOT + 6 * 8192;
constexpr int P3_G = L_SLOT;
constexpr int P3_C = P3_G + 80 * 1024;
constexpr int yat(int j) { return M_CV + (j >> 1) * CV_BUF + 8192 + (j & 1) * 2048; }
constexpr int M_LDS = 163840;
static_assert(3 * CV_BUF + 8192 <= 6 * 8192 && P3_C + 60 * 1024 <= M_LDS && CV_BUF >= 8192 + 4096, "LDS map");

enum { UX, UA, UB, UV };
struct Unit { int kind, idx, need, free, slot; };
#define SCHED gugugugwBuluouk_uuxuu_uuxuxu_uuxuxuu_Euuuuh_vuumumuh_vuumumuhF_vuvumum_DRvuuUHmmmmSptt_VhhhttttttJItttttttt
#define STR_(x) #x
#define STR(x) STR_(x)
constexpr char SCH[] = STR(SCHED);
constexpr int NSCH = sizeof(SCH) - 1;
constexpr int XNEED[10] = {1, 2, 2, 3, 3, 4, 4, 5, 5, 6};
constexpr int VNEED[5] = {5, 6, 7, 7, 8};
constexpr int XSLOT[10] = {17, 16, 15, 13, 10, 14, 11, 17, 9, 16}, WSLOT[10] = {6, 2, 4, 0, 6, 12, 10, 4, 0, 8}, VSLOT[10] = {8, 15, 3, 2, 14, 17, 16, 15, 7, 6}, XW = 6;
constexpr int cnt(char c, int e = NSCH) { int n = 0; for (int i = 0; i < e; ++i) n += SCH[i] == c || (c == 'u' && SCH[i] == 'g'); return n; }
constexpr int pos(char c, int k = 0) { for (int i = 0; i < NSCH; ++i) if (SCH[i] == c && k-- == 0) return i; return NSCH; }
constexpr Unit unit(int n) {
  for (int b = 1; b < 11; ++b) {
    for (int c = 0; c < 10; ++c) if (XNEED[c] == b && n-- == 0) return {UX, c, b, b + 1, XSLOT[c]};
    for (int c = 0; c < 5; ++c) if (VNEED[c] == b && n-- == 0) return {UV, c, b, b + 1, VSLOT[c]};
    if (n < 2) return {n ? UB : UA, b - 1, b < 8 ? b : 8, b < 7 ? b + 2 : 9, WSLOT[b - 1] + n};
    n -= 2;
  }
  return {UV, n + 5, 10, 99, VSLOT[n + 5]};
}
constexpr int FLAGB = cnt('_', pos('D')), GZ = cnt('_', pos('E'));
constexpr int nvm(char c) { return c == 'u' ? 2 : (c == 'H' ? 5 : (c == 'U' ? 4 : (c == 'F' || c == 'h' || c == 't' || c == 'w'))); }
constexpr int behind(int g, int e, char w) {
  const int b = cnt('_', e) + 1;
  int n = 0;
  for (int i = 0; i < e; ++i) {
    const char c = SCH[i];
    if (g == 0) { if (c == 'g') n = w == '_' && unit(cnt('u', i)).need <= b ? 0 : n + 2; if (c == 'B') n += 2; continue; }
    n = (w == '_' ? (c == 'u' && unit(cnt('u', i)).need <= b) || (c == 'h' && b == FLAGB) || (c == 'k' && b == GZ) : c == w) ? 0 : n + nvm(c);
  }
  return g == 0 && b >= GZ ? 0 : n;
}
constexpr bool plan_ok() {
  if (cnt('u') != 40 || cnt('_') != 9 || cnt('x') != 5 || cnt('h') != 6 || cnt('t') != 16 || cnt('I') * cnt('J') * cnt('p') != 1 || cnt('v') != 5 || cnt('m') != 10) return false;
  if (cnt('B') * cnt('D') * cnt('E') * cnt('F') * cnt('l') * cnt('o') * cnt('k') * cnt('H') * cnt('U') * cnt('V') * cnt('R') * cnt('S') != 1) return false;
  if (pos('l') < pos('B') || pos('o') < pos('B') || pos('k') < pos('l') || pos('k') > pos('_') || pos('o') > pos('_') || cnt('g', pos('B')) != cnt('g')) return false;
  if (cnt('_', pos('h', 2)) >= FLAGB || GZ < 3 || GZ > 6 || FLAGB < GZ || FLAGB > 9 || pos('o') > pos('k') || pos('H') > pos('I') || pos('F') > pos('U')) return false;
  if (cnt('_', pos('R')) != 8 || cnt('_', pos('S')) != 8 || pos('S') < pos('R') || cnt('_', pos('I')) != 9 || pos('J') < pos('U') || cnt('_', pos('J')) != 9) return false;
  if (cnt('_', pos('V')) != 9 || cnt('u', pos('V')) < 40) return false;
  for (int c = 0; c < 5; ++c) if (cnt('_', pos('x', c)) != XNEED[c] || cnt('_', pos('v', c)) != VNEED[c] || pos('m', 2 * c) < pos('v', c)) return false;
  if (cnt('_', pos('p')) != 8 || pos('p') < pos('m', 9)) return false;
  int fr[18] = {1, 1};
  for (int i = 0; i < NSCH; ++i) if (SCH[i] == 'u' || SCH[i] == 'g') {
    const Unit u = unit(cnt('u', i));
    const int p = cnt('_', i);
    if (p < fr[u.slot] || p >= u.need) return false;
    fr[u.slot] = u.free;
  }
  return true;
}
static_assert(plan_ok(), "phase A plan");
template <int I, int E, class F> __device__ __forceinline__ void sfor(F&& f) { if constexpr (I < E) { f(IC(I)); sfor<I + 1, E>(f); } }

__device__ __forceinline__ void conv_core(unsigned buf, int lane, unsigned Dw, f32x4 (&ac)[8]) {
  const int l15 = lane & 15, g4 = lane >> 4;
  const int c = (l15 + 1) & 1;
  unsigned aa[4];
#pragma unroll
  for (int k = 0; k < 4; ++k) aa[k] = buf + CV_C + c * CV_CS + (2031 - l15 + 8 * g4 - c) * 2 - 512 - 1024 * k;
  unsigned bb = buf + 512 + 32 * l15 - 32 + 16 * g4;
  u32x2 A[2][8][2];
  u32x4 B[2][8];
  constexpr int TT[36] = {7, 6, 5, 4, 7, 6, 5, 4, 7, 6, 5, 3, 7, 6, 5, 4, 7, 6, 3, 2, 7, 6, 5, 4, 7, 3, 2, 1, 7, 6, 5, 4, 3, 2, 1, 0};
  constexpr int AA[36] = {0, 0, 0, 0, 1, 1, 1, 1, 2, 2, 2, 0, 3, 3, 3, 2, 4, 4, 1, 0, 5, 5, 4, 3, 6, 2, 1, 0, 7, 6, 5, 4, 3, 2, 1, 0};
  auto ld1 = [&](int p, int n) {
    if (n < 16) {
      const int a = n >> 1, o = ((a & 1) ? 0 : 128) + 2 * (n & 1);
      asm volatile("ds_read2_b32 %0, %1 offset0:%2 offset1:%3" : "=&v"(A[p][(n >> 1) & 7][n & 1]) : "v"(aa[(a >> 1) & 3]), "i"(o), "i"(o + 1) : "memory");
    } else
      asm volatile("ds_read_b128 %0, %1 offset:%2" : "=&v"(B[p][n & 7]) : "v"(bb), "i"(512 * (n & 7)) : "memory");
  };
  auto next = [&](int dl) {
#pragma unroll
    for (int k = 0; k < 4; ++k) aa[k] += dl;
    bb += dl;
    SB();
  };
  auto land = [&](int p) {
    asm volatile("s_waitcnt lgkmcnt(0)" : "+v"(A[p][0][0]), "+v"(A[p][0][1]), "+v"(A[p][1][0]), "+v"(A[p][1][1]), "+v"(A[p][2][0]), "+v"(A[p][2][1]),
                 "+v"(A[p][3][0]), "+v"(A[p][3][1]), "+v"(A[p][4][0]), "+v"(A[p][4][1]), "+v"(A[p][5][0]), "+v"(A[p][5][1]), "+v"(A[p][6][0]), "+v"(A[p][6][1]),
                 "+v"(A[p][7][0]), "+v"(A[p][7][1]), "+v"(B[p][0]), "+v"(B[p][1]), "+v"(B[p][2]), "+v"(B[p][3]), "+v"(B[p][4]), "+v"(B[p][5]),
                 "+v"(B[p][6]), "+v"(B[p][7]) :: "memory");
    SB();
  };
  auto mm = [&](int p) {
#pragma unroll
    for (int n = 0; n < 36; ++n) {
      const int t = TT[n], a = AA[n];
      const u32x4 av = {A[p][a][0][0], A[p][a][0][1], A[p][a][1][0], A[p][a][1][1]};
      MFMA(ac[t], av, B[p][t - a]);
      if (n < 24) ld1(p ^ 1, n);
      SB();
    }
    land(p ^ 1);
  };
#pragma unroll
  for (int n = 0; n < 24; ++n) ld1(0, n);
  u32x4 ad;
#pragma unroll
  for (int e = 0; e < 4; ++e) ad[e] = lane == 32 + 2 * e + (lane & 1) + (lane & 8) * 3 ? Dw << (16 * (lane & 1)) : 0u;
  pin(ad);
  land(0);
#pragma unroll
  for (int t = 0; t < 8; ++t) MFMA0(ac[t], ad, B[0][t]);
#pragma unroll 1
  for (int r = 0; r < 8; r += 2) {
    next(-64);
    mm(0);
    next(r < 6 ? -64 : 0);
    mm(1);
  }
  asm volatile("s_nop 15" : "+v"(ac[0]), "+v"(ac[1]), "+v"(ac[2]), "+v"(ac[3]), "+v"(ac[4]), "+v"(ac[5]), "+v"(ac[6]), "+v"(ac[7]));
}

#define ROTA 3
#define RA3 0
#define RB2 4
#define RB4 2
#define THR 24
#define RB5 2
#define FE3 9
#define WSH 1
#define T3L 4
#define PAE 0
#define T7D 1
#define KC 40
#define NWY 4
#define CPR 1
#define PN 10
#define P2P 2
#define SGS 11

__global__ __launch_bounds__(512) void k_f(
    const u16* __restrict__ x, const u16* __restrict__ kin, const u16* __restrict__ vin, const u16* __restrict__ shift_k,
    const u16* __restrict__ ssm_k, const u16* __restrict__ ssm_D, const u16* __restrict__ w_in, const u16* __restrict__ w_sp,
    const u16* __restrict__ b_sp, const u16* __restrict__ w_out, u16* __restrict__ out, char* ws) {
  extern __shared__ __attribute__((aligned(16))) char smem[];
  const int tid = threadIdx.x, lane = tid & 63, wave = __builtin_amdgcn_readfirstlane(tid >> 6);
  const int b = blockIdx.x;
  const int xcd = b & 7, idx = b >> 3;
  const int rb = 8 * (xcd >> 1) + (idx >> 2), cb = 4 * (xcd & 1) + (idx & 3);
  const int t0 = rb * 64, d0 = cb * 80;
  const int g4 = lane >> 4, l15 = lane & 15;
  const unsigned lin = (unsigned)(lane * 16);
  const unsigned lb = lds_addr(smem), flg = lb + L_FLAG;
  AB_DECL;
  AB(0);
  asm volatile("" :: "s"(x), "s"(kin), "s"(vin), "s"(shift_k), "s"(ssm_k), "s"(ssm_D), "s"(w_in), "s"(w_sp), "s"(b_sp), "s"(w_out), "s"(out), "s"(ws));
  u64 tag = *(const __attribute__((address_space(4))) u64*)(uintptr_t)ws;
  int tmo = 1 << 16;
  auto cwarm = [&]() {
    u64 pc;
    unsigned clen;
    asm volatile("s_getpc_b64 %0\n.Lpc_here_%=:\n\ts_mov_b32 %1, .Lcode_end-.Lpc_here_%=\n\t.subsection 2\n.Lcode_end:\n\t.fill 256, 4, 0xBF800000\n\t.subsection 0" : "=s"(pc), "=s"(clen));
    touch(min((unsigned)(idx * 1536 + min(lane, 11) * 128), clen - 4u) & ~3u, (const void*)pc, lb + L_DUM);
  };

  const int cw = wave & 3, wm = wave & 1, wl = (wave >> 1) & 1, ksb = wm ^ wl, odd = cb & 1, pr = cb >> 1;
  const int r8 = lane >> 3, c8 = lane & 7;
  unsigned voX[2], voW[2];
#pragma unroll
  for (int h = 0; h < 2; ++h) {
    voX[h] = (unsigned)((h * 8 + r8) * (D_ * 2) + ((c8 ^ (h * 4 + (r8 & 3))) << 4) - h * 1024);
    voW[h] = (unsigned)((h * 8 + r8) * (F2_ * 2) + ((c8 ^ (h * 4 + (r8 & 2))) << 4) - h * 1024);
  }
  const int rot = (((rb & 7) * 5 >> 2) + ROTA * (cb & 3) + RA3 * (cb >> 2)) % 10, rot2 = (RB2 * (cb >> 2) + RB4 * (rb >> 3) + RB5 * (cb & 1)) & 7;
  const char* gX = (const char*)x + (size_t)(t0 + cw * 16) * (D_ * 2) + rot * 128;
  const char* gW = (const char*)w_in + (size_t)(cw * 16 + rot * 64) * (F2_ * 2) + pr * 384;
  const char *gV = gW + odd * 256, *gB = gW + (odd ? 128 : FH_ * 2 + 128);
  const char* gS = (const char*)w_sp + (size_t)t0 * (S_ * 2);
  const unsigned aoff[2] = {(unsigned)(r8 * 4096 + ((c8 ^ r8) << 4)), (unsigned)(r8 * 4096 + ((c8 ^ r8) << 4) + 32768)};
  const unsigned ldw = lb + cw * 2048;
  auto issue = [&](auto nc) {
    constexpr Unit u = unit(decltype(nc)::value);
    constexpr int lo = L_SLOT + u.slot * 8192;
    if (u.kind == UX) dma32r<lo, u.idx * 128, 1280, 10 - u.idx>(voX[0], voX[1], gX, ldw, rot);
    else dma32r<lo, u.idx * (64 * F2_ * 2) + (u.kind == UA ? FH_ * 2 : 0), 640 * F2_ * 2, 10 - u.idx>(voW[0], voW[1], u.kind == UB ? gB : gV, ldw, rot);
    SB();
  };
  const int kd = d0 + min(wave, 4) * 16 + r8 * 2;
  u32x4 sw;
  {
    const int ks0 = t0 + wave * 8;
#pragma unroll
    for (int i = 0; i < 3; ++i) {
      const int id = min(i * 64 + lane, 189), r = id / 10, c = id % 10;
      const char* kp = (r < 11 ? (const char*)kin + (size_t)max(ks0 - 3 + r, 0) * (D_ * 2) : (const char*)vin + (size_t)(ks0 + r - 11) * (D_ * 2)) + cb * 160 + c * 16;
      asm volatile("s_mov_b32 m0, %1\n\ts_nop 0\n\tglobal_load_lds_dwordx4 %0, off" :: "v"(kp), "s"(lb + L_KV + wave * KV_P + i * 1024) : "memory");
    }
    if (wave == 5) cwarm();
    SB();
  }
  auto ldsw = [&]() { asm volatile("global_load_dwordx4 %0, %1, %2" : "=&v"(sw) : "v"(kd * 8), "s"(shift_k) : "memory"); SB(); };
  unsigned kk[11], vv[8];
  u32x4 oa, ob;
  auto kvld = [&]() {
    const char* kl = smem + L_KV + c8 * KV_P + min(wave, 4) * 32 + r8 * 4;
#pragma unroll
    for (int i = 0; i < 11; ++i) kk[i] = *(const unsigned*)(kl + i * 160);
#pragma unroll
    for (int i = 0; i < 8; ++i) vv[i] = *(const unsigned*)(kl + (11 + i) * 160);
  };
  auto kvm = [&](int e0, int e1) {
    const f32x2 w0 = {lo16(sw[0]), lo16(sw[2])}, w1 = {hi16(sw[0]), hi16(sw[2])};
    const f32x2 w2 = {lo16(sw[1]), lo16(sw[3])}, w3 = {hi16(sw[1]), hi16(sw[3])};
    f32x2 kf[11], rr[8];
#pragma unroll
    for (int i = 2 * e0; i < 2 * e1 + 3; ++i) kf[i] = unpk((i < 3 && t0 + c8 == 0) ? 0u : kk[i]);
#pragma unroll
    for (int i = 2 * e0; i < 2 * e1; ++i) {
      f32x2 a0 = kf[i] * w3;
      a0 = __builtin_elementwise_fma(kf[i + 1], w2, a0);
      a0 = __builtin_elementwise_fma(kf[i + 2], w1, a0);
      a0 = __builtin_elementwise_fma(kf[i + 3], w0, a0);
      rr[i] = a0 * unpk(vv[i]);
    }
#pragma unroll
    for (int e = e0; e < e1; ++e) { oa[e] = pack2(rr[2 * e][0], rr[2 * e + 1][0]); ob[e] = pack2(rr[2 * e][1], rr[2 * e + 1][1]); }
  };
  auto kvst = [&]() {
    const unsigned ko = (unsigned)((kd * S_ + t0 + c8 * 8) * 2);
    st16(ko, ws + WS_KV, oa);
    st16(ko, ws + WS_KV + S_ * 2, ob);
    SB();
  };
  auto barrier0 = [&](int nv) {
    SB();
    asm volatile("s_waitcnt vmcnt(%0) lgkmcnt(0)\n\ts_barrier" :: "n"(nv) : "memory");
    SB();
    asm volatile("" : "+v"(sw), "+s"(tag));
    tag += 1;
    asm volatile("" : "+s"(tag));
  };
  unsigned pX[2], pW[2];
#pragma unroll
  for (int k = 0; k < 2; ++k) {
    const int q = l15 >> 2, p = lane & 3, sz = (q >> 1) | ((g4 & 1) << 1);
    pX[k] = lb + (wm * 32 + q * 8 + p) * 128 + ((((k ^ ksb) * 4 + g4) ^ (l15 & 7)) << 4);
    pW[k] = lb + ((k ^ ksb) * 32 + g4 * 8 + q) * 128 + (((sz << 1) | (p >> 1)) << 4) + (p & 1) * 8;
  }
  bf16x8 ones;
#pragma unroll
  for (int e = 0; e < 8; ++e) ones[e] = (short)0x3F80;
  f32x4 xm[2], xr[2];
  auto ldxs = [&]() {
#pragma unroll
    for (int i = 0; i < 2; ++i) {
      xm[i] = *(const f32x4*)(smem + L_XM + (wm * 32 + g4 * 8 + i * 4) * 4);
      xr[i] = *(const f32x4*)(smem + L_XR + (wm * 32 + g4 * 8 + i * 4) * 4);
    }
  };
  auto act = [&](const f32x4& a, float c, int i) {
    const f32x2 nc = {-c, -c};
    const f32x2 lo = gelu2(__builtin_elementwise_fma((f32x2){xm[i][0], xm[i][1]}, nc, (f32x2){a[0], a[1]}) * (f32x2){xr[i][0], xr[i][1]});
    const f32x2 hi = gelu2(__builtin_elementwise_fma((f32x2){xm[i][2], xm[i][3]}, nc, (f32x2){a[2], a[3]}) * (f32x2){xr[i][2], xr[i][3]});
    return (f32x4){lo[0], lo[1], hi[0], hi[1]};
  };
  auto pack8 = [&](const f32x4& a, const f32x4& c) { return (u32x4){pack2(a[0], a[1]), pack2(a[2], a[3]), pack2(c[0], c[1]), pack2(c[2], c[3])}; };
  auto z1at = [&](int f0) { return (u32x4*)(smem + L_Z1 + (f0 + l15) * 128 + (((wm * 4 + g4) ^ (l15 & 7)) << 4)); };
  auto z1put = [&](int f0, const u32x4& v) { *z1at(f0) = v; };
  f32x4 ac2[2][2], cs2[2];
  s16x4 VF[10][2][2];
  unsigned aV[2][2];
  auto rdV = [&](auto hc_, auto nc, int r) {
    constexpr int n = decltype(nc)::value, c = 5 * decltype(hc_)::value + (n >> 1), so = (VSLOT[c] - 2) * 8192, po = n < 2 ? -1 : ((VSLOT[n < 2 ? c : c - 1] - 2) * 8192) >> 16;
    const int jj = r >> 1;
    if (!(r & 1)) {
      if ((so >> 16) != po) aV[n & 1][jj] = opq((pW[n & 1] ^ (unsigned)(wl * 64 + jj * 32)) + L_SLOT + 2 * 8192 + (so & ~0xFFFF));
      VF[n][jj][0] = tr_asm<(so & 0xFFFF)>(aV[n & 1][jj]);
    } else VF[n][jj][1] = tr_asm<(so & 0xFFFF) + 512>(aV[n & 1][jj]);
    SB();
  };
  auto mm2 = [&](auto hc_, auto nc, bf16x8 (*X)[2][2], int nw, auto&& nxt) {
    constexpr int n = decltype(nc)::value, j = n >> 1, k = n & 1;
    constexpr bool z = n == 0 && !decltype(hc_)::value;
    const bf16x8 w0 = join8(VF[n][0][0], VF[n][0][1]), w1 = join8(VF[n][1][0], VF[n][1][1]);
    if (k == 0) { MFMAW<n == 0>(nw, cs2[0], ones, w0); MFMAW<n == 0>(-1, cs2[1], ones, w1); }
    MFMAW<z>(k ? nw : -1, ac2[0][0], X[j][k][0], w0); nxt(0);
    MFMAW<z>(-1, ac2[1][0], X[j][k][1], w0); nxt(1);
    MFMAW<z>(-1, ac2[0][1], X[j][k][0], w1); nxt(2);
    MFMAW<z>(-1, ac2[1][1], X[j][k][1], w1); nxt(3);
    if constexpr (n == 9) {
      pin(cs2[0], cs2[1]);
      if (g4 < 2) *(float*)(smem + L_CS2 + (wave * 32 + lane) * 4) = g4 ? cs2[1][0] : cs2[0][0];
    }
    SB();
  };
  const unsigned p2a = lb + L_P2 + cw * 4096 + lin;
  auto sttag = [&](const char* p) {
    asm volatile("s_mov_b64 exec, 1\n\tglobal_store_dwordx2 %0, %1, %2 sc1\n\ts_mov_b64 exec, -1" :: "v"(lin), "v"((u32x2){opq((unsigned)tag), opq((unsigned)(tag >> 32))}), "s"(p) : "memory");
  };
  auto ldtag = [&](u32x2& f, unsigned vo, const char* p) { asm volatile("global_load_dwordx2 %0, %1, %2 sc1" : "=&v"(f) : "v"(vo), "s"(p) : "memory"); };
  auto notag = [&](const u32x2& f) { return __builtin_amdgcn_ballot_w64(f[0] != (unsigned)tag || f[1] != (unsigned)(tag >> 32)) != 0; };
  auto waittag = [&](u32x2& f, unsigned vo, const char* p) {
    while (__builtin_expect(notag(f) && tmo > 0, 0)) {
      --tmo;
      __builtin_amdgcn_s_sleep(4);
      ldtag(f, vo, p);
      wait_vm<0>(f);
    }
  };
  u32x2 ef;

  char* const hf = ws + WS_H;
  bf16x8 Wq[15];
  const char* wg = ws + WS_WO + (size_t)(wave * 120 + cb * 5) * 1024;
  auto ldW = [&](int n0, int n1) {
    if (n0 == 0) ldtag(ef, (unsigned)((lane & 3) * 8), ws + WS_LF + (rb * 8 + wave) * 32);
#pragma unroll
    for (int n = n0; n < n1; ++n)
      asm volatile("global_load_dwordx4 %0, %1, %2 offset:%3 sc1" : "=&v"(Wq[n]) : "v"(lin), "s"(wg + (n / 5) * 40960 + 2048), "i"((n % 5) * 1024 - 2048) : "memory");
    SB();
  };
  f32x4 ac3[5];
  auto gemm3 = [&](auto khc, auto&& pre) {
    constexpr int kh = decltype(khc)::value;
    unsigned hoff[4];
#pragma unroll
    for (int it = 0; it < 4; ++it) hoff[it] = (unsigned)(((rb * 4 + ((cw + 1 + it) & 3)) * 24 + 3 * wave) * 1024) + lin;
    u32x4 Hq[4][3];
    auto ldh1 = [&](int it, int k) {
      asm volatile("global_load_dwordx4 %0, %1, %2 offset:%3 " HSC : "=&v"(Hq[it][k]) : "v"(hoff[it]), "s"(hf), "i"(k * 1024) : "memory");
    };
    waittag(ef, (unsigned)((lane & 3) * 8), ws + WS_LF + (rb * 8 + wave) * 32);
    AB(18);
    if (!kh) signal1(flg + 48);
    pre();
    flag_wait_asm(flg + 48, 4u);
    AB(19);
    __builtin_amdgcn_s_setprio(3);
    ldh1(0, 0); ldh1(0, 1); ldh1(0, 2);
    __builtin_amdgcn_s_setprio(2);
    ldh1(1, 0); ldh1(1, 1); ldh1(1, 2);
    __builtin_amdgcn_s_setprio(0);
    SB();
    flag_wait_asm(flg + 64, 4u);
    const unsigned px = lb + (kh ? P3_C + cw * 15360 : P3_G + cw * 20480) + lin;
    f32x4 pa[4][5], ae[5];
    auto put = [&](int it, int j) { asm volatile("ds_write_b128 %0, %1 offset:%2" :: "v"(px), "v"((it & 1) ? ac3[j] : ae[j]), "i"((it * 5 + j) * 1024) : "memory"); };
    auto fetch = [&](int n, int g, int q) {
      const int ms = (cw - 1 - q) & 3;
      const unsigned pn = lb + (g ? P3_C + ms * 15360 : P3_G + ms * 20480) + lin;
#pragma unroll
      for (int j = 0; j < 5; ++j) asm volatile("ds_read_b128 %0, %1 offset:%2" : "=&v"(pa[n][j]) : "v"(pn), "i"((q * 5 + j) * 1024) : "memory");
      SB();
    };
    sfor<0, 4>([&](auto itc) {
      constexpr int it = decltype(itc)::value;
      wait_vm<(it < 3 ? 3 : 0)>(Hq[it][0], Hq[it][1], Hq[it][2]);
      if constexpr (it == 0) pin(Wq[0], Wq[1], Wq[2], Wq[3], Wq[4], Wq[5], Wq[6], Wq[7], Wq[8], Wq[9], Wq[10], Wq[11], Wq[12], Wq[13], Wq[14]);
      auto bad = [&]() { return __builtin_amdgcn_ballot_w64(umx(umx(Hq[it][0][3], Hq[it][1][3]), Hq[it][2][3]) == HOLE) != 0; };
      if (__builtin_expect(bad(), 0)) {
        do {
          --tmo;
          asm volatile("s_sleep 2\n\tglobal_load_dwordx4 %0, %3, %4 sc1\n\tglobal_load_dwordx4 %1, %3, %4 offset:1024 sc1\n\t"
                       "global_load_dwordx4 %2, %3, %4 offset:2048 sc1\n\ts_waitcnt vmcnt(0)"
                       : "+v"(Hq[it][0]), "+v"(Hq[it][1]), "+v"(Hq[it][2]) : "v"(hoff[it]), "s"(hf) : "memory");
        } while (bad() && tmo > 0);
      }
      SB();
#pragma unroll
      for (int k = 0; k < 3; ++k)
#pragma unroll
        for (int j = 0; j < 5; ++j) {
          f32x4& ac = (it & 1) ? ac3[j] : ae[j];
          if (k == 0) MFMA0(ac, Wq[j], Hq[it][0]); else MFMA(ac, Wq[k * 5 + j], Hq[it][k]);
          if constexpr (it + 2 < 4) { if (k == 0 && j < 3) ldh1(it + 2 < 4 ? it + 2 : 0, j); }
          const int m = k * 5 + j;
          if (it > 0 && m < 10 && !(m & 1)) put(it - 1, m >> 1);
          if (it > 0 && m == 8) signal1(flg + 160 + (kh ? 1 + (it > 1) : it == 3) * 16 + ((cw + it) & 3) * 4);
          if (it == 3 && m == FE3 && !kh) { flag_wait_asm(flg + 160 + wave * 4, 2u); fetch(0, 0, 0); fetch(1, 0, 1); }
          SB();
        }
      if constexpr (it == 3) {
        if (kh) {
          flag_wait_asm(flg + 192 + cw * 4, 3u);
          fetch(0, 1, 1); fetch(1, 1, 2); fetch(2, 0, 3);
#pragma unroll
          for (int q = 0; q < 3; ++q) {
            asm volatile("s_waitcnt lgkmcnt(%5)" : "+v"(pa[q][0]), "+v"(pa[q][1]), "+v"(pa[q][2]), "+v"(pa[q][3]), "+v"(pa[q][4]) : "i"(10 - 5 * q) : "memory");
            if (q == 0) pin(ac3[0], ac3[1], ac3[2], ac3[3], ac3[4]);
#pragma unroll
            for (int j = 0; j < 5; ++j) ac3[j] += pa[q][j];
            SB();
          }
        } else {
          flag_wait_asm(flg + 176 + cw * 4, 2u);
          fetch(2, 1, 0); fetch(3, 0, 2);
          pin(pa[0][0], pa[0][1], pa[0][2], pa[0][3], pa[0][4], pa[1][0], pa[1][1], pa[1][2], pa[1][3], pa[1][4], ac3[0], ac3[1], ac3[2], ac3[3], ac3[4]);
#pragma unroll
          for (int j = 0; j < 5; ++j) ac3[j] += pa[0][j] + pa[1][j];
          SB();
          wait_lg<0>(pa[2][0], pa[2][1], pa[2][2], pa[2][3], pa[2][4], pa[3][0], pa[3][1], pa[3][2], pa[3][3], pa[3][4]);
#pragma unroll
          for (int j = 0; j < 5; ++j) {
            ac3[j] += pa[2][j] + pa[3][j];
            put(3, j);
          }
          signal1(flg + 192 + cw * 4);
        }
      }
      SB();
      if constexpr (it == 0) AB(20);
      if constexpr (it == 2) AB(21);
    });
    AB(22);
  };
  if (wave >= 4) {
    const int wid = rb * 4 + cw;
    __builtin_amdgcn_s_setprio(3);
    ldsw();
    if (wid < 120) dma16((unsigned)((lane >> 1) * (D_ * 2) + (lane & 1) * 16), w_out + (size_t)(wid / 5) * 32 * D_ + d0 + (wid % 5) * 16, lb + L_WO + cw * 1024);
    SB();
    u32x4 hole = {HOLE, HOLE, HOLE, HOLE};
    bf16x8 XF[5][2][2];
    auto rdXF = [&](auto cc) {
      constexpr int c = decltype(cc)::value, so = L_SLOT + XSLOT[c] * 8192;
#pragma unroll
      for (int k = 0; k < 2; ++k) {
        const unsigned a = pX[k] + (so & ~0x7FFF);
        ds16<(so & 0x7FFF)>(XF[c][k][0], a);
        ds16<(so & 0x7FFF) + 512>(XF[c][k][1], a);
      }
      SB();
    };
    u32x4 TA[16];
    auto ldT = [&](u32x4& r, int d, int w, int i) {
      asm volatile("global_load_dwordx4 %0, %1, %2" : "=&v"(r) : "v"(aoff[i & 1] + (unsigned)(((d + rot2) & 7) * 128)), "s"(gS + (((w & 2) + 2 + (i >> 1)) & 3) * 65536 + w * 1024) : "memory");
    };
    auto ldx = [&](int c) {
      u32x4 xv;
      asm volatile("global_load_dwordx4 %0, %1, %2\n\ts_waitcnt vmcnt(0)" : "=&v"(xv) : "v"((unsigned)(lane * (D_ * 2) + c * 16)), "s"(x + (size_t)t0 * D_) : "memory");
      return xv;
    };
    const bool docv = cw < ((rb & 1) ? 2 : 3);
    const int d = d0 + (cw < 2 ? 2 * rb + cw : 64 + (rb >> 1));
    u32x4 hc[4], uc[4];
    const unsigned clin = docv ? lin : 0u;
    unsigned Dw;
    auto ldhc = [&]() {
#pragma unroll
      for (int k = 0; k < 4; ++k) asm volatile("global_load_dwordx4 %0, %1, %2 offset:%3" : "=&v"(hc[k]) : "v"(clin), "s"(ssm_k + (size_t)d * S_), "i"(k * 1024) : "memory");
      asm volatile("global_load_ushort %0, %1, %2" : "=&v"(Dw) : "v"(0u), "s"(ssm_D + d) : "memory");
    };
    unsigned rwo;
    f32x2 ra, rc;
    auto rdraw = [&]() { asm volatile("ds_read_b64 %0, %2\n\tds_read_b64 %1, %2 offset:512" : "=&v"(ra), "=&v"(rc) : "v"(rwo) : "memory"); SB(); };
    const char* efp = ws + WS_EF + cb * 256;
    char* cb_ = smem + M_CV + cw * CV_BUF;
    auto run = [&](auto ic) {
      constexpr int i = decltype(ic)::value, p = cnt('_', i) + 1;
      constexpr char c = SCH[i];
      if constexpr (c == '_') {
        CB(3 * (p - XBAR));
        asm volatile("s_waitcnt vmcnt(%0) lgkmcnt(0)" :: "n"(behind(1, i, '_')) : "memory");
        CB(3 * (p - XBAR) + 1);
        asm volatile("s_barrier" ::: "memory");
        CB(3 * (p - XBAR) + 2);
        SB();
#pragma unroll
        for (int q = 0; q < 5; ++q) if (XNEED[q] + 1 == p) pin(XF[q][0][0], XF[q][0][1], XF[q][1][0], XF[q][1][1]);
        if constexpr (p == 7) {
          rwo = lb + L_RAW + ((lane >> 3) * 4 + (lane & 3)) * 16 + (lane & 4) * 2;
          pin(rwo);
        }
        if constexpr (p == 9) __builtin_amdgcn_s_setprio(CPR);
        if constexpr (p == 2) AB(4);
        if constexpr (p == 6) AB(5);
        if constexpr (p == 9) AB(6);
      }
      if constexpr (c == 'u') issue(IC(cnt('u', i)));
      if constexpr (c == 'x') rdXF(IC(cnt('x', i)));
      if constexpr (c == 'B') {
        AB(1);
        barrier0(behind(1, i, 'B'));
        pin(ones, hole);
      }
      if constexpr (c == 'v') sfor<2 * cnt('v', i), 2 * cnt('v', i) + 2>([&](auto nc) { rdV(IC(0), nc, 0); rdV(IC(0), nc, 1); rdV(IC(0), nc, 2); rdV(IC(0), nc, 3); });
      if constexpr (c == 'm') {
        constexpr int n = cnt('m', i);
        mm2(IC(0), IC(n), XF, VNEED[n >> 1] < p - 1 || (n & 1) ? -1 : 0, [](int) {});
      }
      if constexpr (c == 'p') {
        asm volatile("s_nop 7" : "+v"(ac2[0][0]), "+v"(ac2[0][1]), "+v"(ac2[1][0]), "+v"(ac2[1][1]));
        dsw16<0>(p2a, ac2[0][0]); dsw16<1024>(p2a, ac2[1][0]); dsw16<2048>(p2a, ac2[0][1]); dsw16<3072>(p2a, ac2[1][1]);
      }
      if constexpr (c == 'w')
        touch((unsigned)(min(lane, 19) * (F2_ * 2)), (const char*)w_in + (size_t)(((rb + WSH) & 7) * 80 + cw * 20) * (F2_ * 2) + pr * 384 + odd * 256, lb + L_DUM);
      if constexpr (c == 'l') { if (wave == 7) kvld(); }
      if constexpr (c == 'k') { if (wave == 7) { kvm(0, 4); kvst(); } }
      if constexpr (c == 'E' || c == 'D') { if (wave == 5) sttag(ws + WS_EF + (c == 'D' ? 2048 : 0) + (cb * 32 + rb) * 8); }
      if constexpr (c == 'U') {
        wait_vm<behind(1, i, 'F')>(ef);
        waittag(ef, (unsigned)((lane & 31) * 8), efp);
#pragma unroll
        for (int k = 0; k < 4; ++k) asm volatile("global_load_dwordx4 %0, %1, %2 offset:%3 sc1" : "=&v"(uc[k]) : "v"(clin), "s"(ws + WS_KV + (size_t)d * (S_ * 2)), "i"(k * 1024) : "memory");
      }
      if constexpr (c == 'F') ldtag(ef, (unsigned)((lane & 31) * 8), efp);
      if constexpr (c == 'o') {
        if (wid < 120) {
          const unsigned ra = lb + L_WO + cw * 1024 + (8 * g4 + (l15 >> 2)) * 32 + 8 * (lane & 3);
          s16x4 lo = tr_asm<0>(ra), hi = tr_asm<128>(ra);
          wait_lg<0>(lo, hi);
          st16(lin, ws + WS_WO + (size_t)((wid / 5) * 40 + cb * 5 + wid % 5) * 1024, __builtin_bit_cast(u32x4, join8(lo, hi)));
        }
      }
      if constexpr (c == 'h') {
        constexpr int n = cnt('h', i);
        st16(lin, ws + (n < 3 ? WS_Z2 + (size_t)((rb * 2 + wl) * 48 + cb * 6 + wm * 3 + n) * 1024 : WS_H + (size_t)((rb * 4 + cw) * 24 + cb * 3 + n - 3) * 1024), hole);
      }
      if constexpr (c == 't') { constexpr int n = cnt('t', i); ldT(TA[n], 4 + (n >> 3), cw, n & 7); }
      if constexpr (c == 'H') ldhc();
      if constexpr (c == 'R') rdraw();
      if constexpr (c == 'V') {
        wait_vm<behind(1, i, '_')>();
        signal1(flg + 60);
        AB(2);
      }
      if constexpr (c == 'I') {
        wait_vm<behind(1, i, 'H')>(hc[0], hc[1], hc[2], hc[3], Dw);
        if (docv) {
          if (lane < 36) *(u32x4*)(cb_ + (lane < 32 ? lane * 16 : CV_C + ((lane >> 1) & 1) * CV_CS + 4096 + (lane & 1) * 16)) = (u32x4){0u, 0u, 0u, 0u};
#pragma unroll
          for (int k = 0; k < 4; ++k) {
            const int a = k * 64 + lane;
            const int h0 = k ? __builtin_amdgcn_readlane((int)hc[k ? k - 1 : 0][3], 63) : 0;
            const unsigned hp = (unsigned)__builtin_amdgcn_update_dpp(h0, (int)hc[k][3], 0x138, 0xF, 0xF, false);
            const unsigned w[5] = {hp, hc[k][0], hc[k][1], hc[k][2], hc[k][3]};
            u32x4 sw, mx;
#pragma unroll
            for (int q = 0; q < 4; ++q) {
              sw[q] = (w[4 - q] >> 16) | (w[4 - q] << 16);
              mx[q] = (w[4 - q] & 0xFFFFu) | (w[3 - q] & 0xFFFF0000u);
            }
            char* cp = cb_ + CV_C + (2040 - 8 * a) * 2;
            *(u32x4*)(cp) = sw;
            *(u32x4*)(cp + CV_CS) = mx;
          }
        }
      }
      if constexpr (c == 'J') {
        wait_vm<behind(1, i, 'U')>(uc[0], uc[1], uc[2], uc[3]);
        if (docv) {
#pragma unroll
          for (int k = 0; k < 4; ++k) *(u32x4*)(cb_ + 512 + (k * 64 + lane) * 16) = uc[k];
        }
      }
      if constexpr (c == 'S') {
        wait_lg<0>(ra, rc);
        if (wave == 6) {
          const float s1 = nv(ra[0]) + rc[0], fm = s1 * (1.0f / 640.0f), sm = s1 * fm;
          float fq = (ra[1] - sm) + rc[1];
          if (__builtin_expect(__builtin_amdgcn_ballot_w64(!(fq >= 1e-3f * sm)) != 0, 0)) {
            const f32x2 mm = {fm, fm};
            f32x2 q2 = {0.f, 0.f};
#pragma unroll 1
            for (int c = 0; c < 80; ++c) {
              const u32x4 xv = ldx(c);
#pragma unroll
              for (int e = 0; e < 4; ++e) { const f32x2 d = unpk(xv[e]) - mm; q2 = __builtin_elementwise_fma(d, d, q2); }
            }
            fq = q2[0] + q2[1];
          }
          const float vbf = rbf(fmaxf(fq, 0.f) * (1.0f / 640.0f));
          *(float*)(smem + L_XM + lane * 4) = rbf(fm);
          *(float*)(smem + L_XR + lane * 4) = __builtin_amdgcn_rcpf(rbf(__builtin_amdgcn_sqrtf(rbf(vbf + 1e-5f))));
        }
      }
      SB();
    };
    sfor<0, NSCH>(run);
    __builtin_amdgcn_s_setprio(0);
    AB(9);
    auto ldS = [&](auto dc, auto wc) {
      constexpr int dd = decltype(dc)::value, w = decltype(wc)::value;
      const unsigned to = (unsigned)(((dd + rot2) & 7) * 128);
      if (THR < 60) wait_vm<THR>();
      dma32s<M_RING + w * 24576 + (2 - dd) * 8192, (((w & 2) + 2) & 3) * 65536 + w * 1024>(aoff[0] + to, aoff[1] - 1024 + to, gS, lb);
      if (THR < 60) wait_vm<THR>();
      dma32s<M_RING + w * 24576 + (2 - dd) * 8192 + 2048, (((w & 2) + 3) & 3) * 65536 + w * 1024>(aoff[0] + to, aoff[1] - 1024 + to, gS, lb);
      if (THR < 60) wait_vm<THR>();
      dma32s<M_RING + w * 24576 + (2 - dd) * 8192 + 4096, (w & 2) * 65536 + w * 1024>(aoff[0] + to, aoff[1] - 1024 + to, gS, lb);
      if (THR < 60) wait_vm<THR>();
      dma32s<M_RING + w * 24576 + (2 - dd) * 8192 + 6144, ((w & 2) + 1) * 65536 + w * 1024>(aoff[0] + to, aoff[1] - 1024 + to, gS, lb);
      SB();
    };
    auto epi2 = [&]() {
      flag_wait_asm<SLP>(flg + 132, 4u);
#pragma unroll
      for (int jj = 0; jj < 2; ++jj) {
        u32x4* zp = z1at(odd * 32 + wl * 32 + jj * 16);
        u32x4 o = *zp;
#pragma unroll
        for (int q = 0; q < 4; ++q) o[q] = pack2s(gelu1(nv(lo16(o[q]))), gelu1(hi16(o[q])));
        *zp = o;
      }
      signal1(flg + 56);
      SB();
    };
    auto putT = [&](const u32x4* T, unsigned a, unsigned f) {
#pragma unroll
      for (int i = 0; i < 8; ++i) asm volatile("ds_write_b128 %0, %1 offset:%2" :: "v"(a), "v"(T[i]), "i"(i * 1024) : "memory");
      signal1(f);
    };
    const unsigned tx = lb + M_CV + cw * CV_BUF + lin;
    auto landT = [&]() {
      sttag(ws + WS_LF + (rb * 8 + cb) * 32 + cw * 8);
      putT(TA, tx, flg + 112 + wl * 4);
    };
    auto putT4 = [&]() {
      flag_wait2(flg + 96 + wl * 8, 1u);
      putT(TA + 8, lb + M_RING + cw * 24576 + 8192 + lin, flg + 120 + wl * 4);
    };
    if (docv) {
      flag_wait_asm(flg + 36, 4u);
      AB(12);
      f32x4 ac[8];
      conv_core(lds_addr(cb_), lane, Dw, ac);
      AB(13);
      signal1(flg + 40);
      wait_vm<0>();
      landT();
#pragma unroll
      for (int k = 0; k < 4; ++k) {
        u32x2 ov[2];
#pragma unroll
        for (int t = 0; t < 2; ++t)
#pragma unroll
          for (int h = 0; h < 2; ++h) ov[t][h] = pack2(ac[2 * k + t][2 * h], ac[2 * k + t][2 * h + 1]);
        const auto s0 = __builtin_amdgcn_permlane16_swap(ov[0][0], ov[1][0], false, false);
        const auto s1 = __builtin_amdgcn_permlane16_swap(ov[0][1], ov[1][1], false, false);
        st16((unsigned)((256 * (g4 & 1) + 16 * l15 + 8 * (g4 >> 1)) * 2), ws + WS_Y + (size_t)d * (S_ * 2) + k * 1024, (u32x4){s0[0], s1[0], s0[1], s1[1]});
      }
      wait_vm<0>();
      sttag(ws + WS_YF + d * 8);
      epi2();
      putT4();
    } else if (cw != 3) {
      wait_vm<0>();
      landT();
      epi2();
      putT4();
    } else {
      ldS(IC(0), IC(0)); ldS(IC(0), IC(1)); ldS(IC(0), IC(2)); ldS(IC(1), IC(1)); ldS(IC(2), IC(1));
      wait_vm<40>();
      landT();
      AB(12);
      flag_wait_asm(flg + 16, 4u);
      AB(8);
      wait_vm<24>();
      ldS(IC(0), IC(3)); ldS(IC(1), IC(0)); ldS(IC(1), IC(2)); ldS(IC(1), IC(3));
      wait_vm<24>();
      signal1(flg + 32);
      ldS(IC(2), IC(0)); ldS(IC(2), IC(2)); ldS(IC(2), IC(3));
      ldtag(ef, (unsigned)((lane & 1) * 8), ws + WS_XF + (rb * 4 + pr) * 16);
      AB(10);
      u32x4 TB[32];
      auto ring2 = [&]() { dsw4(flg + 224 + (lane & 1) * 4, __uint_as_float(2u)); };
#pragma unroll
      for (int n = 0; n < 32; ++n) {
        if (n == 8) wait_vm<32>();
        if (THR < 60 && !(n & 1)) wait_vm<THR>();
        if (THR < 32 && n == THR) ring2();
        ldT(TB[n], 6, n >> 3, n & 7);
      }
      wait_vm<32>(ef);
      if (THR >= 32) ring2();
      AB(11);
      if (!odd) {
        u32x4 zx[4];
        waittag(ef, (unsigned)((lane & 1) * 8), ws + WS_XF + (rb * 4 + pr) * 16);
#pragma unroll
        for (int c = 0; c < 4; ++c)
          asm volatile("global_load_dwordx4 %0, %1, %2 offset:%3 sc1" : "=&v"(zx[c]) : "v"(lin), "s"(ws + WS_ZX + (size_t)(rb * 4 + pr) * 4096), "i"(c * 1024) : "memory");
        wait_vm<0>(zx[0], zx[1], zx[2], zx[3]);
#pragma unroll
        for (int c = 0; c < 4; ++c) *(u32x4*)(smem + L_Z1 + (64 + (c & 1) * 16 + l15) * 128 + ((((c >> 1) * 4 + g4) ^ (l15 & 7)) << 4)) = zx[c];
      }
      wait_vm<0>();
      epi2();
      signal1(flg + 56);
      putT4();
#pragma unroll
      for (int w = 0; w < 4; ++w) {
        if (!(w & 1)) flag_wait2(flg + 96 + w * 4, 2u);
        putT(TB + 8 * w, lb + M_RING + w * 24576 + lin, flg + 144 + (w >> 1) * 4);
      }
      AB(13);
    }
    SB();
    wait_vm<0>();
    AB(14);
    u32x2 xv[5];
    f32x2 sv[5][2];
    flag_wait_asm<SLP>(flg + 40, (rb & 1) ? 2u : 3u);
    const int yc = min(cw * 24, 56);
    const unsigned yfo = (unsigned)(min(lane, 23) * 8);
    const char* yfp = ws + WS_YF + (d0 + yc) * 8;
    auto ldy = [&]() {
#pragma unroll
      for (int n = 0; n < 3; ++n) {
        const int q = (yc >> 3) + n;
        asm volatile("s_mov_b32 m0, %2\n\ts_nop 0\n\tglobal_load_lds_dwordx4 %0, %1 sc1" :: "v"((unsigned)(r8 * (S_ * 2) + ((c8 ^ (r8 & 6)) << 4))),
                     "s"(ws + WS_Y + ((size_t)(d0 + q * 8) * S_ + t0) * 2), "s"(lb + yat(0) + (q >> 2) * CV_BUF + (q & 3) * 1024) : "memory");
      }
    };
    unsigned xo[3], oo[2] = {(unsigned)(l15 * D_ * 2 + 32 * (g4 & 1) + 16 * (g4 >> 1)), (unsigned)(l15 * D_ * 2 + 8 * g4)};
    const u16* op = out + (size_t)(t0 + cw * 16) * D_ + d0;
    pin(oo[0], oo[1]);
    asm volatile("" : "+s"(op));
#pragma unroll
    for (int n = 0; n < 3; ++n) { const int id = min(n * 64 + lane, 159); xo[n] = opq((unsigned)((id / 10) * (D_ * 2) + (id % 10) * 16)); }
    flag_wait2(flg + 96 + wl * 8, 3u);
    {
      const unsigned to = (unsigned)(((7 + rot2) & 7) * 128);
      for (int w = cw; w < (cw < 2 ? cw + 1 : 4 - (cw & 1)); ++w)
        sfor<0, 4>([&](auto ic) {
          constexpr int i = decltype(ic)::value;
          dma32s<M_RING + 16384 + i * 2048, 0>(aoff[0] + to, aoff[1] - 1024 + to, gS + ((2 * wl + 2 + i) & 3) * 65536 + w * 1024, lb + w * 24576);
          if (T7D) __builtin_amdgcn_s_sleep(T7D);
        });
    }
    flag_wait2(flg + 96 + wl * 8, 4u);
    ldtag(ef, yfo, yfp);
    wait_vm<1>();
    signal1(flg + 152 + wl * 4, cw < 2 ? 1u : 2u - 2u * (cw & 1));
    wait_vm<0>(ef);
    flag_wait_asm<SLP / 2 + 1>(flg + 44, 4u);
    waittag(ef, yfo, yfp);
    AB(15);
    ldy();
    static_assert(SGS >= 10, "");
#pragma unroll
    for (int n = 0; n < 3; ++n) dma16(xo[n], x + (size_t)(t0 + cw * 16) * D_ + d0, lb + M_RING + cw * 24576 + 8192 + n * 1024);
    ldW(0, NWY);
    wait_vm<NWY>(ef);
    AB(17);
    signal1(flg + 52);
    ldW(NWY, 15);
    gemm3(IC(1), [&]() {
      flag_wait_asm(flg + 52, 4u);
      const int yr_ = 4 * g4 + (l15 >> 2);
      const unsigned ya = lb + yr_ * 128 + (((cw * 2 + ((lane >> 1) & 1)) ^ (yr_ & 6)) << 4) + (lane & 1) * 8;
      const float nm = -*(const float*)(smem + L_XM + (cw * 16 + l15) * 4), sr = *(const float*)(smem + L_XR + (cw * 16 + l15) * 4);
      s16x4 yr[5] = {tr_asm<yat(0)>(ya), tr_asm<yat(1)>(ya), tr_asm<yat(2)>(ya), tr_asm<yat(3)>(ya), tr_asm<yat(4)>(ya)};
#pragma unroll
      for (int j = 0; j < 5; ++j) asm volatile("ds_read_b64 %0, %1 offset:%2" : "=&v"(xv[j]) : "v"(lb + M_RING + cw * 24576 + 8192 + l15 * 160 + g4 * 8), "i"(j * 32) : "memory");
      signal1(flg + 64);
      wait_lg<0>(xv[0], xv[1], xv[2], xv[3], xv[4], yr[0], yr[1], yr[2], yr[3], yr[4]);
#pragma unroll
      for (int j = 0; j < 5; ++j) {
        const u32x2 yw = __builtin_bit_cast(u32x2, yr[j]);
#pragma unroll
        for (int h = 0; h < 2; ++h) {
          sv[j][h] = ((unpk(xv[j][h]) + (f32x2){nm, nm}) * (f32x2){sr, sr}) * unpk(yw[h]);
          pin(sv[j][h]);
        }
      }
      SB();
      AB(16);
    });
    unsigned pk[5][2];
#pragma unroll
    for (int j = 0; j < 5; ++j) {
#pragma unroll
      for (int h = 0; h < 2; ++h) {
        const f32x2 o2 = {ac3[j][2 * h], ac3[j][2 * h + 1]};
        const f32x2 a = o2 * (f32x2){-1.4426950f, -1.4426950f};
        const f32x2 e = (f32x2){__builtin_amdgcn_exp2f(a[0]), __builtin_amdgcn_exp2f(a[1])} + (f32x2){1.0f, 1.0f};
        const f32x2 r = {__builtin_amdgcn_rcpf(e[0]), __builtin_amdgcn_rcpf(e[1])};
        pk[j][h] = pk2(__builtin_elementwise_fma(o2 * sv[j][h], r, unpk(xv[j][h])));
      }
      if (j & 1) {
        const auto s0 = __builtin_amdgcn_permlane16_swap(pk[j - 1][0], pk[j][0], false, false);
        const auto s1 = __builtin_amdgcn_permlane16_swap(pk[j - 1][1], pk[j][1], false, false);
        asm volatile("global_store_dwordx4 %0, %1, %2 offset:%3\n\ts_nop 1" :: "v"(oo[0]), "v"((u32x4){s0[0], s1[0], s0[1], s1[1]}), "s"(op), "i"(32 * (j - 1)) : "memory");
        SB();
      }
    }
    asm volatile("global_store_dwordx2 %0, %1, %2 offset:128" :: "v"(oo[1]), "v"((u32x2){pk[4][0], pk[4][1]}), "s"(op) : "memory");
  } else {
    ldsw();
    sfor<0, pos('B')>([&](auto ic) { if constexpr (SCH[ic] == 'g') issue(IC(cnt('u', ic))); });
    if (tid < 64) *(unsigned*)(smem + L_FLAG + tid * 4) = 0u;
    pin(ones);
    AB(1);
    barrier0(behind(0, pos('B'), 'B'));
    kvld();
    kvm(0, 4);
    kvst();
    const bool isz2 = !(odd && wl);
    const char* stp = ws + WS_ST + (size_t)((rb * 12 + pr * 3 + odd * 2 + wl) * 1024 + wm * 512);
    if (isz2) st8(lin >> 1, stp, (u32x2){0u, ~(unsigned)tag});
    const char* Ab[4];
#pragma unroll
    for (int i = 0; i < 4; ++i) Ab[i] = (const char*)w_sp + (size_t)(t0 + ((2 * wl + 2 + i) & 3) * 16) * (S_ * 2) + wave * 1024 + 4096 - i * 2048;
    const unsigned ringl = lb + M_RING + wave * 24576;
    unsigned av[2], xl = ringl + 16384 + 4096;
    unsigned bsv[2];
    auto preact = [&]() {
      flag_wait_asm(flg + 20 + wl * 4, 2u);
      const float* cp2 = (const float*)(smem + L_CS2 + (wl * 64 + l15) * 4);
      pin(ac2[0][0], ac2[0][1], ac2[1][0], ac2[1][1]);
#pragma unroll
      for (int jj = 0; jj < 2; ++jj) {
        const float nct = -add1(add1(cp2[jj * 16], cp2[32 + jj * 16]), add1(cp2[128 + jj * 16], cp2[160 + jj * 16]));
        u32x4 o;
#pragma unroll
        for (int q = 0; q < 4; ++q) {
          const int i = q >> 1, e = (q & 1) * 2;
          o[q] = pack2(mul1(fma1(xm[i][e], nct, ac2[i][jj][e]), xr[i][e]), mul1(fma1(xm[i][e + 1], nct, ac2[i][jj][e + 1]), xr[i][e + 1]));
        }
        z1put(odd * 32 + wl * 32 + jj * 16, o);
      }
      signal1(flg + 132);
      SB();
    };
    {
      f32x4 zv[2][4], cs[4];
      s16x4 WB[20][4][2];
      bf16x8 XG[10][2][2];
      unsigned aX[2][2], aW[2][2][4];
#pragma unroll
      for (int k = 0; k < 2; ++k)
#pragma unroll
        for (int w = 0; w < 2; ++w) {
          aX[w][k] = pX[k] + L_SLOT + XW * 8192 + w * 65536;
          pin(aX[w][k]);
#pragma unroll
          for (int jj = 0; jj < 4; ++jj) { aW[w][k][jj] = (pW[k] ^ (unsigned)(jj * 32)) + L_SLOT + wl * 8192 + w * 65536; pin(aW[w][k][jj]); }
        }
      auto rd1 = [&](auto nc, int r) {
        constexpr int n = decltype(nc)::value, j = n >> 1, k = n & 1, c = n > 0 && n < 11 ? n - 1 : 0, sx = (XSLOT[c] - XW) * 8192, sa = WSLOT[j] * 8192;
        if (r < 0) { if (r & 1) ds16<(sx & 0xFFFF) + 512>(XG[c][(r + 4) >> 1][1], aX[sx >> 16][(r + 4) >> 1]); else ds16<(sx & 0xFFFF)>(XG[c][(r + 4) >> 1][0], aX[sx >> 16][(r + 4) >> 1]); }
        else if (r & 1) WB[n][r >> 1][1] = tr_asm<(sa & 0xFFFF) + 512>(aW[sa >> 16][k][r >> 1]);
        else WB[n][r >> 1][0] = tr_asm<(sa & 0xFFFF)>(aW[sa >> 16][k][r >> 1]);
        SB();
      };
      auto bar = [&](auto bc) {
        SB();
        constexpr int p = decltype(bc)::value;
        CB(3 * (p - XBAR));
        if constexpr (p == 1) asm volatile("s_waitcnt vmcnt(%0) lgkmcnt(0)\n\ts_barrier" :: "n"(behind(0, pos('_', p - 1), '_')) : "memory");
        else if constexpr (p <= GZ) asm volatile("s_waitcnt vmcnt(%0) lgkmcnt(8)\n\ts_barrier" :: "n"(behind(0, pos('_', p - 1), '_')) : "memory");
        else if constexpr (p >= 8) asm volatile("s_waitcnt lgkmcnt(0)\n\ts_barrier" ::: "memory");
        else if constexpr (p < 7) asm volatile("s_waitcnt lgkmcnt(8)\n\ts_barrier" ::: "memory");
        else asm volatile("s_barrier" ::: "memory");
        CB(3 * (p - XBAR) + 2);
        SB();
      };
      float cp[4];
      f32x4 st[2][2], rv;
      const unsigned rwa = lb + L_RAW + (ksb * 2 + wm) * 256 + l15 * 16 + ((l15 >> 2) == g4 ? 0 : 1024);
      const unsigned csa = lb + L_CS + (wave * 64 + lane) * 4;
      auto kstep = [&](auto nc) {
        constexpr int n = decltype(nc)::value, j = n >> 1, k = n & 1, nn = n + 1, r0 = nn >= 2 && nn <= 10 ? -4 : 0;
        constexpr int sc = n == 1 ? 0 : n - 2, ns = !k ? 0 : (n == 1 || n == 11 ? 1 : 2);
        if constexpr (k) bar(IC(nn / 2 + 1));
        int r = r0, c = 0;
        auto nxt = [&]() { if (r < 8) { rd1(IC(nn), r++); ++c; } };
        auto sel = [&](int q) {
          const f32x4& sq = st[q & 1][q >> 1];
          rv[q] = sel4(sq[0], sq[1], sq[2], sq[3], 0xAAAAAAAAAAAAAAAAull, 0xCCCCCCCCCCCCCCCCull);
          SB();
        };
#pragma unroll
        for (int jj = 0; jj < 4; ++jj) {
          const bf16x8 wf = join8(WB[n][jj][0], WB[n][jj][1]);
          MFMAW<n == 0>(jj == 0 ? 4 : (jj == 2 ? c : -1), zv[0][jj], XG[j][k][0], wf);
          SB();
          nxt();
          if (n == 12) nxt();
          if (n == 12 && jj == 2) sel(2);
          MFMAW<n == 0>(-1, zv[1][jj], XG[j][k][1], wf);
          SB();
          nxt();
          if (n == 12) nxt();
          if (n == 12 && (jj == 1 || jj == 2)) sel(jj == 1 ? 0 : 3);
          if (n == 12 && jj == 2) { dsw16<0>(rwa, rv); SB(); }
          if (k == 0) {
            MFMAW<n == 0>(-1, cs[jj], ones, wf);
            SB();
            nxt();
            if (n == 12) nxt();
            if (n == 12 && jj == 1) sel(1);
          }
#pragma unroll
          for (int q = 0; q < ns; ++q) {
            const bf16x8& xs = XG[sc + q][1][jj >> 1];
            MFMAW<n == 1>(-1, st[jj & 1][jj >> 1], xs, (jj & 1) ? xs : ones);
            SB();
            nxt();
          }
        }
      };
      auto lstep = [&](auto nc, auto ac, auto bc, int w) {
        constexpr int n = decltype(nc)::value, j = n >> 1, k = n & 1, na = decltype(ac)::value, nb = decltype(bc)::value;
        int r = 0;
        auto nxt = [&]() { if (na > 0 && r < 16) { if (r < 8) rd1(ac, r); else rd1(bc, r - 8); ++r; } };
#pragma unroll
        for (int jj = 0; jj < 4; ++jj) {
          const bf16x8 wf = join8(WB[n][jj][0], WB[n][jj][1]);
          if (k == 0) { MFMAW<false>(jj == 0 ? w : -1, cs[jj], ones, wf); SB(); nxt(); nxt(); }
          MFMAW<false>(jj == 0 && k ? w : -1, zv[0][jj], XG[j][k][0], wf);
          SB();
          nxt();
          if (k) nxt();
          MFMAW<false>(-1, zv[1][jj], XG[j][k][1], wf);
          SB();
          nxt();
          if (k) nxt();
          if (n == 15 && jj == 2) {
            dsw4(csa, sel4(cs[0][0], cs[1][0], cs[2][0], cs[3][0], 0xFFFF0000FFFF0000ull, 0xFFFFFFFF00000000ull));
            SB();
          }
        }
      };
      bar(IC(1));
      AB(2);
#pragma unroll
      for (int r = -4; r < 8; ++r) rd1(IC(0), r);
      kstep(IC(0)); AB(4); kstep(IC(1)); kstep(IC(2)); kstep(IC(3)); kstep(IC(4)); kstep(IC(5)); kstep(IC(6)); kstep(IC(7)); kstep(IC(8)); AB(5); kstep(IC(9));
      kstep(IC(10)); kstep(IC(11)); kstep(IC(12));
      bar(IC(8));
      lstep(IC(13), IC(14), IC(16), -1);
      lstep(IC(14), IC(18), IC(15), 8);
      lstep(IC(16), IC(17), IC(19), 15);
      lstep(IC(18), IC(0), IC(0), 15);
      lstep(IC(15), IC(0), IC(0), -1);
      lstep(IC(17), IC(0), IC(0), 9);
      bar(IC(9));
      __builtin_amdgcn_s_setprio(2);
#pragma unroll
      for (int q = 0; q < 4; ++q) cp[q] = *(const float*)(smem + L_CS + ((wave ^ 1) * 64 + q * 16 + l15) * 4);
      ldxs();
      SB();
      lstep(IC(19), IC(0), IC(0), -1);
      AB(6);
      float ct[4];
#pragma unroll
      for (int jj = 0; jj < 4; ++jj) ct[jj] = cs[jj][0] + cp[jj];
#pragma unroll
      for (int jj = 0; jj < 4; ++jj) {
        if (jj == 0) asm volatile("s_nop 3");
        if (jj == 2) SB();
        if (!(jj & 1)) pin(zv[0][jj], zv[1][jj], zv[0][jj + 1], zv[1][jj + 1]);
#pragma unroll
        for (int i = 0; i < 2; ++i) zv[i][jj] = act(zv[i][jj], ct[jj], i);
      }
      if (isz2) {
        f32x4 pv[2], s1[2], s2[2];
#pragma unroll
        for (int i = 0; i < 2; ++i) {
#pragma unroll
          for (int e = 0; e < 4; ++e) {
            const float z0 = zv[i][0][e];
            pv[i][e] = __int_as_float(__builtin_amdgcn_update_dpp(0, __float_as_int(z0), 0x150, 0xF, 0xF, true));
          }
          const f32x4 a0 = zv[i][0] - pv[i], a1 = zv[i][1] - pv[i], a2 = zv[i][2] - pv[i], a3 = zv[i][3] - pv[i];
          s1[i] = (a0 + a1) + (a2 + a3);
          s2[i] = __builtin_elementwise_fma(a3, a3, __builtin_elementwise_fma(a2, a2, __builtin_elementwise_fma(a1, a1, a0 * a0)));
        }
        float r[8], q[4];
#define DA(d, s, ctl, bm) "v_add_f32_dpp " d ", " s ", " s " " ctl " row_mask:0xf bank_mask:" bm "\n\t"
        asm volatile("s_nop 1\n\t"
                     DA("%0", "%12", "row_ror:8", "0x3") DA("%1", "%13", "row_ror:8", "0x3") DA("%2", "%14", "row_ror:8", "0x3") DA("%3", "%15", "row_ror:8", "0x3")
                     DA("%4", "%16", "row_ror:8", "0x3") DA("%5", "%17", "row_ror:8", "0x3") DA("%6", "%18", "row_ror:8", "0x3") DA("%7", "%19", "row_ror:8", "0x3")
                     DA("%0", "%20", "row_ror:8", "0xc") DA("%1", "%21", "row_ror:8", "0xc") DA("%2", "%22", "row_ror:8", "0xc") DA("%3", "%23", "row_ror:8", "0xc")
                     DA("%4", "%24", "row_ror:8", "0xc") DA("%5", "%25", "row_ror:8", "0xc") DA("%6", "%26", "row_ror:8", "0xc") DA("%7", "%27", "row_ror:8", "0xc")
                     DA("%8", "%0", "row_half_mirror", "0x5") DA("%9", "%1", "row_half_mirror", "0x5") DA("%10", "%2", "row_half_mirror", "0x5") DA("%11", "%3", "row_half_mirror", "0x5")
                     DA("%8", "%4", "row_half_mirror", "0xa") DA("%9", "%5", "row_half_mirror", "0xa") DA("%10", "%6", "row_half_mirror", "0xa") DA("%11", "%7", "row_half_mirror", "0xa")
                     DA("%8", "%8", "quad_perm:[1,0,3,2]", "0xf") DA("%9", "%9", "quad_perm:[1,0,3,2]", "0xf") DA("%10", "%10", "quad_perm:[1,0,3,2]", "0xf") DA("%11", "%11", "quad_perm:[1,0,3,2]", "0xf")
                     DA("%8", "%8", "quad_perm:[2,3,0,1]", "0xf") DA("%9", "%9", "quad_perm:[2,3,0,1]", "0xf") DA("%10", "%10", "quad_perm:[2,3,0,1]", "0xf") DA("%11", "%11", "quad_perm:[2,3,0,1]", "0xf")
                     : "=&v"(r[0]), "=&v"(r[1]), "=&v"(r[2]), "=&v"(r[3]), "=&v"(r[4]), "=&v"(r[5]), "=&v"(r[6]), "=&v"(r[7]),
                       "=&v"(q[0]), "=&v"(q[1]), "=&v"(q[2]), "=&v"(q[3])
                     : "v"(s1[0][0]), "v"(s2[0][0]), "v"(s1[0][1]), "v"(s2[0][1]), "v"(s1[0][2]), "v"(s2[0][2]), "v"(s1[0][3]), "v"(s2[0][3]),
                       "v"(s1[1][0]), "v"(s2[1][0]), "v"(s1[1][1]), "v"(s2[1][1]), "v"(s1[1][2]), "v"(s2[1][2]), "v"(s1[1][3]), "v"(s2[1][3]));
#undef DA
        const f32x4 p4 = (lane & 8) ? pv[1] : pv[0];
        const float pa = (lane & 4) ? p4[2] : p4[0], pb = (lane & 4) ? p4[3] : p4[1];
        const float pp = (lane & 2) ? pb : pa, S1 = (lane & 2) ? q[2] : q[0], S2 = (lane & 2) ? q[3] : q[1];
        const float dl = S1 * (1.0f / 64.0f);
        st8(lin >> 1, stp, (u32x2){__float_as_uint((lane & 1) ? fmaxf(S2 - S1 * dl, 0.f) : pp + dl), (unsigned)tag});
      } else {
#pragma unroll
        for (int jj = 0; jj < 4; ++jj) {
          u32x4 o = pack8(zv[0][jj], zv[1][jj]);
          if (jj >= 2) z1put(jj * 16 - 32, o);
          else st16(lin, ws + WS_ZX + (size_t)((rb * 4 + pr) * 4 + wm * 2 + jj) * 1024, o);
        }
      }
      signal1(flg + 36);
      SB();
      AB(7);
      __builtin_amdgcn_s_setprio(P2P);
      unsigned po = isz2 ? (unsigned)((lane >> 5) * 6144 + (lane & 31) * 16) : 0u;
      const char* pb = ws + WS_ST + (size_t)rb * 12288 + wm * 512 + 2048;
      u32x4 sg[6];
      auto ldsg = [&]() {
#pragma unroll
        for (int c = 0; c < 6; ++c) asm volatile("global_load_dwordx4 %0, %1, %2 offset:%3 sc1" : "=&v"(sg[c]) : "v"(po), "s"(pb), "i"(c * 1024 - 2048) : "memory");
      };
      auto look = [&]() {
        wait_vm<0>();
        ldsg();
        asm volatile("global_load_ushort %0, %2, %3\n\tglobal_load_ushort %1, %2, %3 offset:32" : "=&v"(bsv[0]), "=&v"(bsv[1]) : "v"((unsigned)(l15 * 2)), "s"(b_sp + t0 + wl * 32) : "memory");
        if (!isz2) sttag(ws + WS_XF + (rb * 4 + pr) * 16 + wm * 8);
        SB();
      };
      flag_wait_asm(flg + 60, 4u);
      ds16<0>(ac2[0][0], p2a); ds16<1024>(ac2[1][0], p2a); ds16<2048>(ac2[0][1], p2a); ds16<3072>(ac2[1][1], p2a);
      sfor<0, 4>([&](auto nc) { rdV(IC(1), nc, 0); rdV(IC(1), nc, 1); rdV(IC(1), nc, 2); rdV(IC(1), nc, 3); });
      sfor<0, 10>([&](auto nc) {
        constexpr int n = decltype(nc)::value;
        mm2(IC(1), nc, XG + 5, n == 0 ? 12 : (n < 6 ? 15 : 4 * (9 - n)), [&](int r) {
          SB();
          if constexpr (n < 3) { if (r < 2) { rdV(IC(1), IC(4 + 2 * n), 2 * r); rdV(IC(1), IC(4 + 2 * n), 2 * r + 1); } else { rdV(IC(1), IC(5 + 2 * n), 2 * r - 4); rdV(IC(1), IC(5 + 2 * n), 2 * r - 3); } }
        });
        if constexpr (n == 2) signal1(flg + 16);
        if constexpr (n == PN) look();
      });
      signal1(flg + 20 + wl * 4);
      AB(3);
      if (PAE == 2) preact();
      if (PN > 9) look();
      AB(8);
      if (PAE == 3) preact();
      wait_vm<2>(sg[0], sg[1], sg[2], sg[3], sg[4], sg[5]);
      if (isz2) {
        const unsigned zw = lb + L_ZS + wave * 256 + lane * 4, zr_ = lb + L_ZS + wave * 256 + g4 * 32;
        const char* zb = ws + WS_Z2 + (size_t)((rb * 2 + wm) * 48 + pr * 12 + odd * 8 + wl * 4) * 1024;
        auto bad = [&]() {
          unsigned an = sg[0][1] & sg[0][3], on = sg[0][1] | sg[0][3];
#pragma unroll
          for (int c = 1; c < 6; ++c) { an &= sg[c][1] & sg[c][3]; on |= sg[c][1] | sg[c][3]; }
          return __builtin_amdgcn_ballot_w64(((an ^ (unsigned)tag) | (on ^ (unsigned)tag)) != 0) != 0;
        };
        if (__builtin_expect(bad(), 0)) {
          do {
            --tmo;
            reld6(sg, po, pb);
            wait_vm<0>(sg[0], sg[1], sg[2], sg[3], sg[4], sg[5]);
          } while (bad() && tmo > 0);
        }
        AB(9);
        auto f = [&](int c, int e) { return __uint_as_float(sg[c][e]); };
        auto sum6 = [&](int e) { return add1(add1(add1(f(0, e), f(1, e)), add1(f(2, e), f(3, e))), add1(f(4, e), f(5, e))); };
        const float nmh = sum6(0) * (-1.0f / 6.0f), mh = -nmh;
        float qa = sum6(2) * (1.0f / 64.0f), qb;
#pragma unroll
        for (int c = 0; c < 6; c += 2) { const float u = add1(f(c, 0), nmh), v = add1(f(c + 1, 0), nmh); qa = fma1(u, u, qa); qb = c ? fma1(v, v, qb) : v * v; }
        const float Mh = add1(qa, qb);
        const auto sm = __builtin_amdgcn_permlane32_swap(__float_as_uint(mh), __float_as_uint(mh), false, false);
        const auto sM = __builtin_amdgcn_permlane32_swap(__float_as_uint(Mh), __float_as_uint(Mh), false, false);
        const float dm = __uint_as_float(sm[0]) - __uint_as_float(sm[1]), mean = rbf(add1(__uint_as_float(sm[0]), __uint_as_float(sm[1])) * 0.5f);
        const float vbf = rbf(__builtin_fmaf(dm * dm, 3.0f, add1(__uint_as_float(sM[0]), __uint_as_float(sM[1]))) * (1.0f / 12.0f));
        const float rz = __builtin_amdgcn_rcpf(rbf(__builtin_amdgcn_sqrtf(rbf(vbf + 1e-5f))));
        dsw4(zw, lane < 32 ? -mean * rz : rz);
        f32x4 zm[2], zr[2];
        ds16<0>(zm[0], zr_); ds16<128>(zr[0], zr_); ds16<16>(zm[1], zr_); ds16<144>(zr[1], zr_);
        wait_lg<0>(zm[0], zm[1], zr[0], zr[1]);
#pragma unroll
        for (int jj = 0; jj < 4; ++jj) {
          u32x4 o;
#pragma unroll
          for (int q = 0; q < 4; ++q) {
            const int i = q >> 1, e = (q & 1) * 2;
            o[q] = pack2(fma1(zv[i][jj][e], zr[i][e], zm[i][e]), fma1(zv[i][jj][e + 1], zr[i][e + 1], zm[i][e + 1]));
          }
          o[3] = nohole(o[3]);
          asm volatile("global_store_dwordx4 %0, %1, %2 offset:%3 sc1\n\ts_nop 1" :: "v"(lin), "v"(o), "s"(zb), "i"(jj * 1024) : "memory");
          SB();
        }
      }
      SB();
      AB(10);
      ldtag(ef, (unsigned)((lane & 31) * 8), ws + WS_EF + 2048 + cb * 256);
    }
    const char* Bs[2] = {ws + WS_Z2 + (size_t)(wl * 32 * 48 + cb * 6 + wm * 3) * 1024, ws + WS_Z2 + (size_t)((wl * 32 + 16) * 48 + cb * 6 + wm * 3) * 1024};
    bf16x8 Bf[3][6];
    unsigned bv[2], bo;
    auto ldB1 = [&](int s, int j) {
      asm volatile("global_load_dwordx4 %0, %1, %2 offset:%3 " HSC : "=&v"(Bf[s % 3][j]) : "v"(bv[s & 1]), "s"(Bs[j / 3]), "i"((j % 3) * 1024) : "memory");
    };
    auto nxb = [&](int s, int k) {
      if (k == 0) asm volatile("s_add_u32 %0, %0, 0xc000\n\ts_cmp_eq_u32 %0, 0xc0000\n\ts_cselect_b32 %0, 0, %0" : "+s"(bo) :: "scc");
      else asm volatile("v_or_b32 %0, %1, %2" : "=v"(bv[s & 1]) : "s"(bo), "v"(lin));
    };
    f32x4 acc[4][3];
    unsigned ara[2][2];
#pragma unroll
    for (int ks = 0; ks < 2; ++ks) {
      ara[0][ks] = lb + M_RING + wl * 49152 + l15 * 128 + (((ks * 4 + g4) ^ (l15 & 7)) << 4);
      ara[1][ks] = ara[0][ks] + (M_CV - M_RING) + wl * (2 * CV_BUF - 49152);
    }
    u32x4 af[3][8];
    auto rdA = [&](int s, int i) {
      const int q = (0x20132012 >> (4 * (s >> 1))) & 3;
      asm volatile("ds_read_b128 %0, %1 offset:%2" : "=&v"(af[s == 15 ? 2 : s & 1][i]) : "v"(ara[q == 3][s & 1]), "i"((q % 3) * 8192 + (i & 3) * 2048 + (i >> 2) * (q == 3 ? CV_BUF : 24576)) : "memory");
    };
    bo = (unsigned)(rot2 * 2 * 49152);
    asm volatile("" : "+s"(xl), "+s"(Ab[0]), "+s"(Ab[1]), "+s"(Ab[2]), "+s"(Ab[3]));
    auto adA = [&](int d) {
#pragma unroll
      for (int q = 0; q < 2; ++q) av[q] = opq(aoff[q] - q * 1024 + (unsigned)(((d + rot2) & 7) * 128));
    };
    auto ldA1 = [&](int a) { asm volatile("global_load_lds_dwordx4 %0, %1 offset:%2" :: "v"(av[a & 1]), "s"(Ab[a >> 1]), "i"(a * 1024 - 4096) : "memory"); };
    adA(3);
    flag_wait_asm(flg + 32, 1u);
#pragma unroll
    for (int i = 0; i < 16; ++i) rdA(i >> 3, i & 7);
    signal1(flg + 28);
    asm volatile("s_mov_b32 m0, %0" :: "s"(xl));
    nxb(0, 1); nxb(1, 0); nxb(1, 1);
    if (PAE == 1) preact();
    flag_wait_asm(flg + 28, 4u);
#pragma unroll
    for (int i = 0; i < 16; ++i) pin(af[i >> 3][i & 7]);
#pragma unroll
    for (int a = 0; a < 8 - T3L; ++a) ldA1(a);
    unsigned fv, fw = flg + wave * 4, fp = flg + wl * 4, cv = 1u;
    unsigned fo = flg, fx = flg + (wave ^ 2) * 4, xa = lb + M_CV + wave * CV_BUF + lin, xb = lb + M_CV + (wave ^ 2) * CV_BUF + lin, za[2];
    unsigned eo = (unsigned)((g4 & 1) * 24576 + (l15 + 16 * (g4 >> 1)) * 16);
    const char* hp = hf + (size_t)((rb * 4 + wl * 2) * 24 + cb * 3) * 1024 + wm * 1536;
#pragma unroll
    for (int q = 0; q < 2; ++q) {
      const int fr = 4 * g4 + (l15 >> 2), p = lane & 3;
      za[q] = lb + L_Z1 + wm * 6144 + fr * 128 + (((wl * 4 + q * 2 + (p >> 1)) ^ (fr & 7)) << 4) + (p & 1) * 8;
    }
    pin(fw, fp, fo, fx, xa, xb, za[0], za[1], eo);
    asm volatile("" : "+s"(hp));
    AB(11);
    wait_vm<8 - T3L>(ef, bsv[0], bsv[1]);
    waittag(ef, (unsigned)((lane & 31) * 8), ws + WS_EF + 2048 + cb * 256);
    AB(12); SS(1 - 3 * XS);
#pragma unroll
    for (int j = 0; j < 6; ++j) ldB1(0, j);
#pragma unroll
    for (int j = 0; j < 6; ++j) ldB1(1, j);
#pragma unroll
    for (int a = 8 - T3L; a < 8; ++a) ldA1(a);
    nxb(2, 0); nxb(2, 1); nxb(3, 0); nxb(3, 1);
    SS(2 - 3 * XS);
    if (!PAE) preact();
    __builtin_amdgcn_s_setprio(0);
    f32x4 bs4[2] = {{lo16(bsv[0]), lo16(bsv[0]), lo16(bsv[0]), lo16(bsv[0])}, {lo16(bsv[1]), lo16(bsv[1]), lo16(bsv[1]), lo16(bsv[1])}};
    pin(bs4[0], bs4[1]);
#define CK0 HIREG("h0", "%0") HIREG("h1", "%1") HIREG("h2", "%2") HIREG("h3", "%3") HIREG("h4", "%4") HIREG("h5", "%5") "s_waitcnt vmcnt(%[nw])\n\tv_max3_u32 %6, v[h0], v[h1], v[h2]\n\t"
#define CK1 "v_max3_u32 %6, %6, v[h3], v[h4]\n\tv_max_u32 %6, %6, v[h5]\n\t"
#define CK2 "v_cmp_eq_u32 vcc, -1, %6\n\ts_waitcnt lgkmcnt(0)\n\t"
#define CKA(d) "s_add_u32 vcc_lo, %[r2], %[st] + " d "\n\ts_and_b32 vcc_lo, vcc_lo, 15\n\ts_mul_i32 vcc_lo, vcc_lo, 0xc000\n\tv_or_b32 %6, vcc_lo, %[li]\n\t"
#define CKL(a, b, c, d, e, f) "global_load_dwordx4 " a ", %6, %[bs] sc1\n\tglobal_load_dwordx4 " b ", %6, %[bs] offset:1024 sc1\n\tglobal_load_dwordx4 " c ", %6, %[bs] offset:2048 sc1\n\t" \
            "global_load_dwordx4 " d ", %6, %[bt] sc1\n\tglobal_load_dwordx4 " e ", %6, %[bt] offset:1024 sc1\n\tglobal_load_dwordx4 " f ", %6, %[bt] offset:2048 sc1\n\t"
#define CK3X(x) "s_cbranch_vccnz .Lck_%=\n.Lcb_%=:\n\t.subsection 1\n.Lck_%=:\n\ts_waitcnt vmcnt(0)\n\ts_sleep 2\n\t" CKA("0") CKL("%0", "%1", "%2", "%3", "%4", "%5") x \
            "s_waitcnt vmcnt(0)\n\tv_max3_u32 %6, v[h0], v[h1], v[h2]\n\t" CK1 "v_cmp_eq_u32 vcc, -1, %6\n\t" \
            "s_sub_i32 %7, %7, 1\n\ts_cmp_lt_i32 %7, 1\n\ts_cbranch_scc1 .Lcb_%=\n\ts_cbranch_vccnz .Lck_%=\n\ts_branch .Lcb_%=\n\t.subsection 0"
#define CK3 CK3X("")
#define CK4 CK3X(CKA("1") CKL("%[n0]", "%[n1]", "%[n2]", "%[n3]", "%[n4]", "%[n5]"))
#define CKN(sl) [n0] "+v"(Bf[sl][0]), [n1] "+v"(Bf[sl][1]), [n2] "+v"(Bf[sl][2]), [n3] "+v"(Bf[sl][3]), [n4] "+v"(Bf[sl][4]), [n5] "+v"(Bf[sl][5])
#define CKO(sl) "+v"(Bf[sl][0]), "+v"(Bf[sl][1]), "+v"(Bf[sl][2]), "+v"(Bf[sl][3]), "+v"(Bf[sl][4]), "+v"(Bf[sl][5]), "=&v"(mx), "+s"(tmo)
#define CKI(s_, nw_) [r2] "s"(2 * rot2), [st] "i"(s_), [li] "v"(lin), [bs] "s"(Bs[0]), [bt] "s"(Bs[1]), [nw] "i"(nw_)
    unsigned mx;
    asm volatile(CK0 CK1 CK2 CK4 : CKO(0), CKN(1) : CKI(0, 6 + T3L) : "memory", "vcc", "scc");
    auto mm = [&](int s, int i, int j) {
      if (s == 0 && j < 3 && i >= 2) asm volatile(MF "%0, %1, %2, %3" : "=&v"(acc[i][j]) : "v"(Bf[0][j]), "v"(af[0][i]), "v"(bs4[i & 1]));
      else if (s == 0 && j < 3) MFMA0(acc[i][j], Bf[0][j], af[0][i]);
      else MFMA(acc[i][j % 3], Bf[s % 3][j], af[s == 15 ? 2 : s & 1][(j / 3) * 4 + i]);
      SB();
    };
    auto mmf = [&](int s, int i, int j, unsigned a, int off, int tg) {
      unsigned sv;
      asm volatile(MF "%0, %3, %4, %0\n\tv_readfirstlane_b32 %2, %1\n\ts_cmp_lt_u32 %2, %7\n\ts_cbranch_scc1 .Lfk_%=\n.Lfb_%=:\n\t.subsection 1\n.Lfk_%=:\n\t"
                   "ds_read_b32 %1, %5 offset:%6\n\ts_waitcnt lgkmcnt(0)\n\tv_readfirstlane_b32 %2, %1\n\ts_cmp_lt_u32 %2, %7\n\ts_cbranch_scc1 .Lfk_%=\n\ts_branch .Lfb_%=\n\t.subsection 0"
                   : "+v"(acc[i][j % 3]), "+v"(fv), "=&s"(sv) : "v"(Bf[s % 3][j]), "v"(af[s == 15 ? 2 : s & 1][(j / 3) * 4 + i]), "v"(a), "i"(off), "i"(tg) : "memory", "scc");
      SB();
    };
    auto step = [&](auto sc) {
      constexpr int s = decltype(sc)::value;
      constexpr int NV = (s == 1 ? 1 : 6) + (s < 13 ? 5 : 0);
      constexpr int FO[8] = {224, 0, 232, 112, 120, 144, 152, 0};
      SS(3 + s);
      SB();
#pragma unroll
      for (int j = 0; j < 5; ++j) {
        auto& B = Bf[s % 3][j];
        if (j == 4 && s > 0 && s < 12)
          asm volatile(MF "%0, %2, %3, %0\n\ts_add_u32 %1, %1, 0xc000\n\ts_cmp_eq_u32 %1, 0xc0000\n\ts_cselect_b32 %1, 0, %1" : "+v"(acc[0][1]), "+s"(bo) : "v"(B), "v"(af[s & 1][4]) : "scc");
        else if (j == 0 && (s & 1) && FO[s >> 1]) mmf(s, 0, 0, fp, FO[s >> 1], 2);
        else mm(s, 0, j);
        if (s == 0) ldB1(2, j);
        if (s == 13 && j > 0) rdA(15, 2 * j - 1);
        SB();
        if (j == 4 && s > 0 && s < 12) asm volatile(MF "%0, %2, %3, %0\n\tv_or_b32 %1, %4, %5" : "+v"(acc[1][1]), "=&v"(bv[s & 1]) : "v"(B), "v"(af[s & 1][5]), "s"(bo), "v"(lin));
        else mm(s, 1, j);
        if (j < 4 && s > 0) rdA(s + 1, 2 * j);
        SB();
        mm(s, 2, j);
        if (j < 4 && s > 0) rdA(s + 1, 2 * j + 1);
        if (j == 4 && (s == 2 || s == 4 || s == 6 || s == 8)) asm volatile("ds_write_b32 %1, %0 offset:96\n\tv_add_u32 %0, 1, %0" : "+v"(cv) : "v"(fw) : "memory");
        if (j == 4 && !(s & 1) && FO[s >> 1]) asm volatile("ds_read_b32 %0, %1 offset:%2" : "=&v"(fv) : "v"(fp), "i"(FO[s >> 1]) : "memory");
        if (j == 4 && s == 3) signal1(fp + 232);
        if (j == 4 && s == SGS) signal1(flg + 44);
        SB();
        mm(s, 3, j);
        if (s + 3 < 16) ldB1(s + 3, j);
        else if (j < 4) rdA(15, 2 * j);
        else asm volatile("ds_read_b32 %0, %1 offset:56" : "=&v"(fv) : "v"(fo) : "memory");
        SB();
      }
      constexpr int sn = (s + 1) % 3;
      if constexpr (s == 0) {
        mm(0, 0, 5); ldB1(2, 5); mm(0, 1, 5); nxb(4, 0); mm(0, 2, 5); nxb(4, 1); mm(0, 3, 5); ldB1(3, 5);
        asm volatile(CK0 CK1 CK2 CK4 : CKO(1), CKN(2) : CKI(1, 12 + T3L) : "memory", "vcc", "scc");
      } else if constexpr (s < 13)
        asm volatile(MF "%8, %12, %19, %8\n\t" CK0 MF "%9, %12, %20, %9\n\t" CK1 MF "%10, %12, %21, %10\n\t" CK2 MF "%11, %12, %22, %11\n\t"
                     "global_load_dwordx4 %12, %23, %[bt] offset:2048 " HSC "\n\t" CK4
                     : CKO(sn), "+v"(acc[0][2]), "+v"(acc[1][2]), "+v"(acc[2][2]), "+v"(acc[3][2]), "+v"(Bf[s % 3][5]), CKN((s + 2) % 3)
                     : "v"(af[s & 1][4]), "v"(af[s & 1][5]), "v"(af[s & 1][6]), "v"(af[s & 1][7]), "v"(bv[~s & 1]), CKI(s + 1, NV) : "memory", "vcc", "scc");
      else
        asm volatile(MF "%8, %12, %13, %8\n\t" CK0 MF "%9, %12, %14, %9\n\t" CK1 MF "%10, %12, %15, %10\n\t" CK2 MF "%11, %12, %16, %11\n\t" CK3
                     : CKO(sn), "+v"(acc[0][2]), "+v"(acc[1][2]), "+v"(acc[2][2]), "+v"(acc[3][2])
                     : "v"(Bf[s % 3][5]), "v"(af[s & 1][4]), "v"(af[s & 1][5]), "v"(af[s & 1][6]), "v"(af[s & 1][7]), CKI(s + 1, NV) : "memory", "vcc", "scc");
#pragma unroll
      for (int i = 0; i < 8; ++i) pin(af[~s & 1][i]);
      SB();
    };
    step(IC(0)); AB(13); step(IC(1)); step(IC(2)); step(IC(3)); AB(14); step(IC(4)); step(IC(5)); step(IC(6)); step(IC(7));
    step(IC(8)); step(IC(9)); step(IC(10)); step(IC(11)); step(IC(12)); step(IC(13));
    SS(17);
#pragma unroll
    for (int i = 0; i < 8; ++i) pin(af[2][i]);
    s16x4 z1r[2][3];
    f32x2 zf[12];
    f32x4 pr_[2][3];
#pragma unroll
    for (int m = 0; m < 48; ++m) {
      const int i = m / 12, n = m % 12;
      if (m == 0) mmf(14, 0, 0, fo, 56, 5);
      else if (m == 2)
        asm volatile(MF "%8, %11, %15, %8\n\t" CK0 MF "%9, %12, %16, %9\n\t" CK1 MF "%10, %13, %16, %10\n\t" CK2 MF "%8, %14, %16, %8\n\t" CK3
                     : CKO(0), "+v"(acc[0][2]), "+v"(acc[0][0]), "+v"(acc[0][1])
                     : "v"(Bf[2][2]), "v"(Bf[2][3]), "v"(Bf[2][4]), "v"(Bf[2][5]), "v"(af[0][0]), "v"(af[0][4]), CKI(15, 0) : "memory", "vcc", "scc");
      else if (m == KC) { wait_lg<0>(fv); mmf(14 + n / 6, i, n % 6, fx, 0, 1); }
      else if (m < 2 || m > 5) mm(14 + n / 6, i, n % 6);
      if (m == 1) {
#pragma unroll
        for (int q = 0; q < 2; ++q) { z1r[q][0] = tr_asm<0>(za[q]); z1r[q][1] = tr_asm<2048>(za[q]); z1r[q][2] = tr_asm<4096>(za[q]); }
      }
      if (m == 5) pin(z1r[0][0], z1r[0][1], z1r[0][2], z1r[1][0], z1r[1][1], z1r[1][2]);
      const int u = m < 14 ? m - 6 : (m - 14) / 2 + 8;
      if (m >= 6 && (m < 14 || !(m & 1)) && u < 12) { zf[u] = unpk(__builtin_bit_cast(u32x2, z1r[u / 6][u / 2 % 3])[u & 1]); pin(zf[u]); }
      if ((i == 1 || i == 2) && n >= 2 && n < 8 && (n & 1)) asm volatile("ds_write_b128 %0, %1 offset:%2" :: "v"(xa), "v"(acc[i - 1][n / 2 - 1]), "i"((i * 3 + n / 2 - 4) * 1024) : "memory");
      if (m == 32) asm volatile("ds_write_b32 %1, %0" :: "v"(cv), "v"(fw) : "memory");
      if (m == KC - 4) asm volatile("ds_read_b32 %0, %1" : "=&v"(fv) : "v"(fx) : "memory");
      if (m >= KC && m < KC + 3)
#pragma unroll
        for (int q = 0; q < 2; ++q) asm volatile("ds_read_b128 %0, %1 offset:%2" : "=&v"(pr_[q][m - KC]) : "v"(xb), "i"((q * 3 + m - KC) * 1024) : "memory");
      SB();
    }
    SS(19);
    AB(15);
    wait_lg<0>(pr_[0][0], pr_[0][1], pr_[0][2], pr_[1][0], pr_[1][1], pr_[1][2]);
    AB(16);
    unsigned pk[2][3][2];
    auto hm = [&](int i, int j) {
      pin(acc[2 + i][j]);
      const f32x4 o = acc[2 + i][j] + pr_[i][j];
#pragma unroll
      for (int h = 0; h < 2; ++h) pk[i][j][h] = pk2(zf[i * 6 + j * 2 + h] * (f32x2){o[2 * h], o[2 * h + 1]});
    };
    hm(0, 0); hm(0, 1);
    SB();
#pragma unroll
    for (int j = 0; j < 3; ++j) {
      if (j == 2) hm(0, 2);
      hm(1, j);
      const auto s0 = __builtin_amdgcn_permlane16_swap(pk[0][j][0], pk[1][j][0], false, false);
      const auto s1 = __builtin_amdgcn_permlane16_swap(pk[0][j][1], pk[1][j][1], false, false);
      asm volatile("global_store_dwordx4 %0, %1, %2 offset:%3 sc1\n\ts_nop 1" :: "v"(eo), "v"((u32x4){s0[0], s1[0], s0[1], nohole(s1[1])}), "s"(hp), "i"(j * 512) : "memory");
      SB();
    }
    AB(17);
    ldW(0, 15);
    wait_vm<15>(ef);
    gemm3(IC(0), []() {});
    if (b == 0 && tid == 0) *(u64*)(ws + WS_EP) = tag;
  }
  AB(24);
  AB_OUT();
}

extern "C" void kernel_launch(void* const* d_in, const int* in_sizes, int n_in,
                              void* d_out, int out_size, void* d_ws, size_t ws_size,
                              hipStream_t stream) {
  (void)hipFuncSetAttribute((const void*)k_f, hipFuncAttributeMaxDynamicSharedMemorySize, M_LDS);
  k_f<<<256, 512, M_LDS, stream>>>((const u16*)d_in[0], (const u16*)d_in[1], (const u16*)d_in[2], (const u16*)d_in[3], (const u16*)d_in[4],
                                   (const u16*)d_in[5], (const u16*)d_in[7], (const u16*)d_in[8], (const u16*)d_in[9], (const u16*)d_in[10],
                                   (u16*)d_out, (char*)d_ws);
}
```

```cpp
#include <hip/hip_runtime.h>
#include <hip/hip_bf16.h>
#include <stdint.h>
#include <type_traits>

typedef unsigned short u16;
typedef unsigned long long u64;
typedef __attribute__((ext_vector_type(8))) short bf16x8;
typedef __attribute__((ext_vector_type(4))) short s16x4;
typedef __attribute__((ext_vector_type(4))) float f32x4;
typedef __attribute__((ext_vector_type(2))) float f32x2;
typedef __attribute__((ext_vector_type(4))) unsigned int u32x4;
typedef __attribute__((ext_vector_type(2))) unsigned int u32x2;
typedef __attribute__((ext_vector_type(2))) __bf16 bf16x2n;

#define LDS_AS __attribute__((address_space(3)))
#define IC(n) std::integral_constant<int, (n)>{}
#define SB() __builtin_amdgcn_sched_barrier(0)

#define AB_DECL do {} while (0)
#define AB(i) do {} while (0)
#define AB_OUT() do {} while (0)
#define CB(i) do {} while (0)
#define SS(i) do {} while (0)
constexpr int S_ = 2048, D_ = 640, FH_ = 768, F2_ = 1536;
constexpr unsigned HOLE = 0xFFFFFFFFu;

constexpr size_t WS_EP = 0;
constexpr size_t WS_EF = 4096;
constexpr size_t WS_LF = 22528;
constexpr size_t WS_YF = 14336;
constexpr size_t WS_XF = 20480;
constexpr size_t WS_ST = 32768;
constexpr size_t WS_Z2 = WS_ST + 32 * 12 * 1024;
constexpr size_t WS_H = WS_Z2 + 64 * 48 * 1024;
constexpr size_t WS_ZX = WS_H + 128 * 24 * 1024;
constexpr size_t WS_KV = WS_ZX + 32 * 4 * 4096;
constexpr size_t WS_Y = WS_KV + D_ * S_ * 2;
constexpr size_t WS_WO = WS_Y + D_ * S_ * 2;

__device__ __forceinline__ unsigned pack2(float lo, float hi) {
  f32x2 v = {lo, hi};
  bf16x2n r = __builtin_convertvector(v, bf16x2n);
  return __builtin_bit_cast(unsigned, r);
}
__device__ __forceinline__ float nv(float v) { asm("" : "+v"(v)); return v; }
__device__ __forceinline__ float fma1(float a, float b, float c) { float r; asm("v_fma_f32 %0, %1, %2, %3" : "=v"(r) : "v"(a), "v"(b), "v"(c)); return r; }
__device__ __forceinline__ float add1(float a, float b) { float r; asm("v_add_f32 %0, %1, %2" : "=v"(r) : "v"(a), "v"(b)); return r; }
__device__ __forceinline__ float mul1(float a, float b) { float r; asm("v_mul_f32 %0, %1, %2" : "=v"(r) : "v"(a), "v"(b)); return r; }
__device__ __forceinline__ unsigned pack2s(float lo, float hi) { return pack2(nv(lo), nv(hi)); }
__device__ __forceinline__ float lo16(unsigned w) { return __uint_as_float(w << 16); }
__device__ __forceinline__ float hi16(unsigned w) { return __uint_as_float(w & 0xFFFF0000u); }
__device__ __forceinline__ float rbf(float f) { return lo16(pack2(f, f)); }
__device__ __forceinline__ f32x2 unpk(unsigned w) { f32x2 r = {lo16(w), hi16(w)}; return r; }
__device__ __forceinline__ unsigned pk2(f32x2 v) { return pack2(v[0], v[1]); }
__device__ __forceinline__ unsigned umx(unsigned a, unsigned b) { return a > b ? a : b; }
__device__ __forceinline__ unsigned nohole(unsigned a) { return a < HOLE - 1 ? a : HOLE - 1; }
__device__ __forceinline__ f32x2 gelu2(f32x2 x) {
  const f32x2 c1 = {-2.3022082f, -2.3022082f}, c2 = {-0.10294324f, -0.10294324f};
  const f32x2 a = x * __builtin_elementwise_fma(x * x, c2, c1);
  f32x2 e = {__builtin_amdgcn_exp2f(a[0]), __builtin_amdgcn_exp2f(a[1])};
  e = e + (f32x2){1.0f, 1.0f};
  const f32x2 r = {__builtin_amdgcn_rcpf(e[0]), __builtin_amdgcn_rcpf(e[1])};
  return x * r;
}
__device__ __forceinline__ float gelu1(float x) {
  const float a = x * __builtin_fmaf(x * x, -0.10294324f, -2.3022082f);
  return x * __builtin_amdgcn_rcpf(__builtin_amdgcn_exp2f(a) + 1.0f);
}
template <int OFF>
__device__ __forceinline__ s16x4 tr_asm(unsigned a) {
  s16x4 r;
  asm volatile("ds_read_b64_tr_b16 %0, %1 offset:%2" : "=&v"(r) : "v"(a), "i"(OFF) : "memory");
  return r;
}
__device__ __forceinline__ bf16x8 join8(s16x4 lo, s16x4 hi) {
  bf16x8 r;
  r[0] = lo[0]; r[1] = lo[1]; r[2] = lo[2]; r[3] = lo[3];
  r[4] = hi[0]; r[5] = hi[1]; r[6] = hi[2]; r[7] = hi[3];
  return r;
}
__device__ __forceinline__ unsigned lds_addr(const void* p) { return (unsigned)(uintptr_t)(LDS_AS const char*)p; }
#define HIREG(sym, r) ".set " sym ", 0\n.set hir_st, 0\n.irpc c, \"" r "\"\n.ifc \\c,:\n.set hir_st, 1\n.else\n.ifc \\c,]\n.set hir_st, 0\n.else\n.if hir_st\n.set " sym ", " sym " * 10 + \\c\n.endif\n.endif\n.endif\n.endr\n"
#define MF "v_mfma_f32_16x16x32_bf16 "
#define SLP 2
template <int SL = 1> __device__ __forceinline__ void flag_wait_asm(unsigned faddr, unsigned target) {
  unsigned v, sv;
  asm volatile(".Lfw_%=:\n\tds_read_b32 %0, %2\n\ts_waitcnt lgkmcnt(0)\n\tv_readfirstlane_b32 %1, %0\n\ts_nop 0\n\t"
               "s_cmp_ge_u32 %1, %3\n\ts_cbranch_scc1 .Lfd_%=\n\ts_sleep %4\n\ts_branch .Lfw_%=\n\t.Lfd_%=:"
               : "=&v"(v), "=&s"(sv) : "v"(faddr), "s"(target), "i"(SL) : "memory", "scc");
}
__device__ __forceinline__ void flag_wait2(unsigned faddr, unsigned target) {
  unsigned v, w, sv;
  asm volatile(".Lgw_%=:\n\tds_read_b32 %0, %3\n\tds_read_b32 %1, %3 offset:4\n\ts_waitcnt lgkmcnt(0)\n\tv_min_u32 %0, %0, %1\n\ts_nop 0\n\tv_readfirstlane_b32 %2, %0\n\ts_nop 0\n\t"
               "s_cmp_ge_u32 %2, %4\n\ts_cbranch_scc1 .Lgd_%=\n\ts_sleep %5\n\ts_branch .Lgw_%=\n\t.Lgd_%=:"
               : "=&v"(v), "=&v"(w), "=&s"(sv) : "v"(faddr), "s"(target), "i"(SLP) : "memory", "scc");
}
__device__ __forceinline__ void signal1(unsigned a, unsigned one = 1u) {
  asm volatile("s_mov_b64 exec, 1\n\tds_add_u32 %0, %1\n\ts_mov_b64 exec, -1" :: "v"(a), "v"(one) : "memory");
}
__device__ __forceinline__ void dma16(unsigned vo, const void* sb, unsigned lds) {
  asm volatile("s_mov_b32 m0, %2\n\ts_nop 0\n\tglobal_load_lds_dwordx4 %0, %1" :: "v"(vo), "s"(sb), "s"(lds) : "memory");
}
template <int LO, int GO> __device__ __forceinline__ void dma32s(unsigned vo0, unsigned vo1, const void* sb, unsigned lds) {
  asm volatile("s_add_u32 m0, %5, %6\n\ts_add_u32 vcc_lo, %2, %4\n\ts_addc_u32 vcc_hi, %3, 0\n\t"
               "global_load_lds_dwordx4 %0, vcc\n\tglobal_load_lds_dwordx4 %1, vcc offset:1024"
               :: "v"(vo0), "v"(vo1), "s"((unsigned)(uintptr_t)sb), "s"((unsigned)((uintptr_t)sb >> 32)), "i"(GO), "s"(lds), "i"(LO) : "memory", "scc", "vcc");
}
template <int LO, int GO, int WRAP, int LIM> __device__ __forceinline__ void dma32r(unsigned vo0, unsigned vo1, const void* sb, unsigned lds, int rot) {
  const char* s2 = (const char*)sb - WRAP;
  asm volatile("s_add_u32 m0, %5, %6\n\ts_cmp_ge_u32 %7, %8\n\ts_cselect_b64 vcc, %3, %2\n\ts_add_u32 vcc_lo, vcc_lo, %4\n\ts_addc_u32 vcc_hi, vcc_hi, 0\n\t"
               "global_load_lds_dwordx4 %0, vcc\n\tglobal_load_lds_dwordx4 %1, vcc offset:1024"
               :: "v"(vo0), "v"(vo1), "s"(sb), "s"(s2), "i"(GO), "s"(lds), "i"(LO), "s"(rot), "i"(LIM) : "memory", "scc", "vcc");
}
__device__ __forceinline__ void st16(unsigned vo, const void* sb, u32x4 v) {
  asm volatile("global_store_dwordx4 %0, %1, %2 sc1\n\ts_nop 1" :: "v"(vo), "v"(v), "s"(sb) : "memory");
}
__device__ __forceinline__ void touch(unsigned vo, const void* sb, unsigned lds) {
  asm volatile("s_mov_b32 m0, %2\n\ts_nop 0\n\tglobal_load_lds_dword %0, %1" :: "v"(vo), "s"(sb), "s"(lds) : "memory");
}
__device__ __forceinline__ void st8(unsigned vo, const void* sb, u32x2 v) { asm volatile("global_store_dwordx2 %0, %1, %2 sc1" :: "v"(vo), "v"(v), "s"(sb) : "memory"); }
template <class A, class B> __device__ __forceinline__ void MFMA(f32x4& acc, const A& a, const B& b) {
  asm volatile("v_mfma_f32_16x16x32_bf16 %0, %1, %2, %0" : "+v"(acc) : "v"(a), "v"(b));
}
template <class A, class B> __device__ __forceinline__ void MFMA0(f32x4& acc, const A& a, const B& b) {
  asm volatile("v_mfma_f32_16x16x32_bf16 %0, %1, %2, 0" : "=&v"(acc) : "v"(a), "v"(b));
}
template <bool Z, class A, class B> __device__ __forceinline__ void MFMAW(int n, f32x4& acc, const A& a, const B& b) {
  if (n < 0) { if (Z) MFMA0(acc, a, b); else MFMA(acc, a, b); }
  else if (Z) asm volatile("s_waitcnt lgkmcnt(%3)\n\tv_mfma_f32_16x16x32_bf16 %0, %1, %2, 0" : "=&v"(acc) : "v"(a), "v"(b), "i"(n));
  else asm volatile("s_waitcnt lgkmcnt(%3)\n\tv_mfma_f32_16x16x32_bf16 %0, %1, %2, %0" : "+v"(acc) : "v"(a), "v"(b), "i"(n));
}
__device__ __forceinline__ float sel4(float a, float b, float c, float d, u64 m1, u64 m2) {
  float r, t;
  asm volatile("v_cndmask_b32_e64 %0, %2, %3, %6\n\tv_cndmask_b32_e64 %1, %4, %5, %6\n\tv_cndmask_b32_e64 %0, %0, %1, %7"
               : "=&v"(r), "=&v"(t) : "v"(a), "v"(b), "v"(c), "v"(d), "s"(m1), "s"(m2));
  return r;
}
template <class T> __device__ __forceinline__ void pin1(T& r) { asm volatile("" : "+v"(r)); }
template <class T> __device__ __forceinline__ T opq(T r) { asm volatile("" : "+v"(r)); return r; }
template <class... T> __device__ __forceinline__ void pin(T&... r) { (pin1(r), ...); }
template <int N, class... T> __device__ __forceinline__ void wait_vm(T&... r) { asm volatile("s_waitcnt vmcnt(%0)" :: "n"(N) : "memory"); (pin1(r), ...); }
template <int N, class... T> __device__ __forceinline__ void wait_lg(T&... r) { asm volatile("s_waitcnt lgkmcnt(%0)" :: "n"(N) : "memory"); (pin1(r), ...); }
template <int OFF, class T> __device__ __forceinline__ void ds16(T& d, unsigned a) {
  asm volatile("ds_read_b128 %0, %1 offset:%2" : "=&v"(d) : "v"(a), "i"(OFF) : "memory");
}
template <int OFF> __device__ __forceinline__ void dsw16(unsigned a, const f32x4& v) {
  asm volatile("ds_write_b128 %0, %1 offset:%2" :: "v"(a), "v"(v), "i"(OFF) : "memory");
}
__device__ __forceinline__ void dsw4(unsigned a, float v) { asm volatile("ds_write_b32 %0, %1" :: "v"(a), "v"(v) : "memory"); }
#define HSC ""
template <class T> __device__ __forceinline__ void reld6(T (&B)[6], unsigned vo, const void* sb) {
  asm volatile("global_load_dwordx4 %0, %6, %7 offset:-2048 sc1\n\tglobal_load_dwordx4 %1, %6, %7 offset:-1024 sc1\n\t"
               "global_load_dwordx4 %2, %6, %7 sc1\n\tglobal_load_dwordx4 %3, %6, %7 offset:1024 sc1\n\t"
               "global_load_dwordx4 %4, %6, %7 offset:2048 sc1\n\tglobal_load_dwordx4 %5, %6, %7 offset:3072 sc1"
               : "+v"(B[0]), "+v"(B[1]), "+v"(B[2]), "+v"(B[3]), "+v"(B[4]), "+v"(B[5]) : "v"(vo), "s"(sb) : "memory");
}

constexpr int L_FLAG = 0;
constexpr int L_DUM = 256;
constexpr int L_XM = 512;
constexpr int L_XR = 768;
constexpr int L_CS = 1024;
constexpr int L_CS2 = 3072;
constexpr int L_Z1 = 4096;
constexpr int L_RAW = L_Z1;
constexpr int L_SLOT = 16384;
constexpr int L_KV = 512, KV_P = 3088, L_WO = L_KV + 8 * KV_P;
static_assert(L_WO % 16 == 0 && L_WO + 4 * 1024 <= L_SLOT + 2 * 8192, "staging");
constexpr int L_P2 = L_SLOT + 12 * 8192;
constexpr int CV_C = 4608, CV_CS = 4160, CV_BUF = CV_C + 2 * CV_CS;
constexpr int M_CV = L_SLOT;
constexpr int M_RING = L_SLOT + 6 * 8192;
constexpr int P3_G = L_SLOT;
constexpr int P3_C = P3_G + 80 * 1024;
constexpr int yat(int j) { return M_CV + (j >> 1) * CV_BUF + 8192 + (j & 1) * 2048; }
constexpr int M_LDS = 163840;
static_assert(3 * CV_BUF + 8192 <= 6 * 8192 && P3_C + 60 * 1024 <= M_LDS && CV_BUF >= 8192 + 4096, "LDS map");

enum { UX, UA, UB, UV };
struct Unit { int kind, idx, need, free, slot; };
#define SCHED gugugugwBuluouk_uuxuu_uuxuxu_uuxuxuu_Euuuuh_vuumumuh_vuumumuhF_vuvumum_DRvuuUHmmmmSptt_VhhhttttttJItttttttt
#define STR_(x) #x
#define STR(x) STR_(x)
constexpr char SCH[] = STR(SCHED);
constexpr int NSCH = sizeof(SCH) - 1;
constexpr int XNEED[10] = {1, 2, 2, 3, 3, 4, 4, 5, 5, 6};
constexpr int VNEED[5] = {5, 6, 7, 7, 8};
constexpr int XSLOT[10] = {17, 16, 15, 13, 10, 14, 11, 17, 9, 16}, WSLOT[10] = {6, 2, 4, 0, 6, 12, 10, 4, 0, 8}, VSLOT[10] = {8, 15, 3, 2, 14, 17, 16, 15, 7, 6}, XW = 6;
constexpr int cnt(char c, int e = NSCH) { int n = 0; for (int i = 0; i < e; ++i) n += SCH[i] == c || (c == 'u' && SCH[i] == 'g'); return n; }
constexpr int pos(char c, int k = 0) { for (int i = 0; i < NSCH; ++i) if (SCH[i] == c && k-- == 0) return i; return NSCH; }
constexpr Unit unit(int n) {
  for (int b = 1; b < 11; ++b) {
    for (int c = 0; c < 10; ++c) if (XNEED[c] == b && n-- == 0) return {UX, c, b, b + 1, XSLOT[c]};
    for (int c = 0; c < 5; ++c) if (VNEED[c] == b && n-- == 0) return {UV, c, b, b + 1, VSLOT[c]};
    if (n < 2) return {n ? UB : UA, b - 1, b < 8 ? b : 8, b < 7 ? b + 2 : 9, WSLOT[b - 1] + n};
    n -= 2;
  }
  return {UV, n + 5, 10, 99, VSLOT[n + 5]};
}
constexpr int FLAGB = cnt('_', pos('D')), GZ = cnt('_', pos('E'));
constexpr int nvm(char c) { return c == 'u' ? 2 : (c == 'H' ? 5 : (c == 'U' ? 4 : (c == 'F' || c == 'h' || c == 't' || c == 'w'))); }
constexpr int behind(int g, int e, char w) {
  const int b = cnt('_', e) + 1;
  int n = 0;
  for (int i = 0; i < e; ++i) {
    const char c = SCH[i];
    if (g == 0) { if (c == 'g') n = w == '_' && unit(cnt('u', i)).need <= b ? 0 : n + 2; if (c == 'B') n += 2; continue; }
    n = (w == '_' ? (c == 'u' && unit(cnt('u', i)).need <= b) || (c == 'h' && b == FLAGB) || (c == 'k' && b == GZ) : c == w) ? 0 : n + nvm(c);
  }
  return g == 0 && b >= GZ ? 0 : n;
}
constexpr bool plan_ok() {
  if (cnt('u') != 40 || cnt('_') != 9 || cnt('x') != 5 || cnt('h') != 6 || cnt('t') != 16 || cnt('I') * cnt('J') * cnt('p') != 1 || cnt('v') != 5 || cnt('m') != 10) return false;
  if (cnt('B') * cnt('D') * cnt('E') * cnt('F') * cnt('l') * cnt('o') * cnt('k') * cnt('H') * cnt('U') * cnt('V') * cnt('R') * cnt('S') != 1) return false;
  if (pos('l') < pos('B') || pos('o') < pos('B') || pos('k') < pos('l') || pos('k') > pos('_') || pos('o') > pos('_') || cnt('g', pos('B')) != cnt('g')) return false;
  if (cnt('_', pos('h', 2)) >= FLAGB || GZ < 3 || GZ > 6 || FLAGB < GZ || FLAGB > 9 || pos('o') > pos('k') || pos('H') > pos('I') || pos('F') > pos('U')) return false;
  if (cnt('_', pos('R')) != 8 || cnt('_', pos('S')) != 8 || pos('S') < pos('R') || cnt('_', pos('I')) != 9 || pos('J') < pos('U') || cnt('_', pos('J')) != 9) return false;
  if (cnt('_', pos('V')) != 9 || cnt('u', pos('V')) < 40) return false;
  for (int c = 0; c < 5; ++c) if (cnt('_', pos('x', c)) != XNEED[c] || cnt('_', pos('v', c)) != VNEED[c] || pos('m', 2 * c) < pos('v', c)) return false;
  if (cnt('_', pos('p')) != 8 || pos('p') < pos('m', 9)) return false;
  int fr[18] = {1, 1};
  for (int i = 0; i < NSCH; ++i) if (SCH[i] == 'u' || SCH[i] == 'g') {
    const Unit u = unit(cnt('u', i));
    const int p = cnt('_', i);
    if (p < fr[u.slot] || p >= u.need) return false;
    fr[u.slot] = u.free;
  }
  return true;
}
static_assert(plan_ok(), "phase A plan");
template <int I, int E, class F> __device__ __forceinline__ void sfor(F&& f) { if constexpr (I < E) { f(IC(I)); sfor<I + 1, E>(f); } }

__device__ __forceinline__ void conv_core(unsigned buf, int lane, unsigned Dw, f32x4 (&ac)[8]) {
  const int l15 = lane & 15, g4 = lane >> 4;
  const int c = (l15 + 1) & 1;
  unsigned aa[4];
#pragma unroll
  for (int k = 0; k < 4; ++k) aa[k] = buf + CV_C + c * CV_CS + (2031 - l15 + 8 * g4 - c) * 2 - 512 - 1024 * k;
  unsigned bb = buf + 512 + 32 * l15 - 32 + 16 * g4;
  u32x2 A[2][8][2];
  u32x4 B[2][8];
  constexpr int TT[36] = {7, 6, 5, 4, 7, 6, 5, 4, 7, 6, 5, 3, 7, 6, 5, 4, 7, 6, 3, 2, 7, 6, 5, 4, 7, 3, 2, 1, 7, 6, 5, 4, 3, 2, 1, 0};
  constexpr int AA[36] = {0, 0, 0, 0, 1, 1, 1, 1, 2, 2, 2, 0, 3, 3, 3, 2, 4, 4, 1, 0, 5, 5, 4, 3, 6, 2, 1, 0, 7, 6, 5, 4, 3, 2, 1, 0};
  auto ld1 = [&](int p, int n) {
    if (n < 16) {
      const int a = n >> 1, o = ((a & 1) ? 0 : 128) + 2 * (n & 1);
      asm volatile("ds_read2_b32 %0, %1 offset0:%2 offset1:%3" : "=&v"(A[p][(n >> 1) & 7][n & 1]) : "v"(aa[(a >> 1) & 3]), "i"(o), "i"(o + 1) : "memory");
    } else
      asm volatile("ds_read_b128 %0, %1 offset:%2" : "=&v"(B[p][n & 7]) : "v"(bb), "i"(512 * (n & 7)) : "memory");
  };
  auto next = [&](int dl) {
#pragma unroll
    for (int k = 0; k < 4; ++k) aa[k] += dl;
    bb += dl;
    SB();
  };
  auto land = [&](int p) {
    asm volatile("s_waitcnt lgkmcnt(0)" : "+v"(A[p][0][0]), "+v"(A[p][0][1]), "+v"(A[p][1][0]), "+v"(A[p][1][1]), "+v"(A[p][2][0]), "+v"(A[p][2][1]),
                 "+v"(A[p][3][0]), "+v"(A[p][3][1]), "+v"(A[p][4][0]), "+v"(A[p][4][1]), "+v"(A[p][5][0]), "+v"(A[p][5][1]), "+v"(A[p][6][0]), "+v"(A[p][6][1]),
                 "+v"(A[p][7][0]), "+v"(A[p][7][1]), "+v"(B[p][0]), "+v"(B[p][1]), "+v"(B[p][2]), "+v"(B[p][3]), "+v"(B[p][4]), "+v"(B[p][5]),
                 "+v"(B[p][6]), "+v"(B[p][7]) :: "memory");
    SB();
  };
  auto mm = [&](int p) {
#pragma unroll
    for (int n = 0; n < 36; ++n) {
      const int t = TT[n], a = AA[n];
      const u32x4 av = {A[p][a][0][0], A[p][a][0][1], A[p][a][1][0], A[p][a][1][1]};
      MFMA(ac[t], av, B[p][t - a]);
      if (n < 24) ld1(p ^ 1, n);
      SB();
    }
    land(p ^ 1);
  };
#pragma unroll
  for (int n = 0; n < 24; ++n) ld1(0, n);
  u32x4 ad;
#pragma unroll
  for (int e = 0; e < 4; ++e) ad[e] = lane == 32 + 2 * e + (lane & 1) + (lane & 8) * 3 ? Dw << (16 * (lane & 1)) : 0u;
  pin(ad);
  land(0);
#pragma unroll
  for (int t = 0; t < 8; ++t) MFMA0(ac[t], ad, B[0][t]);
#pragma unroll 1
  for (int r = 0; r < 8; r += 2) {
    next(-64);
    mm(0);
    next(r < 6 ? -64 : 0);
    mm(1);
  }
  asm volatile("s_nop 15" : "+v"(ac[0]), "+v"(ac[1]), "+v"(ac[2]), "+v"(ac[3]), "+v"(ac[4]), "+v"(ac[5]), "+v"(ac[6]), "+v"(ac[7]));
}

#define ROTA 3
#define RA3 0
#define RB2 4
#define RB4 2
#define THR 24
#define RB5 2
#define FE3 9
#define WSH 1
#define T3L 4
#define PAE 0
#define T7D 1
#define KC 40
#define NWY 8
#define CPR 1
#define PN 10
#define P2P 2
#define SGS 11

__global__ __launch_bounds__(512) void k_f(
    const u16* __restrict__ x, const u16* __restrict__ kin, const u16* __restrict__ vin, const u16* __restrict__ shift_k,
    const u16* __restrict__ ssm_k, const u16* __restrict__ ssm_D, const u16* __restrict__ w_in, const u16* __restrict__ w_sp,
    const u16* __restrict__ b_sp, const u16* __restrict__ w_out, u16* __restrict__ out, char* ws) {
  extern __shared__ __attribute__((aligned(16))) char smem[];
  const int tid = threadIdx.x, lane = tid & 63, wave = __builtin_amdgcn_readfirstlane(tid >> 6);
  const int b = blockIdx.x;
  const int xcd = b & 7, idx = b >> 3;
  const int rb = 8 * (xcd >> 1) + (idx >> 2), cb = 4 * (xcd & 1) + (idx & 3);
  const int t0 = rb * 64, d0 = cb * 80;
  const int g4 = lane >> 4, l15 = lane & 15;
  const unsigned lin = (unsigned)(lane * 16);
  const unsigned lb = lds_addr(smem), flg = lb + L_FLAG;
  AB_DECL;
  AB(0);
  asm volatile("" :: "s"(x), "s"(kin), "s"(vin), "s"(shift_k), "s"(ssm_k), "s"(ssm_D), "s"(w_in), "s"(w_sp), "s"(b_sp), "s"(w_out), "s"(out), "s"(ws));
  u64 tag = *(const __attribute__((address_space(4))) u64*)(uintptr_t)ws;
  int tmo = 1 << 16;
  auto cwarm = [&]() {
    u64 pc;
    unsigned clen;
    asm volatile("s_getpc_b64 %0\n.Lpc_here_%=:\n\ts_mov_b32 %1, .Lcode_end-.Lpc_here_%=\n\t.subsection 2\n.Lcode_end:\n\t.fill 256, 4, 0xBF800000\n\t.subsection 0" : "=s"(pc), "=s"(clen));
    touch(min((unsigned)(idx * 1536 + min(lane, 11) * 128), clen - 4u) & ~3u, (const void*)pc, lb + L_DUM);
  };

  const int cw = wave & 3, wm = wave & 1, wl = (wave >> 1) & 1, ksb = wm ^ wl, odd = cb & 1, pr = cb >> 1;
  const int r8 = lane >> 3, c8 = lane & 7;
  unsigned voX[2], voW[2];
#pragma unroll
  for (int h = 0; h < 2; ++h) {
    voX[h] = (unsigned)((h * 8 + r8) * (D_ * 2) + ((c8 ^ (h * 4 + (r8 & 3))) << 4) - h * 1024);
    voW[h] = (unsigned)((h * 8 + r8) * (F2_ * 2) + ((c8 ^ (h * 4 + (r8 & 2))) << 4) - h * 1024);
  }
  const int rot = (((rb & 7) * 5 >> 2) + ROTA * (cb & 3) + RA3 * (cb >> 2)) % 10, rot2 = (RB2 * (cb >> 2) + RB4 * (rb >> 3) + RB5 * (cb & 1)) & 7;
  const char* gX = (const char*)x + (size_t)(t0 + cw * 16) * (D_ * 2) + rot * 128;
  const char* gW = (const char*)w_in + (size_t)(cw * 16 + rot * 64) * (F2_ * 2) + pr * 384;
  const char *gV = gW + odd * 256, *gB = gW + (odd ? 128 : FH_ * 2 + 128);
  const char* gS = (const char*)w_sp + (size_t)t0 * (S_ * 2);
  const unsigned aoff[2] = {(unsigned)(r8 * 4096 + ((c8 ^ r8) << 4)), (unsigned)(r8 * 4096 + ((c8 ^ r8) << 4) + 32768)};
  const unsigned ldw = lb + cw * 2048;
  auto issue = [&](auto nc) {
    constexpr Unit u = unit(decltype(nc)::value);
    constexpr int lo = L_SLOT + u.slot * 8192;
    if (u.kind == UX) dma32r<lo, u.idx * 128, 1280, 10 - u.idx>(voX[0], voX[1], gX, ldw, rot);
    else dma32r<lo, u.idx * (64 * F2_ * 2) + (u.kind == UA ? FH_ * 2 : 0), 640 * F2_ * 2, 10 - u.idx>(voW[0], voW[1], u.kind == UB ? gB : gV, ldw, rot);
    SB();
  };
  const int kd = d0 + min(wave, 4) * 16 + r8 * 2;
  u32x4 sw;
  {
    const int ks0 = t0 + wave * 8;
#pragma unroll
    for (int i = 0; i < 3; ++i) {
      const int id = min(i * 64 + lane, 189), r = id / 10, c = id % 10;
      const char* kp = (r < 11 ? (const char*)kin + (size_t)max(ks0 - 3 + r, 0) * (D_ * 2) : (const char*)vin + (size_t)(ks0 + r - 11) * (D_ * 2)) + cb * 160 + c * 16;
      asm volatile("s_mov_b32 m0, %1\n\ts_nop 0\n\tglobal_load_lds_dwordx4 %0, off" :: "v"(kp), "s"(lb + L_KV + wave * KV_P + i * 1024) : "memory");
    }
    if (wave == 5) cwarm();
    SB();
  }
  auto ldsw = [&]() { asm volatile("global_load_dwordx4 %0, %1, %2" : "=&v"(sw) : "v"(kd * 8), "s"(shift_k) : "memory"); SB(); };
  unsigned kk[11], vv[8];
  u32x4 oa, ob;
  auto kvld = [&]() {
    const char* kl = smem + L_KV + c8 * KV_P + min(wave, 4) * 32 + r8 * 4;
#pragma unroll
    for (int i = 0; i < 11; ++i) kk[i] = *(const unsigned*)(kl + i * 160);
#pragma unroll
    for (int i = 0; i < 8; ++i) vv[i] = *(const unsigned*)(kl + (11 + i) * 160);
  };
  auto kvm = [&](int e0, int e1) {
    const f32x2 w0 = {lo16(sw[0]), lo16(sw[2])}, w1 = {hi16(sw[0]), hi16(sw[2])};
    const f32x2 w2 = {lo16(sw[1]), lo16(sw[3])}, w3 = {hi16(sw[1]), hi16(sw[3])};
    f32x2 kf[11], rr[8];
#pragma unroll
    for (int i = 2 * e0; i < 2 * e1 + 3; ++i) kf[i] = unpk((i < 3 && t0 + c8 == 0) ? 0u : kk[i]);
#pragma unroll
    for (int i = 2 * e0; i < 2 * e1; ++i) {
      f32x2 a0 = kf[i] * w3;
      a0 = __builtin_elementwise_fma(kf[i + 1], w2, a0);
      a0 = __builtin_elementwise_fma(kf[i + 2], w1, a0);
      a0 = __builtin_elementwise_fma(kf[i + 3], w0, a0);
      rr[i] = a0 * unpk(vv[i]);
    }
#pragma unroll
    for (int e = e0; e < e1; ++e) { oa[e] = pack2(rr[2 * e][0], rr[2 * e + 1][0]); ob[e] = pack2(rr[2 * e][1], rr[2 * e + 1][1]); }
  };
  auto kvst = [&]() {
    const unsigned ko = (unsigned)((kd * S_ + t0 + c8 * 8) * 2);
    st16(ko, ws + WS_KV, oa);
    st16(ko, ws + WS_KV + S_ * 2, ob);
    SB();
  };
  auto barrier0 = [&](int nv) {
    SB();
    asm volatile("s_waitcnt vmcnt(%0) lgkmcnt(0)\n\ts_barrier" :: "n"(nv) : "memory");
    SB();
    asm volatile("" : "+v"(sw), "+s"(tag));
    tag += 1;
    asm volatile("" : "+s"(tag));
  };
  unsigned pX[2], pW[2];
#pragma unroll
  for (int k = 0; k < 2; ++k) {
    const int q = l15 >> 2, p = lane & 3, sz = (q >> 1) | ((g4 & 1) << 1);
    pX[k] = lb + (wm * 32 + q * 8 + p) * 128 + ((((k ^ ksb) * 4 + g4) ^ (l15 & 7)) << 4);
    pW[k] = lb + ((k ^ ksb) * 32 + g4 * 8 + q) * 128 + (((sz << 1) | (p >> 1)) << 4) + (p & 1) * 8;
  }
  bf16x8 ones;
#pragma unroll
  for (int e = 0; e < 8; ++e) ones[e] = (short)0x3F80;
  f32x4 xm[2], xr[2];
  auto ldxs = [&]() {
#pragma unroll
    for (int i = 0; i < 2; ++i) {
      xm[i] = *(const f32x4*)(smem + L_XM + (wm * 32 + g4 * 8 + i * 4) * 4);
      xr[i] = *(const f32x4*)(smem + L_XR + (wm * 32 + g4 * 8 + i * 4) * 4);
    }
  };
  auto act = [&](const f32x4& a, float c, int i) {
    const f32x2 nc = {-c, -c};
    const f32x2 lo = gelu2(__builtin_elementwise_fma((f32x2){xm[i][0], xm[i][1]}, nc, (f32x2){a[0], a[1]}) * (f32x2){xr[i][0], xr[i][1]});
    const f32x2 hi = gelu2(__builtin_elementwise_fma((f32x2){xm[i][2], xm[i][3]}, nc, (f32x2){a[2], a[3]}) * (f32x2){xr[i][2], xr[i][3]});
    return (f32x4){lo[0], lo[1], hi[0], hi[1]};
  };
  auto pack8 = [&](const f32x4& a, const f32x4& c) { return (u32x4){pack2(a[0], a[1]), pack2(a[2], a[3]), pack2(c[0], c[1]), pack2(c[2], c[3])}; };
  auto z1at = [&](int f0) { return (u32x4*)(smem + L_Z1 + (f0 + l15) * 128 + (((wm * 4 + g4) ^ (l15 & 7)) << 4)); };
  auto z1put = [&](int f0, const u32x4& v) { *z1at(f0) = v; };
  f32x4 ac2[2][2], cs2[2];
  s16x4 VF[10][2][2];
  unsigned aV[2][2];
  auto rdV = [&](auto hc_, auto nc, int r) {
    constexpr int n = decltype(nc)::value, c = 5 * decltype(hc_)::value + (n >> 1), so = (VSLOT[c] - 2) * 8192, po = n < 2 ? -1 : ((VSLOT[n < 2 ? c : c - 1] - 2) * 8192) >> 16;
    const int jj = r >> 1;
    if (!(r & 1)) {
      if ((so >> 16) != po) aV[n & 1][jj] = opq((pW[n & 1] ^ (unsigned)(wl * 64 + jj * 32)) + L_SLOT + 2 * 8192 + (so & ~0xFFFF));
      VF[n][jj][0] = tr_asm<(so & 0xFFFF)>(aV[n & 1][jj]);
    } else VF[n][jj][1] = tr_asm<(so & 0xFFFF) + 512>(aV[n & 1][jj]);
    SB();
  };
  auto mm2 = [&](auto hc_, auto nc, bf16x8 (*X)[2][2], int nw, auto&& nxt) {
    constexpr int n = decltype(nc)::value, j = n >> 1, k = n & 1;
    constexpr bool z = n == 0 && !decltype(hc_)::value;
    const bf16x8 w0 = join8(VF[n][0][0], VF[n][0][1]), w1 = join8(VF[n][1][0], VF[n][1][1]);
    if (k == 0) { MFMAW<n == 0>(nw, cs2[0], ones, w0); MFMAW<n == 0>(-1, cs2[1], ones, w1); }
    MFMAW<z>(k ? nw : -1, ac2[0][0], X[j][k][0], w0); nxt(0);
    MFMAW<z>(-1, ac2[1][0], X[j][k][1], w0); nxt(1);
    MFMAW<z>(-1, ac2[0][1], X[j][k][0], w1); nxt(2);
    MFMAW<z>(-1, ac2[1][1], X[j][k][1], w1); nxt(3);
    if constexpr (n == 9) {
      pin(cs2[0], cs2[1]);
      if (g4 < 2) *(float*)(smem + L_CS2 + (wave * 32 + lane) * 4) = g4 ? cs2[1][0] : cs2[0][0];
    }
    SB();
  };
  const unsigned p2a = lb + L_P2 + cw * 4096 + lin;
  auto sttag = [&](const char* p) {
    asm volatile("s_mov_b64 exec, 1\n\tglobal_store_dwordx2 %0, %1, %2 sc1\n\ts_mov_b64 exec, -1" :: "v"(lin), "v"((u32x2){opq((unsigned)tag), opq((unsigned)(tag >> 32))}), "s"(p) : "memory");
  };
  auto ldtag = [&](u32x2& f, unsigned vo, const char* p) { asm volatile("global_load_dwordx2 %0, %1, %2 sc1" : "=&v"(f) : "v"(vo), "s"(p) : "memory"); };
  auto notag = [&](const u32x2& f) { return __builtin_amdgcn_ballot_w64(f[0] != (unsigned)tag || f[1] != (unsigned)(tag >> 32)) != 0; };
  auto waittag = [&](u32x2& f, unsigned vo, const char* p) {
    while (__builtin_expect(notag(f) && tmo > 0, 0)) {
      --tmo;
      __builtin_amdgcn_s_sleep(4);
      ldtag(f, vo, p);
      wait_vm<0>(f);
    }
  };
  u32x2 ef;

  char* const hf = ws + WS_H;
  bf16x8 Wq[15];
  const char* wg = ws + WS_WO + (size_t)(wave * 120 + cb * 5) * 1024;
  auto ldW = [&](int n0, int n1) {
    if (n0 == 0) ldtag(ef, (unsigned)((lane & 3) * 8), ws + WS_LF + (rb * 8 + wave) * 32);
#pragma unroll
    for (int n = n0; n < n1; ++n)
      asm volatile("global_load_dwordx4 %0, %1, %2 offset:%3 sc1" : "=&v"(Wq[n]) : "v"(lin), "s"(wg + (n / 5) * 40960 + 2048), "i"((n % 5) * 1024 - 2048) : "memory");
    SB();
  };
  f32x4 ac3[5];
  auto gemm3 = [&](auto khc, auto&& pre) {
    constexpr int kh = decltype(khc)::value;
    unsigned hoff[4];
#pragma unroll
    for (int it = 0; it < 4; ++it) hoff[it] = (unsigned)(((rb * 4 + ((cw + 1 + it) & 3)) * 24 + 3 * wave) * 1024) + lin;
    u32x4 Hq[4][3];
    auto ldh1 = [&](int it, int k) {
      asm volatile("global_load_dwordx4 %0, %1, %2 offset:%3 " HSC : "=&v"(Hq[it][k]) : "v"(hoff[it]), "s"(hf), "i"(k * 1024) : "memory");
    };
    waittag(ef, (unsigned)((lane & 3) * 8), ws + WS_LF + (rb * 8 + wave) * 32);
    AB(18);
    if (!kh) signal1(flg + 48);
    pre();
    flag_wait_asm(flg + 48, 4u);
    AB(19);
    __builtin_amdgcn_s_setprio(3);
    ldh1(0, 0); ldh1(0, 1); ldh1(0, 2);
    __builtin_amdgcn_s_setprio(2);
    ldh1(1, 0); ldh1(1, 1); ldh1(1, 2);
    __builtin_amdgcn_s_setprio(0);
    SB();
    flag_wait_asm(flg + 64, 4u);
    const unsigned px = lb + (kh ? P3_C + cw * 15360 : P3_G + cw * 20480) + lin;
    f32x4 pa[4][5], ae[5];
    auto put = [&](int it, int j) { asm volatile("ds_write_b128 %0, %1 offset:%2" :: "v"(px), "v"((it & 1) ? ac3[j] : ae[j]), "i"((it * 5 + j) * 1024) : "memory"); };
    auto fetch = [&](int n, int g, int q) {
      const int ms = (cw - 1 - q) & 3;
      const unsigned pn = lb + (g ? P3_C + ms * 15360 : P3_G + ms * 20480) + lin;
#pragma unroll
      for (int j = 0; j < 5; ++j) asm volatile("ds_read_b128 %0, %1 offset:%2" : "=&v"(pa[n][j]) : "v"(pn), "i"((q * 5 + j) * 1024) : "memory");
      SB();
    };
    sfor<0, 4>([&](auto itc) {
      constexpr int it = decltype(itc)::value;
      wait_vm<(it < 3 ? 3 : 0)>(Hq[it][0], Hq[it][1], Hq[it][2]);
      if constexpr (it == 0) pin(Wq[0], Wq[1], Wq[2], Wq[3], Wq[4], Wq[5], Wq[6], Wq[7], Wq[8], Wq[9], Wq[10], Wq[11], Wq[12], Wq[13], Wq[14]);
      auto bad = [&]() { return __builtin_amdgcn_ballot_w64(umx(umx(Hq[it][0][3], Hq[it][1][3]), Hq[it][2][3]) == HOLE) != 0; };
      if (__builtin_expect(bad(), 0)) {
        do {
          --tmo;
          asm volatile("s_sleep 2\n\tglobal_load_dwordx4 %0, %3, %4 sc1\n\tglobal_load_dwordx4 %1, %3, %4 offset:1024 sc1\n\t"
                       "global_load_dwordx4 %2, %3, %4 offset:2048 sc1\n\ts_waitcnt vmcnt(0)"
                       : "+v"(Hq[it][0]), "+v"(Hq[it][1]), "+v"(Hq[it][2]) : "v"(hoff[it]), "s"(hf) : "memory");
        } while (bad() && tmo > 0);
      }
      SB();
#pragma unroll
      for (int k = 0; k < 3; ++k)
#pragma unroll
        for (int j = 0; j < 5; ++j) {
          f32x4& ac = (it & 1) ? ac3[j] : ae[j];
          if (k == 0) MFMA0(ac, Wq[j], Hq[it][0]); else MFMA(ac, Wq[k * 5 + j], Hq[it][k]);
          if constexpr (it + 2 < 4) { if (k == 0 && j < 3) ldh1(it + 2 < 4 ? it + 2 : 0, j); }
          const int m = k * 5 + j;
          if (it > 0 && m < 10 && !(m & 1)) put(it - 1, m >> 1);
          if (it > 0 && m == 8) signal1(flg + 160 + (kh ? 1 + (it > 1) : it == 3) * 16 + ((cw + it) & 3) * 4);
          if (it == 3 && m == FE3 && !kh) { flag_wait_asm(flg + 160 + wave * 4, 2u); fetch(0, 0, 0); fetch(1, 0, 1); }
          SB();
        }
      if constexpr (it == 3) {
        if (kh) {
          flag_wait_asm(flg + 192 + cw * 4, 3u);
          fetch(0, 1, 1); fetch(1, 1, 2); fetch(2, 0, 3);
#pragma unroll
          for (int q = 0; q < 3; ++q) {
            asm volatile("s_waitcnt lgkmcnt(%5)" : "+v"(pa[q][0]), "+v"(pa[q][1]), "+v"(pa[q][2]), "+v"(pa[q][3]), "+v"(pa[q][4]) : "i"(10 - 5 * q) : "memory");
            if (q == 0) pin(ac3[0], ac3[1], ac3[2], ac3[3], ac3[4]);
#pragma unroll
            for (int j = 0; j < 5; ++j) ac3[j] += pa[q][j];
            SB();
          }
        } else {
          flag_wait_asm(flg + 176 + cw * 4, 2u);
          fetch(2, 1, 0); fetch(3, 0, 2);
          pin(pa[0][0], pa[0][1], pa[0][2], pa[0][3], pa[0][4], pa[1][0], pa[1][1], pa[1][2], pa[1][3], pa[1][4], ac3[0], ac3[1], ac3[2], ac3[3], ac3[4]);
#pragma unroll
          for (int j = 0; j < 5; ++j) ac3[j] += pa[0][j] + pa[1][j];
          SB();
          wait_lg<0>(pa[2][0], pa[2][1], pa[2][2], pa[2][3], pa[2][4], pa[3][0], pa[3][1], pa[3][2], pa[3][3], pa[3][4]);
#pragma unroll
          for (int j = 0; j < 5; ++j) {
            ac3[j] += pa[2][j] + pa[3][j];
            put(3, j);
          }
          signal1(flg + 192 + cw * 4);
        }
      }
      SB();
      if constexpr (it == 0) AB(20);
      if constexpr (it == 2) AB(21);
    });
    AB(22);
  };
  if (wave >= 4) {
    const int wid = rb * 4 + cw;
    __builtin_amdgcn_s_setprio(3);
    ldsw();
    if (wid < 120) dma16((unsigned)((lane >> 1) * (D_ * 2) + (lane & 1) * 16), w_out + (size_t)(wid / 5) * 32 * D_ + d0 + (wid % 5) * 16, lb + L_WO + cw * 1024);
    SB();
    u32x4 hole = {HOLE, HOLE, HOLE, HOLE};
    bf16x8 XF[5][2][2];
    auto rdXF = [&](auto cc) {
      constexpr int c = decltype(cc)::value, so = L_SLOT + XSLOT[c] * 8192;
#pragma unroll
      for (int k = 0; k < 2; ++k) {
        const unsigned a = pX[k] + (so & ~0x7FFF);
        ds16<(so & 0x7FFF)>(XF[c][k][0], a);
        ds16<(so & 0x7FFF) + 512>(XF[c][k][1], a);
      }
      SB();
    };
    u32x4 TA[16];
    auto ldT = [&](u32x4& r, int d, int w, int i) {
      asm volatile("global_load_dwordx4 %0, %1, %2" : "=&v"(r) : "v"(aoff[i & 1] + (unsigned)(((d + rot2) & 7) * 128)), "s"(gS + (((w & 2) + 2 + (i >> 1)) & 3) * 65536 + w * 1024) : "memory");
    };
    auto ldx = [&](int c) {
      u32x4 xv;
      asm volatile("global_load_dwordx4 %0, %1, %2\n\ts_waitcnt vmcnt(0)" : "=&v"(xv) : "v"((unsigned)(lane * (D_ * 2) + c * 16)), "s"(x + (size_t)t0 * D_) : "memory");
      return xv;
    };
    const bool docv = cw < ((rb & 1) ? 2 : 3);
    const int d = d0 + (cw < 2 ? 2 * rb + cw : 64 + (rb >> 1));
    u32x4 hc[4], uc[4];
    const unsigned clin = docv ? lin : 0u;
    unsigned Dw;
    auto ldhc = [&]() {
#pragma unroll
      for (int k = 0; k < 4; ++k) asm volatile("global_load_dwordx4 %0, %1, %2 offset:%3" : "=&v"(hc[k]) : "v"(clin), "s"(ssm_k + (size_t)d * S_), "i"(k * 1024) : "memory");
      asm volatile("global_load_ushort %0, %1, %2" : "=&v"(Dw) : "v"(0u), "s"(ssm_D + d) : "memory");
    };
    unsigned rwo;
    f32x2 ra, rc;
    auto rdraw = [&]() { asm volatile("ds_read_b64 %0, %2\n\tds_read_b64 %1, %2 offset:512" : "=&v"(ra), "=&v"(rc) : "v"(rwo) : "memory"); SB(); };
    const char* efp = ws + WS_EF + cb * 256;
    char* cb_ = smem + M_CV + cw * CV_BUF;
    auto run = [&](auto ic) {
      constexpr int i = decltype(ic)::value, p = cnt('_', i) + 1;
      constexpr char c = SCH[i];
      if constexpr (c == '_') {
        CB(3 * (p - XBAR));
        asm volatile("s_waitcnt vmcnt(%0) lgkmcnt(0)" :: "n"(behind(1, i, '_')) : "memory");
        CB(3 * (p - XBAR) + 1);
        asm volatile("s_barrier" ::: "memory");
        CB(3 * (p - XBAR) + 2);
        SB();
#pragma unroll
        for (int q = 0; q < 5; ++q) if (XNEED[q] + 1 == p) pin(XF[q][0][0], XF[q][0][1], XF[q][1][0], XF[q][1][1]);
        if constexpr (p == 7) {
          rwo = lb + L_RAW + ((lane >> 3) * 4 + (lane & 3)) * 16 + (lane & 4) * 2;
          pin(rwo);
        }
        if constexpr (p == 9) __builtin_amdgcn_s_setprio(CPR);
        if constexpr (p == 2) AB(4);
        if constexpr (p == 6) AB(5);
        if constexpr (p == 9) AB(6);
      }
      if constexpr (c == 'u') issue(IC(cnt('u', i)));
      if constexpr (c == 'x') rdXF(IC(cnt('x', i)));
      if constexpr (c == 'B') {
        AB(1);
        barrier0(behind(1, i, 'B'));
        pin(ones, hole);
      }
      if constexpr (c == 'v') sfor<2 * cnt('v', i), 2 * cnt('v', i) + 2>([&](auto nc) { rdV(IC(0), nc, 0); rdV(IC(0), nc, 1); rdV(IC(0), nc, 2); rdV(IC(0), nc, 3); });
      if constexpr (c == 'm') {
        constexpr int n = cnt('m', i);
        mm2(IC(0), IC(n), XF, VNEED[n >> 1] < p - 1 || (n & 1) ? -1 : 0, [](int) {});
      }
      if constexpr (c == 'p') {
        asm volatile("s_nop 7" : "+v"(ac2[0][0]), "+v"(ac2[0][1]), "+v"(ac2[1][0]), "+v"(ac2[1][1]));
        dsw16<0>(p2a, ac2[0][0]); dsw16<1024>(p2a, ac2[1][0]); dsw16<2048>(p2a, ac2[0][1]); dsw16<3072>(p2a, ac2[1][1]);
      }
      if constexpr (c == 'w')
        touch((unsigned)(min(lane, 19) * (F2_ * 2)), (const char*)w_in + (size_t)(((rb + WSH) & 7) * 80 + cw * 20) * (F2_ * 2) + pr * 384 + odd * 256, lb + L_DUM);
      if constexpr (c == 'l') { if (wave == 7) kvld(); }
      if constexpr (c == 'k') { if (wave == 7) { kvm(0, 4); kvst(); } }
      if constexpr (c == 'E' || c == 'D') { if (wave == 5) sttag(ws + WS_EF + (c == 'D' ? 2048 : 0) + (cb * 32 + rb) * 8); }
      if constexpr (c == 'U') {
        wait_vm<behind(1, i, 'F')>(ef);
        waittag(ef, (unsigned)((lane & 31) * 8), efp);
#pragma unroll
        for (int k = 0; k < 4; ++k) asm volatile("global_load_dwordx4 %0, %1, %2 offset:%3 sc1" : "=&v"(uc[k]) : "v"(clin), "s"(ws + WS_KV + (size_t)d * (S_ * 2)), "i"(k * 1024) : "memory");
      }
      if constexpr (c == 'F') ldtag(ef, (unsigned)((lane & 31) * 8), efp);
      if constexpr (c == 'o') {
        if (wid < 120) {
          const unsigned ra = lb + L_WO + cw * 1024 + (8 * g4 + (l15 >> 2)) * 32 + 8 * (lane & 3);
          s16x4 lo = tr_asm<0>(ra), hi = tr_asm<128>(ra);
          wait_lg<0>(lo, hi);
          st16(lin, ws + WS_WO + (size_t)((wid / 5) * 40 + cb * 5 + wid % 5) * 1024, __builtin_bit_cast(u32x4, join8(lo, hi)));
        }
      }
      if constexpr (c == 'h') {
        constexpr int n = cnt('h', i);
        st16(lin, ws + (n < 3 ? WS_Z2 + (size_t)((rb * 2 + wl) * 48 + cb * 6 + wm * 3 + n) * 1024 : WS_H + (size_t)((rb * 4 + cw) * 24 + cb * 3 + n - 3) * 1024), hole);
      }
      if constexpr (c == 't') { constexpr int n = cnt('t', i); ldT(TA[n], 4 + (n >> 3), cw, n & 7); }
      if constexpr (c == 'H') ldhc();
      if constexpr (c == 'R') rdraw();
      if constexpr (c == 'V') {
        wait_vm<behind(1, i, '_')>();
        signal1(flg + 60);
        AB(2);
      }
      if constexpr (c == 'I') {
        wait_vm<behind(1, i, 'H')>(hc[0], hc[1], hc[2], hc[3], Dw);
        if (docv) {
          if (lane < 36) *(u32x4*)(cb_ + (lane < 32 ? lane * 16 : CV_C + ((lane >> 1) & 1) * CV_CS + 4096 + (lane & 1) * 16)) = (u32x4){0u, 0u, 0u, 0u};
#pragma unroll
          for (int k = 0; k < 4; ++k) {
            const int a = k * 64 + lane;
            const int h0 = k ? __builtin_amdgcn_readlane((int)hc[k ? k - 1 : 0][3], 63) : 0;
            const unsigned hp = (unsigned)__builtin_amdgcn_update_dpp(h0, (int)hc[k][3], 0x138, 0xF, 0xF, false);
            const unsigned w[5] = {hp, hc[k][0], hc[k][1], hc[k][2], hc[k][3]};
            u32x4 sw, mx;
#pragma unroll
            for (int q = 0; q < 4; ++q) {
              sw[q] = (w[4 - q] >> 16) | (w[4 - q] << 16);
              mx[q] = (w[4 - q] & 0xFFFFu) | (w[3 - q] & 0xFFFF0000u);
            }
            char* cp = cb_ + CV_C + (2040 - 8 * a) * 2;
            *(u32x4*)(cp) = sw;
            *(u32x4*)(cp + CV_CS) = mx;
          }
        }
      }
      if constexpr (c == 'J') {
        wait_vm<behind(1, i, 'U')>(uc[0], uc[1], uc[2], uc[3]);
        if (docv) {
#pragma unroll
          for (int k = 0; k < 4; ++k) *(u32x4*)(cb_ + 512 + (k * 64 + lane) * 16) = uc[k];
        }
      }
      if constexpr (c == 'S') {
        wait_lg<0>(ra, rc);
        if (wave == 6) {
          const float s1 = nv(ra[0]) + rc[0], fm = s1 * (1.0f / 640.0f), sm = s1 * fm;
          float fq = (ra[1] - sm) + rc[1];
          if (__builtin_expect(__builtin_amdgcn_ballot_w64(!(fq >= 1e-3f * sm)) != 0, 0)) {
            const f32x2 mm = {fm, fm};
            f32x2 q2 = {0.f, 0.f};
#pragma unroll 1
            for (int c = 0; c < 80; ++c) {
              const u32x4 xv = ldx(c);
#pragma unroll
              for (int e = 0; e < 4; ++e) { const f32x2 d = unpk(xv[e]) - mm; q2 = __builtin_elementwise_fma(d, d, q2); }
            }
            fq = q2[0] + q2[1];
          }
          const float vbf = rbf(fmaxf(fq, 0.f) * (1.0f / 640.0f));
          *(float*)(smem + L_XM + lane * 4) = rbf(fm);
          *(float*)(smem + L_XR + lane * 4) = __builtin_amdgcn_rcpf(rbf(__builtin_amdgcn_sqrtf(rbf(vbf + 1e-5f))));
        }
      }
      SB();
    };
    sfor<0, NSCH>(run);
    __builtin_amdgcn_s_setprio(0);
    AB(9);
    auto ldS = [&](auto dc, auto wc) {
      constexpr int dd = decltype(dc)::value, w = decltype(wc)::value;
      const unsigned to = (unsigned)(((dd + rot2) & 7) * 128);
      if (THR < 60) wait_vm<THR>();
      dma32s<M_RING + w * 24576 + (2 - dd) * 8192, (((w & 2) + 2) & 3) * 65536 + w * 1024>(aoff[0] + to, aoff[1] - 1024 + to, gS, lb);
      if (THR < 60) wait_vm<THR>();
      dma32s<M_RING + w * 24576 + (2 - dd) * 8192 + 2048, (((w & 2) + 3) & 3) * 65536 + w * 1024>(aoff[0] + to, aoff[1] - 1024 + to, gS, lb);
      if (THR < 60) wait_vm<THR>();
      dma32s<M_RING + w * 24576 + (2 - dd) * 8192 + 4096, (w & 2) * 65536 + w * 1024>(aoff[0] + to, aoff[1] - 1024 + to, gS, lb);
      if (THR < 60) wait_vm<THR>();
      dma32s<M_RING + w * 24576 + (2 - dd) * 8192 + 6144, ((w & 2) + 1) * 65536 + w * 1024>(aoff[0] + to, aoff[1] - 1024 + to, gS, lb);
      SB();
    };
    auto epi2 = [&]() {
      flag_wait_asm<SLP>(flg + 132, 4u);
#pragma unroll
      for (int jj = 0; jj < 2; ++jj) {
        u32x4* zp = z1at(odd * 32 + wl * 32 + jj * 16);
        u32x4 o = *zp;
#pragma unroll
        for (int q = 0; q < 4; ++q) o[q] = pack2s(gelu1(nv(lo16(o[q]))), gelu1(hi16(o[q])));
        *zp = o;
      }
      signal1(flg + 56);
      SB();
    };
    auto putT = [&](const u32x4* T, unsigned a, unsigned f) {
#pragma unroll
      for (int i = 0; i < 8; ++i) asm volatile("ds_write_b128 %0, %1 offset:%2" :: "v"(a), "v"(T[i]), "i"(i * 1024) : "memory");
      signal1(f);
    };
    const unsigned tx = lb + M_CV + cw * CV_BUF + lin;
    auto landT = [&]() {
      sttag(ws + WS_LF + (rb * 8 + cb) * 32 + cw * 8);
      putT(TA, tx, flg + 112 + wl * 4);
    };
    auto putT4 = [&]() {
      flag_wait2(flg + 96 + wl * 8, 1u);
      putT(TA + 8, lb + M_RING + cw * 24576 + 8192 + lin, flg + 120 + wl * 4);
    };
    if (docv) {
      flag_wait_asm(flg + 36, 4u);
      AB(12);
      f32x4 ac[8];
      conv_core(lds_addr(cb_), lane, Dw, ac);
      AB(13);
      signal1(flg + 40);
#pragma unroll
      for (int k = 0; k < 4; ++k) {
        u32x2 ov[2];
#pragma unroll
        for (int t = 0; t < 2; ++t)
#pragma unroll
          for (int h = 0; h < 2; ++h) ov[t][h] = pack2(ac[2 * k + t][2 * h], ac[2 * k + t][2 * h + 1]);
        const auto s0 = __builtin_amdgcn_permlane16_swap(ov[0][0], ov[1][0], false, false);
        const auto s1 = __builtin_amdgcn_permlane16_swap(ov[0][1], ov[1][1], false, false);
        st16((unsigned)((256 * (g4 & 1) + 16 * l15 + 8 * (g4 >> 1)) * 2), ws + WS_Y + (size_t)d * (S_ * 2) + k * 1024, (u32x4){s0[0], s1[0], s0[1], s1[1]});
      }
      wait_vm<4>();
      landT();
      wait_vm<1>();
      sttag(ws + WS_YF + d * 8);
      epi2();
      putT4();
    } else if (cw != 3) {
      wait_vm<0>();
      landT();
      epi2();
      putT4();
    } else {
      flag_wait_asm(flg + 36, 4u);
      ldS(IC(0), IC(0)); ldS(IC(0), IC(1)); ldS(IC(0), IC(2)); ldS(IC(1), IC(1)); ldS(IC(2), IC(1));
      wait_vm<40>();
      landT();
      AB(12);
      flag_wait_asm(flg + 16, 4u);
      AB(8);
      wait_vm<24>();
      ldS(IC(0), IC(3)); ldS(IC(1), IC(0)); ldS(IC(1), IC(2)); ldS(IC(1), IC(3));
      wait_vm<24>();
      signal1(flg + 32);
      ldS(IC(2), IC(0)); ldS(IC(2), IC(2)); ldS(IC(2), IC(3));
      ldtag(ef, (unsigned)((lane & 1) * 8), ws + WS_XF + (rb * 4 + pr) * 16);
      AB(10);
      u32x4 TB[32];
      auto ring2 = [&]() { dsw4(flg + 224 + (lane & 1) * 4, __uint_as_float(2u)); };
#pragma unroll
      for (int n = 0; n < 32; ++n) {
        if (n == 8) wait_vm<32>();
        if (THR < 60 && !(n & 1)) wait_vm<THR>();
        if (THR < 32 && n == THR) ring2();
        ldT(TB[n], 6, n >> 3, n & 7);
      }
      wait_vm<32>(ef);
      if (THR >= 32) ring2();
      AB(11);
      if (!odd) {
        u32x4 zx[4];
        waittag(ef, (unsigned)((lane & 1) * 8), ws + WS_XF + (rb * 4 + pr) * 16);
#pragma unroll
        for (int c = 0; c < 4; ++c)
          asm volatile("global_load_dwordx4 %0, %1, %2 offset:%3 sc1" : "=&v"(zx[c]) : "v"(lin), "s"(ws + WS_ZX + (size_t)(rb * 4 + pr) * 4096), "i"(c * 1024) : "memory");
        wait_vm<0>(zx[0], zx[1], zx[2], zx[3]);
#pragma unroll
        for (int c = 0; c < 4; ++c) *(u32x4*)(smem + L_Z1 + (64 + (c & 1) * 16 + l15) * 128 + ((((c >> 1) * 4 + g4) ^ (l15 & 7)) << 4)) = zx[c];
      }
      wait_vm<0>();
      epi2();
      signal1(flg + 56);
      putT4();
#pragma unroll
      for (int w = 0; w < 4; ++w) {
        if (!(w & 1)) flag_wait2(flg + 96 + w * 4, 2u);
        putT(TB + 8 * w, lb + M_RING + w * 24576 + lin, flg + 144 + (w >> 1) * 4);
      }
      AB(13);
    }
    SB();
    wait_vm<0>();
    AB(14);
    u32x2 xv[5];
    f32x2 sv[5][2];
    flag_wait_asm<SLP>(flg + 40, (rb & 1) ? 2u : 3u);
    const int yc = min(cw * 24, 56);
    const unsigned yfo = (unsigned)(min(lane, 23) * 8);
    const char* yfp = ws + WS_YF + (d0 + yc) * 8;
    auto ldy = [&]() {
#pragma unroll
      for (int n = 0; n < 3; ++n) {
        const int q = (yc >> 3) + n;
        asm volatile("s_mov_b32 m0, %2\n\ts_nop 0\n\tglobal_load_lds_dwordx4 %0, %1 sc1" :: "v"((unsigned)(r8 * (S_ * 2) + ((c8 ^ (r8 & 6)) << 4))),
                     "s"(ws + WS_Y + ((size_t)(d0 + q * 8) * S_ + t0) * 2), "s"(lb + yat(0) + (q >> 2) * CV_BUF + (q & 3) * 1024) : "memory");
      }
    };
    unsigned xo[3], oo[2] = {(unsigned)(l15 * D_ * 2 + 32 * (g4 & 1) + 16 * (g4 >> 1)), (unsigned)(l15 * D_ * 2 + 8 * g4)};
    const u16* op = out + (size_t)(t0 + cw * 16) * D_ + d0;
    pin(oo[0], oo[1]);
    asm volatile("" : "+s"(op));
#pragma unroll
    for (int n = 0; n < 3; ++n) { const int id = min(n * 64 + lane, 159); xo[n] = opq((unsigned)((id / 10) * (D_ * 2) + (id % 10) * 16)); }
    flag_wait2(flg + 96 + wl * 8, 3u);
    {
      const unsigned to = (unsigned)(((7 + rot2) & 7) * 128);
      for (int w = cw; w < (cw < 2 ? cw + 1 : 4 - (cw & 1)); ++w)
        sfor<0, 4>([&](auto ic) {
          constexpr int i = decltype(ic)::value;
          dma32s<M_RING + 16384 + i * 2048, 0>(aoff[0] + to, aoff[1] - 1024 + to, gS + ((2 * wl + 2 + i) & 3) * 65536 + w * 1024, lb + w * 24576);
          if (T7D) __builtin_amdgcn_s_sleep(T7D);
        });
    }
    flag_wait2(flg + 96 + wl * 8, 4u);
    ldtag(ef, yfo, yfp);
    wait_vm<1>();
    signal1(flg + 152 + wl * 4, cw < 2 ? 1u : 2u - 2u * (cw & 1));
    wait_vm<0>(ef);
    flag_wait_asm<SLP / 2 + 1>(flg + 44, 4u);
    waittag(ef, yfo, yfp);
    AB(15);
    ldy();
    static_assert(SGS >= 10, "");
#pragma unroll
    for (int n = 0; n < 3; ++n) dma16(xo[n], x + (size_t)(t0 + cw * 16) * D_ + d0, lb + M_RING + cw * 24576 + 8192 + n * 1024);
    ldW(0, NWY);
    wait_vm<NWY>(ef);
    AB(17);
    signal1(flg + 52);
    gemm3(IC(1), [&]() {
      flag_wait_asm(flg + 52, 4u);
      const int yr_ = 4 * g4 + (l15 >> 2);
      const unsigned ya = lb + yr_ * 128 + (((cw * 2 + ((lane >> 1) & 1)) ^ (yr_ & 6)) << 4) + (lane & 1) * 8;
      const float nm = -*(const float*)(smem + L_XM + (cw * 16 + l15) * 4), sr = *(const float*)(smem + L_XR + (cw * 16 + l15) * 4);
      s16x4 yr[5] = {tr_asm<yat(0)>(ya), tr_asm<yat(1)>(ya), tr_asm<yat(2)>(ya), tr_asm<yat(3)>(ya), tr_asm<yat(4)>(ya)};
#pragma unroll
      for (int j = 0; j < 5; ++j) asm volatile("ds_read_b64 %0, %1 offset:%2" : "=&v"(xv[j]) : "v"(lb + M_RING + cw * 24576 + 8192 + l15 * 160 + g4 * 8), "i"(j * 32) : "memory");
      signal1(flg + 64);
      wait_lg<0>(xv[0], xv[1], xv[2], xv[3], xv[4], yr[0], yr[1], yr[2], yr[3], yr[4]);
#pragma unroll
      for (int j = 0; j < 5; ++j) {
        const u32x2 yw = __builtin_bit_cast(u32x2, yr[j]);
#pragma unroll
        for (int h = 0; h < 2; ++h) {
          sv[j][h] = ((unpk(xv[j][h]) + (f32x2){nm, nm}) * (f32x2){sr, sr}) * unpk(yw[h]);
          pin(sv[j][h]);
        }
      }
      SB();
      AB(16);
      ldW(NWY, 15);
    });
    unsigned pk[5][2];
#pragma unroll
    for (int j = 0; j < 5; ++j) {
#pragma unroll
      for (int h = 0; h < 2; ++h) {
        const f32x2 o2 = {ac3[j][2 * h], ac3[j][2 * h + 1]};
        const f32x2 a = o2 * (f32x2){-1.4426950f, -1.4426950f};
        const f32x2 e = (f32x2){__builtin_amdgcn_exp2f(a[0]), __builtin_amdgcn_exp2f(a[1])} + (f32x2){1.0f, 1.0f};
        const f32x2 r = {__builtin_amdgcn_rcpf(e[0]), __builtin_amdgcn_rcpf(e[1])};
        pk[j][h] = pk2(__builtin_elementwise_fma(o2 * sv[j][h], r, unpk(xv[j][h])));
      }
      if (j & 1) {
        const auto s0 = __builtin_amdgcn_permlane16_swap(pk[j - 1][0], pk[j][0], false, false);
        const auto s1 = __builtin_amdgcn_permlane16_swap(pk[j - 1][1], pk[j][1], false, false);
        asm volatile("global_store_dwordx4 %0, %1, %2 offset:%3\n\ts_nop 1" :: "v"(oo[0]), "v"((u32x4){s0[0], s1[0], s0[1], s1[1]}), "s"(op), "i"(32 * (j - 1)) : "memory");
        SB();
      }
    }
    asm volatile("global_store_dwordx2 %0, %1, %2 offset:128" :: "v"(oo[1]), "v"((u32x2){pk[4][0], pk[4][1]}), "s"(op) : "memory");
  } else {
    ldsw();
    sfor<0, pos('B')>([&](auto ic) { if constexpr (SCH[ic] == 'g') issue(IC(cnt('u', ic))); });
    if (tid < 64) *(unsigned*)(smem + L_FLAG + tid * 4) = 0u;
    pin(ones);
    AB(1);
    barrier0(behind(0, pos('B'), 'B'));
    kvld();
    kvm(0, 4);
    kvst();
    const bool isz2 = !(odd && wl);
    const char* stp = ws + WS_ST + (size_t)((rb * 12 + pr * 3 + odd * 2 + wl) * 1024 + wm * 512);
    if (isz2) st8(lin >> 1, stp, (u32x2){0u, ~(unsigned)tag});
    const char* Ab[4];
#pragma unroll
    for (int i = 0; i < 4; ++i) Ab[i] = (const char*)w_sp + (size_t)(t0 + ((2 * wl + 2 + i) & 3) * 16) * (S_ * 2) + wave * 1024 + 4096 - i * 2048;
    const unsigned ringl = lb + M_RING + wave * 24576;
    unsigned av[2], xl = ringl + 16384 + 4096;
    unsigned bsv[2];
    auto preact = [&]() {
      flag_wait_asm(flg + 20 + wl * 4, 2u);
      const float* cp2 = (const float*)(smem + L_CS2 + (wl * 64 + l15) * 4);
      pin(ac2[0][0], ac2[0][1], ac2[1][0], ac2[1][1]);
#pragma unroll
      for (int jj = 0; jj < 2; ++jj) {
        const float nct = -add1(add1(cp2[jj * 16], cp2[32 + jj * 16]), add1(cp2[128 + jj * 16], cp2[160 + jj * 16]));
        u32x4 o;
#pragma unroll
        for (int q = 0; q < 4; ++q) {
          const int i = q >> 1, e = (q & 1) * 2;
          o[q] = pack2(mul1(fma1(xm[i][e], nct, ac2[i][jj][e]), xr[i][e]), mul1(fma1(xm[i][e + 1], nct, ac2[i][jj][e + 1]), xr[i][e + 1]));
        }
        z1put(odd * 32 + wl * 32 + jj * 16, o);
      }
      signal1(flg + 132);
      SB();
    };
    {
      f32x4 zv[2][4], cs[4];
      s16x4 WB[20][4][2];
      bf16x8 XG[10][2][2];
      unsigned aX[2][2], aW[2][2][4];
#pragma unroll
      for (int k = 0; k < 2; ++k)
#pragma unroll
        for (int w = 0; w < 2; ++w) {
          aX[w][k] = pX[k] + L_SLOT + XW * 8192 + w * 65536;
          pin(aX[w][k]);
#pragma unroll
          for (int jj = 0; jj < 4; ++jj) { aW[w][k][jj] = (pW[k] ^ (unsigned)(jj * 32)) + L_SLOT + wl * 8192 + w * 65536; pin(aW[w][k][jj]); }
        }
      auto rd1 = [&](auto nc, int r) {
        constexpr int n = decltype(nc)::value, j = n >> 1, k = n & 1, c = n > 0 && n < 11 ? n - 1 : 0, sx = (XSLOT[c] - XW) * 8192, sa = WSLOT[j] * 8192;
        if (r < 0) { if (r & 1) ds16<(sx & 0xFFFF) + 512>(XG[c][(r + 4) >> 1][1], aX[sx >> 16][(r + 4) >> 1]); else ds16<(sx & 0xFFFF)>(XG[c][(r + 4) >> 1][0], aX[sx >> 16][(r + 4) >> 1]); }
        else if (r & 1) WB[n][r >> 1][1] = tr_asm<(sa & 0xFFFF) + 512>(aW[sa >> 16][k][r >> 1]);
        else WB[n][r >> 1][0] = tr_asm<(sa & 0xFFFF)>(aW[sa >> 16][k][r >> 1]);
        SB();
      };
      auto bar = [&](auto bc) {
        SB();
        constexpr int p = decltype(bc)::value;
        CB(3 * (p - XBAR));
        if constexpr (p == 1) asm volatile("s_waitcnt vmcnt(%0) lgkmcnt(0)\n\ts_barrier" :: "n"(behind(0, pos('_', p - 1), '_')) : "memory");
        else if constexpr (p <= GZ) asm volatile("s_waitcnt vmcnt(%0) lgkmcnt(8)\n\ts_barrier" :: "n"(behind(0, pos('_', p - 1), '_')) : "memory");
        else if constexpr (p >= 8) asm volatile("s_waitcnt lgkmcnt(0)\n\ts_barrier" ::: "memory");
        else if constexpr (p < 7) asm volatile("s_waitcnt lgkmcnt(8)\n\ts_barrier" ::: "memory");
        else asm volatile("s_barrier" ::: "memory");
        CB(3 * (p - XBAR) + 2);
        SB();
      };
      float cp[4];
      f32x4 st[2][2], rv;
      const unsigned rwa = lb + L_RAW + (ksb * 2 + wm) * 256 + l15 * 16 + ((l15 >> 2) == g4 ? 0 : 1024);
      const unsigned csa = lb + L_CS + (wave * 64 + lane) * 4;
      auto kstep = [&](auto nc) {
        constexpr int n = decltype(nc)::value, j = n >> 1, k = n & 1, nn = n + 1, r0 = nn >= 2 && nn <= 10 ? -4 : 0;
        constexpr int sc = n == 1 ? 0 : n - 2, ns = !k ? 0 : (n == 1 || n == 11 ? 1 : 2);
        if constexpr (k) bar(IC(nn / 2 + 1));
        int r = r0, c = 0;
        auto nxt = [&]() { if (r < 8) { rd1(IC(nn), r++); ++c; } };
        auto sel = [&](int q) {
          const f32x4& sq = st[q & 1][q >> 1];
          rv[q] = sel4(sq[0], sq[1], sq[2], sq[3], 0xAAAAAAAAAAAAAAAAull, 0xCCCCCCCCCCCCCCCCull);
          SB();
        };
#pragma unroll
        for (int jj = 0; jj < 4; ++jj) {
          const bf16x8 wf = join8(WB[n][jj][0], WB[n][jj][1]);
          MFMAW<n == 0>(jj == 0 ? 4 : (jj == 2 ? c : -1), zv[0][jj], XG[j][k][0], wf);
          SB();
          nxt();
          if (n == 12) nxt();
          if (n == 12 && jj == 2) sel(2);
          MFMAW<n == 0>(-1, zv[1][jj], XG[j][k][1], wf);
          SB();
          nxt();
          if (n == 12) nxt();
          if (n == 12 && (jj == 1 || jj == 2)) sel(jj == 1 ? 0 : 3);
          if (n == 12 && jj == 2) { dsw16<0>(rwa, rv); SB(); }
          if (k == 0) {
            MFMAW<n == 0>(-1, cs[jj], ones, wf);
            SB();
            nxt();
            if (n == 12) nxt();
            if (n == 12 && jj == 1) sel(1);
          }
#pragma unroll
          for (int q = 0; q < ns; ++q) {
            const bf16x8& xs = XG[sc + q][1][jj >> 1];
            MFMAW<n == 1>(-1, st[jj & 1][jj >> 1], xs, (jj & 1) ? xs : ones);
            SB();
            nxt();
          }
        }
      };
      auto lstep = [&](auto nc, auto ac, auto bc, int w) {
        constexpr int n = decltype(nc)::value, j = n >> 1, k = n & 1, na = decltype(ac)::value, nb = decltype(bc)::value;
        int r = 0;
        auto nxt = [&]() { if (na > 0 && r < 16) { if (r < 8) rd1(ac, r); else rd1(bc, r - 8); ++r; } };
#pragma unroll
        for (int jj = 0; jj < 4; ++jj) {
          const bf16x8 wf = join8(WB[n][jj][0], WB[n][jj][1]);
          if (k == 0) { MFMAW<false>(jj == 0 ? w : -1, cs[jj], ones, wf); SB(); nxt(); nxt(); }
          MFMAW<false>(jj == 0 && k ? w : -1, zv[0][jj], XG[j][k][0], wf);
          SB();
          nxt();
          if (k) nxt();
          MFMAW<false>(-1, zv[1][jj], XG[j][k][1], wf);
          SB();
          nxt();
          if (k) nxt();
          if (n == 15 && jj == 2) {
            dsw4(csa, sel4(cs[0][0], cs[1][0], cs[2][0], cs[3][0], 0xFFFF0000FFFF0000ull, 0xFFFFFFFF00000000ull));
            SB();
          }
        }
      };
      bar(IC(1));
      AB(2);
#pragma unroll
      for (int r = -4; r < 8; ++r) rd1(IC(0), r);
      kstep(IC(0)); AB(4); kstep(IC(1)); kstep(IC(2)); kstep(IC(3)); kstep(IC(4)); kstep(IC(5)); kstep(IC(6)); kstep(IC(7)); kstep(IC(8)); AB(5); kstep(IC(9));
      kstep(IC(10)); kstep(IC(11)); kstep(IC(12));
      bar(IC(8));
      lstep(IC(13), IC(14), IC(16), -1);
      lstep(IC(14), IC(18), IC(15), 8);
      lstep(IC(16), IC(17), IC(19), 15);
      lstep(IC(18), IC(0), IC(0), 15);
      lstep(IC(15), IC(0), IC(0), -1);
      lstep(IC(17), IC(0), IC(0), 9);
      bar(IC(9));
      __builtin_amdgcn_s_setprio(2);
#pragma unroll
      for (int q = 0; q < 4; ++q) cp[q] = *(const float*)(smem + L_CS + ((wave ^ 1) * 64 + q * 16 + l15) * 4);
      ldxs();
      SB();
      lstep(IC(19), IC(0), IC(0), -1);
      AB(6);
      float ct[4];
#pragma unroll
      for (int jj = 0; jj < 4; ++jj) ct[jj] = cs[jj][0] + cp[jj];
#pragma unroll
      for (int jj = 0; jj < 4; ++jj) {
        if (jj == 0) asm volatile("s_nop 3");
        if (jj == 2) SB();
        if (!(jj & 1)) pin(zv[0][jj], zv[1][jj], zv[0][jj + 1], zv[1][jj + 1]);
#pragma unroll
        for (int i = 0; i < 2; ++i) zv[i][jj] = act(zv[i][jj], ct[jj], i);
      }
      if (isz2) {
        f32x4 pv[2], s1[2], s2[2];
#pragma unroll
        for (int i = 0; i < 2; ++i) {
#pragma unroll
          for (int e = 0; e < 4; ++e) {
            const float z0 = zv[i][0][e];
            pv[i][e] = __int_as_float(__builtin_amdgcn_update_dpp(0, __float_as_int(z0), 0x150, 0xF, 0xF, true));
          }
          const f32x4 a0 = zv[i][0] - pv[i], a1 = zv[i][1] - pv[i], a2 = zv[i][2] - pv[i], a3 = zv[i][3] - pv[i];
          s1[i] = (a0 + a1) + (a2 + a3);
          s2[i] = __builtin_elementwise_fma(a3, a3, __builtin_elementwise_fma(a2, a2, __builtin_elementwise_fma(a1, a1, a0 * a0)));
        }
        float r[8], q[4];
#define DA(d, s, ctl, bm) "v_add_f32_dpp " d ", " s ", " s " " ctl " row_mask:0xf bank_mask:" bm "\n\t"
        asm volatile("s_nop 1\n\t"
                     DA("%0", "%12", "row_ror:8", "0x3") DA("%1", "%13", "row_ror:8", "0x3") DA("%2", "%14", "row_ror:8", "0x3") DA("%3", "%15", "row_ror:8", "0x3")
                     DA("%4", "%16", "row_ror:8", "0x3") DA("%5", "%17", "row_ror:8", "0x3") DA("%6", "%18", "row_ror:8", "0x3") DA("%7", "%19", "row_ror:8", "0x3")
                     DA("%0", "%20", "row_ror:8", "0xc") DA("%1", "%21", "row_ror:8", "0xc") DA("%2", "%22", "row_ror:8", "0xc") DA("%3", "%23", "row_ror:8", "0xc")
                     DA("%4", "%24", "row_ror:8", "0xc") DA("%5", "%25", "row_ror:8", "0xc") DA("%6", "%26", "row_ror:8", "0xc") DA("%7", "%27", "row_ror:8", "0xc")
                     DA("%8", "%0", "row_half_mirror", "0x5") DA("%9", "%1", "row_half_mirror", "0x5") DA("%10", "%2", "row_half_mirror", "0x5") DA("%11", "%3", "row_half_mirror", "0x5")
                     DA("%8", "%4", "row_half_mirror", "0xa") DA("%9", "%5", "row_half_mirror", "0xa") DA("%10", "%6", "row_half_mirror", "0xa") DA("%11", "%7", "row_half_mirror", "0xa")
                     DA("%8", "%8", "quad_perm:[1,0,3,2]", "0xf") DA("%9", "%9", "quad_perm:[1,0,3,2]", "0xf") DA("%10", "%10", "quad_perm:[1,0,3,2]", "0xf") DA("%11", "%11", "quad_perm:[1,0,3,2]", "0xf")
                     DA("%8", "%8", "quad_perm:[2,3,0,1]", "0xf") DA("%9", "%9", "quad_perm:[2,3,0,1]", "0xf") DA("%10", "%10", "quad_perm:[2,3,0,1]", "0xf") DA("%11", "%11", "quad_perm:[2,3,0,1]", "0xf")
                     : "=&v"(r[0]), "=&v"(r[1]), "=&v"(r[2]), "=&v"(r[3]), "=&v"(r[4]), "=&v"(r[5]), "=&v"(r[6]), "=&v"(r[7]),
                       "=&v"(q[0]), "=&v"(q[1]), "=&v"(q[2]), "=&v"(q[3])
                     : "v"(s1[0][0]), "v"(s2[0][0]), "v"(s1[0][1]), "v"(s2[0][1]), "v"(s1[0][2]), "v"(s2[0][2]), "v"(s1[0][3]), "v"(s2[0][3]),
                       "v"(s1[1][0]), "v"(s2[1][0]), "v"(s1[1][1]), "v"(s2[1][1]), "v"(s1[1][2]), "v"(s2[1][2]), "v"(s1[1][3]), "v"(s2[1][3]));
#undef DA
        const f32x4 p4 = (lane & 8) ? pv[1] : pv[0];
        const float pa = (lane & 4) ? p4[2] : p4[0], pb = (lane & 4) ? p4[3] : p4[1];
        const float pp = (lane & 2) ? pb : pa, S1 = (lane & 2) ? q[2] : q[0], S2 = (lane & 2) ? q[3] : q[1];
        const float dl = S1 * (1.0f / 64.0f);
        st8(lin >> 1, stp, (u32x2){__float_as_uint((lane & 1) ? fmaxf(S2 - S1 * dl, 0.f) : pp + dl), (unsigned)tag});
      } else {
#pragma unroll
        for (int jj = 0; jj < 4; ++jj) {
          u32x4 o = pack8(zv[0][jj], zv[1][jj]);
          if (jj >= 2) z1put(jj * 16 - 32, o);
          else st16(lin, ws + WS_ZX + (size_t)((rb * 4 + pr) * 4 + wm * 2 + jj) * 1024, o);
        }
      }
      signal1(flg + 36);
      SB();
      AB(7);
      __builtin_amdgcn_s_setprio(P2P);
      unsigned po = isz2 ? (unsigned)((l15 >> 3) * 6144 + (g4 * 8 + c8) * 16) : 0u;
      const char* pb = ws + WS_ST + (size_t)rb * 12288 + wm * 512 + 2048;
      u32x4 sg[6];
      auto ldsg = [&]() {
#pragma unroll
        for (int c = 0; c < 6; ++c) asm volatile("global_load_dwordx4 %0, %1, %2 offset:%3 sc1" : "=&v"(sg[c]) : "v"(po), "s"(pb), "i"(c * 1024 - 2048) : "memory");
      };
      auto look = [&]() {
        wait_vm<0>();
        ldsg();
        asm volatile("global_load_ushort %0, %2, %3\n\tglobal_load_ushort %1, %2, %3 offset:32" : "=&v"(bsv[0]), "=&v"(bsv[1]) : "v"((unsigned)(l15 * 2)), "s"(b_sp + t0 + wl * 32) : "memory");
        if (!isz2) sttag(ws + WS_XF + (rb * 4 + pr) * 16 + wm * 8);
        SB();
      };
      flag_wait_asm(flg + 60, 4u);
      ds16<0>(ac2[0][0], p2a); ds16<1024>(ac2[1][0], p2a); ds16<2048>(ac2[0][1], p2a); ds16<3072>(ac2[1][1], p2a);
      sfor<0, 4>([&](auto nc) { rdV(IC(1), nc, 0); rdV(IC(1), nc, 1); rdV(IC(1), nc, 2); rdV(IC(1), nc, 3); });
      sfor<0, 10>([&](auto nc) {
        constexpr int n = decltype(nc)::value;
        mm2(IC(1), nc, XG + 5, n == 0 ? 12 : (n < 6 ? 15 : 4 * (9 - n)), [&](int r) {
          SB();
          if constexpr (n < 3) { if (r < 2) { rdV(IC(1), IC(4 + 2 * n), 2 * r); rdV(IC(1), IC(4 + 2 * n), 2 * r + 1); } else { rdV(IC(1), IC(5 + 2 * n), 2 * r - 4); rdV(IC(1), IC(5 + 2 * n), 2 * r - 3); } }
        });
        if constexpr (n == 2) signal1(flg + 16);
        if constexpr (n == PN) look();
      });
      signal1(flg + 20 + wl * 4);
      AB(3);
      if (PAE == 2) preact();
      if (PN > 9) look();
      AB(8);
      if (PAE == 3) preact();
      wait_vm<2>(sg[0], sg[1], sg[2], sg[3], sg[4], sg[5]);
      if (isz2) {
        const char* zb = ws + WS_Z2 + (size_t)((rb * 2 + wm) * 48 + pr * 12 + odd * 8 + wl * 4) * 1024;
        auto bad = [&]() {
          unsigned an = sg[0][1] & sg[0][3], on = sg[0][1] | sg[0][3];
#pragma unroll
          for (int c = 1; c < 6; ++c) { an &= sg[c][1] & sg[c][3]; on |= sg[c][1] | sg[c][3]; }
          return __builtin_amdgcn_ballot_w64(((an ^ (unsigned)tag) | (on ^ (unsigned)tag)) != 0) != 0;
        };
        if (__builtin_expect(bad(), 0)) {
          do {
            --tmo;
            reld6(sg, po, pb);
            wait_vm<0>(sg[0], sg[1], sg[2], sg[3], sg[4], sg[5]);
          } while (bad() && tmo > 0);
        }
        AB(9);
        auto f = [&](int c, int e) { return __uint_as_float(sg[c][e]); };
        auto sum6 = [&](int e) { return add1(add1(add1(f(0, e), f(1, e)), add1(f(2, e), f(3, e))), add1(f(4, e), f(5, e))); };
        auto both = [&](float v) { float r; asm("s_nop 1\n\tv_add_f32_dpp %0, %1, %1 row_ror:8 row_mask:0xf bank_mask:0xf" : "=v"(r) : "v"(v)); return r; };
        const float nm = both(sum6(0)) * (-1.0f / 12.0f);
        float qa = sum6(2) * (1.0f / 64.0f), qb;
#pragma unroll
        for (int c = 0; c < 6; c += 2) { const float u = add1(f(c, 0), nm), v = add1(f(c + 1, 0), nm); qa = fma1(u, u, qa); qb = c ? fma1(v, v, qb) : v * v; }
        const float vbf = rbf(both(add1(qa, qb)) * (1.0f / 12.0f));
        const float rz = __builtin_amdgcn_rcpf(rbf(__builtin_amdgcn_sqrtf(rbf(vbf + 1e-5f)))), mz = rbf(nm) * rz;
        float zm[2][4], zr[2][4];
        sfor<0, 8>([&](auto qc) {
          constexpr int q = decltype(qc)::value;
          zr[q >> 2][q & 3] = __int_as_float(__builtin_amdgcn_update_dpp(0, __float_as_int(rz), 0x150 + q, 0xF, 0xF, true));
          zm[q >> 2][q & 3] = __int_as_float(__builtin_amdgcn_update_dpp(0, __float_as_int(mz), 0x150 + q, 0xF, 0xF, true));
        });
#pragma unroll
        for (int jj = 0; jj < 4; ++jj) {
          u32x4 o;
#pragma unroll
          for (int q = 0; q < 4; ++q) {
            const int i = q >> 1, e = (q & 1) * 2;
            o[q] = pack2(fma1(zv[i][jj][e], zr[i][e], zm[i][e]), fma1(zv[i][jj][e + 1], zr[i][e + 1], zm[i][e + 1]));
          }
          o[3] = nohole(o[3]);
          asm volatile("global_store_dwordx4 %0, %1, %2 offset:%3 sc1\n\ts_nop 1" :: "v"(lin), "v"(o), "s"(zb), "i"(jj * 1024) : "memory");
          SB();
        }
      }
      SB();
      AB(10);
      ldtag(ef, (unsigned)((lane & 31) * 8), ws + WS_EF + 2048 + cb * 256);
    }
    const char* Bs[2] = {ws + WS_Z2 + (size_t)(wl * 32 * 48 + cb * 6 + wm * 3) * 1024, ws + WS_Z2 + (size_t)((wl * 32 + 16) * 48 + cb * 6 + wm * 3) * 1024};
    bf16x8 Bf[3][6];
    unsigned bv[2], bo;
    auto ldB1 = [&](int s, int j) {
      asm volatile("global_load_dwordx4 %0, %1, %2 offset:%3 " HSC : "=&v"(Bf[s % 3][j]) : "v"(bv[s & 1]), "s"(Bs[j / 3]), "i"((j % 3) * 1024) : "memory");
    };
    auto nxb = [&](int s, int k) {
      if (k == 0) asm volatile("s_add_u32 %0, %0, 0xc000\n\ts_cmp_eq_u32 %0, 0xc0000\n\ts_cselect_b32 %0, 0, %0" : "+s"(bo) :: "scc");
      else asm volatile("v_or_b32 %0, %1, %2" : "=v"(bv[s & 1]) : "s"(bo), "v"(lin));
    };
    f32x4 acc[4][3];
    unsigned ara[2][2];
#pragma unroll
    for (int ks = 0; ks < 2; ++ks) {
      ara[0][ks] = lb + M_RING + wl * 49152 + l15 * 128 + (((ks * 4 + g4) ^ (l15 & 7)) << 4);
      ara[1][ks] = ara[0][ks] + (M_CV - M_RING) + wl * (2 * CV_BUF - 49152);
    }
    u32x4 af[3][8];
    auto rdA = [&](int s, int i) {
      const int q = (0x20132012 >> (4 * (s >> 1))) & 3;
      asm volatile("ds_read_b128 %0, %1 offset:%2" : "=&v"(af[s == 15 ? 2 : s & 1][i]) : "v"(ara[q == 3][s & 1]), "i"((q % 3) * 8192 + (i & 3) * 2048 + (i >> 2) * (q == 3 ? CV_BUF : 24576)) : "memory");
    };
    bo = (unsigned)(rot2 * 2 * 49152);
    asm volatile("" : "+s"(xl), "+s"(Ab[0]), "+s"(Ab[1]), "+s"(Ab[2]), "+s"(Ab[3]));
    auto adA = [&](int d) {
#pragma unroll
      for (int q = 0; q < 2; ++q) av[q] = opq(aoff[q] - q * 1024 + (unsigned)(((d + rot2) & 7) * 128));
    };
    auto ldA1 = [&](int a) { asm volatile("global_load_lds_dwordx4 %0, %1 offset:%2" :: "v"(av[a & 1]), "s"(Ab[a >> 1]), "i"(a * 1024 - 4096) : "memory"); };
    adA(3);
    flag_wait_asm(flg + 32, 1u);
#pragma unroll
    for (int i = 0; i < 16; ++i) rdA(i >> 3, i & 7);
    signal1(flg + 28);
    asm volatile("s_mov_b32 m0, %0" :: "s"(xl));
    nxb(0, 1); nxb(1, 0); nxb(1, 1);
    if (PAE == 1) preact();
    flag_wait_asm(flg + 28, 4u);
#pragma unroll
    for (int i = 0; i < 16; ++i) pin(af[i >> 3][i & 7]);
#pragma unroll
    for (int a = 0; a < 8 - T3L; ++a) ldA1(a);
    unsigned fv, fw = flg + wave * 4, fp = flg + wl * 4, cv = 1u;
    unsigned fo = flg, fx = flg + (wave ^ 2) * 4, xa = lb + M_CV + wave * CV_BUF + lin, xb = lb + M_CV + (wave ^ 2) * CV_BUF + lin, za[2];
    unsigned eo = (unsigned)((g4 & 1) * 24576 + (l15 + 16 * (g4 >> 1)) * 16);
    const char* hp = hf + (size_t)((rb * 4 + wl * 2) * 24 + cb * 3) * 1024 + wm * 1536;
#pragma unroll
    for (int q = 0; q < 2; ++q) {
      const int fr = 4 * g4 + (l15 >> 2), p = lane & 3;
      za[q] = lb + L_Z1 + wm * 6144 + fr * 128 + (((wl * 4 + q * 2 + (p >> 1)) ^ (fr & 7)) << 4) + (p & 1) * 8;
    }
    pin(fw, fp, fo, fx, xa, xb, za[0], za[1], eo);
    asm volatile("" : "+s"(hp));
    AB(11);
    wait_vm<8 - T3L>(ef, bsv[0], bsv[1]);
    waittag(ef, (unsigned)((lane & 31) * 8), ws + WS_EF + 2048 + cb * 256);
    AB(12); SS(1 - 3 * XS);
#pragma unroll
    for (int j = 0; j < 6; ++j) ldB1(0, j);
#pragma unroll
    for (int j = 0; j < 6; ++j) ldB1(1, j);
#pragma unroll
    for (int a = 8 - T3L; a < 8; ++a) ldA1(a);
    nxb(2, 0); nxb(2, 1); nxb(3, 0); nxb(3, 1);
    SS(2 - 3 * XS);
    if (!PAE) preact();
    __builtin_amdgcn_s_setprio(0);
    f32x4 bs4[2] = {{lo16(bsv[0]), lo16(bsv[0]), lo16(bsv[0]), lo16(bsv[0])}, {lo16(bsv[1]), lo16(bsv[1]), lo16(bsv[1]), lo16(bsv[1])}};
    pin(bs4[0], bs4[1]);
#define CK0 HIREG("h0", "%0") HIREG("h1", "%1") HIREG("h2", "%2") HIREG("h3", "%3") HIREG("h4", "%4") HIREG("h5", "%5") "s_waitcnt vmcnt(%[nw])\n\tv_max3_u32 %6, v[h0], v[h1], v[h2]\n\t"
#define CK1 "v_max3_u32 %6, %6, v[h3], v[h4]\n\tv_max_u32 %6, %6, v[h5]\n\t"
#define CK2 "v_cmp_eq_u32 vcc, -1, %6\n\ts_waitcnt lgkmcnt(0)\n\t"
#define CKA(d) "s_add_u32 vcc_lo, %[r2], %[st] + " d "\n\ts_and_b32 vcc_lo, vcc_lo, 15\n\ts_mul_i32 vcc_lo, vcc_lo, 0xc000\n\tv_or_b32 %6, vcc_lo, %[li]\n\t"
#define CKL(a, b, c, d, e, f) "global_load_dwordx4 " a ", %6, %[bs] sc1\n\tglobal_load_dwordx4 " b ", %6, %[bs] offset:1024 sc1\n\tglobal_load_dwordx4 " c ", %6, %[bs] offset:2048 sc1\n\t" \
            "global_load_dwordx4 " d ", %6, %[bt] sc1\n\tglobal_load_dwordx4 " e ", %6, %[bt] offset:1024 sc1\n\tglobal_load_dwordx4 " f ", %6, %[bt] offset:2048 sc1\n\t"
#define CK3X(x) "s_cbranch_vccnz .Lck_%=\n.Lcb_%=:\n\t.subsection 1\n.Lck_%=:\n\ts_waitcnt vmcnt(0)\n\ts_sleep 2\n\t" CKA("0") CKL("%0", "%1", "%2", "%3", "%4", "%5") x \
            "s_waitcnt vmcnt(0)\n\tv_max3_u32 %6, v[h0], v[h1], v[h2]\n\t" CK1 "v_cmp_eq_u32 vcc, -1, %6\n\t" \
            "s_sub_i32 %7, %7, 1\n\ts_cmp_lt_i32 %7, 1\n\ts_cbranch_scc1 .Lcb_%=\n\ts_cbranch_vccnz .Lck_%=\n\ts_branch .Lcb_%=\n\t.subsection 0"
#define CK3 CK3X("")
#define CK4 CK3X(CKA("1") CKL("%[n0]", "%[n1]", "%[n2]", "%[n3]", "%[n4]", "%[n5]"))
#define CKN(sl) [n0] "+v"(Bf[sl][0]), [n1] "+v"(Bf[sl][1]), [n2] "+v"(Bf[sl][2]), [n3] "+v"(Bf[sl][3]), [n4] "+v"(Bf[sl][4]), [n5] "+v"(Bf[sl][5])
#define CKO(sl) "+v"(Bf[sl][0]), "+v"(Bf[sl][1]), "+v"(Bf[sl][2]), "+v"(Bf[sl][3]), "+v"(Bf[sl][4]), "+v"(Bf[sl][5]), "=&v"(mx), "+s"(tmo)
#define CKI(s_, nw_) [r2] "s"(2 * rot2), [st] "i"(s_), [li] "v"(lin), [bs] "s"(Bs[0]), [bt] "s"(Bs[1]), [nw] "i"(nw_)
    unsigned mx;
    asm volatile(CK0 CK1 CK2 CK4 : CKO(0), CKN(1) : CKI(0, 6 + T3L) : "memory", "vcc", "scc");
    auto mm = [&](int s, int i, int j) {
      if (s == 0 && j < 3 && i >= 2) asm volatile(MF "%0, %1, %2, %3" : "=&v"(acc[i][j]) : "v"(Bf[0][j]), "v"(af[0][i]), "v"(bs4[i & 1]));
      else if (s == 0 && j < 3) MFMA0(acc[i][j], Bf[0][j], af[0][i]);
      else MFMA(acc[i][j % 3], Bf[s % 3][j], af[s == 15 ? 2 : s & 1][(j / 3) * 4 + i]);
      SB();
    };
    auto mmf = [&](int s, int i, int j, unsigned a, int off, int tg) {
      unsigned sv;
      asm volatile(MF "%0, %3, %4, %0\n\tv_readfirstlane_b32 %2, %1\n\ts_cmp_lt_u32 %2, %7\n\ts_cbranch_scc1 .Lfk_%=\n.Lfb_%=:\n\t.subsection 1\n.Lfk_%=:\n\t"
                   "ds_read_b32 %1, %5 offset:%6\n\ts_waitcnt lgkmcnt(0)\n\tv_readfirstlane_b32 %2, %1\n\ts_cmp_lt_u32 %2, %7\n\ts_cbranch_scc1 .Lfk_%=\n\ts_branch .Lfb_%=\n\t.subsection 0"
                   : "+v"(acc[i][j % 3]), "+v"(fv), "=&s"(sv) : "v"(Bf[s % 3][j]), "v"(af[s == 15 ? 2 : s & 1][(j / 3) * 4 + i]), "v"(a), "i"(off), "i"(tg) : "memory", "scc");
      SB();
    };
    auto step = [&](auto sc) {
      constexpr int s = decltype(sc)::value;
      constexpr int NV = (s == 1 ? 1 : 6) + (s < 13 ? 5 : 0);
      constexpr int FO[8] = {224, 0, 232, 112, 120, 144, 152, 0};
      SS(3 + s);
      SB();
#pragma unroll
      for (int j = 0; j < 5; ++j) {
        auto& B = Bf[s % 3][j];
        if (j == 4 && s > 0 && s < 12)
          asm volatile(MF "%0, %2, %3, %0\n\ts_add_u32 %1, %1, 0xc000\n\ts_cmp_eq_u32 %1, 0xc0000\n\ts_cselect_b32 %1, 0, %1" : "+v"(acc[0][1]), "+s"(bo) : "v"(B), "v"(af[s & 1][4]) : "scc");
        else if (j == 0 && (s & 1) && FO[s >> 1]) mmf(s, 0, 0, fp, FO[s >> 1], 2);
        else mm(s, 0, j);
        if (s == 0) ldB1(2, j);
        if (s == 13 && j > 0) rdA(15, 2 * j - 1);
        SB();
        if (j == 4 && s > 0 && s < 12) asm volatile(MF "%0, %2, %3, %0\n\tv_or_b32 %1, %4, %5" : "+v"(acc[1][1]), "=&v"(bv[s & 1]) : "v"(B), "v"(af[s & 1][5]), "s"(bo), "v"(lin));
        else mm(s, 1, j);
        if (j < 4 && s > 0) rdA(s + 1, 2 * j);
        SB();
        mm(s, 2, j);
        if (j < 4 && s > 0) rdA(s + 1, 2 * j + 1);
        if (j == 4 && (s == 2 || s == 4 || s == 6 || s == 8)) asm volatile("ds_write_b32 %1, %0 offset:96\n\tv_add_u32 %0, 1, %0" : "+v"(cv) : "v"(fw) : "memory");
        if (j == 4 && !(s & 1) && FO[s >> 1]) asm volatile("ds_read_b32 %0, %1 offset:%2" : "=&v"(fv) : "v"(fp), "i"(FO[s >> 1]) : "memory");
        if (j == 4 && s == 3) signal1(fp + 232);
        if (j == 4 && s == SGS) signal1(flg + 44);
        SB();
        mm(s, 3, j);
        if (s + 3 < 16) ldB1(s + 3, j);
        else if (j < 4) rdA(15, 2 * j);
        else asm volatile("ds_read_b32 %0, %1 offset:56" : "=&v"(fv) : "v"(fo) : "memory");
        SB();
      }
      constexpr int sn = (s + 1) % 3;
      if constexpr (s == 0) {
        mm(0, 0, 5); ldB1(2, 5); mm(0, 1, 5); nxb(4, 0); mm(0, 2, 5); nxb(4, 1); mm(0, 3, 5); ldB1(3, 5);
        asm volatile(CK0 CK1 CK2 CK4 : CKO(1), CKN(2) : CKI(1, 12 + T3L) : "memory", "vcc", "scc");
      } else if constexpr (s < 13)
        asm volatile(MF "%8, %12, %19, %8\n\t" CK0 MF "%9, %12, %20, %9\n\t" CK1 MF "%10, %12, %21, %10\n\t" CK2 MF "%11, %12, %22, %11\n\t"
                     "global_load_dwordx4 %12, %23, %[bt] offset:2048 " HSC "\n\t" CK4
                     : CKO(sn), "+v"(acc[0][2]), "+v"(acc[1][2]), "+v"(acc[2][2]), "+v"(acc[3][2]), "+v"(Bf[s % 3][5]), CKN((s + 2) % 3)
                     : "v"(af[s & 1][4]), "v"(af[s & 1][5]), "v"(af[s & 1][6]), "v"(af[s & 1][7]), "v"(bv[~s & 1]), CKI(s + 1, NV) : "memory", "vcc", "scc");
      else
        asm volatile(MF "%8, %12, %13, %8\n\t" CK0 MF "%9, %12, %14, %9\n\t" CK1 MF "%10, %12, %15, %10\n\t" CK2 MF "%11, %12, %16, %11\n\t" CK3
                     : CKO(sn), "+v"(acc[0][2]), "+v"(acc[1][2]), "+v"(acc[2][2]), "+v"(acc[3][2])
                     : "v"(Bf[s % 3][5]), "v"(af[s & 1][4]), "v"(af[s & 1][5]), "v"(af[s & 1][6]), "v"(af[s & 1][7]), CKI(s + 1, NV) : "memory", "vcc", "scc");
#pragma unroll
      for (int i = 0; i < 8; ++i) pin(af[~s & 1][i]);
      SB();
    };
    step(IC(0)); AB(13); step(IC(1)); step(IC(2)); step(IC(3)); AB(14); step(IC(4)); step(IC(5)); step(IC(6)); step(IC(7));
    step(IC(8)); step(IC(9)); step(IC(10)); step(IC(11)); step(IC(12)); step(IC(13));
    SS(17);
#pragma unroll
    for (int i = 0; i < 8; ++i) pin(af[2][i]);
    s16x4 z1r[2][3];
    f32x2 zf[12];
    f32x4 pr_[2][3];
#pragma unroll
    for (int m = 0; m < 48; ++m) {
      const int i = m / 12, n = m % 12;
      if (m == 0) mmf(14, 0, 0, fo, 56, 5);
      else if (m == 2)
        asm volatile(MF "%8, %11, %15, %8\n\t" CK0 MF "%9, %12, %16, %9\n\t" CK1 MF "%10, %13, %16, %10\n\t" CK2 MF "%8, %14, %16, %8\n\t" CK3
                     : CKO(0), "+v"(acc[0][2]), "+v"(acc[0][0]), "+v"(acc[0][1])
                     : "v"(Bf[2][2]), "v"(Bf[2][3]), "v"(Bf[2][4]), "v"(Bf[2][5]), "v"(af[0][0]), "v"(af[0][4]), CKI(15, 0) : "memory", "vcc", "scc");
      else if (m == KC) { wait_lg<0>(fv); mmf(14 + n / 6, i, n % 6, fx, 0, 1); }
      else if (m < 2 || m > 5) mm(14 + n / 6, i, n % 6);
      if (m == 1) {
#pragma unroll
        for (int q = 0; q < 2; ++q) { z1r[q][0] = tr_asm<0>(za[q]); z1r[q][1] = tr_asm<2048>(za[q]); z1r[q][2] = tr_asm<4096>(za[q]); }
      }
      if (m == 5) pin(z1r[0][0], z1r[0][1], z1r[0][2], z1r[1][0], z1r[1][1], z1r[1][2]);
      const int u = m < 14 ? m - 6 : (m - 14) / 2 + 8;
      if (m >= 6 && (m < 14 || !(m & 1)) && u < 12) { zf[u] = unpk(__builtin_bit_cast(u32x2, z1r[u / 6][u / 2 % 3])[u & 1]); pin(zf[u]); }
      if ((i == 1 || i == 2) && n >= 2 && n < 8 && (n & 1)) asm volatile("ds_write_b128 %0, %1 offset:%2" :: "v"(xa), "v"(acc[i - 1][n / 2 - 1]), "i"((i * 3 + n / 2 - 4) * 1024) : "memory");
      if (m == 32) asm volatile("ds_write_b32 %1, %0" :: "v"(cv), "v"(fw) : "memory");
      if (m == KC - 4) asm volatile("ds_read_b32 %0, %1" : "=&v"(fv) : "v"(fx) : "memory");
      if (m >= KC && m < KC + 3)
#pragma unroll
        for (int q = 0; q < 2; ++q) asm volatile("ds_read_b128 %0, %1 offset:%2" : "=&v"(pr_[q][m - KC]) : "v"(xb), "i"((q * 3 + m - KC) * 1024) : "memory");
      SB();
    }
    SS(19);
    AB(15);
    wait_lg<0>(pr_[0][0], pr_[0][1], pr_[0][2], pr_[1][0], pr_[1][1], pr_[1][2]);
    AB(16);
    unsigned pk[2][3][2];
    auto hm = [&](int i, int j) {
      pin(acc[2 + i][j]);
      const f32x4 o = acc[2 + i][j] + pr_[i][j];
#pragma unroll
      for (int h = 0; h < 2; ++h) pk[i][j][h] = pk2(zf[i * 6 + j * 2 + h] * (f32x2){o[2 * h], o[2 * h + 1]});
    };
    hm(0, 0); hm(0, 1);
    SB();
#pragma unroll
    for (int j = 0; j < 3; ++j) {
      if (j == 2) hm(0, 2);
      hm(1, j);
      const auto s0 = __builtin_amdgcn_permlane16_swap(pk[0][j][0], pk[1][j][0], false, false);
      const auto s1 = __builtin_amdgcn_permlane16_swap(pk[0][j][1], pk[1][j][1], false, false);
      asm volatile("global_store_dwordx4 %0, %1, %2 offset:%3 sc1\n\ts_nop 1" :: "v"(eo), "v"((u32x4){s0[0], s1[0], s0[1], nohole(s1[1])}), "s"(hp), "i"(j * 512) : "memory");
      SB();
    }
    AB(17);
    ldW(0, 15);
    wait_vm<15>(ef);
    gemm3(IC(0), []() {});
    if (b == 0 && tid == 0) *(u64*)(ws + WS_EP) = tag;
  }
  AB(24);
  AB_OUT();
}

extern "C" void kernel_launch(void* const* d_in, const int* in_sizes, int n_in,
                              void* d_out, int out_size, void* d_ws, size_t ws_size,
                              hipStream_t stream) {
  (void)hipFuncSetAttribute((const void*)k_f, hipFuncAttributeMaxDynamicSharedMemorySize, M_LDS);
  k_f<<<256, 512, M_LDS, stream>>>((const u16*)d_in[0], (const u16*)d_in[1], (const u16*)d_in[2], (const u16*)d_in[3], (const u16*)d_in[4],
                                   (const u16*)d_in[5], (const u16*)d_in[7], (const u16*)d_in[8], (const u16*)d_in[9], (const u16*)d_in[10],
                                   (u16*)d_out, (char*)d_ws);
}
```
